```python
import torch

def _softmax(x, axis=-1):
    return torch.softmax(x, dim=axis)
def _gelu(x):
    return torch.nn.functional.gelu(x, approximate="tanh")
def _silu(x):
    return torch.nn.functional.silu(x)
def _sigmoid(x):
    return torch.sigmoid(x)
def _layernorm(x, axis=-1, eps=1e-5):
    m = x.mean(dim=axis, keepdim=True)
    v = x.var(dim=axis, keepdim=True, unbiased=False)
    return (x - m) / torch.sqrt(v + eps)
def _scatter_add(tgt, idx, src, axis=0):
    return tgt.clone().index_add_(axis, idx, src)
def _segment_sum(src, seg, n):
    out = torch.zeros((n,) + src.shape[1:], dtype=src.dtype)
    return out.index_add_(0, seg, src)
def _pad(x, pw):
    flat = tuple(v for pair in reversed(pw) for v in pair)
    return torch.nn.functional.pad(x, flat)

kernel_name = 'standalone__modern_conv_block_convnext__vision'

def setup_inputs():

    _w = (lambda a: (lambda t: t.to(torch.bfloat16) if t.is_floating_point() else t)(torch.as_tensor(a)))
    H = W = 224
    C = 192
    K = 3
    return {
        "x": _w((lambda *s: torch.randn(*s, dtype=torch.bfloat16) * 0.05)(1, H, W, C)),
        "dw": _w((lambda *s: torch.randn(*s, dtype=torch.bfloat16) * 0.05)(K, K, C)),
        "w1": _w((lambda *s: torch.randn(*s, dtype=torch.bfloat16) * 0.05)(C, 4 * C)),
        "b1": _w((lambda *s: torch.randn(*s, dtype=torch.bfloat16) * 0.2)(4 * C)),
        "w2": _w((lambda *s: torch.randn(*s, dtype=torch.bfloat16) * 0.05)(4 * C, C)),
        "b2": _w((lambda *s: torch.randn(*s, dtype=torch.bfloat16) * 0.4)(C)),
        "gamma": _w((lambda *s: torch.randn(*s, dtype=torch.bfloat16) * 0.05)(C)),
    }

def reference(x, dw, w1, b1, w2, b2, gamma):
    B, H, W, C = x.shape
    K = dw.shape[0]
    pad = K // 2
    xp = _pad(x, ((0,0),(pad,pad),(pad,pad),(0,0)))
    out = torch.zeros_like(x)
    for i in range(K):
        for j in range(K):
            out = out + xp[:, i:i+H, j:j+W, :] * dw[i, j][None, None, None, :]
    h = _layernorm(out, axis=-1)
    h = torch.einsum('bhwc,ce->bhwe', h, w1) + b1
    h = _gelu(h)
    h = torch.einsum('bhwe,ec->bhwc', h, w2) + b2
    return x + gamma[None, None, None, :] * h
```

```cpp
#include <hip/hip_runtime.h>
#include <hip/hip_bf16.h>
typedef __bf16 bf16x8 __attribute__((ext_vector_type(8)));
typedef float f32x16 __attribute__((ext_vector_type(16)));
typedef unsigned short u16;
constexpr int H = 224, W = 224, C = 192, E = 768;
__device__ __forceinline__ float bf2f(u16 v) { return __uint_as_float((unsigned)v << 16); }
__device__ __forceinline__ u16 f2bf(float f) { __bf16 b = (__bf16)f; return __builtin_bit_cast(u16, b); }

__global__ void k_conv_ln(const u16* __restrict__ x, const u16* __restrict__ dw, u16* __restrict__ a) {
  int wave = (blockIdx.x * blockDim.x + threadIdx.x) >> 6, lane = threadIdx.x & 63;
  if (wave >= H * W) return;
  int y = wave / W, xx = wave % W;
  float v[3];
  for (int j = 0; j < 3; ++j) {
    int c = lane + 64 * j; float s = 0;
    for (int dy = 0; dy < 3; ++dy) for (int dx = 0; dx < 3; ++dx) {
      int yy = y + dy - 1, xc = xx + dx - 1;
      if (yy >= 0 && yy < H && xc >= 0 && xc < W) s += bf2f(x[(yy * W + xc) * C + c]) * bf2f(dw[(dy * 3 + dx) * C + c]);
    }
    v[j] = s;
  }
  float m = v[0] + v[1] + v[2];
  for (int o = 32; o; o >>= 1) m += __shfl_xor(m, o);
  m *= (1.f / C);
  float q = 0; for (int j = 0; j < 3; ++j) { v[j] -= m; q += v[j] * v[j]; }
  for (int o = 32; o; o >>= 1) q += __shfl_xor(q, o);
  float r = rsqrtf(q * (1.f / C) + 1e-5f);
  for (int j = 0; j < 3; ++j) a[wave * C + lane + 64 * j] = f2bf(v[j] * r);
}

__global__ __launch_bounds__(64) void k_mlp(const u16* __restrict__ a, const u16* __restrict__ x, const u16* __restrict__ w1, const u16* __restrict__ b1,
                      const u16* __restrict__ w2, const u16* __restrict__ b2, const u16* __restrict__ gamma, u16* __restrict__ out) {
  int lane = threadIdx.x, n = lane & 31, h = lane >> 5;
  int px = blockIdx.x * 32 + n;
  bf16x8 B[12];
  for (int s = 0; s < 12; ++s) B[s] = *(const bf16x8*)(a + px * C + 16 * s + 8 * h);
  f32x16 O[6];
  for (int t = 0; t < 6; ++t) for (int r = 0; r < 16; ++r) O[t][r] = 0.f;
  for (int ch = 0; ch < 24; ++ch) {
    f32x16 Hh;
    for (int r = 0; r < 16; ++r) Hh[r] = bf2f(b1[32 * ch + (r & 3) + 8 * (r >> 2) + 4 * h]);
    for (int s = 0; s < 12; ++s) {
      bf16x8 A;
      for (int j = 0; j < 8; ++j) A[j] = __builtin_bit_cast(__bf16, w1[(16 * s + 8 * h + j) * E + 32 * ch + n]);
      Hh = __builtin_amdgcn_mfma_f32_32x32x16_bf16(A, B[s], Hh, 0, 0, 0);
    }
    bf16x8 G[2];
    for (int r = 0; r < 16; ++r) {
      float v = Hh[r];
      float u = 0.7978845608028654f * (v + 0.044715f * v * v * v);
      float g = v / (1.f + __expf(-2.f * u));
      G[r >> 3][r & 7] = (__bf16)g;
    }
    for (int t = 0; t < 6; ++t) for (int s = 0; s < 2; ++s) {
      bf16x8 A;
      for (int j = 0; j < 8; ++j) A[j] = __builtin_bit_cast(__bf16, w2[(32 * ch + 16 * s + 8 * (j >> 2) + 4 * h + (j & 3)) * C + 32 * t + n]);
      O[t] = __builtin_amdgcn_mfma_f32_32x32x16_bf16(A, G[s], O[t], 0, 0, 0);
    }
  }
  for (int t = 0; t < 6; ++t) for (int r = 0; r < 16; ++r) {
    int c = 32 * t + (r & 3) + 8 * (r >> 2) + 4 * h;
    out[px * C + c] = f2bf(bf2f(x[px * C + c]) + bf2f(gamma[c]) * (O[t][r] + bf2f(b2[c])));
  }
}

extern "C" void kernel_launch(void* const* d_in, const int* in_sizes, int n_in, void* d_out, int out_size, void* d_ws, size_t ws_size, hipStream_t stream) {
  const u16* x = (const u16*)d_in[0]; const u16* dw = (const u16*)d_in[1]; const u16* w1 = (const u16*)d_in[2]; const u16* b1 = (const u16*)d_in[3];
  const u16* w2 = (const u16*)d_in[4]; const u16* b2 = (const u16*)d_in[5]; const u16* g = (const u16*)d_in[6];
  u16* a = (u16*)d_ws;
  k_conv_ln<<<H * W / 4, 256, 0, stream>>>(x, dw, a);
  k_mlp<<<H * W / 32, 64, 0, stream>>>(a, x, w1, b1, w2, b2, g, (u16*)d_out);
}
```

```cpp
#include <hip/hip_runtime.h>
__global__ __launch_bounds__(256) void convnext_block_kernel(const void* x, const void* dw, const void* w1, const void* b1, const void* w2, const void* b2,
                                                             const void* gamma, void* out) {
  __shared__ __attribute__((aligned(16))) char lds[163840];
  asm volatile("" :: "v"(lds) : "memory");
  const unsigned wid = __builtin_amdgcn_readfirstlane(threadIdx.x >> 6);
  asm volatile(
    "s_load_dwordx16 s[84:99], %0, 0\n\t"
    "v_mbcnt_lo_u32_b32 v0, -1, 0\n\t"
    "s_lshr_b32 s59, %1, 3\n\t"
    "v_mbcnt_hi_u32_b32 v0, -1, v0\n\t"
    "s_getpc_b64 s[76:77]\n\t"
    "pc_%=:\n\t"
    "s_lshl2_add_u32 s62, s59, %2\n\t"
    "s_and_b32 s62, s62, 63\n\t"
    "s_lshl_b32 s62, s62, 11\n\t"
    "v_lshl_add_u32 v40, v0, 5, s62\n\t"
    "v_min_u32 v40, end_a_%=-pc_%=-4, v40\n\t"
    "global_load_dword v56, v40, s[76:77]\n\t"
    "s_mov_b32 s56, %2\n\t"
    "s_and_b32 s61, %1, 7\n\t"
    "s_cmp_ge_u32 s59, 14\n\t"
    "s_cbranch_scc1 small_%=\n\t"
    "s_mul_i32 s62, s61, 14\n\t"
    "s_add_u32 s62, s62, s59\n\t"
    "s_lshr_b32 s58, s62, 3\n\t"
    "s_lshl_b32 s58, s58, 4\n\t"
    "s_and_b32 s62, s62, 7\n\t"
    "s_mul_i32 s57, s62, 13\n\t"
    "s_sub_u32 s79, s56, 1\n\t"
    "s_mul_i32 s75, s56, 3\n\t"
    "s_min_u32 s62, s56, 1\n\t"
    "s_add_u32 s75, s75, s62\n\t"
    "s_mov_b32 s81, 1\n\t"
    "s_mov_b32 s82, 270\n\t"
    "s_mov_b32 s83, 34816\n\t"
    "s_branch front_%=\n\t"
    "small_%=:\n\t"
    "s_sub_u32 s62, s59, 14\n\t"
    "s_and_b32 s63, s61, 1\n\t"
    "s_sub_u32 s63, 18, s63\n\t"
    "s_cmp_ge_u32 s62, s63\n\t"
    "s_cbranch_scc0 go_%=\n\t"
    "s_endpgm\n\t"
    "go_%=:\n\t"
    "s_mul_i32 s63, s61, 35\n\t"
    "s_add_u32 s63, s63, 1\n\t"
    "s_lshr_b32 s63, s63, 1\n\t"
    "s_add_u32 s62, s62, s63\n\t"
    "s_mul_i32 s63, s62, 205\n\t"
    "s_lshr_b32 s63, s63, 11\n\t"
    "s_lshl_b32 s58, s63, 4\n\t"
    "s_mul_i32 s63, s63, 10\n\t"
    "s_sub_u32 s62, s62, s63\n\t"
    "s_mul_i32 s57, s62, 12\n\t"
    "s_add_u32 s57, s57, 104\n\t"
    "s_mov_b32 s79, s56\n\t"
    "s_mul_i32 s75, s56, 3\n\t"
    "s_mov_b32 s81, 0\n\t"
    "s_mov_b32 s82, 252\n\t"
    "s_sleep 15\n\t"
    "front_%=:\n\t"
    "v_lshlrev_b32 v3, 4, v0\n\t"
    "s_lshl_b32 s62, s56, 6\n\t"
    "v_add_u32 v57, s62, v0\n\t"
    "v_lshlrev_b32 v4, 1, v57\n\t"
    "v_min_u32 v6, 382, v4\n\t"
    "s_lshl_b32 s78, s56, 10\n\t"
    "v_lshrrev_b32 v7, 3, v57\n\t"
    "v_and_b32 v8, 7, v0\n\t"
    "v_mov_b32 v9, 1073741824\n\t"
    "s_mul_i32 s63, s57, 224\n\t"
    "s_add_u32 s63, s63, s58\n\t"
    "s_sub_u32 s63, s63, 225\n\t"
    "s_mul_i32 s63, s63, 384\n\t"
    "v_mul_u32_u24 v5, 384, v7\n\t"
    "v_add_u32 v5, s63, v5\n\t"
    "s_cmp_eq_u32 s58, 0\n\t"
    "s_cselect_b32 s64, 0, 99\n\t"
    "s_cmp_eq_u32 s58, 208\n\t"
    "s_cselect_b32 s64, 17, s64\n\t"
    "s_mov_b32 s63, 79104\n\t"
    "s_mov_b32 s70, 0\n\t"
    "s_mov_b32 s71, 128\n\t"
    "s_mov_b32 s72, 256\n\t"
    "s_mov_b32 s69, -18\n\t"
    "v_add_u32 v10, 0, v7\n\t"
    "v_mul_u32_u24 v11, 3641, v10\n\t"
    "v_lshrrev_b32 v11, 16, v11\n\t"
    "v_mad_i32_i24 v12, v11, s69, v10\n\t"
    "v_cmp_ne_u32 vcc, s64, v12\n\t"
    "v_bitop3_b32 v13, v12, 6, v8 bitop3:0x6a\n\t"
    "v_add_u32 v12, 0, v5\n\t"
    "v_mad_u32_u24 v12, v11, s63, v12\n\t"
    "v_lshl_add_u32 v12, v13, 4, v12\n\t"
    "v_cndmask_b32 v96, v9, v12, vcc\n\t"
    "v_add_u32 v10, 32, v7\n\t"
    "v_mul_u32_u24 v11, 3641, v10\n\t"
    "v_lshrrev_b32 v11, 16, v11\n\t"
    "v_mad_i32_i24 v12, v11, s69, v10\n\t"
    "v_cmp_ne_u32 vcc, s64, v12\n\t"
    "v_bitop3_b32 v13, v12, 6, v8 bitop3:0x6a\n\t"
    "v_add_u32 v12, 12288, v5\n\t"
    "v_mad_u32_u24 v12, v11, s63, v12\n\t"
    "v_lshl_add_u32 v12, v13, 4, v12\n\t"
    "v_cndmask_b32 v97, v9, v12, vcc\n\t"
    "s_waitcnt lgkmcnt(0)\n\t"
    "s_mov_b64 s[36:37], s[84:85]\n\t"
    "s_mov_b32 s38, 19267584\n\t"
    "s_mov_b32 s39, 0x20000\n\t"
    "s_add_u32 m0, s78, 24576\n\t"
    "s_nop 0\n\t"
    "buffer_load_dwordx4 v96, s[36:39], s70 offen lds\n\t"
    "s_add_u32 m0, s78, 28672\n\t"
    "s_nop 0\n\t"
    "buffer_load_dwordx4 v97, s[36:39], s70 offen lds\n\t"
    "s_add_u32 m0, s78, 158208\n\t"
    "s_mov_b64 s[52:53], s[86:87]\n\t"
    "s_mov_b32 s54, 3456\n\t"
    "s_mov_b32 s55, 0x20000\n\t"
    "buffer_load_dwordx4 v3, s[52:55], s78 offen lds\n\t"
    "global_load_ushort v48, v4, s[90:91]\n\t"
    "global_load_ushort v49, v4, s[90:91] offset:512\n\t"
    "global_load_ushort v50, v4, s[90:91] offset:1024\n\t"
    "global_load_ushort v51, v6, s[94:95]\n\t"
    "global_load_ushort v52, v6, s[96:97]\n\t"
    "s_add_u32 m0, s78, 32768\n\t"
    "v_add_u32 v10, 64, v7\n\t"
    "v_mul_u32_u24 v11, 3641, v10\n\t"
    "v_lshrrev_b32 v11, 16, v11\n\t"
    "v_mad_i32_i24 v12, v11, s69, v10\n\t"
    "v_cmp_ne_u32 vcc, s64, v12\n\t"
    "v_bitop3_b32 v13, v12, 6, v8 bitop3:0x6a\n\t"
    "v_add_u32 v12, 24576, v5\n\t"
    "v_mad_u32_u24 v12, v11, s63, v12\n\t"
    "v_lshl_add_u32 v12, v13, 4, v12\n\t"
    "v_cndmask_b32 v98, v9, v12, vcc\n\t"
    "buffer_load_dwordx4 v98, s[36:39], s70 offen lds\n\t"
    "s_add_u32 m0, s78, 36864\n\t"
    "v_add_u32 v10, 96, v7\n\t"
    "v_mul_u32_u24 v11, 3641, v10\n\t"
    "v_lshrrev_b32 v11, 16, v11\n\t"
    "v_mad_i32_i24 v12, v11, s69, v10\n\t"
    "v_cmp_ne_u32 vcc, s64, v12\n\t"
    "v_bitop3_b32 v13, v12, 6, v8 bitop3:0x6a\n\t"
    "v_add_u32 v12, 36864, v5\n\t"
    "v_mad_u32_u24 v12, v11, s63, v12\n\t"
    "v_lshl_add_u32 v12, v13, 4, v12\n\t"
    "v_cndmask_b32 v99, v9, v12, vcc\n\t"
    "buffer_load_dwordx4 v99, s[36:39], s70 offen lds\n\t"
    "s_add_u32 m0, s78, 40960\n\t"
    "v_add_u32 v10, 128, v7\n\t"
    "v_mul_u32_u24 v11, 3641, v10\n\t"
    "v_lshrrev_b32 v11, 16, v11\n\t"
    "v_mad_i32_i24 v12, v11, s69, v10\n\t"
    "v_cmp_ne_u32 vcc, s64, v12\n\t"
    "v_bitop3_b32 v13, v12, 6, v8 bitop3:0x6a\n\t"
    "v_add_u32 v12, 49152, v5\n\t"
    "v_mad_u32_u24 v12, v11, s63, v12\n\t"
    "v_lshl_add_u32 v12, v13, 4, v12\n\t"
    "v_cndmask_b32 v100, v9, v12, vcc\n\t"
    "buffer_load_dwordx4 v100, s[36:39], s70 offen lds\n\t"
    "s_add_u32 m0, s78, 45056\n\t"
    "v_add_u32 v10, 160, v7\n\t"
    "v_mul_u32_u24 v11, 3641, v10\n\t"
    "v_lshrrev_b32 v11, 16, v11\n\t"
    "v_mad_i32_i24 v12, v11, s69, v10\n\t"
    "v_cmp_ne_u32 vcc, s64, v12\n\t"
    "v_bitop3_b32 v13, v12, 6, v8 bitop3:0x6a\n\t"
    "v_add_u32 v12, 61440, v5\n\t"
    "v_mad_u32_u24 v12, v11, s63, v12\n\t"
    "v_lshl_add_u32 v12, v13, 4, v12\n\t"
    "v_cndmask_b32 v101, v9, v12, vcc\n\t"
    "buffer_load_dwordx4 v101, s[36:39], s70 offen lds\n\t"
    "s_add_u32 m0, s78, 49152\n\t"
    "v_add_u32 v10, 192, v7\n\t"
    "v_mul_u32_u24 v11, 3641, v10\n\t"
    "v_lshrrev_b32 v11, 16, v11\n\t"
    "v_mad_i32_i24 v12, v11, s69, v10\n\t"
    "v_cmp_ne_u32 vcc, s64, v12\n\t"
    "v_bitop3_b32 v13, v12, 6, v8 bitop3:0x6a\n\t"
    "v_add_u32 v12, 73728, v5\n\t"
    "v_mad_u32_u24 v12, v11, s63, v12\n\t"
    "v_lshl_add_u32 v12, v13, 4, v12\n\t"
    "v_cndmask_b32 v102, v9, v12, vcc\n\t"
    "buffer_load_dwordx4 v102, s[36:39], s70 offen lds\n\t"
    "s_add_u32 m0, s78, 53248\n\t"
    "v_add_u32 v10, 224, v7\n\t"
    "v_mul_u32_u24 v11, 3641, v10\n\t"
    "v_lshrrev_b32 v11, 16, v11\n\t"
    "v_mad_i32_i24 v12, v11, s69, v10\n\t"
    "v_cmp_ne_u32 vcc, s64, v12\n\t"
    "v_bitop3_b32 v13, v12, 6, v8 bitop3:0x6a\n\t"
    "v_add_u32 v12, 86016, v5\n\t"
    "v_mad_u32_u24 v12, v11, s63, v12\n\t"
    "v_lshl_add_u32 v12, v13, 4, v12\n\t"
    "v_cndmask_b32 v103, v9, v12, vcc\n\t"
    "v_cmp_gt_u32 vcc, s82, v10\n\t"
    "s_nop 1\n\t"
    "v_cndmask_b32 v103, v9, v103, vcc\n\t"
    "buffer_load_dwordx4 v103, s[36:39], s70 offen lds\n\t"
    "s_cmp_eq_u32 s81, 0\n\t"
    "s_cselect_b32 s61, 9, s56\n\t"
    "s_cmp_ge_u32 s61, 2\n\t"
    "s_cbranch_scc1 odd_x_%=\n\t"
    "v_add_u32 v10, 256, v7\n\t"
    "v_mul_u32_u24 v11, 3641, v10\n\t"
    "v_lshrrev_b32 v11, 16, v11\n\t"
    "v_mad_i32_i24 v12, v11, s69, v10\n\t"
    "v_cmp_ne_u32 vcc, s64, v12\n\t"
    "v_bitop3_b32 v13, v12, 6, v8 bitop3:0x6a\n\t"
    "v_add_u32 v12, 98304, v5\n\t"
    "v_mad_u32_u24 v12, v11, s63, v12\n\t"
    "v_lshl_add_u32 v12, v13, 4, v12\n\t"
    "v_cndmask_b32 v104, v9, v12, vcc\n\t"
    "v_cmp_gt_u32 vcc, s82, v10\n\t"
    "s_nop 1\n\t"
    "v_cndmask_b32 v104, v9, v104, vcc\n\t"
    "s_add_u32 s61, s78, 57344\n\t"
    "s_mov_b32 m0, s61\n\t"
    "s_add_u32 s61, s61, s83\n\t"
    "buffer_load_dwordx4 v104, s[36:39], s70 offen lds\n\t"
    "s_mov_b32 m0, s61\n\t"
    "s_add_u32 s61, s61, s83\n\t"
    "buffer_load_dwordx4 v104, s[36:39], s71 offen lds\n\t"
    "s_mov_b32 m0, s61\n\t"
    "s_add_u32 s61, s61, s83\n\t"
    "buffer_load_dwordx4 v104, s[36:39], s72 offen lds\n\t"
    "odd_x_%=:\n\t"
    "s_mul_i32 s61, s81, s56\n\t"
    "s_cmp_eq_u32 s61, 0\n\t"
    "s_cbranch_scc1 odd_y_%=\n\t"
    "s_mul_i32 s61, s79, 32\n\t"
    "s_lshl_b32 s62, s56, 3\n\t"
    "s_sub_u32 s61, s61, s62\n\t"
    "v_add_u32 v7, s61, v7\n\t"
    "s_mul_i32 s61, s61, 384\n\t"
    "v_add_u32 v5, s61, v5\n\t"
    "s_lshl_b32 s76, s79, 12\n\t"
    "v_add_u32 v10, 0, v7\n\t"
    "v_mul_u32_u24 v11, 3641, v10\n\t"
    "v_lshrrev_b32 v11, 16, v11\n\t"
    "v_mad_i32_i24 v12, v11, s69, v10\n\t"
    "v_cmp_ne_u32 vcc, s64, v12\n\t"
    "v_bitop3_b32 v13, v12, 6, v8 bitop3:0x6a\n\t"
    "v_add_u32 v12, 0, v5\n\t"
    "v_mad_u32_u24 v12, v11, s63, v12\n\t"
    "v_lshl_add_u32 v12, v13, 4, v12\n\t"
    "v_cndmask_b32 v105, v9, v12, vcc\n\t"
    "v_add_u32 v10, 96, v7\n\t"
    "v_mul_u32_u24 v11, 3641, v10\n\t"
    "v_lshrrev_b32 v11, 16, v11\n\t"
    "v_mad_i32_i24 v12, v11, s69, v10\n\t"
    "v_cmp_ne_u32 vcc, s64, v12\n\t"
    "v_bitop3_b32 v13, v12, 6, v8 bitop3:0x6a\n\t"
    "v_add_u32 v12, 36864, v5\n\t"
    "v_mad_u32_u24 v12, v11, s63, v12\n\t"
    "v_lshl_add_u32 v12, v13, 4, v12\n\t"
    "v_cndmask_b32 v106, v9, v12, vcc\n\t"
    "v_add_u32 v10, 192, v7\n\t"
    "v_mul_u32_u24 v11, 3641, v10\n\t"
    "v_lshrrev_b32 v11, 16, v11\n\t"
    "v_mad_i32_i24 v12, v11, s69, v10\n\t"
    "v_cmp_ne_u32 vcc, s64, v12\n\t"
    "v_bitop3_b32 v13, v12, 6, v8 bitop3:0x6a\n\t"
    "v_add_u32 v12, 73728, v5\n\t"
    "v_mad_u32_u24 v12, v11, s63, v12\n\t"
    "v_lshl_add_u32 v12, v13, 4, v12\n\t"
    "v_cndmask_b32 v107, v9, v12, vcc\n\t"
    "v_cmp_gt_u32 vcc, s82, v10\n\t"
    "s_nop 1\n\t"
    "v_cndmask_b32 v107, v9, v107, vcc\n\t"
    "odd_y_%=:\n\t"
    "s_cmp_eq_u32 s81, 0\n\t"
    "s_cbranch_scc1 prog_a_%=\n\t"
    "s_cmp_eq_u32 s56, 0\n\t"
    "s_cbranch_scc1 prog_c_%=\n\t"
    "s_add_u32 m0, s78, 59392\n\t"
    "s_nop 0\n\t"
    "buffer_load_dwordx4 v96, s[36:39], s71 offen lds\n\t"
    "v_and_b32 v61, 15, v0\n\t"
    "v_lshrrev_b32 v62, 4, v0\n\t"
    "v_and_b32 v53, 3, v0\n\t"
    "v_mul_u32_u24 v9, 0x5555, v53\n\t"
    "v_and_b32 v53, 1, v0\n\t"
    "v_lshlrev_b32 v53, 4, v53\n\t"
    "v_mov_b32 v54, 0xffff\n\t"
    "v_lshlrev_b32 v53, v53, v54\n\t"
    "v_lshrrev_b32 v54, 2, v61\n\t"
    "v_cmp_eq_u32 vcc, 0, v54\n\t"
    "s_nop 1\n\t"
    "v_cndmask_b32 v1, 0, v53, vcc\n\t"
    "v_cmp_eq_u32 vcc, 1, v54\n\t"
    "s_nop 1\n\t"
    "v_cndmask_b32 v2, 0, v53, vcc\n\t"
    "v_cmp_eq_u32 vcc, 2, v54\n\t"
    "s_nop 1\n\t"
    "v_cndmask_b32 v3, 0, v53, vcc\n\t"
    "v_cmp_eq_u32 vcc, 3, v54\n\t"
    "s_nop 1\n\t"
    "v_cndmask_b32 v4, 0, v53, vcc\n\t"
    "v_lshrrev_b32 v54, 3, v61\n\t"
    "v_and_b32 v55, 1, v62\n\t"
    "v_cmp_eq_u32 vcc, v54, v55\n\t"
    "v_bfe_u32 v54, v61, 1, 2\n\t"
    "s_nop 0\n\t"
    "v_cndmask_b32 v53, 0, v53, vcc\n\t"
    "v_cmp_eq_u32 vcc, 0, v54\n\t"
    "s_nop 1\n\t"
    "v_cndmask_b32 v5, 0, v53, vcc\n\t"
    "v_cmp_eq_u32 vcc, 1, v54\n\t"
    "s_nop 1\n\t"
    "v_cndmask_b32 v6, 0, v53, vcc\n\t"
    "v_cmp_eq_u32 vcc, 2, v54\n\t"
    "s_nop 1\n\t"
    "v_cndmask_b32 v7, 0, v53, vcc\n\t"
    "v_cmp_eq_u32 vcc, 3, v54\n\t"
    "s_nop 1\n\t"
    "v_cndmask_b32 v8, 0, v53, vcc\n\t"
    "v_lshrrev_b32 v53, 1, v62\n\t"
    "v_and_b32 v54, 14, v61\n\t"
    "v_lshlrev_b32 v54, 1, v54\n\t"
    "v_add_u32 v54, 158208, v54\n\t"
    "v_mul_u32_u24 v55, 384, v53\n\t"
    "v_add_u32 v10, v54, v55\n\t"
    "v_add_u32 v10, 2304, v10\n\t"
    "v_mul_u32_u24 v55, 3, v53\n\t"
    "v_and_b32 v53, 1, v62\n\t"
    "v_add_u32 v55, v55, v53\n\t"
    "v_mul_u32_u24 v55, 384, v55\n\t"
    "v_add_u32 v11, v54, v55\n\t"
    "v_mul_u32_u24 v55, 1152, v62\n\t"
    "v_add_u32 v53, 768, v55\n\t"
    "v_min_u32 v53, 3456, v53\n\t"
    "v_add_u32 v12, v54, v53\n\t"
    "v_add_u32 v53, -384, v55\n\t"
    "v_min_u32 v53, 3456, v53\n\t"
    "v_add_u32 v13, v54, v53\n\t"
    "s_waitcnt vmcnt(7)\n\t"
    "s_barrier\n\t"
    "v_lshrrev_b32 v53, 1, v62\n\t"
    "s_mul_i32 s61, s75, 2304\n\t"
    "s_add_u32 s61, s61, 24576\n\t"
    "s_add_u32 m0, s78, 63488\n\t"
    "s_nop 0\n\t"
    "buffer_load_dwordx4 v97, s[36:39], s71 offen lds\n\t"
    "v_add_u32 v54, v53, v61\n\t"
    "v_lshlrev_b32 v14, 7, v54\n\t"
    "v_and_b32 v54, 6, v54\n\t"
    "v_and_or_b32 v54, v62, 1, v54\n\t"
    "v_lshl_add_u32 v14, v54, 4, v14\n\t"
    "v_add_u32 v14, s61, v14\n\t"
    "v_add_u32 v54, 2, v61\n\t"
    "v_lshlrev_b32 v18, 7, v54\n\t"
    "v_and_b32 v54, 6, v54\n\t"
    "v_and_or_b32 v54, v62, 1, v54\n\t"
    "v_lshl_add_u32 v18, v54, 4, v18\n\t"
    "v_add_u32 v18, s61, v18\n\t"
    "v_add_u32 v22, 9216, v18\n\t"
    "v_mul_u32_u24 v54, 2304, v53\n\t"
    "v_add_u32 v18, v18, v54\n\t"
    "v_xor_b32 v15, 32, v14\n\t"
    "v_xor_b32 v16, 64, v14\n\t"
    "v_xor_b32 v17, 96, v14\n\t"
    "v_xor_b32 v19, 32, v18\n\t"
    "v_xor_b32 v20, 64, v18\n\t"
    "v_xor_b32 v21, 96, v18\n\t"
    "v_xor_b32 v23, 32, v22\n\t"
    "v_xor_b32 v24, 64, v22\n\t"
    "v_xor_b32 v25, 96, v22\n\t"
    "s_mov_b64 s[48:49], s[98:99]\n\t"
    "s_mov_b32 s50, 19267584\n\t"
    "s_mov_b32 s51, 0x20000\n\t"
    "s_mov_b32 s60, 0x44ea44ea\n\t"
    "s_mov_b64 s[40:41], s[88:89]\n\t"
    "s_mov_b32 s42, 294912\n\t"
    "s_mov_b32 s43, 0x20000\n\t"
    "s_mov_b64 s[44:45], s[92:93]\n\t"
    "s_sub_u32 s44, s44, 4096\n\t"
    "s_subb_u32 s45, s45, 0\n\t"
    "s_mov_b32 s46, 299008\n\t"
    "s_mov_b32 s47, 0x20000\n\t"
    "v_lshrrev_b32 v53, 3, v0\n\t"
    "v_bfe_u32 v54, v0, 4, 1\n\t"
    "v_lshlrev_b32 v54, 2, v54\n\t"
    "v_bfe_u32 v55, v0, 5, 1\n\t"
    "v_lshl_add_u32 v54, v55, 1, v54\n\t"
    "v_and_b32 v55, 7, v0\n\t"
    "v_xor_b32 v54, v54, v55\n\t"
    "v_lshlrev_b32 v54, 4, v54\n\t"
    "v_mul_u32_u24 v55, 1536, v53\n\t"
    "v_add_u32 v26, v55, v54\n\t"
    "v_add_u32 v27, 11264, v26\n\t"
    "v_add_u32 v28, 22528, v26\n\t"
    "v_add_u32 v58, 33792, v26\n\t"
    "v_mul_u32_u24 v55, 384, v53\n\t"
    "v_add_u32 v55, v55, v54\n\t"
    "s_mul_i32 s61, s79, 4\n\t"
    "s_add_u32 s61, s61, 0\n\t"
    "s_mul_i32 s62, s61, 86\n\t"
    "s_lshr_b32 s62, s62, 8\n\t"
    "s_mul_i32 s62, s62, 2688\n\t"
    "s_lshl_b32 s61, s61, 7\n\t"
    "s_add_u32 s61, s61, s62\n\t"
    "s_add_u32 s61, s61, 4096\n\t"
    "v_add_u32 v29, s61, v55\n\t"
    "s_add_u32 m0, s78, 67584\n\t"
    "s_nop 0\n\t"
    "buffer_load_dwordx4 v98, s[36:39], s71 offen lds\n\t"
    "s_mul_i32 s61, s79, 4\n\t"
    "s_add_u32 s61, s61, 1\n\t"
    "s_mul_i32 s62, s61, 86\n\t"
    "s_lshr_b32 s62, s62, 8\n\t"
    "s_mul_i32 s62, s62, 2688\n\t"
    "s_lshl_b32 s61, s61, 7\n\t"
    "s_add_u32 s61, s61, s62\n\t"
    "s_add_u32 s61, s61, 3072\n\t"
    "v_add_u32 v30, s61, v55\n\t"
    "s_mul_i32 s61, s79, 4\n\t"
    "s_add_u32 s61, s61, 2\n\t"
    "s_mul_i32 s62, s61, 86\n\t"
    "s_lshr_b32 s62, s62, 8\n\t"
    "s_mul_i32 s62, s62, 2688\n\t"
    "s_lshl_b32 s61, s61, 7\n\t"
    "s_add_u32 s61, s61, s62\n\t"
    "s_add_u32 s61, s61, 2048\n\t"
    "v_add_u32 v31, s61, v55\n\t"
    "s_mul_i32 s61, s79, 4\n\t"
    "s_add_u32 s61, s61, 3\n\t"
    "s_mul_i32 s62, s61, 86\n\t"
    "s_lshr_b32 s62, s62, 8\n\t"
    "s_mul_i32 s62, s62, 2688\n\t"
    "s_lshl_b32 s61, s61, 7\n\t"
    "s_add_u32 s61, s61, s62\n\t"
    "s_add_u32 s61, s61, 1024\n\t"
    "v_add_u32 v59, s61, v55\n\t"
    "s_mov_b32 s66, 0\n\t"
    "s_mul_i32 s67, s79, 4096\n\t"
    "s_mul_i32 s65, s79, 49152\n\t"
    "s_add_u32 s68, s67, 55296\n\t"
    "v_lshlrev_b32 v53, 2, v57\n\t"
    "v_min_u32 v54, 764, v53\n\t"
    "v_add_u32 v53, 0x20000, v53\n\t"
    "v_add_u32 v54, 0x20000, v54\n\t"
    "s_mov_b32 s61, 0x3eefe414\n\t"
    "s_mov_b32 s62, 0x4008986d\n\t"
    "v_lshlrev_b32 v48, 16, v48\n\t"
    "v_lshlrev_b32 v49, 16, v49\n\t"
    "v_lshlrev_b32 v50, 16, v50\n\t"
    "v_lshlrev_b32 v51, 16, v51\n\t"
    "v_lshlrev_b32 v52, 16, v52\n\t"
    "v_mul_f32 v48, s61, v48\n\t"
    "v_mul_f32 v49, s61, v49\n\t"
    "v_mul_f32 v50, s61, v50\n\t"
    "v_mul_f32 v51, s61, v51\n\t"
    "v_mul_f32 v52, s62, v52\n\t"
    "ds_write_b32 v53, v48 offset:22528\n\t"
    "ds_write_b32 v53, v49 offset:23552\n\t"
    "ds_write_b32 v53, v50 offset:24576\n\t"
    "ds_write_b32 v54, v51 offset:25600\n\t"
    "ds_write_b32 v54, v52 offset:26368\n\t"
    "s_add_u32 m0, s78, 71680\n\t"
    "s_nop 0\n\t"
    "buffer_load_dwordx4 v99, s[36:39], s71 offen lds\n\t"
    "s_add_u32 m0, s78, 75776\n\t"
    "s_nop 0\n\t"
    "buffer_load_dwordx4 v100, s[36:39], s71 offen lds\n\t"
    "s_waitcnt vmcnt(5) lgkmcnt(0)\n\t"
    "s_barrier\n\t"
    "ds_read2_b32 v[48:49], v10 offset0:0 offset1:8\n\t"
    "ds_read2_b32 v[50:51], v11 offset0:0 offset1:8\n\t"
    "ds_read2_b32 v[52:53], v12 offset0:0 offset1:8\n\t"
    "ds_read2_b32 v[54:55], v13 offset0:0 offset1:8\n\t"
    "ds_read_b128 a[8:11], v14 offset:4608\n\t"
    "ds_read_b128 a[12:15], v14 offset:6912\n\t"
    "ds_read_b128 a[16:19], v14 offset:9216\n\t"
    "ds_read_b128 a[0:3], v14 offset:0\n\t"
    "ds_read_b128 a[4:7], v14 offset:2304\n\t"
    "ds_read_b128 a[20:23], v18 offset:0\n\t"
    "ds_read_b128 a[24:27], v18 offset:4608\n\t"
    "ds_read_b128 a[28:31], v22 offset:0\n\t"
    "s_waitcnt lgkmcnt(8)\n\t"
    "v_and_b32 v32, v5, v48\n\t"
    "v_and_b32 v33, v6, v48\n\t"
    "v_and_b32 v34, v7, v48\n\t"
    "v_and_b32 v35, v8, v48\n\t"
    "v_and_b32 v36, v1, v50\n\t"
    "v_and_b32 v37, v2, v50\n\t"
    "v_and_b32 v38, v3, v50\n\t"
    "v_and_b32 v39, v4, v50\n\t"
    "v_and_b32 v40, v1, v52\n\t"
    "v_and_b32 v41, v2, v52\n\t"
    "v_and_b32 v42, v3, v52\n\t"
    "v_and_b32 v43, v4, v52\n\t"
    "v_and_b32 v44, v1, v54\n\t"
    "v_and_b32 v45, v2, v54\n\t"
    "v_and_b32 v46, v3, v54\n\t"
    "v_and_b32 v47, v4, v54\n\t"
    "s_waitcnt lgkmcnt(5)\n\t"
    "v_mfma_f32_16x16x32_bf16 v[64:67], v[32:35], a[8:11], 0\n\t"
    "ds_read_b128 a[40:43], v15 offset:4608\n\t"
    "v_mfma_f32_16x16x32_bf16 v[112:115], v[32:35], a[12:15], 0\n\t"
    "ds_read_b128 a[44:47], v15 offset:6912\n\t"
    "s_add_u32 m0, s78, 79872\n\t"
    "s_nop 0\n\t"
    "buffer_load_dwordx4 v101, s[36:39], s71 offen lds\n\t"
    "v_mfma_f32_16x16x32_bf16 v[208:211], v[32:35], a[16:19], 0\n\t"
    "ds_read_b128 a[48:51], v15 offset:9216\n\t"
    "s_waitcnt lgkmcnt(6)\n\t"
    "v_smfmac_f32_16x16x64_bf16 v[64:67], v[36:39], a[0:7], v9\n\t"
    "ds_read_b128 a[32:35], v15 offset:0\n\t"
    "v_and_b32 v32, v5, v49\n\t"
    "v_and_b32 v33, v6, v49\n\t"
    "v_smfmac_f32_16x16x64_bf16 v[112:115], v[36:39], a[4:11], v9\n\t"
    "ds_read_b128 a[36:39], v15 offset:2304\n\t"
    "s_add_u32 m0, s78, 83968\n\t"
    "s_nop 0\n\t"
    "buffer_load_dwordx4 v102, s[36:39], s71 offen lds\n\t"
    "v_and_b32 v34, v7, v49\n\t"
    "v_smfmac_f32_16x16x64_bf16 v[208:211], v[36:39], a[8:15], v9\n\t"
    "ds_read_b128 a[52:55], v19 offset:0\n\t"
    "v_and_b32 v35, v8, v49\n\t"
    "s_waitcnt lgkmcnt(6)\n\t"
    "v_smfmac_f32_16x16x64_bf16 v[64:67], v[40:43], a[20:27], v9\n\t"
    "ds_read_b128 a[56:59], v19 offset:4608\n\t"
    "ds_read2_b32 v[48:49], v10 offset0:16 offset1:24\n\t"
    "v_and_b32 v36, v1, v51\n\t"
    "v_and_b32 v37, v2, v51\n\t"
    "v_smfmac_f32_16x16x64_bf16 v[208:211], v[40:43], a[24:31], v9\n\t"
    "ds_read_b128 a[60:63], v23 offset:0\n\t"
    "s_add_u32 m0, s78, 88064\n\t"
    "s_nop 0\n\t"
    "buffer_load_dwordx4 v103, s[36:39], s71 offen lds\n\t"
    "v_and_b32 v38, v3, v51\n\t"
    "v_smfmac_f32_16x16x64_bf16 v[112:115], v[44:47], a[20:27], v9\n\t"
    "v_and_b32 v39, v4, v51\n\t"
    "v_and_b32 v40, v1, v53\n\t"
    "v_and_b32 v41, v2, v53\n\t"
    "v_and_b32 v42, v3, v53\n\t"
    "v_and_b32 v43, v4, v53\n\t"
    "v_and_b32 v44, v1, v55\n\t"
    "v_and_b32 v45, v2, v55\n\t"
    "v_and_b32 v46, v3, v55\n\t"
    "v_and_b32 v47, v4, v55\n\t"
    "v_sub_f32 v180, 0, v64\n\t"
    "v_sub_f32 v182, 0, v112\n\t"
    "v_sub_f32 v184, 0, v208\n\t"
    "v_sub_f32 v186, 0, v208\n\t"
    "v_permlane16_swap_b32 v180, v182\n\t"
    "s_nop 0\n\t"
    "v_permlane16_swap_b32 v184, v186\n\t"
    "s_nop 1\n\t"
    "v_permlane32_swap_b32 v180, v184\n\t"
    "v_mov_b32 v184, v180\n\t"
    "s_nop 1\n\t"
    "v_permlane32_swap_b32 v180, v184\n\t"
    "v_mov_b32 v182, v180\n\t"
    "v_mov_b32 v186, v184\n\t"
    "s_nop 0\n\t"
    "v_permlane16_swap_b32 v180, v182\n\t"
    "v_permlane16_swap_b32 v184, v186\n\t"
    "v_mov_b32 v168, v180\n\t"
    "v_mov_b32 v169, v180\n\t"
    "v_mov_b32 v170, v180\n\t"
    "v_mov_b32 v171, v180\n\t"
    "v_mov_b32 v172, v182\n\t"
    "v_mov_b32 v173, v182\n\t"
    "v_mov_b32 v174, v182\n\t"
    "v_mov_b32 v175, v182\n\t"
    "v_mov_b32 v176, v184\n\t"
    "v_mov_b32 v177, v184\n\t"
    "v_mov_b32 v178, v184\n\t"
    "v_mov_b32 v179, v184\n\t"
    "v_add_f32 v64, v64, v180\n\t"
    "v_add_f32 v65, v65, v180\n\t"
    "v_add_f32 v66, v66, v180\n\t"
    "v_add_f32 v67, v67, v180\n\t"
    "v_add_f32 v112, v112, v182\n\t"
    "v_add_f32 v113, v113, v182\n\t"
    "v_add_f32 v114, v114, v182\n\t"
    "v_add_f32 v115, v115, v182\n\t"
    "v_add_f32 v208, v208, v184\n\t"
    "v_add_f32 v209, v209, v184\n\t"
    "v_add_f32 v210, v210, v184\n\t"
    "v_add_f32 v211, v211, v184\n\t"
    "s_waitcnt lgkmcnt(6)\n\t"
    "v_mfma_f32_16x16x32_bf16 v[68:71], v[32:35], a[40:43], v[168:171]\n\t"
    "ds_read_b128 a[72:75], v16 offset:4608\n\t"
    "ds_read2_b32 v[50:51], v11 offset0:16 offset1:24\n\t"
    "v_mfma_f32_16x16x32_bf16 v[116:119], v[32:35], a[44:47], v[172:175]\n\t"
    "ds_read_b128 a[76:79], v16 offset:6912\n\t"
    "ds_read2_b32 v[52:53], v12 offset0:16 offset1:24\n\t"
    "v_mfma_f32_16x16x32_bf16 v[212:215], v[32:35], a[48:51], v[176:179]\n\t"
    "ds_read_b128 a[80:83], v16 offset:9216\n\t"
    "ds_read2_b32 v[54:55], v13 offset0:16 offset1:24\n\t"
    "s_waitcnt lgkmcnt(7)\n\t"
    "v_smfmac_f32_16x16x64_bf16 v[68:71], v[36:39], a[32:39], v9\n\t"
    "ds_read_b128 a[64:67], v16 offset:0\n\t"
    "v_and_b32 v32, v5, v48\n\t"
    "v_and_b32 v33, v6, v48\n\t"
    "v_smfmac_f32_16x16x64_bf16 v[116:119], v[36:39], a[36:43], v9\n\t"
    "ds_read_b128 a[68:71], v16 offset:2304\n\t"
    "s_add_u32 m0, s76, 59392\n\t"
    "s_nop 0\n\t"
    "buffer_load_dwordx4 v105, s[36:39], s71 offen lds\n\t"
    "v_and_b32 v34, v7, v48\n\t"
    "v_smfmac_f32_16x16x64_bf16 v[212:215], v[36:39], a[40:47], v9\n\t"
    "ds_read_b128 a[84:87], v20 offset:0\n\t"
    "v_and_b32 v35, v8, v48\n\t"
    "s_waitcnt lgkmcnt(7)\n\t"
    "v_smfmac_f32_16x16x64_bf16 v[68:71], v[40:43], a[52:59], v9\n\t"
    "ds_read_b128 a[88:91], v20 offset:4608\n\t"
    "v_and_b32 v36, v1, v50\n\t"
    "v_and_b32 v37, v2, v50\n\t"
    "v_smfmac_f32_16x16x64_bf16 v[212:215], v[40:43], a[56:63], v9\n\t"
    "ds_read_b128 a[92:95], v24 offset:0\n\t"
    "s_add_u32 m0, s76, 71680\n\t"
    "s_nop 0\n\t"
    "buffer_load_dwordx4 v106, s[36:39], s71 offen lds\n\t"
    "v_and_b32 v38, v3, v50\n\t"
    "v_smfmac_f32_16x16x64_bf16 v[116:119], v[44:47], a[52:59], v9\n\t"
    "v_and_b32 v39, v4, v50\n\t"
    "s_waitcnt lgkmcnt(5)\n\t"
    "v_and_b32 v40, v1, v52\n\t"
    "v_and_b32 v41, v2, v52\n\t"
    "v_and_b32 v42, v3, v52\n\t"
    "v_and_b32 v43, v4, v52\n\t"
    "v_and_b32 v44, v1, v54\n\t"
    "v_and_b32 v45, v2, v54\n\t"
    "v_and_b32 v46, v3, v54\n\t"
    "v_and_b32 v47, v4, v54\n\t"
    "v_mfma_f32_16x16x32_bf16 v[72:75], v[32:35], a[72:75], v[168:171]\n\t"
    "ds_read_b128 a[8:11], v17 offset:4608\n\t"
    "v_mfma_f32_16x16x32_bf16 v[120:123], v[32:35], a[76:79], v[172:175]\n\t"
    "ds_read_b128 a[12:15], v17 offset:6912\n\t"
    "s_add_u32 m0, s76, 83968\n\t"
    "s_nop 0\n\t"
    "buffer_load_dwordx4 v107, s[36:39], s71 offen lds\n\t"
    "v_mfma_f32_16x16x32_bf16 v[216:219], v[32:35], a[80:83], v[176:179]\n\t"
    "ds_read_b128 a[16:19], v17 offset:9216\n\t"
    "s_waitcnt lgkmcnt(6)\n\t"
    "v_smfmac_f32_16x16x64_bf16 v[72:75], v[36:39], a[64:71], v9\n\t"
    "ds_read_b128 a[0:3], v17 offset:0\n\t"
    "v_and_b32 v32, v5, v49\n\t"
    "v_and_b32 v33, v6, v49\n\t"
    "v_smfmac_f32_16x16x64_bf16 v[120:123], v[36:39], a[68:75], v9\n\t"
    "ds_read_b128 a[4:7], v17 offset:2304\n\t"
    "s_add_u32 m0, s78, 94208\n\t"
    "s_nop 0\n\t"
    "buffer_load_dwordx4 v96, s[36:39], s72 offen lds\n\t"
    "v_and_b32 v34, v7, v49\n\t"
    "v_smfmac_f32_16x16x64_bf16 v[216:219], v[36:39], a[72:79], v9\n\t"
    "ds_read_b128 a[20:23], v21 offset:0\n\t"
    "v_and_b32 v35, v8, v49\n\t"
    "s_waitcnt lgkmcnt(6)\n\t"
    "v_smfmac_f32_16x16x64_bf16 v[72:75], v[40:43], a[84:91], v9\n\t"
    "ds_read_b128 a[24:27], v21 offset:4608\n\t"
    "ds_read2_b32 v[48:49], v10 offset0:32 offset1:40\n\t"
    "v_and_b32 v36, v1, v51\n\t"
    "v_and_b32 v37, v2, v51\n\t"
    "v_smfmac_f32_16x16x64_bf16 v[216:219], v[40:43], a[88:95], v9\n\t"
    "ds_read_b128 a[28:31], v25 offset:0\n\t"
    "s_add_u32 m0, s78, 98304\n\t"
    "s_nop 0\n\t"
    "buffer_load_dwordx4 v97, s[36:39], s72 offen lds\n\t"
    "v_and_b32 v38, v3, v51\n\t"
    "v_smfmac_f32_16x16x64_bf16 v[120:123], v[44:47], a[84:91], v9\n\t"
    "v_and_b32 v39, v4, v51\n\t"
    "v_and_b32 v40, v1, v53\n\t"
    "v_and_b32 v41, v2, v53\n\t"
    "v_and_b32 v42, v3, v53\n\t"
    "v_and_b32 v43, v4, v53\n\t"
    "v_and_b32 v44, v1, v55\n\t"
    "v_and_b32 v45, v2, v55\n\t"
    "v_and_b32 v46, v3, v55\n\t"
    "v_and_b32 v47, v4, v55\n\t"
    "s_waitcnt lgkmcnt(6)\n\t"
    "v_mfma_f32_16x16x32_bf16 v[76:79], v[32:35], a[8:11], v[168:171]\n\t"
    "ds_read2_b32 v[50:51], v11 offset0:32 offset1:40\n\t"
    "v_mfma_f32_16x16x32_bf16 v[124:127], v[32:35], a[12:15], v[172:175]\n\t"
    "ds_read2_b32 v[52:53], v12 offset0:32 offset1:40\n\t"
    "v_mfma_f32_16x16x32_bf16 v[220:223], v[32:35], a[16:19], v[176:179]\n\t"
    "ds_read2_b32 v[54:55], v13 offset0:32 offset1:40\n\t"
    "s_waitcnt lgkmcnt(4)\n\t"
    "v_smfmac_f32_16x16x64_bf16 v[76:79], v[36:39], a[0:7], v9\n\t"
    "v_and_b32 v32, v5, v48\n\t"
    "v_and_b32 v33, v6, v48\n\t"
    "v_smfmac_f32_16x16x64_bf16 v[124:127], v[36:39], a[4:11], v9\n\t"
    "s_add_u32 m0, s78, 102400\n\t"
    "s_nop 0\n\t"
    "buffer_load_dwordx4 v98, s[36:39], s72 offen lds\n\t"
    "v_and_b32 v34, v7, v48\n\t"
    "v_smfmac_f32_16x16x64_bf16 v[220:223], v[36:39], a[8:15], v9\n\t"
    "v_and_b32 v35, v8, v48\n\t"
    "s_waitcnt lgkmcnt(2)\n\t"
    "v_smfmac_f32_16x16x64_bf16 v[76:79], v[40:43], a[20:27], v9\n\t"
    "v_and_b32 v36, v1, v50\n\t"
    "v_and_b32 v37, v2, v50\n\t"
    "v_smfmac_f32_16x16x64_bf16 v[220:223], v[40:43], a[24:31], v9\n\t"
    "s_add_u32 m0, s78, 106496\n\t"
    "s_nop 0\n\t"
    "buffer_load_dwordx4 v99, s[36:39], s72 offen lds\n\t"
    "v_and_b32 v38, v3, v50\n\t"
    "v_smfmac_f32_16x16x64_bf16 v[124:127], v[44:47], a[20:27], v9\n\t"
    "v_and_b32 v39, v4, v50\n\t"
    "s_waitcnt lgkmcnt(0)\n\t"
    "v_and_b32 v40, v1, v52\n\t"
    "v_and_b32 v41, v2, v52\n\t"
    "v_and_b32 v42, v3, v52\n\t"
    "v_and_b32 v43, v4, v52\n\t"
    "v_and_b32 v44, v1, v54\n\t"
    "v_and_b32 v45, v2, v54\n\t"
    "v_and_b32 v46, v3, v54\n\t"
    "v_and_b32 v47, v4, v54\n\t"
    "v_pk_mul_f32 v[190:191], v[64:65], v[64:65]\n\t"
    "v_pk_mul_f32 v[194:195], v[112:113], v[112:113]\n\t"
    "v_pk_mul_f32 v[198:199], v[208:209], v[208:209]\n\t"
    "v_pk_fma_f32 v[190:191], v[66:67], v[66:67], v[190:191]\n\t"
    "v_pk_fma_f32 v[194:195], v[114:115], v[114:115], v[194:195]\n\t"
    "v_pk_fma_f32 v[198:199], v[210:211], v[210:211], v[198:199]\n\t"
    "v_pk_add_f32 v[188:189], v[64:65], v[66:67]\n\t"
    "v_pk_add_f32 v[192:193], v[112:113], v[114:115]\n\t"
    "v_pk_add_f32 v[196:197], v[208:209], v[210:211]\n\t"
    "v_pk_fma_f32 v[190:191], v[68:69], v[68:69], v[190:191]\n\t"
    "v_pk_fma_f32 v[194:195], v[116:117], v[116:117], v[194:195]\n\t"
    "v_pk_fma_f32 v[198:199], v[212:213], v[212:213], v[198:199]\n\t"
    "v_pk_add_f32 v[188:189], v[188:189], v[68:69]\n\t"
    "v_pk_add_f32 v[192:193], v[192:193], v[116:117]\n\t"
    "v_pk_add_f32 v[196:197], v[196:197], v[212:213]\n\t"
    "v_pk_fma_f32 v[190:191], v[70:71], v[70:71], v[190:191]\n\t"
    "v_pk_fma_f32 v[194:195], v[118:119], v[118:119], v[194:195]\n\t"
    "v_pk_fma_f32 v[198:199], v[214:215], v[214:215], v[198:199]\n\t"
    "v_pk_add_f32 v[188:189], v[188:189], v[70:71]\n\t"
    "v_pk_add_f32 v[192:193], v[192:193], v[118:119]\n\t"
    "v_pk_add_f32 v[196:197], v[196:197], v[214:215]\n\t"
    "v_pk_fma_f32 v[190:191], v[72:73], v[72:73], v[190:191]\n\t"
    "v_pk_fma_f32 v[194:195], v[120:121], v[120:121], v[194:195]\n\t"
    "v_pk_fma_f32 v[198:199], v[216:217], v[216:217], v[198:199]\n\t"
    "v_pk_add_f32 v[188:189], v[188:189], v[72:73]\n\t"
    "v_pk_add_f32 v[192:193], v[192:193], v[120:121]\n\t"
    "v_pk_add_f32 v[196:197], v[196:197], v[216:217]\n\t"
    "v_pk_fma_f32 v[190:191], v[74:75], v[74:75], v[190:191]\n\t"
    "v_pk_fma_f32 v[194:195], v[122:123], v[122:123], v[194:195]\n\t"
    "v_pk_fma_f32 v[198:199], v[218:219], v[218:219], v[198:199]\n\t"
    "v_pk_add_f32 v[188:189], v[188:189], v[74:75]\n\t"
    "v_pk_add_f32 v[192:193], v[192:193], v[122:123]\n\t"
    "v_pk_add_f32 v[196:197], v[196:197], v[218:219]\n\t"
    "v_pk_fma_f32 v[190:191], v[76:77], v[76:77], v[190:191]\n\t"
    "v_pk_fma_f32 v[194:195], v[124:125], v[124:125], v[194:195]\n\t"
    "v_pk_fma_f32 v[198:199], v[220:221], v[220:221], v[198:199]\n\t"
    "v_pk_add_f32 v[188:189], v[188:189], v[76:77]\n\t"
    "v_pk_add_f32 v[192:193], v[192:193], v[124:125]\n\t"
    "v_pk_add_f32 v[196:197], v[196:197], v[220:221]\n\t"
    "s_waitcnt vmcnt(4)\n\t"
    "s_barrier\n\t"
    "ds_read_b128 a[40:43], v14 offset:39424\n\t"
    "ds_read_b128 a[44:47], v14 offset:41728\n\t"
    "ds_read_b128 a[48:51], v14 offset:44032\n\t"
    "ds_read_b128 a[32:35], v14 offset:34816\n\t"
    "ds_read_b128 a[36:39], v14 offset:37120\n\t"
    "ds_read_b128 a[52:55], v18 offset:34816\n\t"
    "ds_read_b128 a[56:59], v18 offset:39424\n\t"
    "ds_read_b128 a[60:63], v22 offset:34816\n\t"
    "v_pk_fma_f32 v[190:191], v[78:79], v[78:79], v[190:191]\n\t"
    "v_pk_fma_f32 v[194:195], v[126:127], v[126:127], v[194:195]\n\t"
    "v_pk_fma_f32 v[198:199], v[222:223], v[222:223], v[198:199]\n\t"
    "v_pk_add_f32 v[188:189], v[188:189], v[78:79]\n\t"
    "v_pk_add_f32 v[192:193], v[192:193], v[126:127]\n\t"
    "v_pk_add_f32 v[196:197], v[196:197], v[222:223]\n\t"
    "s_waitcnt lgkmcnt(5)\n\t"
    "v_mfma_f32_16x16x32_bf16 v[80:83], v[32:35], a[40:43], v[168:171]\n\t"
    "ds_read_b128 a[72:75], v15 offset:39424\n\t"
    "v_mfma_f32_16x16x32_bf16 v[128:131], v[32:35], a[44:47], v[172:175]\n\t"
    "ds_read_b128 a[76:79], v15 offset:41728\n\t"
    "s_add_u32 m0, s78, 110592\n\t"
    "s_nop 0\n\t"
    "buffer_load_dwordx4 v100, s[36:39], s72 offen lds\n\t"
    "v_mfma_f32_16x16x32_bf16 v[224:227], v[32:35], a[48:51], v[176:179]\n\t"
    "ds_read_b128 a[80:83], v15 offset:44032\n\t"
    "s_waitcnt lgkmcnt(6)\n\t"
    "v_smfmac_f32_16x16x64_bf16 v[80:83], v[36:39], a[32:39], v9\n\t"
    "ds_read_b128 a[64:67], v15 offset:34816\n\t"
    "v_and_b32 v32, v5, v49\n\t"
    "v_and_b32 v33, v6, v49\n\t"
    "v_smfmac_f32_16x16x64_bf16 v[128:131], v[36:39], a[36:43], v9\n\t"
    "ds_read_b128 a[68:71], v15 offset:37120\n\t"
    "s_add_u32 m0, s78, 114688\n\t"
    "s_nop 0\n\t"
    "buffer_load_dwordx4 v101, s[36:39], s72 offen lds\n\t"
    "v_and_b32 v34, v7, v49\n\t"
    "v_smfmac_f32_16x16x64_bf16 v[224:227], v[36:39], a[40:47], v9\n\t"
    "ds_read_b128 a[84:87], v19 offset:34816\n\t"
    "v_and_b32 v35, v8, v49\n\t"
    "s_waitcnt lgkmcnt(6)\n\t"
    "v_smfmac_f32_16x16x64_bf16 v[80:83], v[40:43], a[52:59], v9\n\t"
    "ds_read_b128 a[88:91], v19 offset:39424\n\t"
    "ds_read2_b32 v[48:49], v10 offset0:48 offset1:56\n\t"
    "v_and_b32 v36, v1, v51\n\t"
    "v_and_b32 v37, v2, v51\n\t"
    "v_smfmac_f32_16x16x64_bf16 v[224:227], v[40:43], a[56:63], v9\n\t"
    "ds_read_b128 a[92:95], v23 offset:34816\n\t"
    "s_add_u32 m0, s78, 118784\n\t"
    "s_nop 0\n\t"
    "buffer_load_dwordx4 v102, s[36:39], s72 offen lds\n\t"
    "v_and_b32 v38, v3, v51\n\t"
    "v_smfmac_f32_16x16x64_bf16 v[128:131], v[44:47], a[52:59], v9\n\t"
    "v_and_b32 v39, v4, v51\n\t"
    "v_and_b32 v40, v1, v53\n\t"
    "v_and_b32 v41, v2, v53\n\t"
    "v_and_b32 v42, v3, v53\n\t"
    "v_and_b32 v43, v4, v53\n\t"
    "v_and_b32 v44, v1, v55\n\t"
    "v_and_b32 v45, v2, v55\n\t"
    "v_and_b32 v46, v3, v55\n\t"
    "v_and_b32 v47, v4, v55\n\t"
    "s_waitcnt lgkmcnt(6)\n\t"
    "v_mfma_f32_16x16x32_bf16 v[84:87], v[32:35], a[72:75], v[168:171]\n\t"
    "ds_read_b128 a[8:11], v16 offset:39424\n\t"
    "ds_read2_b32 v[50:51], v11 offset0:48 offset1:56\n\t"
    "v_mfma_f32_16x16x32_bf16 v[132:135], v[32:35], a[76:79], v[172:175]\n\t"
    "ds_read_b128 a[12:15], v16 offset:41728\n\t"
    "ds_read2_b32 v[52:53], v12 offset0:48 offset1:56\n\t"
    "v_mfma_f32_16x16x32_bf16 v[228:231], v[32:35], a[80:83], v[176:179]\n\t"
    "ds_read_b128 a[16:19], v16 offset:44032\n\t"
    "ds_read2_b32 v[54:55], v13 offset0:48 offset1:56\n\t"
    "s_waitcnt lgkmcnt(7)\n\t"
    "v_smfmac_f32_16x16x64_bf16 v[84:87], v[36:39], a[64:71], v9\n\t"
    "ds_read_b128 a[0:3], v16 offset:34816\n\t"
    "v_and_b32 v32, v5, v48\n\t"
    "v_and_b32 v33, v6, v48\n\t"
    "v_smfmac_f32_16x16x64_bf16 v[132:135], v[36:39], a[68:75], v9\n\t"
    "ds_read_b128 a[4:7], v16 offset:37120\n\t"
    "s_add_u32 m0, s78, 122880\n\t"
    "s_nop 0\n\t"
    "buffer_load_dwordx4 v103, s[36:39], s72 offen lds\n\t"
    "v_and_b32 v34, v7, v48\n\t"
    "v_smfmac_f32_16x16x64_bf16 v[228:231], v[36:39], a[72:79], v9\n\t"
    "ds_read_b128 a[20:23], v20 offset:34816\n\t"
    "v_and_b32 v35, v8, v48\n\t"
    "s_waitcnt lgkmcnt(7)\n\t"
    "v_smfmac_f32_16x16x64_bf16 v[84:87], v[40:43], a[84:91], v9\n\t"
    "ds_read_b128 a[24:27], v20 offset:39424\n\t"
    "v_and_b32 v36, v1, v50\n\t"
    "v_and_b32 v37, v2, v50\n\t"
    "v_smfmac_f32_16x16x64_bf16 v[228:231], v[40:43], a[88:95], v9\n\t"
    "ds_read_b128 a[28:31], v24 offset:34816\n\t"
    "s_add_u32 m0, s76, 94208\n\t"
    "s_nop 0\n\t"
    "buffer_load_dwordx4 v105, s[36:39], s72 offen lds\n\t"
    "v_and_b32 v38, v3, v50\n\t"
    "v_smfmac_f32_16x16x64_bf16 v[132:135], v[44:47], a[84:91], v9\n\t"
    "v_and_b32 v39, v4, v50\n\t"
    "s_waitcnt lgkmcnt(5)\n\t"
    "v_and_b32 v40, v1, v52\n\t"
    "v_and_b32 v41, v2, v52\n\t"
    "v_and_b32 v42, v3, v52\n\t"
    "v_and_b32 v43, v4, v52\n\t"
    "v_and_b32 v44, v1, v54\n\t"
    "v_and_b32 v45, v2, v54\n\t"
    "v_and_b32 v46, v3, v54\n\t"
    "v_and_b32 v47, v4, v54\n\t"
    "v_mfma_f32_16x16x32_bf16 v[88:91], v[32:35], a[8:11], v[168:171]\n\t"
    "ds_read_b128 a[40:43], v17 offset:39424\n\t"
    "v_mfma_f32_16x16x32_bf16 v[136:139], v[32:35], a[12:15], v[172:175]\n\t"
    "ds_read_b128 a[44:47], v17 offset:41728\n\t"
    "s_add_u32 m0, s76, 106496\n\t"
    "s_nop 0\n\t"
    "buffer_load_dwordx4 v106, s[36:39], s72 offen lds\n\t"
    "v_mfma_f32_16x16x32_bf16 v[232:235], v[32:35], a[16:19], v[176:179]\n\t"
    "ds_read_b128 a[48:51], v17 offset:44032\n\t"
    "s_waitcnt lgkmcnt(6)\n\t"
    "v_smfmac_f32_16x16x64_bf16 v[88:91], v[36:39], a[0:7], v9\n\t"
    "ds_read_b128 a[32:35], v17 offset:34816\n\t"
    "v_and_b32 v32, v5, v49\n\t"
    "v_and_b32 v33, v6, v49\n\t"
    "v_smfmac_f32_16x16x64_bf16 v[136:139], v[36:39], a[4:11], v9\n\t"
    "ds_read_b128 a[36:39], v17 offset:37120\n\t"
    "s_add_u32 m0, s76, 118784\n\t"
    "s_nop 0\n\t"
    "buffer_load_dwordx4 v107, s[36:39], s72 offen lds\n\t"
    "v_and_b32 v34, v7, v49\n\t"
    "v_smfmac_f32_16x16x64_bf16 v[232:235], v[36:39], a[8:15], v9\n\t"
    "ds_read_b128 a[52:55], v21 offset:34816\n\t"
    "v_and_b32 v35, v8, v49\n\t"
    "s_waitcnt lgkmcnt(6)\n\t"
    "v_smfmac_f32_16x16x64_bf16 v[88:91], v[40:43], a[20:27], v9\n\t"
    "ds_read_b128 a[56:59], v21 offset:39424\n\t"
    "ds_read2_b32 v[48:49], v10 offset0:64 offset1:72\n\t"
    "v_and_b32 v36, v1, v51\n\t"
    "v_and_b32 v37, v2, v51\n\t"
    "v_smfmac_f32_16x16x64_bf16 v[232:235], v[40:43], a[24:31], v9\n\t"
    "ds_read_b128 a[60:63], v25 offset:34816\n\t"
    "s_add_u32 m0, s67, 0\n\t"
    "s_add_u32 s63, s65, 0\n\t"
    "buffer_load_dwordx4 v26, s[40:43], s63 offen offset:0 lds\n\t"
    "v_and_b32 v38, v3, v51\n\t"
    "v_smfmac_f32_16x16x64_bf16 v[136:139], v[44:47], a[20:27], v9\n\t"
    "v_and_b32 v39, v4, v51\n\t"
    "v_and_b32 v40, v1, v53\n\t"
    "v_and_b32 v41, v2, v53\n\t"
    "v_and_b32 v42, v3, v53\n\t"
    "v_and_b32 v43, v4, v53\n\t"
    "v_and_b32 v44, v1, v55\n\t"
    "v_and_b32 v45, v2, v55\n\t"
    "v_and_b32 v46, v3, v55\n\t"
    "v_and_b32 v47, v4, v55\n\t"
    "s_waitcnt lgkmcnt(6)\n\t"
    "v_mfma_f32_16x16x32_bf16 v[92:95], v[32:35], a[40:43], v[168:171]\n\t"
    "ds_read2_b32 v[50:51], v11 offset0:64 offset1:72\n\t"
    "v_mfma_f32_16x16x32_bf16 v[140:143], v[32:35], a[44:47], v[172:175]\n\t"
    "ds_read2_b32 v[52:53], v12 offset0:64 offset1:72\n\t"
    "v_mfma_f32_16x16x32_bf16 v[236:239], v[32:35], a[48:51], v[176:179]\n\t"
    "ds_read2_b32 v[54:55], v13 offset0:64 offset1:72\n\t"
    "s_waitcnt lgkmcnt(4)\n\t"
    "v_smfmac_f32_16x16x64_bf16 v[92:95], v[36:39], a[32:39], v9\n\t"
    "v_and_b32 v32, v5, v48\n\t"
    "v_and_b32 v33, v6, v48\n\t"
    "v_smfmac_f32_16x16x64_bf16 v[140:143], v[36:39], a[36:43], v9\n\t"
    "buffer_load_dwordx4 v27, s[40:43], s63 offen offset:1024 lds\n\t"
    "v_and_b32 v34, v7, v48\n\t"
    "v_smfmac_f32_16x16x64_bf16 v[236:239], v[36:39], a[40:47], v9\n\t"
    "v_and_b32 v35, v8, v48\n\t"
    "s_waitcnt lgkmcnt(2)\n\t"
    "v_smfmac_f32_16x16x64_bf16 v[92:95], v[40:43], a[52:59], v9\n\t"
    "v_and_b32 v36, v1, v50\n\t"
    "v_and_b32 v37, v2, v50\n\t"
    "v_smfmac_f32_16x16x64_bf16 v[236:239], v[40:43], a[56:63], v9\n\t"
    "buffer_load_dwordx4 v28, s[40:43], s63 offen offset:2048 lds\n\t"
    "v_and_b32 v38, v3, v50\n\t"
    "v_smfmac_f32_16x16x64_bf16 v[140:143], v[44:47], a[52:59], v9\n\t"
    "v_and_b32 v39, v4, v50\n\t"
    "s_waitcnt lgkmcnt(0)\n\t"
    "v_and_b32 v40, v1, v52\n\t"
    "v_and_b32 v41, v2, v52\n\t"
    "v_and_b32 v42, v3, v52\n\t"
    "v_and_b32 v43, v4, v52\n\t"
    "v_and_b32 v44, v1, v54\n\t"
    "v_and_b32 v45, v2, v54\n\t"
    "v_and_b32 v46, v3, v54\n\t"
    "v_and_b32 v47, v4, v54\n\t"
    "v_pk_fma_f32 v[190:191], v[80:81], v[80:81], v[190:191]\n\t"
    "v_pk_fma_f32 v[194:195], v[128:129], v[128:129], v[194:195]\n\t"
    "v_pk_fma_f32 v[198:199], v[224:225], v[224:225], v[198:199]\n\t"
    "v_pk_add_f32 v[188:189], v[188:189], v[80:81]\n\t"
    "v_pk_add_f32 v[192:193], v[192:193], v[128:129]\n\t"
    "v_pk_add_f32 v[196:197], v[196:197], v[224:225]\n\t"
    "v_pk_fma_f32 v[190:191], v[82:83], v[82:83], v[190:191]\n\t"
    "v_pk_fma_f32 v[194:195], v[130:131], v[130:131], v[194:195]\n\t"
    "v_pk_fma_f32 v[198:199], v[226:227], v[226:227], v[198:199]\n\t"
    "v_pk_add_f32 v[188:189], v[188:189], v[82:83]\n\t"
    "v_pk_add_f32 v[192:193], v[192:193], v[130:131]\n\t"
    "v_pk_add_f32 v[196:197], v[196:197], v[226:227]\n\t"
    "v_pk_fma_f32 v[190:191], v[84:85], v[84:85], v[190:191]\n\t"
    "v_pk_fma_f32 v[194:195], v[132:133], v[132:133], v[194:195]\n\t"
    "v_pk_fma_f32 v[198:199], v[228:229], v[228:229], v[198:199]\n\t"
    "v_pk_add_f32 v[188:189], v[188:189], v[84:85]\n\t"
    "v_pk_add_f32 v[192:193], v[192:193], v[132:133]\n\t"
    "v_pk_add_f32 v[196:197], v[196:197], v[228:229]\n\t"
    "v_pk_fma_f32 v[190:191], v[86:87], v[86:87], v[190:191]\n\t"
    "v_pk_fma_f32 v[194:195], v[134:135], v[134:135], v[194:195]\n\t"
    "v_pk_fma_f32 v[198:199], v[230:231], v[230:231], v[198:199]\n\t"
    "v_pk_add_f32 v[188:189], v[188:189], v[86:87]\n\t"
    "v_pk_add_f32 v[192:193], v[192:193], v[134:135]\n\t"
    "v_pk_add_f32 v[196:197], v[196:197], v[230:231]\n\t"
    "v_pk_fma_f32 v[190:191], v[88:89], v[88:89], v[190:191]\n\t"
    "v_pk_fma_f32 v[194:195], v[136:137], v[136:137], v[194:195]\n\t"
    "v_pk_fma_f32 v[198:199], v[232:233], v[232:233], v[198:199]\n\t"
    "v_pk_add_f32 v[188:189], v[188:189], v[88:89]\n\t"
    "v_pk_add_f32 v[192:193], v[192:193], v[136:137]\n\t"
    "v_pk_add_f32 v[196:197], v[196:197], v[232:233]\n\t"
    "v_pk_fma_f32 v[190:191], v[90:91], v[90:91], v[190:191]\n\t"
    "v_pk_fma_f32 v[194:195], v[138:139], v[138:139], v[194:195]\n\t"
    "v_pk_fma_f32 v[198:199], v[234:235], v[234:235], v[198:199]\n\t"
    "v_pk_add_f32 v[188:189], v[188:189], v[90:91]\n\t"
    "v_pk_add_f32 v[192:193], v[192:193], v[138:139]\n\t"
    "v_pk_add_f32 v[196:197], v[196:197], v[234:235]\n\t"
    "v_pk_fma_f32 v[190:191], v[92:93], v[92:93], v[190:191]\n\t"
    "v_pk_fma_f32 v[194:195], v[140:141], v[140:141], v[194:195]\n\t"
    "v_pk_fma_f32 v[198:199], v[236:237], v[236:237], v[198:199]\n\t"
    "v_pk_add_f32 v[188:189], v[188:189], v[92:93]\n\t"
    "v_pk_add_f32 v[192:193], v[192:193], v[140:141]\n\t"
    "v_pk_add_f32 v[196:197], v[196:197], v[236:237]\n\t"
    "v_add_u32 v14, 69632, v14\n\t"
    "v_add_u32 v15, 69632, v15\n\t"
    "v_add_u32 v16, 69632, v16\n\t"
    "v_add_u32 v17, 69632, v17\n\t"
    "v_add_u32 v18, 69632, v18\n\t"
    "v_add_u32 v19, 69632, v19\n\t"
    "v_add_u32 v20, 69632, v20\n\t"
    "v_add_u32 v21, 69632, v21\n\t"
    "v_add_u32 v22, 69632, v22\n\t"
    "v_add_u32 v23, 69632, v23\n\t"
    "v_add_u32 v24, 69632, v24\n\t"
    "v_add_u32 v25, 69632, v25\n\t"
    "s_waitcnt vmcnt(3)\n\t"
    "s_barrier\n\t"
    "ds_read_b128 a[72:75], v14 offset:4608\n\t"
    "ds_read_b128 a[76:79], v14 offset:6912\n\t"
    "ds_read_b128 a[80:83], v14 offset:9216\n\t"
    "ds_read_b128 a[64:67], v14 offset:0\n\t"
    "ds_read_b128 a[68:71], v14 offset:2304\n\t"
    "ds_read_b128 a[84:87], v18 offset:0\n\t"
    "ds_read_b128 a[88:91], v18 offset:4608\n\t"
    "ds_read_b128 a[92:95], v22 offset:0\n\t"
    "v_pk_fma_f32 v[190:191], v[94:95], v[94:95], v[190:191]\n\t"
    "v_pk_fma_f32 v[194:195], v[142:143], v[142:143], v[194:195]\n\t"
    "v_pk_fma_f32 v[198:199], v[238:239], v[238:239], v[198:199]\n\t"
    "v_pk_add_f32 v[188:189], v[188:189], v[94:95]\n\t"
    "v_pk_add_f32 v[192:193], v[192:193], v[142:143]\n\t"
    "v_pk_add_f32 v[196:197], v[196:197], v[238:239]\n\t"
    "s_waitcnt lgkmcnt(5)\n\t"
    "v_mfma_f32_16x16x32_bf16 v[96:99], v[32:35], a[72:75], v[168:171]\n\t"
    "ds_read_b128 a[8:11], v15 offset:4608\n\t"
    "v_mfma_f32_16x16x32_bf16 v[144:147], v[32:35], a[76:79], v[172:175]\n\t"
    "ds_read_b128 a[12:15], v15 offset:6912\n\t"
    "v_mfma_f32_16x16x32_bf16 v[240:243], v[32:35], a[80:83], v[176:179]\n\t"
    "ds_read_b128 a[16:19], v15 offset:9216\n\t"
    "s_waitcnt lgkmcnt(6)\n\t"
    "v_smfmac_f32_16x16x64_bf16 v[96:99], v[36:39], a[64:71], v9\n\t"
    "ds_read_b128 a[0:3], v15 offset:0\n\t"
    "v_and_b32 v32, v5, v49\n\t"
    "v_and_b32 v33, v6, v49\n\t"
    "v_smfmac_f32_16x16x64_bf16 v[144:147], v[36:39], a[68:75], v9\n\t"
    "ds_read_b128 a[4:7], v15 offset:2304\n\t"
    "v_and_b32 v34, v7, v49\n\t"
    "v_smfmac_f32_16x16x64_bf16 v[240:243], v[36:39], a[72:79], v9\n\t"
    "ds_read_b128 a[20:23], v19 offset:0\n\t"
    "v_and_b32 v35, v8, v49\n\t"
    "s_waitcnt lgkmcnt(6)\n\t"
    "v_smfmac_f32_16x16x64_bf16 v[96:99], v[40:43], a[84:91], v9\n\t"
    "ds_read_b128 a[24:27], v19 offset:4608\n\t"
    "ds_read2_b32 v[48:49], v10 offset0:80 offset1:88\n\t"
    "v_and_b32 v36, v1, v51\n\t"
    "v_and_b32 v37, v2, v51\n\t"
    "v_smfmac_f32_16x16x64_bf16 v[240:243], v[40:43], a[88:95], v9\n\t"
    "ds_read_b128 a[28:31], v23 offset:0\n\t"
    "v_and_b32 v38, v3, v51\n\t"
    "v_smfmac_f32_16x16x64_bf16 v[144:147], v[44:47], a[84:91], v9\n\t"
    "v_and_b32 v39, v4, v51\n\t"
    "v_and_b32 v40, v1, v53\n\t"
    "v_and_b32 v41, v2, v53\n\t"
    "v_and_b32 v42, v3, v53\n\t"
    "v_and_b32 v43, v4, v53\n\t"
    "v_and_b32 v44, v1, v55\n\t"
    "v_and_b32 v45, v2, v55\n\t"
    "v_and_b32 v46, v3, v55\n\t"
    "v_and_b32 v47, v4, v55\n\t"
    "s_waitcnt lgkmcnt(6)\n\t"
    "v_mfma_f32_16x16x32_bf16 v[100:103], v[32:35], a[8:11], v[168:171]\n\t"
    "ds_read_b128 a[40:43], v16 offset:4608\n\t"
    "ds_read2_b32 v[50:51], v11 offset0:80 offset1:88\n\t"
    "v_mfma_f32_16x16x32_bf16 v[148:151], v[32:35], a[12:15], v[172:175]\n\t"
    "ds_read_b128 a[44:47], v16 offset:6912\n\t"
    "ds_read2_b32 v[52:53], v12 offset0:80 offset1:88\n\t"
    "v_mfma_f32_16x16x32_bf16 v[244:247], v[32:35], a[16:19], v[176:179]\n\t"
    "ds_read_b128 a[48:51], v16 offset:9216\n\t"
    "ds_read2_b32 v[54:55], v13 offset0:80 offset1:88\n\t"
    "s_waitcnt lgkmcnt(7)\n\t"
    "v_smfmac_f32_16x16x64_bf16 v[100:103], v[36:39], a[0:7], v9\n\t"
    "ds_read_b128 a[32:35], v16 offset:0\n\t"
    "v_and_b32 v32, v5, v48\n\t"
    "v_and_b32 v33, v6, v48\n\t"
    "v_smfmac_f32_16x16x64_bf16 v[148:151], v[36:39], a[4:11], v9\n\t"
    "ds_read_b128 a[36:39], v16 offset:2304\n\t"
    "v_and_b32 v34, v7, v48\n\t"
    "v_smfmac_f32_16x16x64_bf16 v[244:247], v[36:39], a[8:15], v9\n\t"
    "ds_read_b128 a[52:55], v20 offset:0\n\t"
    "v_and_b32 v35, v8, v48\n\t"
    "s_waitcnt lgkmcnt(7)\n\t"
    "v_smfmac_f32_16x16x64_bf16 v[100:103], v[40:43], a[20:27], v9\n\t"
    "ds_read_b128 a[56:59], v20 offset:4608\n\t"
    "v_and_b32 v36, v1, v50\n\t"
    "v_and_b32 v37, v2, v50\n\t"
    "v_smfmac_f32_16x16x64_bf16 v[244:247], v[40:43], a[24:31], v9\n\t"
    "ds_read_b128 a[60:63], v24 offset:0\n\t"
    "v_and_b32 v38, v3, v50\n\t"
    "v_smfmac_f32_16x16x64_bf16 v[148:151], v[44:47], a[20:27], v9\n\t"
    "v_and_b32 v39, v4, v50\n\t"
    "s_waitcnt lgkmcnt(5)\n\t"
    "v_and_b32 v40, v1, v52\n\t"
    "v_and_b32 v41, v2, v52\n\t"
    "v_and_b32 v42, v3, v52\n\t"
    "v_and_b32 v43, v4, v52\n\t"
    "v_and_b32 v44, v1, v54\n\t"
    "v_and_b32 v45, v2, v54\n\t"
    "v_and_b32 v46, v3, v54\n\t"
    "v_and_b32 v47, v4, v54\n\t"
    "v_mfma_f32_16x16x32_bf16 v[104:107], v[32:35], a[40:43], v[168:171]\n\t"
    "ds_read_b128 a[72:75], v17 offset:4608\n\t"
    "v_mfma_f32_16x16x32_bf16 v[152:155], v[32:35], a[44:47], v[172:175]\n\t"
    "ds_read_b128 a[76:79], v17 offset:6912\n\t"
    "v_mfma_f32_16x16x32_bf16 v[248:251], v[32:35], a[48:51], v[176:179]\n\t"
    "ds_read_b128 a[80:83], v17 offset:9216\n\t"
    "s_waitcnt lgkmcnt(6)\n\t"
    "v_smfmac_f32_16x16x64_bf16 v[104:107], v[36:39], a[32:39], v9\n\t"
    "ds_read_b128 a[64:67], v17 offset:0\n\t"
    "v_and_b32 v32, v5, v49\n\t"
    "v_and_b32 v33, v6, v49\n\t"
    "v_smfmac_f32_16x16x64_bf16 v[152:155], v[36:39], a[36:43], v9\n\t"
    "ds_read_b128 a[68:71], v17 offset:2304\n\t"
    "v_and_b32 v34, v7, v49\n\t"
    "v_smfmac_f32_16x16x64_bf16 v[248:251], v[36:39], a[40:47], v9\n\t"
    "ds_read_b128 a[84:87], v21 offset:0\n\t"
    "v_and_b32 v35, v8, v49\n\t"
    "s_waitcnt lgkmcnt(6)\n\t"
    "v_smfmac_f32_16x16x64_bf16 v[104:107], v[40:43], a[52:59], v9\n\t"
    "ds_read_b128 a[88:91], v21 offset:4608\n\t"
    "v_and_b32 v36, v1, v51\n\t"
    "v_and_b32 v37, v2, v51\n\t"
    "v_smfmac_f32_16x16x64_bf16 v[248:251], v[40:43], a[56:63], v9\n\t"
    "ds_read_b128 a[92:95], v25 offset:0\n\t"
    "v_and_b32 v38, v3, v51\n\t"
    "v_smfmac_f32_16x16x64_bf16 v[152:155], v[44:47], a[52:59], v9\n\t"
    "v_and_b32 v39, v4, v51\n\t"
    "v_and_b32 v40, v1, v53\n\t"
    "v_and_b32 v41, v2, v53\n\t"
    "v_and_b32 v42, v3, v53\n\t"
    "v_and_b32 v43, v4, v53\n\t"
    "v_and_b32 v44, v1, v55\n\t"
    "v_and_b32 v45, v2, v55\n\t"
    "v_and_b32 v46, v3, v55\n\t"
    "v_and_b32 v47, v4, v55\n\t"
    "s_waitcnt lgkmcnt(5)\n\t"
    "v_mfma_f32_16x16x32_bf16 v[108:111], v[32:35], a[72:75], v[168:171]\n\t"
    "v_mfma_f32_16x16x32_bf16 v[156:159], v[32:35], a[76:79], v[172:175]\n\t"
    "v_mfma_f32_16x16x32_bf16 v[252:255], v[32:35], a[80:83], v[176:179]\n\t"
    "s_waitcnt lgkmcnt(3)\n\t"
    "v_smfmac_f32_16x16x64_bf16 v[108:111], v[36:39], a[64:71], v9\n\t"
    "v_smfmac_f32_16x16x64_bf16 v[156:159], v[36:39], a[68:75], v9\n\t"
    "v_smfmac_f32_16x16x64_bf16 v[252:255], v[36:39], a[72:79], v9\n\t"
    "s_waitcnt lgkmcnt(0)\n\t"
    "v_smfmac_f32_16x16x64_bf16 v[108:111], v[40:43], a[84:91], v9\n\t"
    "v_smfmac_f32_16x16x64_bf16 v[252:255], v[40:43], a[88:95], v9\n\t"
    "v_smfmac_f32_16x16x64_bf16 v[156:159], v[44:47], a[84:91], v9\n\t"
    "v_pk_fma_f32 v[190:191], v[96:97], v[96:97], v[190:191]\n\t"
    "v_pk_fma_f32 v[194:195], v[144:145], v[144:145], v[194:195]\n\t"
    "v_pk_fma_f32 v[198:199], v[240:241], v[240:241], v[198:199]\n\t"
    "v_pk_add_f32 v[188:189], v[188:189], v[96:97]\n\t"
    "v_pk_add_f32 v[192:193], v[192:193], v[144:145]\n\t"
    "v_pk_add_f32 v[196:197], v[196:197], v[240:241]\n\t"
    "v_pk_fma_f32 v[190:191], v[98:99], v[98:99], v[190:191]\n\t"
    "v_pk_fma_f32 v[194:195], v[146:147], v[146:147], v[194:195]\n\t"
    "v_pk_fma_f32 v[198:199], v[242:243], v[242:243], v[198:199]\n\t"
    "v_pk_add_f32 v[188:189], v[188:189], v[98:99]\n\t"
    "v_pk_add_f32 v[192:193], v[192:193], v[146:147]\n\t"
    "v_pk_add_f32 v[196:197], v[196:197], v[242:243]\n\t"
    "v_pk_fma_f32 v[190:191], v[100:101], v[100:101], v[190:191]\n\t"
    "v_pk_fma_f32 v[194:195], v[148:149], v[148:149], v[194:195]\n\t"
    "v_pk_fma_f32 v[198:199], v[244:245], v[244:245], v[198:199]\n\t"
    "v_pk_add_f32 v[188:189], v[188:189], v[100:101]\n\t"
    "v_pk_add_f32 v[192:193], v[192:193], v[148:149]\n\t"
    "v_pk_add_f32 v[196:197], v[196:197], v[244:245]\n\t"
    "v_pk_fma_f32 v[190:191], v[102:103], v[102:103], v[190:191]\n\t"
    "v_pk_fma_f32 v[194:195], v[150:151], v[150:151], v[194:195]\n\t"
    "v_pk_fma_f32 v[198:199], v[246:247], v[246:247], v[198:199]\n\t"
    "v_pk_add_f32 v[188:189], v[188:189], v[102:103]\n\t"
    "v_pk_add_f32 v[192:193], v[192:193], v[150:151]\n\t"
    "v_pk_add_f32 v[196:197], v[196:197], v[246:247]\n\t"
    "v_pk_fma_f32 v[190:191], v[104:105], v[104:105], v[190:191]\n\t"
    "v_pk_fma_f32 v[194:195], v[152:153], v[152:153], v[194:195]\n\t"
    "v_pk_fma_f32 v[198:199], v[248:249], v[248:249], v[198:199]\n\t"
    "v_pk_add_f32 v[188:189], v[188:189], v[104:105]\n\t"
    "v_pk_add_f32 v[192:193], v[192:193], v[152:153]\n\t"
    "v_pk_add_f32 v[196:197], v[196:197], v[248:249]\n\t"
    "v_pk_fma_f32 v[190:191], v[106:107], v[106:107], v[190:191]\n\t"
    "v_pk_fma_f32 v[194:195], v[154:155], v[154:155], v[194:195]\n\t"
    "v_pk_fma_f32 v[198:199], v[250:251], v[250:251], v[198:199]\n\t"
    "v_pk_add_f32 v[188:189], v[188:189], v[106:107]\n\t"
    "v_pk_add_f32 v[192:193], v[192:193], v[154:155]\n\t"
    "v_pk_add_f32 v[196:197], v[196:197], v[250:251]\n\t"
    "v_pk_fma_f32 v[190:191], v[108:109], v[108:109], v[190:191]\n\t"
    "v_pk_fma_f32 v[194:195], v[156:157], v[156:157], v[194:195]\n\t"
    "v_pk_fma_f32 v[198:199], v[252:253], v[252:253], v[198:199]\n\t"
    "v_pk_add_f32 v[188:189], v[188:189], v[108:109]\n\t"
    "v_pk_add_f32 v[192:193], v[192:193], v[156:157]\n\t"
    "v_pk_add_f32 v[196:197], v[196:197], v[252:253]\n\t"
    "v_pk_fma_f32 v[190:191], v[110:111], v[110:111], v[190:191]\n\t"
    "v_pk_fma_f32 v[194:195], v[158:159], v[158:159], v[194:195]\n\t"
    "v_pk_fma_f32 v[198:199], v[254:255], v[254:255], v[198:199]\n\t"
    "v_pk_add_f32 v[188:189], v[188:189], v[110:111]\n\t"
    "v_pk_add_f32 v[192:193], v[192:193], v[158:159]\n\t"
    "v_pk_add_f32 v[196:197], v[196:197], v[254:255]\n\t"
    "v_add_f32 v188, v188, v189\n\t"
    "v_add_f32 v190, v190, v191\n\t"
    "v_add_f32 v192, v192, v193\n\t"
    "v_add_f32 v194, v194, v195\n\t"
    "v_add_f32 v196, v196, v197\n\t"
    "v_add_f32 v198, v198, v199\n\t"
    "v_mov_b32 v200, v196\n\t"
    "v_mov_b32 v202, v198\n\t"
    "v_permlane16_swap_b32 v188, v192\n\t"
    "v_permlane16_swap_b32 v190, v194\n\t"
    "v_permlane16_swap_b32 v196, v200\n\t"
    "v_permlane16_swap_b32 v198, v202\n\t"
    "v_add_f32 v188, v188, v192\n\t"
    "v_add_f32 v190, v190, v194\n\t"
    "v_add_f32 v196, v196, v200\n\t"
    "v_add_f32 v198, v198, v202\n\t"
    "s_nop 0\n\t"
    "v_permlane32_swap_b32 v188, v196\n\t"
    "v_permlane32_swap_b32 v190, v198\n\t"
    "v_add_f32 v204, v188, v196\n\t"
    "v_add_f32 v205, v190, v198\n\t"
    "buffer_load_dwordx4 v58, s[40:43], s63 offen offset:3072 lds\n\t"
    "s_add_u32 m0, s67, 12288\n\t"
    "s_add_u32 s63, s65, 147456\n\t"
    "buffer_load_dwordx4 v26, s[40:43], s63 offen offset:0 lds\n\t"
    "buffer_load_dwordx4 v27, s[40:43], s63 offen offset:1024 lds\n\t"
    "buffer_load_dwordx4 v28, s[40:43], s63 offen offset:2048 lds\n\t"
    "buffer_load_dwordx4 v58, s[40:43], s63 offen offset:3072 lds\n\t"
    "s_add_u32 s65, s65, 128\n\t"
    "s_waitcnt vmcnt(0)\n\t"
    "s_barrier\n\t"
    "v_and_b32 v1, 15, v0\n\t"
    "v_lshrrev_b32 v2, 4, v0\n\t"
    "v_bfe_u32 v4, v0, 2, 2\n\t"
    "v_and_b32 v5, 3, v0\n\t"
    "v_lshlrev_b32 v5, 3, v5\n\t"
    "v_lshl_add_u32 v5, v4, 7, v5\n\t"
    "v_and_b32 v4, 2, v4\n\t"
    "v_lshl_add_u32 v5, v4, 5, v5\n\t"
    "v_and_b32 v3, 1, v2\n\t"
    "v_mul_u32_u24 v3, 544, v3\n\t"
    "v_add_u32 v5, v5, v3\n\t"
    "v_lshrrev_b32 v3, 1, v2\n\t"
    "v_lshl_add_u32 v14, v3, 10, v5\n\t"
    "v_mul_u32_u24 v3, 3072, v3\n\t"
    "v_add_u32 v3, 55296, v3\n\t"
    "v_add_u32 v18, v3, v5\n\t"
    "v_xor_b32 v15, 32, v14\n\t"
    "v_xor_b32 v19, 32, v18\n\t"
    "v_xor_b32 v16, 64, v14\n\t"
    "v_xor_b32 v20, 64, v18\n\t"
    "v_xor_b32 v17, 96, v14\n\t"
    "v_xor_b32 v21, 96, v18\n\t"
    "v_lshlrev_b32 v22, 4, v2\n\t"
    "v_add_u32 v22, 153600, v22\n\t"
    "v_mul_f32 v204, 0x3baaaaab, v204\n\t"
    "v_mul_f32 v205, 0x3baaaaab, v205\n\t"
    "v_fma_f32 v205, -v204, v204, v205\n\t"
    "v_max_f32 v205, 0, v205\n\t"
    "v_add_f32 v205, 0x3727c5ac, v205\n\t"
    "v_rsq_f32 v205, v205\n\t"
    "s_nop 0\n\t"
    "v_mul_f32 v32, 0x3eefe414, v205\n\t"
    "v_mul_f32_e64 v33, -v204, v32\n\t"
    "s_add_u32 m0, s68, 0\n\t"
    "s_nop 0\n\t"
    "buffer_load_dwordx4 v29, s[44:47], s66 offen offset:0 lds\n\t"
    "v_mov_b32 v36, v32\n\t"
    "v_mov_b32 v37, v33\n\t"
    "s_nop 0\n\t"
    "v_permlane32_swap_b32 v32, v36\n\t"
    "v_permlane32_swap_b32 v33, v37\n\t"
    "v_mov_b32 v34, v32\n\t"
    "v_mov_b32 v35, v33\n\t"
    "v_mov_b32 v38, v36\n\t"
    "v_mov_b32 v39, v37\n\t"
    "v_permlane16_swap_b32 v32, v34\n\t"
    "buffer_load_dwordx4 v30, s[44:47], s66 offen offset:1024 lds\n\t"
    "v_permlane16_swap_b32 v33, v35\n\t"
    "v_permlane16_swap_b32 v36, v38\n\t"
    "v_permlane16_swap_b32 v37, v39\n\t"
    "v_pk_fma_f32 v[64:65], v[64:65], v[32:33], v[32:33] op_sel:[0,0,1] op_sel_hi:[1,0,1]\n\t"
    "v_pk_fma_f32 v[112:113], v[112:113], v[34:35], v[34:35] op_sel:[0,0,1] op_sel_hi:[1,0,1]\n\t"
    "v_cvt_pk_bf16_f32 v160, v64, v65\n\t"
    "v_pk_fma_f32 v[208:209], v[208:209], v[36:37], v[36:37] op_sel:[0,0,1] op_sel_hi:[1,0,1]\n\t"
    "v_cvt_pk_bf16_f32 v184, v112, v113\n\t"
    "v_pk_fma_f32 v[66:67], v[66:67], v[32:33], v[32:33] op_sel:[0,0,1] op_sel_hi:[1,0,1]\n\t"
    "buffer_load_dwordx4 v31, s[44:47], s66 offen offset:2048 lds\n\t"
    "v_cvt_pk_bf16_f32 v208, v208, v209\n\t"
    "v_pk_fma_f32 v[114:115], v[114:115], v[34:35], v[34:35] op_sel:[0,0,1] op_sel_hi:[1,0,1]\n\t"
    "v_cvt_pk_bf16_f32 v161, v66, v67\n\t"
    "v_pk_fma_f32 v[210:211], v[210:211], v[36:37], v[36:37] op_sel:[0,0,1] op_sel_hi:[1,0,1]\n\t"
    "v_cvt_pk_bf16_f32 v185, v114, v115\n\t"
    "v_pk_fma_f32 v[68:69], v[68:69], v[32:33], v[32:33] op_sel:[0,0,1] op_sel_hi:[1,0,1]\n\t"
    "v_cvt_pk_bf16_f32 v209, v210, v211\n\t"
    "v_pk_fma_f32 v[116:117], v[116:117], v[34:35], v[34:35] op_sel:[0,0,1] op_sel_hi:[1,0,1]\n\t"
    "v_cvt_pk_bf16_f32 v162, v68, v69\n\t"
    "buffer_load_dwordx4 v59, s[44:47], s66 offen offset:3072 lds\n\t"
    "s_add_u32 s66, s66, 12288\n\t"
    "v_pk_fma_f32 v[212:213], v[212:213], v[36:37], v[36:37] op_sel:[0,0,1] op_sel_hi:[1,0,1]\n\t"
    "v_cvt_pk_bf16_f32 v186, v116, v117\n\t"
    "v_pk_fma_f32 v[70:71], v[70:71], v[32:33], v[32:33] op_sel:[0,0,1] op_sel_hi:[1,0,1]\n\t"
    "v_cvt_pk_bf16_f32 v210, v212, v213\n\t"
    "v_pk_fma_f32 v[118:119], v[118:119], v[34:35], v[34:35] op_sel:[0,0,1] op_sel_hi:[1,0,1]\n\t"
    "v_cvt_pk_bf16_f32 v163, v70, v71\n\t"
    "v_pk_fma_f32 v[214:215], v[214:215], v[36:37], v[36:37] op_sel:[0,0,1] op_sel_hi:[1,0,1]\n\t"
    "v_cvt_pk_bf16_f32 v187, v118, v119\n\t"
    "v_pk_fma_f32 v[72:73], v[72:73], v[32:33], v[32:33] op_sel:[0,0,1] op_sel_hi:[1,0,1]\n\t"
    "v_cvt_pk_bf16_f32 v211, v214, v215\n\t"
    "v_pk_fma_f32 v[120:121], v[120:121], v[34:35], v[34:35] op_sel:[0,0,1] op_sel_hi:[1,0,1]\n\t"
    "v_cvt_pk_bf16_f32 v164, v72, v73\n\t"
    "v_pk_fma_f32 v[216:217], v[216:217], v[36:37], v[36:37] op_sel:[0,0,1] op_sel_hi:[1,0,1]\n\t"
    "v_cvt_pk_bf16_f32 v188, v120, v121\n\t"
    "v_pk_fma_f32 v[74:75], v[74:75], v[32:33], v[32:33] op_sel:[0,0,1] op_sel_hi:[1,0,1]\n\t"
    "v_cvt_pk_bf16_f32 v212, v216, v217\n\t"
    "v_pk_fma_f32 v[122:123], v[122:123], v[34:35], v[34:35] op_sel:[0,0,1] op_sel_hi:[1,0,1]\n\t"
    "v_cvt_pk_bf16_f32 v165, v74, v75\n\t"
    "v_pk_fma_f32 v[218:219], v[218:219], v[36:37], v[36:37] op_sel:[0,0,1] op_sel_hi:[1,0,1]\n\t"
    "v_cvt_pk_bf16_f32 v189, v122, v123\n\t"
    "v_pk_fma_f32 v[76:77], v[76:77], v[32:33], v[32:33] op_sel:[0,0,1] op_sel_hi:[1,0,1]\n\t"
    "v_cvt_pk_bf16_f32 v213, v218, v219\n\t"
    "v_pk_fma_f32 v[124:125], v[124:125], v[34:35], v[34:35] op_sel:[0,0,1] op_sel_hi:[1,0,1]\n\t"
    "v_cvt_pk_bf16_f32 v166, v76, v77\n\t"
    "v_pk_fma_f32 v[220:221], v[220:221], v[36:37], v[36:37] op_sel:[0,0,1] op_sel_hi:[1,0,1]\n\t"
    "v_cvt_pk_bf16_f32 v190, v124, v125\n\t"
    "v_pk_fma_f32 v[78:79], v[78:79], v[32:33], v[32:33] op_sel:[0,0,1] op_sel_hi:[1,0,1]\n\t"
    "v_cvt_pk_bf16_f32 v214, v220, v221\n\t"
    "v_pk_fma_f32 v[126:127], v[126:127], v[34:35], v[34:35] op_sel:[0,0,1] op_sel_hi:[1,0,1]\n\t"
    "v_cvt_pk_bf16_f32 v167, v78, v79\n\t"
    "v_pk_fma_f32 v[222:223], v[222:223], v[36:37], v[36:37] op_sel:[0,0,1] op_sel_hi:[1,0,1]\n\t"
    "v_cvt_pk_bf16_f32 v191, v126, v127\n\t"
    "v_pk_fma_f32 v[80:81], v[80:81], v[32:33], v[32:33] op_sel:[0,0,1] op_sel_hi:[1,0,1]\n\t"
    "v_cvt_pk_bf16_f32 v215, v222, v223\n\t"
    "v_pk_fma_f32 v[128:129], v[128:129], v[34:35], v[34:35] op_sel:[0,0,1] op_sel_hi:[1,0,1]\n\t"
    "v_cvt_pk_bf16_f32 v168, v80, v81\n\t"
    "v_pk_fma_f32 v[224:225], v[224:225], v[36:37], v[36:37] op_sel:[0,0,1] op_sel_hi:[1,0,1]\n\t"
    "v_cvt_pk_bf16_f32 v192, v128, v129\n\t"
    "v_pk_fma_f32 v[82:83], v[82:83], v[32:33], v[32:33] op_sel:[0,0,1] op_sel_hi:[1,0,1]\n\t"
    "v_cvt_pk_bf16_f32 v216, v224, v225\n\t"
    "v_pk_fma_f32 v[130:131], v[130:131], v[34:35], v[34:35] op_sel:[0,0,1] op_sel_hi:[1,0,1]\n\t"
    "v_cvt_pk_bf16_f32 v169, v82, v83\n\t"
    "v_pk_fma_f32 v[226:227], v[226:227], v[36:37], v[36:37] op_sel:[0,0,1] op_sel_hi:[1,0,1]\n\t"
    "v_cvt_pk_bf16_f32 v193, v130, v131\n\t"
    "v_pk_fma_f32 v[84:85], v[84:85], v[32:33], v[32:33] op_sel:[0,0,1] op_sel_hi:[1,0,1]\n\t"
    "v_cvt_pk_bf16_f32 v217, v226, v227\n\t"
    "v_pk_fma_f32 v[132:133], v[132:133], v[34:35], v[34:35] op_sel:[0,0,1] op_sel_hi:[1,0,1]\n\t"
    "v_cvt_pk_bf16_f32 v170, v84, v85\n\t"
    "v_pk_fma_f32 v[228:229], v[228:229], v[36:37], v[36:37] op_sel:[0,0,1] op_sel_hi:[1,0,1]\n\t"
    "v_cvt_pk_bf16_f32 v194, v132, v133\n\t"
    "v_pk_fma_f32 v[86:87], v[86:87], v[32:33], v[32:33] op_sel:[0,0,1] op_sel_hi:[1,0,1]\n\t"
    "v_cvt_pk_bf16_f32 v218, v228, v229\n\t"
    "v_pk_fma_f32 v[134:135], v[134:135], v[34:35], v[34:35] op_sel:[0,0,1] op_sel_hi:[1,0,1]\n\t"
    "v_cvt_pk_bf16_f32 v171, v86, v87\n\t"
    "v_pk_fma_f32 v[230:231], v[230:231], v[36:37], v[36:37] op_sel:[0,0,1] op_sel_hi:[1,0,1]\n\t"
    "v_cvt_pk_bf16_f32 v195, v134, v135\n\t"
    "v_pk_fma_f32 v[88:89], v[88:89], v[32:33], v[32:33] op_sel:[0,0,1] op_sel_hi:[1,0,1]\n\t"
    "v_cvt_pk_bf16_f32 v219, v230, v231\n\t"
    "v_pk_fma_f32 v[136:137], v[136:137], v[34:35], v[34:35] op_sel:[0,0,1] op_sel_hi:[1,0,1]\n\t"
    "v_cvt_pk_bf16_f32 v172, v88, v89\n\t"
    "v_pk_fma_f32 v[232:233], v[232:233], v[36:37], v[36:37] op_sel:[0,0,1] op_sel_hi:[1,0,1]\n\t"
    "v_cvt_pk_bf16_f32 v196, v136, v137\n\t"
    "v_pk_fma_f32 v[90:91], v[90:91], v[32:33], v[32:33] op_sel:[0,0,1] op_sel_hi:[1,0,1]\n\t"
    "v_cvt_pk_bf16_f32 v220, v232, v233\n\t"
    "v_pk_fma_f32 v[138:139], v[138:139], v[34:35], v[34:35] op_sel:[0,0,1] op_sel_hi:[1,0,1]\n\t"
    "v_cvt_pk_bf16_f32 v173, v90, v91\n\t"
    "v_pk_fma_f32 v[234:235], v[234:235], v[36:37], v[36:37] op_sel:[0,0,1] op_sel_hi:[1,0,1]\n\t"
    "v_cvt_pk_bf16_f32 v197, v138, v139\n\t"
    "v_pk_fma_f32 v[92:93], v[92:93], v[32:33], v[32:33] op_sel:[0,0,1] op_sel_hi:[1,0,1]\n\t"
    "v_cvt_pk_bf16_f32 v221, v234, v235\n\t"
    "v_pk_fma_f32 v[140:141], v[140:141], v[34:35], v[34:35] op_sel:[0,0,1] op_sel_hi:[1,0,1]\n\t"
    "v_cvt_pk_bf16_f32 v174, v92, v93\n\t"
    "v_pk_fma_f32 v[236:237], v[236:237], v[36:37], v[36:37] op_sel:[0,0,1] op_sel_hi:[1,0,1]\n\t"
    "v_cvt_pk_bf16_f32 v198, v140, v141\n\t"
    "v_pk_fma_f32 v[94:95], v[94:95], v[32:33], v[32:33] op_sel:[0,0,1] op_sel_hi:[1,0,1]\n\t"
    "v_cvt_pk_bf16_f32 v222, v236, v237\n\t"
    "v_pk_fma_f32 v[142:143], v[142:143], v[34:35], v[34:35] op_sel:[0,0,1] op_sel_hi:[1,0,1]\n\t"
    "v_cvt_pk_bf16_f32 v175, v94, v95\n\t"
    "v_pk_fma_f32 v[238:239], v[238:239], v[36:37], v[36:37] op_sel:[0,0,1] op_sel_hi:[1,0,1]\n\t"
    "v_cvt_pk_bf16_f32 v199, v142, v143\n\t"
    "ds_read_b64_tr_b16 a[192:193], v14 offset:0\n\t"
    "ds_read_b64_tr_b16 a[194:195], v14 offset:2048\n\t"
    "ds_read_b64_tr_b16 a[196:197], v14 offset:4096\n\t"
    "ds_read_b64_tr_b16 a[198:199], v14 offset:6144\n\t"
    "ds_read_b64_tr_b16 a[200:201], v14 offset:8192\n\t"
    "ds_read_b64_tr_b16 a[202:203], v14 offset:10240\n\t"
    "ds_read_b64_tr_b16 a[204:205], v14 offset:12288\n\t"
    "ds_read_b64_tr_b16 a[206:207], v14 offset:14336\n\t"
    "v_pk_fma_f32 v[96:97], v[96:97], v[32:33], v[32:33] op_sel:[0,0,1] op_sel_hi:[1,0,1]\n\t"
    "v_cvt_pk_bf16_f32 v223, v238, v239\n\t"
    "v_pk_fma_f32 v[144:145], v[144:145], v[34:35], v[34:35] op_sel:[0,0,1] op_sel_hi:[1,0,1]\n\t"
    "v_cvt_pk_bf16_f32 v176, v96, v97\n\t"
    "v_pk_fma_f32 v[240:241], v[240:241], v[36:37], v[36:37] op_sel:[0,0,1] op_sel_hi:[1,0,1]\n\t"
    "v_cvt_pk_bf16_f32 v200, v144, v145\n\t"
    "v_pk_fma_f32 v[98:99], v[98:99], v[32:33], v[32:33] op_sel:[0,0,1] op_sel_hi:[1,0,1]\n\t"
    "v_cvt_pk_bf16_f32 v224, v240, v241\n\t"
    "v_pk_fma_f32 v[146:147], v[146:147], v[34:35], v[34:35] op_sel:[0,0,1] op_sel_hi:[1,0,1]\n\t"
    "v_cvt_pk_bf16_f32 v177, v98, v99\n\t"
    "v_pk_fma_f32 v[242:243], v[242:243], v[36:37], v[36:37] op_sel:[0,0,1] op_sel_hi:[1,0,1]\n\t"
    "v_cvt_pk_bf16_f32 v201, v146, v147\n\t"
    "v_pk_fma_f32 v[100:101], v[100:101], v[32:33], v[32:33] op_sel:[0,0,1] op_sel_hi:[1,0,1]\n\t"
    "v_cvt_pk_bf16_f32 v225, v242, v243\n\t"
    "v_pk_fma_f32 v[148:149], v[148:149], v[34:35], v[34:35] op_sel:[0,0,1] op_sel_hi:[1,0,1]\n\t"
    "v_cvt_pk_bf16_f32 v178, v100, v101\n\t"
    "v_pk_fma_f32 v[244:245], v[244:245], v[36:37], v[36:37] op_sel:[0,0,1] op_sel_hi:[1,0,1]\n\t"
    "v_cvt_pk_bf16_f32 v202, v148, v149\n\t"
    "v_pk_fma_f32 v[102:103], v[102:103], v[32:33], v[32:33] op_sel:[0,0,1] op_sel_hi:[1,0,1]\n\t"
    "v_cvt_pk_bf16_f32 v226, v244, v245\n\t"
    "v_pk_fma_f32 v[150:151], v[150:151], v[34:35], v[34:35] op_sel:[0,0,1] op_sel_hi:[1,0,1]\n\t"
    "v_cvt_pk_bf16_f32 v179, v102, v103\n\t"
    "v_pk_fma_f32 v[246:247], v[246:247], v[36:37], v[36:37] op_sel:[0,0,1] op_sel_hi:[1,0,1]\n\t"
    "v_cvt_pk_bf16_f32 v203, v150, v151\n\t"
    "ds_read_b64_tr_b16 a[208:209], v14 offset:16384\n\t"
    "ds_read_b64_tr_b16 a[210:211], v14 offset:18432\n\t"
    "ds_read_b64_tr_b16 a[212:213], v14 offset:20480\n\t"
    "ds_read_b64_tr_b16 a[214:215], v14 offset:22528\n\t"
    "ds_read_b128 v[48:51], v22 offset:0\n\t"
    "ds_read_b128 v[52:55], v22 offset:64\n\t"
    "v_pk_fma_f32 v[104:105], v[104:105], v[32:33], v[32:33] op_sel:[0,0,1] op_sel_hi:[1,0,1]\n\t"
    "v_cvt_pk_bf16_f32 v227, v246, v247\n\t"
    "v_pk_fma_f32 v[152:153], v[152:153], v[34:35], v[34:35] op_sel:[0,0,1] op_sel_hi:[1,0,1]\n\t"
    "v_cvt_pk_bf16_f32 v180, v104, v105\n\t"
    "v_pk_fma_f32 v[248:249], v[248:249], v[36:37], v[36:37] op_sel:[0,0,1] op_sel_hi:[1,0,1]\n\t"
    "v_cvt_pk_bf16_f32 v204, v152, v153\n\t"
    "v_pk_fma_f32 v[106:107], v[106:107], v[32:33], v[32:33] op_sel:[0,0,1] op_sel_hi:[1,0,1]\n\t"
    "v_cvt_pk_bf16_f32 v228, v248, v249\n\t"
    "v_pk_fma_f32 v[154:155], v[154:155], v[34:35], v[34:35] op_sel:[0,0,1] op_sel_hi:[1,0,1]\n\t"
    "v_cvt_pk_bf16_f32 v181, v106, v107\n\t"
    "v_pk_fma_f32 v[250:251], v[250:251], v[36:37], v[36:37] op_sel:[0,0,1] op_sel_hi:[1,0,1]\n\t"
    "v_cvt_pk_bf16_f32 v205, v154, v155\n\t"
    "v_pk_fma_f32 v[108:109], v[108:109], v[32:33], v[32:33] op_sel:[0,0,1] op_sel_hi:[1,0,1]\n\t"
    "v_cvt_pk_bf16_f32 v229, v250, v251\n\t"
    "v_pk_fma_f32 v[156:157], v[156:157], v[34:35], v[34:35] op_sel:[0,0,1] op_sel_hi:[1,0,1]\n\t"
    "v_cvt_pk_bf16_f32 v182, v108, v109\n\t"
    "v_pk_fma_f32 v[252:253], v[252:253], v[36:37], v[36:37] op_sel:[0,0,1] op_sel_hi:[1,0,1]\n\t"
    "v_cvt_pk_bf16_f32 v206, v156, v157\n\t"
    "v_pk_fma_f32 v[110:111], v[110:111], v[32:33], v[32:33] op_sel:[0,0,1] op_sel_hi:[1,0,1]\n\t"
    "v_cvt_pk_bf16_f32 v230, v252, v253\n\t"
    "v_pk_fma_f32 v[158:159], v[158:159], v[34:35], v[34:35] op_sel:[0,0,1] op_sel_hi:[1,0,1]\n\t"
    "v_cvt_pk_bf16_f32 v183, v110, v111\n\t"
    "v_pk_fma_f32 v[254:255], v[254:255], v[36:37], v[36:37] op_sel:[0,0,1] op_sel_hi:[1,0,1]\n\t"
    "v_cvt_pk_bf16_f32 v207, v158, v159\n\t"
    "v_cvt_pk_bf16_f32 v231, v254, v255\n\t"
    "v_add_u32 v23, 3072, v22\n\t"
    "s_waitcnt lgkmcnt(0)\n\t"
    "v_mfma_f32_16x16x32_bf16 v[64:67], a[192:195], v[160:163], v[48:51]\n\t"
    "ds_read_b64_tr_b16 a[216:217], v15 offset:0\n\t"
    "ds_read_b128 a[0:3], v23 offset:0\n\t"
    "v_mfma_f32_16x16x32_bf16 v[68:71], a[192:195], v[184:187], v[48:51]\n\t"
    "ds_read_b64_tr_b16 a[218:219], v15 offset:2048\n\t"
    "ds_read_b128 a[4:7], v23 offset:0\n\t"
    "s_add_u32 m0, s67, 24576\n\t"
    "s_add_u32 s63, s65, 0\n\t"
    "v_mfma_f32_16x16x32_bf16 v[72:75], a[192:195], v[208:211], v[48:51]\n\t"
    "buffer_load_dwordx4 v26, s[40:43], s63 offen offset:0 lds\n\t"
    "ds_read_b128 a[8:11], v23 offset:0\n\t"
    "v_mfma_f32_16x16x32_bf16 v[64:67], a[196:199], v[164:167], v[64:67]\n\t"
    "ds_read_b64_tr_b16 a[220:221], v15 offset:4096\n\t"
    "ds_read_b128 a[12:15], v23 offset:64\n\t"
    "v_mfma_f32_16x16x32_bf16 v[68:71], a[196:199], v[188:191], v[68:71]\n\t"
    "ds_read_b64_tr_b16 a[222:223], v15 offset:6144\n\t"
    "buffer_load_dwordx4 v27, s[40:43], s63 offen offset:1024 lds\n\t"
    "ds_read_b128 a[16:19], v23 offset:64\n\t"
    "v_mfma_f32_16x16x32_bf16 v[72:75], a[196:199], v[212:215], v[72:75]\n\t"
    "ds_read_b128 a[20:23], v23 offset:64\n\t"
    "s_waitcnt lgkmcnt(7)\n\t"
    "v_mfma_f32_16x16x32_bf16 v[64:67], a[200:203], v[168:171], v[64:67]\n\t"
    "ds_read_b64_tr_b16 a[224:225], v15 offset:8192\n\t"
    "buffer_load_dwordx4 v28, s[40:43], s63 offen offset:2048 lds\n\t"
    "ds_read_b128 a[24:27], v23 offset:128\n\t"
    "v_mfma_f32_16x16x32_bf16 v[68:71], a[200:203], v[192:195], v[68:71]\n\t"
    "ds_read_b64_tr_b16 a[226:227], v15 offset:10240\n\t"
    "ds_read_b128 a[28:31], v23 offset:128\n\t"
    "v_mfma_f32_16x16x32_bf16 v[72:75], a[200:203], v[216:219], v[72:75]\n\t"
    "ds_read_b128 v[48:51], v22 offset:128\n\t"
    "buffer_load_dwordx4 v58, s[40:43], s63 offen offset:3072 lds\n\t"
    "ds_read_b128 a[32:35], v23 offset:128\n\t"
    "s_waitcnt lgkmcnt(8)\n\t"
    "v_mfma_f32_16x16x32_bf16 v[64:67], a[204:207], v[172:175], v[64:67]\n\t"
    "ds_read_b64_tr_b16 a[228:229], v15 offset:12288\n\t"
    "ds_read_b128 a[36:39], v23 offset:192\n\t"
    "s_add_u32 m0, s67, 36864\n\t"
    "s_add_u32 s63, s65, 147456\n\t"
    "v_mfma_f32_16x16x32_bf16 v[68:71], a[204:207], v[196:199], v[68:71]\n\t"
    "ds_read_b64_tr_b16 a[230:231], v15 offset:14336\n\t"
    "buffer_load_dwordx4 v26, s[40:43], s63 offen offset:0 lds\n\t"
    "ds_read_b128 a[40:43], v23 offset:192\n\t"
    "v_mfma_f32_16x16x32_bf16 v[72:75], a[204:207], v[220:223], v[72:75]\n\t"
    "ds_read_b128 a[44:47], v23 offset:192\n\t"
    "s_waitcnt lgkmcnt(8)\n\t"
    "v_mfma_f32_16x16x32_bf16 v[64:67], a[208:211], v[176:179], v[64:67]\n\t"
    "ds_read_b64_tr_b16 a[232:233], v15 offset:16384\n\t"
    "buffer_load_dwordx4 v27, s[40:43], s63 offen offset:1024 lds\n\t"
    "ds_read_b128 a[48:51], v23 offset:256\n\t"
    "v_mfma_f32_16x16x32_bf16 v[68:71], a[208:211], v[200:203], v[68:71]\n\t"
    "ds_read_b64_tr_b16 a[234:235], v15 offset:18432\n\t"
    "ds_read_b128 a[52:55], v23 offset:256\n\t"
    "v_mfma_f32_16x16x32_bf16 v[72:75], a[208:211], v[224:227], v[72:75]\n\t"
    "buffer_load_dwordx4 v28, s[40:43], s63 offen offset:2048 lds\n\t"
    "ds_read_b128 a[56:59], v23 offset:256\n\t"
    "s_waitcnt lgkmcnt(7)\n\t"
    "v_mfma_f32_16x16x32_bf16 v[64:67], a[212:215], v[180:183], v[64:67]\n\t"
    "ds_read_b64_tr_b16 a[236:237], v15 offset:20480\n\t"
    "ds_read_b128 a[60:63], v23 offset:320\n\t"
    "v_mfma_f32_16x16x32_bf16 v[68:71], a[212:215], v[204:207], v[68:71]\n\t"
    "ds_read_b64_tr_b16 a[238:239], v15 offset:22528\n\t"
    "buffer_load_dwordx4 v58, s[40:43], s63 offen offset:3072 lds\n\t"
    "ds_read_b128 a[64:67], v23 offset:320\n\t"
    "v_mfma_f32_16x16x32_bf16 v[72:75], a[212:215], v[228:231], v[72:75]\n\t"
    "ds_read_b128 a[68:71], v23 offset:320\n\t"
    "s_add_u32 s65, s65, 128\n\t"
    "s_add_u32 m0, s68, 12288\n\t"
    "s_waitcnt lgkmcnt(7)\n\t"
    "v_mfma_f32_16x16x32_bf16 v[80:83], a[216:219], v[160:163], v[52:55]\n\t"
    "ds_read_b64_tr_b16 a[192:193], v16 offset:0\n\t"
    "buffer_load_dwordx4 v29, s[44:47], s66 offen offset:0 lds\n\t"
    "ds_read_b128 a[72:75], v23 offset:384\n\t"
    "v_mfma_f32_16x16x32_bf16 v[84:87], a[216:219], v[184:187], v[52:55]\n\t"
    "ds_read_b64_tr_b16 a[194:195], v16 offset:2048\n\t"
    "ds_read_b128 a[76:79], v23 offset:384\n\t"
    "v_mfma_f32_16x16x32_bf16 v[88:91], a[216:219], v[208:211], v[52:55]\n\t"
    "buffer_load_dwordx4 v30, s[44:47], s66 offen offset:1024 lds\n\t"
    "ds_read_b128 a[80:83], v23 offset:384\n\t"
    "s_waitcnt lgkmcnt(7)\n\t"
    "v_mfma_f32_16x16x32_bf16 v[80:83], a[220:223], v[164:167], v[80:83]\n\t"
    "ds_read_b64_tr_b16 a[196:197], v16 offset:4096\n\t"
    "ds_read_b128 a[84:87], v23 offset:448\n\t"
    "v_mfma_f32_16x16x32_bf16 v[84:87], a[220:223], v[188:191], v[84:87]\n\t"
    "ds_read_b64_tr_b16 a[198:199], v16 offset:6144\n\t"
    "buffer_load_dwordx4 v31, s[44:47], s66 offen offset:2048 lds\n\t"
    "ds_read_b128 a[88:91], v23 offset:448\n\t"
    "v_mfma_f32_16x16x32_bf16 v[88:91], a[220:223], v[212:215], v[88:91]\n\t"
    "ds_read_b128 a[92:95], v23 offset:448\n\t"
    "s_waitcnt lgkmcnt(7)\n\t"
    "v_mfma_f32_16x16x32_bf16 v[80:83], a[224:227], v[168:171], v[80:83]\n\t"
    "ds_read_b64_tr_b16 a[200:201], v16 offset:8192\n\t"
    "buffer_load_dwordx4 v59, s[44:47], s66 offen offset:3072 lds\n\t"
    "ds_read_b128 a[96:99], v23 offset:512\n\t"
    "v_cvt_pk_f16_f32 v32, v64, v65\n\t"
    "v_mfma_f32_16x16x32_bf16 v[84:87], a[224:227], v[192:195], v[84:87]\n\t"
    "ds_read_b64_tr_b16 a[202:203], v16 offset:10240\n\t"
    "ds_read_b128 a[100:103], v23 offset:512\n\t"
    "v_cvt_pk_f16_f32 v33, v66, v67\n\t"
    "v_pk_fma_f16 v34, v32, v32, s60 neg_lo:[1,0,1] neg_hi:[1,0,1]\n\t"
    "s_add_u32 s66, s66, 12288\n\t"
    "v_mfma_f32_16x16x32_bf16 v[88:91], a[224:227], v[216:219], v[88:91]\n\t"
    "ds_read_b128 v[52:55], v22 offset:192\n\t"
    "ds_read_b128 a[104:107], v23 offset:512\n\t"
    "v_pk_fma_f16 v35, v33, v33, s60 neg_lo:[1,0,1] neg_hi:[1,0,1]\n\t"
    "s_waitcnt lgkmcnt(8)\n\t"
    "v_mfma_f32_16x16x32_bf16 v[80:83], a[228:231], v[172:175], v[80:83]\n\t"
    "ds_read_b64_tr_b16 a[204:205], v16 offset:12288\n\t"
    "ds_read_b128 a[108:111], v23 offset:576\n\t"
    "v_pk_mul_f16 v34, v34, v32\n\t"
    "v_pk_mul_f16 v35, v35, v33\n\t"
    "v_exp_f16 v36, v34\n\t"
    "v_mfma_f32_16x16x32_bf16 v[84:87], a[228:231], v[196:199], v[84:87]\n\t"
    "ds_read_b64_tr_b16 a[206:207], v16 offset:14336\n\t"
    "ds_read_b128 a[112:115], v23 offset:576\n\t"
    "v_exp_f16 v37, v35\n\t"
    "v_mfma_f32_16x16x32_bf16 v[88:91], a[228:231], v[220:223], v[88:91]\n\t"
    "ds_read_b128 a[116:119], v23 offset:576\n\t"
    "v_exp_f16_sdwa v36, v34 dst_sel:WORD_1 dst_unused:UNUSED_PRESERVE src0_sel:WORD_1\n\t"
    "s_waitcnt lgkmcnt(8)\n\t"
    "v_mfma_f32_16x16x32_bf16 v[80:83], a[232:235], v[176:179], v[80:83]\n\t"
    "ds_read_b64_tr_b16 a[208:209], v16 offset:16384\n\t"
    "ds_read_b128 a[120:123], v23 offset:640\n\t"
    "v_exp_f16_sdwa v37, v35 dst_sel:WORD_1 dst_unused:UNUSED_PRESERVE src0_sel:WORD_1\n\t"
    "v_pk_add_f16 v36, v36, 1.0 op_sel_hi:[1,0]\n\t"
    "v_mfma_f32_16x16x32_bf16 v[84:87], a[232:235], v[200:203], v[84:87]\n\t"
    "ds_read_b64_tr_b16 a[210:211], v16 offset:18432\n\t"
    "ds_read_b128 a[124:127], v23 offset:640\n\t"
    "v_pk_add_f16 v37, v37, 1.0 op_sel_hi:[1,0]\n\t"
    "v_rcp_f16 v34, v36\n\t"
    "v_mfma_f32_16x16x32_bf16 v[88:91], a[232:235], v[224:227], v[88:91]\n\t"
    "ds_read_b128 a[128:131], v23 offset:640\n\t"
    "v_rcp_f16 v35, v37\n\t"
    "s_waitcnt lgkmcnt(7)\n\t"
    "v_mfma_f32_16x16x32_bf16 v[80:83], a[236:239], v[180:183], v[80:83]\n\t"
    "ds_read_b64_tr_b16 a[212:213], v16 offset:20480\n\t"
    "ds_read_b128 a[132:135], v23 offset:704\n\t"
    "v_rcp_f16_sdwa v34, v36 dst_sel:WORD_1 dst_unused:UNUSED_PRESERVE src0_sel:WORD_1\n\t"
    "v_rcp_f16_sdwa v35, v37 dst_sel:WORD_1 dst_unused:UNUSED_PRESERVE src0_sel:WORD_1\n\t"
    "v_mfma_f32_16x16x32_bf16 v[84:87], a[236:239], v[204:207], v[84:87]\n\t"
    "ds_read_b64_tr_b16 a[214:215], v16 offset:22528\n\t"
    "ds_read_b128 a[136:139], v23 offset:704\n\t"
    "v_fma_mix_f32 v64, v64, v34, 0 op_sel_hi:[0,1,0]\n\t"
    "v_fma_mix_f32 v66, v66, v35, 0 op_sel_hi:[0,1,0]\n\t"
    "v_mfma_f32_16x16x32_bf16 v[88:91], a[236:239], v[228:231], v[88:91]\n\t"
    "ds_read_b128 a[140:143], v23 offset:704\n\t"
    "v_fma_mix_f32 v65, v65, v34, 0 op_sel:[0,1,0] op_sel_hi:[0,1,0]\n\t"
    "v_fma_mix_f32 v67, v67, v35, 0 op_sel:[0,1,0] op_sel_hi:[0,1,0]\n\t"
    "v_cvt_pk_bf16_f32 v128, v64, v65\n\t"
    "v_cvt_pk_bf16_f32 v129, v66, v67\n\t"
    "v_mfma_f32_16x16x32_bf16 v[96:99], a[192:195], v[160:163], v[48:51]\n\t"
    "ds_read_b64_tr_b16 a[216:217], v17 offset:0\n\t"
    "v_cvt_pk_f16_f32 v38, v68, v69\n\t"
    "v_cvt_pk_f16_f32 v39, v70, v71\n\t"
    "v_pk_fma_f16 v40, v38, v38, s60 neg_lo:[1,0,1] neg_hi:[1,0,1]\n\t"
    "v_pk_fma_f16 v41, v39, v39, s60 neg_lo:[1,0,1] neg_hi:[1,0,1]\n\t"
    "v_pk_mul_f16 v40, v40, v38\n\t"
    "v_mfma_f32_16x16x32_bf16 v[100:103], a[192:195], v[184:187], v[48:51]\n\t"
    "ds_read_b64_tr_b16 a[218:219], v17 offset:2048\n\t"
    "v_pk_mul_f16 v41, v41, v39\n\t"
    "v_exp_f16 v42, v40\n\t"
    "v_exp_f16 v43, v41\n\t"
    "v_mfma_f32_16x16x32_bf16 v[104:107], a[192:195], v[208:211], v[48:51]\n\t"
    "v_exp_f16_sdwa v42, v40 dst_sel:WORD_1 dst_unused:UNUSED_PRESERVE src0_sel:WORD_1\n\t"
    "v_exp_f16_sdwa v43, v41 dst_sel:WORD_1 dst_unused:UNUSED_PRESERVE src0_sel:WORD_1\n\t"
    "v_pk_add_f16 v42, v42, 1.0 op_sel_hi:[1,0]\n\t"
    "v_pk_add_f16 v43, v43, 1.0 op_sel_hi:[1,0]\n\t"
    "v_mfma_f32_16x16x32_bf16 v[96:99], a[196:199], v[164:167], v[96:99]\n\t"
    "ds_read_b64_tr_b16 a[220:221], v17 offset:4096\n\t"
    "v_rcp_f16 v40, v42\n\t"
    "v_rcp_f16 v41, v43\n\t"
    "v_rcp_f16_sdwa v40, v42 dst_sel:WORD_1 dst_unused:UNUSED_PRESERVE src0_sel:WORD_1\n\t"
    "v_mfma_f32_16x16x32_bf16 v[100:103], a[196:199], v[188:191], v[100:103]\n\t"
    "s_waitcnt lgkmcnt(11)\n\t"
    "ds_read_b64_tr_b16 a[222:223], v17 offset:6144\n\t"
    "v_rcp_f16_sdwa v41, v43 dst_sel:WORD_1 dst_unused:UNUSED_PRESERVE src0_sel:WORD_1\n\t"
    "v_fma_mix_f32 v68, v68, v40, 0 op_sel_hi:[0,1,0]\n\t"
    "v_fma_mix_f32 v70, v70, v41, 0 op_sel_hi:[0,1,0]\n\t"
    "v_mfma_f32_16x16x32_bf16 v[104:107], a[196:199], v[212:215], v[104:107]\n\t"
    "v_fma_mix_f32 v69, v69, v40, 0 op_sel:[0,1,0] op_sel_hi:[0,1,0]\n\t"
    "v_fma_mix_f32 v71, v71, v41, 0 op_sel:[0,1,0] op_sel_hi:[0,1,0]\n\t"
    "v_cvt_pk_bf16_f32 v132, v68, v69\n\t"
    "v_cvt_pk_bf16_f32 v133, v70, v71\n\t"
    "v_cvt_pk_f16_f32 v32, v72, v73\n\t"
    "v_cvt_pk_f16_f32 v33, v74, v75\n\t"
    "v_mfma_f32_16x16x32_bf16 v[96:99], a[200:203], v[168:171], v[96:99]\n\t"
    "ds_read_b64_tr_b16 a[224:225], v17 offset:8192\n\t"
    "v_pk_fma_f16 v34, v32, v32, s60 neg_lo:[1,0,1] neg_hi:[1,0,1]\n\t"
    "v_pk_fma_f16 v35, v33, v33, s60 neg_lo:[1,0,1] neg_hi:[1,0,1]\n\t"
    "v_pk_mul_f16 v34, v34, v32\n\t"
    "v_pk_mul_f16 v35, v35, v33\n\t"
    "v_exp_f16 v36, v34\n\t"
    "v_mfma_f32_16x16x32_bf16 v[100:103], a[200:203], v[192:195], v[100:103]\n\t"
    "ds_read_b64_tr_b16 a[226:227], v17 offset:10240\n\t"
    "v_exp_f16 v37, v35\n\t"
    "v_exp_f16_sdwa v36, v34 dst_sel:WORD_1 dst_unused:UNUSED_PRESERVE src0_sel:WORD_1\n\t"
    "v_mfma_f32_16x16x32_bf16 v[104:107], a[200:203], v[216:219], v[104:107]\n\t"
    "v_exp_f16_sdwa v37, v35 dst_sel:WORD_1 dst_unused:UNUSED_PRESERVE src0_sel:WORD_1\n\t"
    "v_pk_add_f16 v36, v36, 1.0 op_sel_hi:[1,0]\n\t"
    "v_pk_add_f16 v37, v37, 1.0 op_sel_hi:[1,0]\n\t"
    "v_rcp_f16 v34, v36\n\t"
    "v_mfma_f32_16x16x32_bf16 v[96:99], a[204:207], v[172:175], v[96:99]\n\t"
    "ds_read_b64_tr_b16 a[228:229], v17 offset:12288\n\t"
    "v_rcp_f16 v35, v37\n\t"
    "v_rcp_f16_sdwa v34, v36 dst_sel:WORD_1 dst_unused:UNUSED_PRESERVE src0_sel:WORD_1\n\t"
    "v_rcp_f16_sdwa v35, v37 dst_sel:WORD_1 dst_unused:UNUSED_PRESERVE src0_sel:WORD_1\n\t"
    "v_mfma_f32_16x16x32_bf16 v[100:103], a[204:207], v[196:199], v[100:103]\n\t"
    "s_waitcnt lgkmcnt(11)\n\t"
    "ds_read_b64_tr_b16 a[230:231], v17 offset:14336\n\t"
    "v_fma_mix_f32 v72, v72, v34, 0 op_sel_hi:[0,1,0]\n\t"
    "v_fma_mix_f32 v74, v74, v35, 0 op_sel_hi:[0,1,0]\n\t"
    "v_fma_mix_f32 v73, v73, v34, 0 op_sel:[0,1,0] op_sel_hi:[0,1,0]\n\t"
    "v_fma_mix_f32 v75, v75, v35, 0 op_sel:[0,1,0] op_sel_hi:[0,1,0]\n\t"
    "v_cvt_pk_bf16_f32 v136, v72, v73\n\t"
    "v_mfma_f32_16x16x32_bf16 v[104:107], a[204:207], v[220:223], v[104:107]\n\t"
    "v_cvt_pk_bf16_f32 v137, v74, v75\n\t"
    "v_cvt_pk_f16_f32 v38, v80, v81\n\t"
    "v_cvt_pk_f16_f32 v39, v82, v83\n\t"
    "s_waitcnt vmcnt(12)\n\t"
    "s_barrier\n\t"
    "s_waitcnt lgkmcnt(4)\n\t"
    "v_mfma_f32_16x16x32_bf16 v[96:99], a[208:211], v[176:179], v[96:99]\n\t"
    "ds_read_b64_tr_b16 a[232:233], v17 offset:16384\n\t"
    "v_pk_fma_f16 v40, v38, v38, s60 neg_lo:[1,0,1] neg_hi:[1,0,1]\n\t"
    "v_pk_fma_f16 v41, v39, v39, s60 neg_lo:[1,0,1] neg_hi:[1,0,1]\n\t"
    "v_pk_mul_f16 v40, v40, v38\n\t"
    "v_pk_mul_f16 v41, v41, v39\n\t"
    "v_mfma_f32_16x16x32_bf16 v[100:103], a[208:211], v[200:203], v[100:103]\n\t"
    "ds_read_b64_tr_b16 a[234:235], v17 offset:18432\n\t"
    "v_exp_f16 v42, v40\n\t"
    "v_exp_f16 v43, v41\n\t"
    "s_add_u32 m0, s68, 24576\n\t"
    "v_mfma_f32_16x16x32_bf16 v[104:107], a[208:211], v[224:227], v[104:107]\n\t"
    "buffer_load_dwordx4 v29, s[44:47], s66 offen offset:0 lds\n\t"
    "v_exp_f16_sdwa v42, v40 dst_sel:WORD_1 dst_unused:UNUSED_PRESERVE src0_sel:WORD_1\n\t"
    "v_exp_f16_sdwa v43, v41 dst_sel:WORD_1 dst_unused:UNUSED_PRESERVE src0_sel:WORD_1\n\t"
    "v_pk_add_f16 v42, v42, 1.0 op_sel_hi:[1,0]\n\t"
    "v_mfma_f32_16x16x32_bf16 v[96:99], a[212:215], v[180:183], v[96:99]\n\t"
    "ds_read_b64_tr_b16 a[236:237], v17 offset:20480\n\t"
    "v_pk_add_f16 v43, v43, 1.0 op_sel_hi:[1,0]\n\t"
    "v_rcp_f16 v40, v42\n\t"
    "v_rcp_f16 v41, v43\n\t"
    "v_mfma_f32_16x16x32_bf16 v[100:103], a[212:215], v[204:207], v[100:103]\n\t"
    "ds_read_b64_tr_b16 a[238:239], v17 offset:22528\n\t"
    "v_rcp_f16_sdwa v40, v42 dst_sel:WORD_1 dst_unused:UNUSED_PRESERVE src0_sel:WORD_1\n\t"
    "v_rcp_f16_sdwa v41, v43 dst_sel:WORD_1 dst_unused:UNUSED_PRESERVE src0_sel:WORD_1\n\t"
    "v_fma_mix_f32 v80, v80, v40, 0 op_sel_hi:[0,1,0]\n\t"
    "v_mfma_f32_16x16x32_bf16 v[104:107], a[212:215], v[228:231], v[104:107]\n\t"
    "buffer_load_dwordx4 v30, s[44:47], s66 offen offset:1024 lds\n\t"
    "v_fma_mix_f32 v82, v82, v41, 0 op_sel_hi:[0,1,0]\n\t"
    "v_fma_mix_f32 v81, v81, v40, 0 op_sel:[0,1,0] op_sel_hi:[0,1,0]\n\t"
    "v_fma_mix_f32 v83, v83, v41, 0 op_sel:[0,1,0] op_sel_hi:[0,1,0]\n\t"
    "v_cvt_pk_bf16_f32 v130, v80, v81\n\t"
    "v_mfma_f32_16x16x32_bf16 v[112:115], a[216:219], v[160:163], v[52:55]\n\t"
    "ds_read_b64_tr_b16 a[192:193], v18 offset:0\n\t"
    "v_cvt_pk_bf16_f32 v131, v82, v83\n\t"
    "v_cvt_pk_f16_f32 v32, v84, v85\n\t"
    "v_cvt_pk_f16_f32 v33, v86, v87\n\t"
    "v_pk_fma_f16 v34, v32, v32, s60 neg_lo:[1,0,1] neg_hi:[1,0,1]\n\t"
    "v_pk_fma_f16 v35, v33, v33, s60 neg_lo:[1,0,1] neg_hi:[1,0,1]\n\t"
    "v_mfma_f32_16x16x32_bf16 v[116:119], a[216:219], v[184:187], v[52:55]\n\t"
    "ds_read_b64_tr_b16 a[194:195], v18 offset:6144\n\t"
    "v_pk_mul_f16 v34, v34, v32\n\t"
    "v_pk_mul_f16 v35, v35, v33\n\t"
    "v_exp_f16 v36, v34\n\t"
    "v_exp_f16 v37, v35\n\t"
    "v_mfma_f32_16x16x32_bf16 v[120:123], a[216:219], v[208:211], v[52:55]\n\t"
    "buffer_load_dwordx4 v31, s[44:47], s66 offen offset:2048 lds\n\t"
    "v_exp_f16_sdwa v36, v34 dst_sel:WORD_1 dst_unused:UNUSED_PRESERVE src0_sel:WORD_1\n\t"
    "v_exp_f16_sdwa v37, v35 dst_sel:WORD_1 dst_unused:UNUSED_PRESERVE src0_sel:WORD_1\n\t"
    "v_mfma_f32_16x16x32_bf16 v[112:115], a[220:223], v[164:167], v[112:115]\n\t"
    "ds_read_b64_tr_b16 a[196:197], v19 offset:0\n\t"
    "v_pk_add_f16 v36, v36, 1.0 op_sel_hi:[1,0]\n\t"
    "v_pk_add_f16 v37, v37, 1.0 op_sel_hi:[1,0]\n\t"
    "v_rcp_f16 v34, v36\n\t"
    "v_mfma_f32_16x16x32_bf16 v[116:119], a[220:223], v[188:191], v[116:119]\n\t"
    "ds_read_b64_tr_b16 a[198:199], v19 offset:6144\n\t"
    "v_rcp_f16 v35, v37\n\t"
    "v_rcp_f16_sdwa v34, v36 dst_sel:WORD_1 dst_unused:UNUSED_PRESERVE src0_sel:WORD_1\n\t"
    "v_rcp_f16_sdwa v35, v37 dst_sel:WORD_1 dst_unused:UNUSED_PRESERVE src0_sel:WORD_1\n\t"
    "v_mfma_f32_16x16x32_bf16 v[120:123], a[220:223], v[212:215], v[120:123]\n\t"
    "buffer_load_dwordx4 v59, s[44:47], s66 offen offset:3072 lds\n\t"
    "v_fma_mix_f32 v84, v84, v34, 0 op_sel_hi:[0,1,0]\n\t"
    "v_fma_mix_f32 v86, v86, v35, 0 op_sel_hi:[0,1,0]\n\t"
    "s_waitcnt lgkmcnt(4)\n\t"
    "v_mfma_f32_16x16x32_bf16 v[112:115], a[224:227], v[168:171], v[112:115]\n\t"
    "ds_read_b64_tr_b16 a[200:201], v20 offset:0\n\t"
    "v_fma_mix_f32 v85, v85, v34, 0 op_sel:[0,1,0] op_sel_hi:[0,1,0]\n\t"
    "v_fma_mix_f32 v87, v87, v35, 0 op_sel:[0,1,0] op_sel_hi:[0,1,0]\n\t"
    "v_cvt_pk_bf16_f32 v134, v84, v85\n\t"
    "v_cvt_pk_bf16_f32 v135, v86, v87\n\t"
    "v_cvt_pk_f16_f32 v38, v88, v89\n\t"
    "v_mfma_f32_16x16x32_bf16 v[116:119], a[224:227], v[192:195], v[116:119]\n\t"
    "ds_read_b64_tr_b16 a[202:203], v20 offset:6144\n\t"
    "v_cvt_pk_f16_f32 v39, v90, v91\n\t"
    "v_pk_fma_f16 v40, v38, v38, s60 neg_lo:[1,0,1] neg_hi:[1,0,1]\n\t"
    "v_pk_fma_f16 v41, v39, v39, s60 neg_lo:[1,0,1] neg_hi:[1,0,1]\n\t"
    "v_pk_mul_f16 v40, v40, v38\n\t"
    "s_add_u32 s66, s66, 12288\n\t"
    "v_mfma_f32_16x16x32_bf16 v[120:123], a[224:227], v[216:219], v[120:123]\n\t"
    "v_pk_mul_f16 v41, v41, v39\n\t"
    "v_exp_f16 v42, v40\n\t"
    "v_exp_f16 v43, v41\n\t"
    "v_exp_f16_sdwa v42, v40 dst_sel:WORD_1 dst_unused:UNUSED_PRESERVE src0_sel:WORD_1\n\t"
    "v_mfma_f32_16x16x32_bf16 v[112:115], a[228:231], v[172:175], v[112:115]\n\t"
    "ds_read_b64_tr_b16 a[204:205], v21 offset:0\n\t"
    "v_exp_f16_sdwa v43, v41 dst_sel:WORD_1 dst_unused:UNUSED_PRESERVE src0_sel:WORD_1\n\t"
    "v_pk_add_f16 v42, v42, 1.0 op_sel_hi:[1,0]\n\t"
    "v_pk_add_f16 v43, v43, 1.0 op_sel_hi:[1,0]\n\t"
    "v_mfma_f32_16x16x32_bf16 v[116:119], a[228:231], v[196:199], v[116:119]\n\t"
    "ds_read_b64_tr_b16 a[206:207], v21 offset:6144\n\t"
    "v_rcp_f16 v40, v42\n\t"
    "v_rcp_f16 v41, v43\n\t"
    "v_rcp_f16_sdwa v40, v42 dst_sel:WORD_1 dst_unused:UNUSED_PRESERVE src0_sel:WORD_1\n\t"
    "v_mfma_f32_16x16x32_bf16 v[120:123], a[228:231], v[220:223], v[120:123]\n\t"
    "v_rcp_f16_sdwa v41, v43 dst_sel:WORD_1 dst_unused:UNUSED_PRESERVE src0_sel:WORD_1\n\t"
    "v_fma_mix_f32 v88, v88, v40, 0 op_sel_hi:[0,1,0]\n\t"
    "v_fma_mix_f32 v90, v90, v41, 0 op_sel_hi:[0,1,0]\n\t"
    "v_fma_mix_f32 v89, v89, v40, 0 op_sel:[0,1,0] op_sel_hi:[0,1,0]\n\t"
    "v_fma_mix_f32 v91, v91, v41, 0 op_sel:[0,1,0] op_sel_hi:[0,1,0]\n\t"
    "v_mfma_f32_16x16x32_bf16 v[112:115], a[232:235], v[176:179], v[112:115]\n\t"
    "ds_read_b64_tr_b16 a[208:209], v18 offset:1024\n\t"
    "v_cvt_pk_bf16_f32 v138, v88, v89\n\t"
    "v_cvt_pk_bf16_f32 v139, v90, v91\n\t"
    "v_cvt_pk_f16_f32 v32, v96, v97\n\t"
    "v_cvt_pk_f16_f32 v33, v98, v99\n\t"
    "v_pk_fma_f16 v34, v32, v32, s60 neg_lo:[1,0,1] neg_hi:[1,0,1]\n\t"
    "v_mfma_f32_16x16x32_bf16 v[116:119], a[232:235], v[200:203], v[116:119]\n\t"
    "ds_read_b64_tr_b16 a[210:211], v18 offset:7168\n\t"
    "v_pk_fma_f16 v35, v33, v33, s60 neg_lo:[1,0,1] neg_hi:[1,0,1]\n\t"
    "v_pk_mul_f16 v34, v34, v32\n\t"
    "v_pk_mul_f16 v35, v35, v33\n\t"
    "v_exp_f16 v36, v34\n\t"
    "v_mfma_f32_16x16x32_bf16 v[120:123], a[232:235], v[224:227], v[120:123]\n\t"
    "v_exp_f16 v37, v35\n\t"
    "v_exp_f16_sdwa v36, v34 dst_sel:WORD_1 dst_unused:UNUSED_PRESERVE src0_sel:WORD_1\n\t"
    "v_exp_f16_sdwa v37, v35 dst_sel:WORD_1 dst_unused:UNUSED_PRESERVE src0_sel:WORD_1\n\t"
    "v_mfma_f32_16x16x32_bf16 v[112:115], a[236:239], v[180:183], v[112:115]\n\t"
    "ds_read_b64_tr_b16 a[212:213], v19 offset:1024\n\t"
    "v_pk_add_f16 v36, v36, 1.0 op_sel_hi:[1,0]\n\t"
    "v_pk_add_f16 v37, v37, 1.0 op_sel_hi:[1,0]\n\t"
    "v_rcp_f16 v34, v36\n\t"
    "v_mfma_f32_16x16x32_bf16 v[116:119], a[236:239], v[204:207], v[116:119]\n\t"
    "ds_read_b64_tr_b16 a[214:215], v19 offset:7168\n\t"
    "v_rcp_f16 v35, v37\n\t"
    "v_rcp_f16_sdwa v34, v36 dst_sel:WORD_1 dst_unused:UNUSED_PRESERVE src0_sel:WORD_1\n\t"
    "v_rcp_f16_sdwa v35, v37 dst_sel:WORD_1 dst_unused:UNUSED_PRESERVE src0_sel:WORD_1\n\t"
    "v_mfma_f32_16x16x32_bf16 v[120:123], a[236:239], v[228:231], v[120:123]\n\t"
    "v_fma_mix_f32 v96, v96, v34, 0 op_sel_hi:[0,1,0]\n\t"
    "v_fma_mix_f32 v98, v98, v35, 0 op_sel_hi:[0,1,0]\n\t"
    "v_fma_mix_f32 v97, v97, v34, 0 op_sel:[0,1,0] op_sel_hi:[0,1,0]\n\t"
    "v_fma_mix_f32 v99, v99, v35, 0 op_sel:[0,1,0] op_sel_hi:[0,1,0]\n\t"
    "v_cvt_pk_bf16_f32 v144, v96, v97\n\t"
    "v_cvt_pk_bf16_f32 v145, v98, v99\n\t"
    "s_mov_b32 s69, 6\n\t"
    "s_mov_b32 s30, 49152\n\t"
    "s_branch P2_b_%=\n\t"
    ".p2align 6\n\t"
    "loop_b_%=:\n\t"
    "s_waitcnt lgkmcnt(4)\n\t"
    "v_mfma_f32_16x16x32_bf16 a[0:3], a[192:195], v[128:131], a[0:3]\n\t"
    "ds_read_b64_tr_b16 a[216:217], v20 offset:25600\n\t"
    "v_cvt_pk_f16_f32 v38, v100, v101\n\t"
    "v_cvt_pk_f16_f32 v39, v102, v103\n\t"
    "v_pk_fma_f16 v40, v38, v38, s60 neg_lo:[1,0,1] neg_hi:[1,0,1]\n\t"
    "v_mfma_f32_16x16x32_bf16 a[4:7], a[192:195], v[132:135], a[4:7]\n\t"
    "ds_read_b64_tr_b16 a[218:219], v20 offset:31744\n\t"
    "v_pk_fma_f16 v41, v39, v39, s60 neg_lo:[1,0,1] neg_hi:[1,0,1]\n\t"
    "v_pk_mul_f16 v40, v40, v38\n\t"
    "v_mfma_f32_16x16x32_bf16 a[8:11], a[192:195], v[136:139], a[8:11]\n\t"
    "v_pk_mul_f16 v41, v41, v39\n\t"
    "v_exp_f16 v42, v40\n\t"
    "v_mfma_f32_16x16x32_bf16 a[12:15], a[196:199], v[128:131], a[12:15]\n\t"
    "ds_read_b64_tr_b16 a[220:221], v21 offset:25600\n\t"
    "v_exp_f16 v43, v41\n\t"
    "v_exp_f16_sdwa v42, v40 dst_sel:WORD_1 dst_unused:UNUSED_PRESERVE src0_sel:WORD_1\n\t"
    "v_mfma_f32_16x16x32_bf16 a[16:19], a[196:199], v[132:135], a[16:19]\n\t"
    "ds_read_b64_tr_b16 a[222:223], v21 offset:31744\n\t"
    "v_exp_f16_sdwa v43, v41 dst_sel:WORD_1 dst_unused:UNUSED_PRESERVE src0_sel:WORD_1\n\t"
    "v_mfma_f32_16x16x32_bf16 a[20:23], a[196:199], v[136:139], a[20:23]\n\t"
    "v_pk_add_f16 v42, v42, 1.0 op_sel_hi:[1,0]\n\t"
    "v_pk_add_f16 v43, v43, 1.0 op_sel_hi:[1,0]\n\t"
    "v_rcp_f16 v40, v42\n\t"
    "v_mfma_f32_16x16x32_bf16 a[24:27], a[200:203], v[128:131], a[24:27]\n\t"
    "ds_read_b64_tr_b16 a[224:225], v18 offset:26624\n\t"
    "v_rcp_f16 v41, v43\n\t"
    "v_mfma_f32_16x16x32_bf16 a[28:31], a[200:203], v[132:135], a[28:31]\n\t"
    "ds_read_b64_tr_b16 a[226:227], v18 offset:32768\n\t"
    "v_rcp_f16_sdwa v40, v42 dst_sel:WORD_1 dst_unused:UNUSED_PRESERVE src0_sel:WORD_1\n\t"
    "v_mfma_f32_16x16x32_bf16 a[32:35], a[200:203], v[136:139], a[32:35]\n\t"
    "v_rcp_f16_sdwa v41, v43 dst_sel:WORD_1 dst_unused:UNUSED_PRESERVE src0_sel:WORD_1\n\t"
    "v_fma_mix_f32 v100, v100, v40, 0 op_sel_hi:[0,1,0]\n\t"
    "v_fma_mix_f32 v102, v102, v41, 0 op_sel_hi:[0,1,0]\n\t"
    "v_mfma_f32_16x16x32_bf16 a[36:39], a[204:207], v[128:131], a[36:39]\n\t"
    "ds_read_b64_tr_b16 a[228:229], v19 offset:26624\n\t"
    "v_fma_mix_f32 v101, v101, v40, 0 op_sel:[0,1,0] op_sel_hi:[0,1,0]\n\t"
    "v_fma_mix_f32 v103, v103, v41, 0 op_sel:[0,1,0] op_sel_hi:[0,1,0]\n\t"
    "v_cvt_pk_bf16_f32 v148, v100, v101\n\t"
    "v_mfma_f32_16x16x32_bf16 a[40:43], a[204:207], v[132:135], a[40:43]\n\t"
    "ds_read_b64_tr_b16 a[230:231], v19 offset:32768\n\t"
    "v_cvt_pk_bf16_f32 v149, v102, v103\n\t"
    "v_cvt_pk_f16_f32 v32, v104, v105\n\t"
    "v_mfma_f32_16x16x32_bf16 a[44:47], a[204:207], v[136:139], a[44:47]\n\t"
    "v_cvt_pk_f16_f32 v33, v106, v107\n\t"
    "s_waitcnt vmcnt(0)\n\t"
    "s_barrier\n\t"
    "s_waitcnt lgkmcnt(4)\n\t"
    "v_mfma_f32_16x16x32_bf16 a[48:51], a[208:211], v[128:131], a[48:51]\n\t"
    "ds_read_b64_tr_b16 a[232:233], v20 offset:26624\n\t"
    "v_pk_fma_f16 v34, v32, v32, s60 neg_lo:[1,0,1] neg_hi:[1,0,1]\n\t"
    "v_pk_fma_f16 v35, v33, v33, s60 neg_lo:[1,0,1] neg_hi:[1,0,1]\n\t"
    "v_mfma_f32_16x16x32_bf16 a[52:55], a[208:211], v[132:135], a[52:55]\n\t"
    "ds_read_b64_tr_b16 a[234:235], v20 offset:32768\n\t"
    "v_pk_mul_f16 v34, v34, v32\n\t"
    "s_add_u32 m0, s67, 24576\n\t"
    "s_add_u32 s63, s65, 0\n\t"
    "v_mfma_f32_16x16x32_bf16 a[56:59], a[208:211], v[136:139], a[56:59]\n\t"
    "buffer_load_dwordx4 v26, s[40:43], s63 offen offset:0 lds\n\t"
    "v_pk_mul_f16 v35, v35, v33\n\t"
    "v_mfma_f32_16x16x32_bf16 a[60:63], a[212:215], v[128:131], a[60:63]\n\t"
    "ds_read_b64_tr_b16 a[236:237], v21 offset:26624\n\t"
    "v_exp_f16 v36, v34\n\t"
    "v_exp_f16 v37, v35\n\t"
    "v_mfma_f32_16x16x32_bf16 a[64:67], a[212:215], v[132:135], a[64:67]\n\t"
    "ds_read_b64_tr_b16 a[238:239], v21 offset:32768\n\t"
    "v_exp_f16_sdwa v36, v34 dst_sel:WORD_1 dst_unused:UNUSED_PRESERVE src0_sel:WORD_1\n\t"
    "v_mfma_f32_16x16x32_bf16 a[68:71], a[212:215], v[136:139], a[68:71]\n\t"
    "v_exp_f16_sdwa v37, v35 dst_sel:WORD_1 dst_unused:UNUSED_PRESERVE src0_sel:WORD_1\n\t"
    "v_pk_add_f16 v36, v36, 1.0 op_sel_hi:[1,0]\n\t"
    "v_mfma_f32_16x16x32_bf16 a[72:75], a[216:219], v[128:131], a[72:75]\n\t"
    "ds_read_b64_tr_b16 a[192:193], v14 offset:0\n\t"
    "v_pk_add_f16 v37, v37, 1.0 op_sel_hi:[1,0]\n\t"
    "v_rcp_f16 v34, v36\n\t"
    "v_mfma_f32_16x16x32_bf16 a[76:79], a[216:219], v[132:135], a[76:79]\n\t"
    "ds_read_b64_tr_b16 a[194:195], v14 offset:2048\n\t"
    "v_rcp_f16 v35, v37\n\t"
    "v_mfma_f32_16x16x32_bf16 a[80:83], a[216:219], v[136:139], a[80:83]\n\t"
    "buffer_load_dwordx4 v27, s[40:43], s63 offen offset:1024 lds\n\t"
    "v_rcp_f16_sdwa v34, v36 dst_sel:WORD_1 dst_unused:UNUSED_PRESERVE src0_sel:WORD_1\n\t"
    "v_mfma_f32_16x16x32_bf16 a[84:87], a[220:223], v[128:131], a[84:87]\n\t"
    "ds_read_b64_tr_b16 a[196:197], v14 offset:4096\n\t"
    "v_rcp_f16_sdwa v35, v37 dst_sel:WORD_1 dst_unused:UNUSED_PRESERVE src0_sel:WORD_1\n\t"
    "v_fma_mix_f32 v104, v104, v34, 0 op_sel_hi:[0,1,0]\n\t"
    "v_mfma_f32_16x16x32_bf16 a[88:91], a[220:223], v[132:135], a[88:91]\n\t"
    "ds_read_b64_tr_b16 a[198:199], v14 offset:6144\n\t"
    "v_fma_mix_f32 v106, v106, v35, 0 op_sel_hi:[0,1,0]\n\t"
    "v_fma_mix_f32 v105, v105, v34, 0 op_sel:[0,1,0] op_sel_hi:[0,1,0]\n\t"
    "v_mfma_f32_16x16x32_bf16 a[92:95], a[220:223], v[136:139], a[92:95]\n\t"
    "ds_read_b128 v[52:55], v22 offset:64\n\t"
    "v_fma_mix_f32 v107, v107, v35, 0 op_sel:[0,1,0] op_sel_hi:[0,1,0]\n\t"
    "v_cvt_pk_bf16_f32 v152, v104, v105\n\t"
    "s_waitcnt lgkmcnt(5)\n\t"
    "v_mfma_f32_16x16x32_bf16 a[96:99], a[224:227], v[128:131], a[96:99]\n\t"
    "ds_read_b64_tr_b16 a[200:201], v14 offset:8192\n\t"
    "v_cvt_pk_bf16_f32 v153, v106, v107\n\t"
    "v_cvt_pk_f16_f32 v38, v112, v113\n\t"
    "v_mfma_f32_16x16x32_bf16 a[100:103], a[224:227], v[132:135], a[100:103]\n\t"
    "ds_read_b64_tr_b16 a[202:203], v14 offset:10240\n\t"
    "v_cvt_pk_f16_f32 v39, v114, v115\n\t"
    "v_pk_fma_f16 v40, v38, v38, s60 neg_lo:[1,0,1] neg_hi:[1,0,1]\n\t"
    "v_pk_fma_f16 v41, v39, v39, s60 neg_lo:[1,0,1] neg_hi:[1,0,1]\n\t"
    "v_mfma_f32_16x16x32_bf16 a[104:107], a[224:227], v[136:139], a[104:107]\n\t"
    "buffer_load_dwordx4 v28, s[40:43], s63 offen offset:2048 lds\n\t"
    "v_pk_mul_f16 v40, v40, v38\n\t"
    "v_mfma_f32_16x16x32_bf16 a[108:111], a[228:231], v[128:131], a[108:111]\n\t"
    "ds_read_b64_tr_b16 a[204:205], v14 offset:12288\n\t"
    "v_pk_mul_f16 v41, v41, v39\n\t"
    "v_exp_f16 v42, v40\n\t"
    "v_mfma_f32_16x16x32_bf16 a[112:115], a[228:231], v[132:135], a[112:115]\n\t"
    "ds_read_b64_tr_b16 a[206:207], v14 offset:14336\n\t"
    "v_exp_f16 v43, v41\n\t"
    "v_mfma_f32_16x16x32_bf16 a[116:119], a[228:231], v[136:139], a[116:119]\n\t"
    "ds_read_b128 v[48:51], v22 offset:0\n\t"
    "v_exp_f16_sdwa v42, v40 dst_sel:WORD_1 dst_unused:UNUSED_PRESERVE src0_sel:WORD_1\n\t"
    "v_exp_f16_sdwa v43, v41 dst_sel:WORD_1 dst_unused:UNUSED_PRESERVE src0_sel:WORD_1\n\t"
    "v_mfma_f32_16x16x32_bf16 a[120:123], a[232:235], v[128:131], a[120:123]\n\t"
    "ds_read_b64_tr_b16 a[208:209], v14 offset:16384\n\t"
    "v_pk_add_f16 v42, v42, 1.0 op_sel_hi:[1,0]\n\t"
    "v_pk_add_f16 v43, v43, 1.0 op_sel_hi:[1,0]\n\t"
    "v_mfma_f32_16x16x32_bf16 a[124:127], a[232:235], v[132:135], a[124:127]\n\t"
    "ds_read_b64_tr_b16 a[210:211], v14 offset:18432\n\t"
    "v_rcp_f16 v40, v42\n\t"
    "v_mfma_f32_16x16x32_bf16 a[128:131], a[232:235], v[136:139], a[128:131]\n\t"
    "buffer_load_dwordx4 v58, s[40:43], s63 offen offset:3072 lds\n\t"
    "v_rcp_f16 v41, v43\n\t"
    "v_mfma_f32_16x16x32_bf16 a[132:135], a[236:239], v[128:131], a[132:135]\n\t"
    "ds_read_b64_tr_b16 a[212:213], v14 offset:20480\n\t"
    "v_rcp_f16_sdwa v40, v42 dst_sel:WORD_1 dst_unused:UNUSED_PRESERVE src0_sel:WORD_1\n\t"
    "v_mfma_f32_16x16x32_bf16 a[136:139], a[236:239], v[132:135], a[136:139]\n\t"
    "ds_read_b64_tr_b16 a[214:215], v14 offset:22528\n\t"
    "v_rcp_f16_sdwa v41, v43 dst_sel:WORD_1 dst_unused:UNUSED_PRESERVE src0_sel:WORD_1\n\t"
    "v_fma_mix_f32 v112, v112, v40, 0 op_sel_hi:[0,1,0]\n\t"
    "v_mfma_f32_16x16x32_bf16 a[140:143], a[236:239], v[136:139], a[140:143]\n\t"
    "v_fma_mix_f32 v114, v114, v41, 0 op_sel_hi:[0,1,0]\n\t"
    "v_fma_mix_f32 v113, v113, v40, 0 op_sel:[0,1,0] op_sel_hi:[0,1,0]\n\t"
    "v_fma_mix_f32 v115, v115, v41, 0 op_sel:[0,1,0] op_sel_hi:[0,1,0]\n\t"
    "s_waitcnt lgkmcnt(4)\n\t"
    "v_mfma_f32_16x16x32_bf16 v[64:67], a[192:195], v[160:163], v[48:51]\n\t"
    "ds_read_b64_tr_b16 a[216:217], v15 offset:0\n\t"
    "v_cvt_pk_bf16_f32 v146, v112, v113\n\t"
    "v_cvt_pk_bf16_f32 v147, v114, v115\n\t"
    "v_mfma_f32_16x16x32_bf16 v[68:71], a[192:195], v[184:187], v[48:51]\n\t"
    "ds_read_b64_tr_b16 a[218:219], v15 offset:2048\n\t"
    "v_cvt_pk_f16_f32 v32, v116, v117\n\t"
    "v_cvt_pk_f16_f32 v33, v118, v119\n\t"
    "s_add_u32 m0, s68, 12288\n\t"
    "v_mfma_f32_16x16x32_bf16 v[72:75], a[192:195], v[208:211], v[48:51]\n\t"
    "buffer_load_dwordx4 v29, s[44:47], s66 offen offset:0 lds\n\t"
    "v_pk_fma_f16 v34, v32, v32, s60 neg_lo:[1,0,1] neg_hi:[1,0,1]\n\t"
    "v_mfma_f32_16x16x32_bf16 v[64:67], a[196:199], v[164:167], v[64:67]\n\t"
    "ds_read_b64_tr_b16 a[220:221], v15 offset:4096\n\t"
    "v_pk_fma_f16 v35, v33, v33, s60 neg_lo:[1,0,1] neg_hi:[1,0,1]\n\t"
    "v_pk_mul_f16 v34, v34, v32\n\t"
    "v_pk_mul_f16 v35, v35, v33\n\t"
    "v_mfma_f32_16x16x32_bf16 v[68:71], a[196:199], v[188:191], v[68:71]\n\t"
    "ds_read_b64_tr_b16 a[222:223], v15 offset:6144\n\t"
    "v_exp_f16 v36, v34\n\t"
    "v_mfma_f32_16x16x32_bf16 v[72:75], a[196:199], v[212:215], v[72:75]\n\t"
    "v_exp_f16 v37, v35\n\t"
    "v_exp_f16_sdwa v36, v34 dst_sel:WORD_1 dst_unused:UNUSED_PRESERVE src0_sel:WORD_1\n\t"
    "v_mfma_f32_16x16x32_bf16 v[64:67], a[200:203], v[168:171], v[64:67]\n\t"
    "ds_read_b64_tr_b16 a[224:225], v15 offset:8192\n\t"
    "v_exp_f16_sdwa v37, v35 dst_sel:WORD_1 dst_unused:UNUSED_PRESERVE src0_sel:WORD_1\n\t"
    "v_mfma_f32_16x16x32_bf16 v[68:71], a[200:203], v[192:195], v[68:71]\n\t"
    "ds_read_b64_tr_b16 a[226:227], v15 offset:10240\n\t"
    "v_pk_add_f16 v36, v36, 1.0 op_sel_hi:[1,0]\n\t"
    "v_pk_add_f16 v37, v37, 1.0 op_sel_hi:[1,0]\n\t"
    "v_rcp_f16 v34, v36\n\t"
    "v_mfma_f32_16x16x32_bf16 v[72:75], a[200:203], v[216:219], v[72:75]\n\t"
    "buffer_load_dwordx4 v30, s[44:47], s66 offen offset:1024 lds\n\t"
    "v_mfma_f32_16x16x32_bf16 v[64:67], a[204:207], v[172:175], v[64:67]\n\t"
    "ds_read_b64_tr_b16 a[228:229], v15 offset:12288\n\t"
    "v_rcp_f16 v35, v37\n\t"
    "v_rcp_f16_sdwa v34, v36 dst_sel:WORD_1 dst_unused:UNUSED_PRESERVE src0_sel:WORD_1\n\t"
    "v_mfma_f32_16x16x32_bf16 v[68:71], a[204:207], v[196:199], v[68:71]\n\t"
    "ds_read_b64_tr_b16 a[230:231], v15 offset:14336\n\t"
    "v_rcp_f16_sdwa v35, v37 dst_sel:WORD_1 dst_unused:UNUSED_PRESERVE src0_sel:WORD_1\n\t"
    "v_mfma_f32_16x16x32_bf16 v[72:75], a[204:207], v[220:223], v[72:75]\n\t"
    "v_fma_mix_f32 v116, v116, v34, 0 op_sel_hi:[0,1,0]\n\t"
    "v_fma_mix_f32 v118, v118, v35, 0 op_sel_hi:[0,1,0]\n\t"
    "s_waitcnt lgkmcnt(4)\n\t"
    "v_mfma_f32_16x16x32_bf16 v[64:67], a[208:211], v[176:179], v[64:67]\n\t"
    "ds_read_b64_tr_b16 a[232:233], v15 offset:16384\n\t"
    "v_fma_mix_f32 v117, v117, v34, 0 op_sel:[0,1,0] op_sel_hi:[0,1,0]\n\t"
    "v_fma_mix_f32 v119, v119, v35, 0 op_sel:[0,1,0] op_sel_hi:[0,1,0]\n\t"
    "v_cvt_pk_bf16_f32 v150, v116, v117\n\t"
    "v_mfma_f32_16x16x32_bf16 v[68:71], a[208:211], v[200:203], v[68:71]\n\t"
    "ds_read_b64_tr_b16 a[234:235], v15 offset:18432\n\t"
    "v_cvt_pk_bf16_f32 v151, v118, v119\n\t"
    "v_cvt_pk_f16_f32 v38, v120, v121\n\t"
    "v_mfma_f32_16x16x32_bf16 v[72:75], a[208:211], v[224:227], v[72:75]\n\t"
    "buffer_load_dwordx4 v31, s[44:47], s66 offen offset:2048 lds\n\t"
    "v_cvt_pk_f16_f32 v39, v122, v123\n\t"
    "v_pk_fma_f16 v40, v38, v38, s60 neg_lo:[1,0,1] neg_hi:[1,0,1]\n\t"
    "v_mfma_f32_16x16x32_bf16 v[64:67], a[212:215], v[180:183], v[64:67]\n\t"
    "ds_read_b64_tr_b16 a[236:237], v15 offset:20480\n\t"
    "v_pk_fma_f16 v41, v39, v39, s60 neg_lo:[1,0,1] neg_hi:[1,0,1]\n\t"
    "v_pk_mul_f16 v40, v40, v38\n\t"
    "v_pk_mul_f16 v41, v41, v39\n\t"
    "v_mfma_f32_16x16x32_bf16 v[68:71], a[212:215], v[204:207], v[68:71]\n\t"
    "ds_read_b64_tr_b16 a[238:239], v15 offset:22528\n\t"
    "v_exp_f16 v42, v40\n\t"
    "v_mfma_f32_16x16x32_bf16 v[72:75], a[212:215], v[228:231], v[72:75]\n\t"
    "v_exp_f16 v43, v41\n\t"
    "v_exp_f16_sdwa v42, v40 dst_sel:WORD_1 dst_unused:UNUSED_PRESERVE src0_sel:WORD_1\n\t"
    "v_mfma_f32_16x16x32_bf16 v[80:83], a[216:219], v[160:163], v[52:55]\n\t"
    "ds_read_b64_tr_b16 a[192:193], v18 offset:36864\n\t"
    "v_exp_f16_sdwa v43, v41 dst_sel:WORD_1 dst_unused:UNUSED_PRESERVE src0_sel:WORD_1\n\t"
    "v_mfma_f32_16x16x32_bf16 v[84:87], a[216:219], v[184:187], v[52:55]\n\t"
    "ds_read_b64_tr_b16 a[194:195], v18 offset:43008\n\t"
    "v_pk_add_f16 v42, v42, 1.0 op_sel_hi:[1,0]\n\t"
    "v_pk_add_f16 v43, v43, 1.0 op_sel_hi:[1,0]\n\t"
    "v_mfma_f32_16x16x32_bf16 v[88:91], a[216:219], v[208:211], v[52:55]\n\t"
    "buffer_load_dwordx4 v59, s[44:47], s66 offen offset:3072 lds\n\t"
    "v_rcp_f16 v40, v42\n\t"
    "v_mfma_f32_16x16x32_bf16 v[80:83], a[220:223], v[164:167], v[80:83]\n\t"
    "ds_read_b64_tr_b16 a[196:197], v19 offset:36864\n\t"
    "v_rcp_f16 v41, v43\n\t"
    "v_rcp_f16_sdwa v40, v42 dst_sel:WORD_1 dst_unused:UNUSED_PRESERVE src0_sel:WORD_1\n\t"
    "v_mfma_f32_16x16x32_bf16 v[84:87], a[220:223], v[188:191], v[84:87]\n\t"
    "ds_read_b64_tr_b16 a[198:199], v19 offset:43008\n\t"
    "v_rcp_f16_sdwa v41, v43 dst_sel:WORD_1 dst_unused:UNUSED_PRESERVE src0_sel:WORD_1\n\t"
    "v_mfma_f32_16x16x32_bf16 v[88:91], a[220:223], v[212:215], v[88:91]\n\t"
    "v_fma_mix_f32 v120, v120, v40, 0 op_sel_hi:[0,1,0]\n\t"
    "v_fma_mix_f32 v122, v122, v41, 0 op_sel_hi:[0,1,0]\n\t"
    "s_waitcnt lgkmcnt(4)\n\t"
    "v_mfma_f32_16x16x32_bf16 v[80:83], a[224:227], v[168:171], v[80:83]\n\t"
    "ds_read_b64_tr_b16 a[200:201], v20 offset:36864\n\t"
    "v_fma_mix_f32 v121, v121, v40, 0 op_sel:[0,1,0] op_sel_hi:[0,1,0]\n\t"
    "v_fma_mix_f32 v123, v123, v41, 0 op_sel:[0,1,0] op_sel_hi:[0,1,0]\n\t"
    "v_cvt_pk_bf16_f32 v154, v120, v121\n\t"
    "v_mfma_f32_16x16x32_bf16 v[84:87], a[224:227], v[192:195], v[84:87]\n\t"
    "ds_read_b64_tr_b16 a[202:203], v20 offset:43008\n\t"
    "v_cvt_pk_bf16_f32 v155, v122, v123\n\t"
    "s_add_u32 s66, s66, 12288\n\t"
    "v_mfma_f32_16x16x32_bf16 v[88:91], a[224:227], v[216:219], v[88:91]\n\t"
    "v_cvt_pk_f16_f32 v32, v64, v65\n\t"
    "v_cvt_pk_f16_f32 v33, v66, v67\n\t"
    "v_pk_fma_f16 v34, v32, v32, s60 neg_lo:[1,0,1] neg_hi:[1,0,1]\n\t"
    "v_pk_fma_f16 v35, v33, v33, s60 neg_lo:[1,0,1] neg_hi:[1,0,1]\n\t"
    "v_mfma_f32_16x16x32_bf16 v[80:83], a[228:231], v[172:175], v[80:83]\n\t"
    "ds_read_b64_tr_b16 a[204:205], v21 offset:36864\n\t"
    "v_pk_mul_f16 v34, v34, v32\n\t"
    "v_pk_mul_f16 v35, v35, v33\n\t"
    "v_mfma_f32_16x16x32_bf16 v[84:87], a[228:231], v[196:199], v[84:87]\n\t"
    "ds_read_b64_tr_b16 a[206:207], v21 offset:43008\n\t"
    "v_exp_f16 v36, v34\n\t"
    "v_exp_f16 v37, v35\n\t"
    "v_mfma_f32_16x16x32_bf16 v[88:91], a[228:231], v[220:223], v[88:91]\n\t"
    "v_exp_f16_sdwa v36, v34 dst_sel:WORD_1 dst_unused:UNUSED_PRESERVE src0_sel:WORD_1\n\t"
    "v_mfma_f32_16x16x32_bf16 v[80:83], a[232:235], v[176:179], v[80:83]\n\t"
    "ds_read_b64_tr_b16 a[208:209], v18 offset:37888\n\t"
    "v_exp_f16_sdwa v37, v35 dst_sel:WORD_1 dst_unused:UNUSED_PRESERVE src0_sel:WORD_1\n\t"
    "v_pk_add_f16 v36, v36, 1.0 op_sel_hi:[1,0]\n\t"
    "v_mfma_f32_16x16x32_bf16 v[84:87], a[232:235], v[200:203], v[84:87]\n\t"
    "ds_read_b64_tr_b16 a[210:211], v18 offset:44032\n\t"
    "v_pk_add_f16 v37, v37, 1.0 op_sel_hi:[1,0]\n\t"
    "v_rcp_f16 v34, v36\n\t"
    "v_mfma_f32_16x16x32_bf16 v[88:91], a[232:235], v[224:227], v[88:91]\n\t"
    "v_rcp_f16 v35, v37\n\t"
    "v_rcp_f16_sdwa v34, v36 dst_sel:WORD_1 dst_unused:UNUSED_PRESERVE src0_sel:WORD_1\n\t"
    "v_mfma_f32_16x16x32_bf16 v[80:83], a[236:239], v[180:183], v[80:83]\n\t"
    "ds_read_b64_tr_b16 a[212:213], v19 offset:37888\n\t"
    "v_rcp_f16_sdwa v35, v37 dst_sel:WORD_1 dst_unused:UNUSED_PRESERVE src0_sel:WORD_1\n\t"
    "v_mfma_f32_16x16x32_bf16 v[84:87], a[236:239], v[204:207], v[84:87]\n\t"
    "ds_read_b64_tr_b16 a[214:215], v19 offset:44032\n\t"
    "v_fma_mix_f32 v64, v64, v34, 0 op_sel_hi:[0,1,0]\n\t"
    "v_fma_mix_f32 v66, v66, v35, 0 op_sel_hi:[0,1,0]\n\t"
    "v_mfma_f32_16x16x32_bf16 v[88:91], a[236:239], v[228:231], v[88:91]\n\t"
    "v_fma_mix_f32 v65, v65, v34, 0 op_sel:[0,1,0] op_sel_hi:[0,1,0]\n\t"
    "v_fma_mix_f32 v67, v67, v35, 0 op_sel:[0,1,0] op_sel_hi:[0,1,0]\n\t"
    "v_cvt_pk_bf16_f32 v128, v64, v65\n\t"
    "v_cvt_pk_bf16_f32 v129, v66, v67\n\t"
    "s_waitcnt lgkmcnt(4)\n\t"
    "v_mfma_f32_16x16x32_bf16 a[0:3], a[192:195], v[144:147], a[0:3]\n\t"
    "ds_read_b64_tr_b16 a[216:217], v20 offset:37888\n\t"
    "v_cvt_pk_f16_f32 v38, v68, v69\n\t"
    "v_cvt_pk_f16_f32 v39, v70, v71\n\t"
    "v_pk_fma_f16 v40, v38, v38, s60 neg_lo:[1,0,1] neg_hi:[1,0,1]\n\t"
    "v_mfma_f32_16x16x32_bf16 a[4:7], a[192:195], v[148:151], a[4:7]\n\t"
    "ds_read_b64_tr_b16 a[218:219], v20 offset:44032\n\t"
    "s_add_u32 m0, s67, 36864\n\t"
    "s_add_u32 s63, s65, 147456\n\t"
    "v_mfma_f32_16x16x32_bf16 a[8:11], a[192:195], v[152:155], a[8:11]\n\t"
    "buffer_load_dwordx4 v26, s[40:43], s63 offen offset:0 lds\n\t"
    "v_pk_fma_f16 v41, v39, v39, s60 neg_lo:[1,0,1] neg_hi:[1,0,1]\n\t"
    "v_pk_mul_f16 v40, v40, v38\n\t"
    "v_mfma_f32_16x16x32_bf16 a[12:15], a[196:199], v[144:147], a[12:15]\n\t"
    "ds_read_b64_tr_b16 a[220:221], v21 offset:37888\n\t"
    "v_pk_mul_f16 v41, v41, v39\n\t"
    "v_exp_f16 v42, v40\n\t"
    "v_mfma_f32_16x16x32_bf16 a[16:19], a[196:199], v[148:151], a[16:19]\n\t"
    "ds_read_b64_tr_b16 a[222:223], v21 offset:44032\n\t"
    "v_exp_f16 v43, v41\n\t"
    "v_mfma_f32_16x16x32_bf16 a[20:23], a[196:199], v[152:155], a[20:23]\n\t"
    "buffer_load_dwordx4 v27, s[40:43], s63 offen offset:1024 lds\n\t"
    "v_exp_f16_sdwa v42, v40 dst_sel:WORD_1 dst_unused:UNUSED_PRESERVE src0_sel:WORD_1\n\t"
    "v_mfma_f32_16x16x32_bf16 a[24:27], a[200:203], v[144:147], a[24:27]\n\t"
    "ds_read_b64_tr_b16 a[224:225], v18 offset:38912\n\t"
    "v_exp_f16_sdwa v43, v41 dst_sel:WORD_1 dst_unused:UNUSED_PRESERVE src0_sel:WORD_1\n\t"
    "v_mfma_f32_16x16x32_bf16 a[28:31], a[200:203], v[148:151], a[28:31]\n\t"
    "ds_read_b64_tr_b16 a[226:227], v18 offset:45056\n\t"
    "v_pk_add_f16 v42, v42, 1.0 op_sel_hi:[1,0]\n\t"
    "v_pk_add_f16 v43, v43, 1.0 op_sel_hi:[1,0]\n\t"
    "v_mfma_f32_16x16x32_bf16 a[32:35], a[200:203], v[152:155], a[32:35]\n\t"
    "buffer_load_dwordx4 v28, s[40:43], s63 offen offset:2048 lds\n\t"
    "v_rcp_f16 v40, v42\n\t"
    "v_mfma_f32_16x16x32_bf16 a[36:39], a[204:207], v[144:147], a[36:39]\n\t"
    "ds_read_b64_tr_b16 a[228:229], v19 offset:38912\n\t"
    "v_rcp_f16 v41, v43\n\t"
    "v_rcp_f16_sdwa v40, v42 dst_sel:WORD_1 dst_unused:UNUSED_PRESERVE src0_sel:WORD_1\n\t"
    "v_mfma_f32_16x16x32_bf16 a[40:43], a[204:207], v[148:151], a[40:43]\n\t"
    "ds_read_b64_tr_b16 a[230:231], v19 offset:45056\n\t"
    "v_rcp_f16_sdwa v41, v43 dst_sel:WORD_1 dst_unused:UNUSED_PRESERVE src0_sel:WORD_1\n\t"
    "v_mfma_f32_16x16x32_bf16 a[44:47], a[204:207], v[152:155], a[44:47]\n\t"
    "buffer_load_dwordx4 v58, s[40:43], s63 offen offset:3072 lds\n\t"
    "s_waitcnt lgkmcnt(4)\n\t"
    "v_mfma_f32_16x16x32_bf16 a[48:51], a[208:211], v[144:147], a[48:51]\n\t"
    "ds_read_b64_tr_b16 a[232:233], v20 offset:38912\n\t"
    "v_fma_mix_f32 v68, v68, v40, 0 op_sel_hi:[0,1,0]\n\t"
    "v_fma_mix_f32 v70, v70, v41, 0 op_sel_hi:[0,1,0]\n\t"
    "v_fma_mix_f32 v69, v69, v40, 0 op_sel:[0,1,0] op_sel_hi:[0,1,0]\n\t"
    "v_mfma_f32_16x16x32_bf16 a[52:55], a[208:211], v[148:151], a[52:55]\n\t"
    "ds_read_b64_tr_b16 a[234:235], v20 offset:45056\n\t"
    "v_fma_mix_f32 v71, v71, v41, 0 op_sel:[0,1,0] op_sel_hi:[0,1,0]\n\t"
    "s_add_u32 s65, s65, 128\n\t"
    "s_add_u32 m0, s68, 24576\n\t"
    "v_mfma_f32_16x16x32_bf16 a[56:59], a[208:211], v[152:155], a[56:59]\n\t"
    "buffer_load_dwordx4 v29, s[44:47], s66 offen offset:0 lds\n\t"
    "v_cvt_pk_bf16_f32 v132, v68, v69\n\t"
    "v_mfma_f32_16x16x32_bf16 a[60:63], a[212:215], v[144:147], a[60:63]\n\t"
    "ds_read_b64_tr_b16 a[236:237], v21 offset:38912\n\t"
    "v_cvt_pk_bf16_f32 v133, v70, v71\n\t"
    "v_cvt_pk_f16_f32 v32, v72, v73\n\t"
    "v_cvt_pk_f16_f32 v33, v74, v75\n\t"
    "v_mfma_f32_16x16x32_bf16 a[64:67], a[212:215], v[148:151], a[64:67]\n\t"
    "ds_read_b64_tr_b16 a[238:239], v21 offset:45056\n\t"
    "v_pk_fma_f16 v34, v32, v32, s60 neg_lo:[1,0,1] neg_hi:[1,0,1]\n\t"
    "v_pk_fma_f16 v35, v33, v33, s60 neg_lo:[1,0,1] neg_hi:[1,0,1]\n\t"
    "v_mfma_f32_16x16x32_bf16 a[68:71], a[212:215], v[152:155], a[68:71]\n\t"
    "buffer_load_dwordx4 v30, s[44:47], s66 offen offset:1024 lds\n\t"
    "v_pk_mul_f16 v34, v34, v32\n\t"
    "v_pk_mul_f16 v35, v35, v33\n\t"
    "v_mfma_f32_16x16x32_bf16 a[72:75], a[216:219], v[144:147], a[72:75]\n\t"
    "ds_read_b64_tr_b16 a[192:193], v16 offset:0\n\t"
    "v_exp_f16 v36, v34\n\t"
    "v_mfma_f32_16x16x32_bf16 a[76:79], a[216:219], v[148:151], a[76:79]\n\t"
    "ds_read_b64_tr_b16 a[194:195], v16 offset:2048\n\t"
    "v_exp_f16 v37, v35\n\t"
    "v_exp_f16_sdwa v36, v34 dst_sel:WORD_1 dst_unused:UNUSED_PRESERVE src0_sel:WORD_1\n\t"
    "v_mfma_f32_16x16x32_bf16 a[80:83], a[216:219], v[152:155], a[80:83]\n\t"
    "buffer_load_dwordx4 v31, s[44:47], s66 offen offset:2048 lds\n\t"
    "v_mfma_f32_16x16x32_bf16 a[84:87], a[220:223], v[144:147], a[84:87]\n\t"
    "ds_read_b64_tr_b16 a[196:197], v16 offset:4096\n\t"
    "v_exp_f16_sdwa v37, v35 dst_sel:WORD_1 dst_unused:UNUSED_PRESERVE src0_sel:WORD_1\n\t"
    "v_pk_add_f16 v36, v36, 1.0 op_sel_hi:[1,0]\n\t"
    "v_mfma_f32_16x16x32_bf16 a[88:91], a[220:223], v[148:151], a[88:91]\n\t"
    "ds_read_b64_tr_b16 a[198:199], v16 offset:6144\n\t"
    "v_pk_add_f16 v37, v37, 1.0 op_sel_hi:[1,0]\n\t"
    "v_rcp_f16 v34, v36\n\t"
    "v_mfma_f32_16x16x32_bf16 a[92:95], a[220:223], v[152:155], a[92:95]\n\t"
    "ds_read_b128 v[52:55], v22 offset:192\n\t"
    "buffer_load_dwordx4 v59, s[44:47], s66 offen offset:3072 lds\n\t"
    "s_waitcnt lgkmcnt(5)\n\t"
    "v_mfma_f32_16x16x32_bf16 a[96:99], a[224:227], v[144:147], a[96:99]\n\t"
    "ds_read_b64_tr_b16 a[200:201], v16 offset:8192\n\t"
    "v_rcp_f16 v35, v37\n\t"
    "v_mfma_f32_16x16x32_bf16 a[100:103], a[224:227], v[148:151], a[100:103]\n\t"
    "ds_read_b64_tr_b16 a[202:203], v16 offset:10240\n\t"
    "v_rcp_f16_sdwa v34, v36 dst_sel:WORD_1 dst_unused:UNUSED_PRESERVE src0_sel:WORD_1\n\t"
    "s_add_u32 s66, s66, 12288\n\t"
    "v_mfma_f32_16x16x32_bf16 a[104:107], a[224:227], v[152:155], a[104:107]\n\t"
    "v_rcp_f16_sdwa v35, v37 dst_sel:WORD_1 dst_unused:UNUSED_PRESERVE src0_sel:WORD_1\n\t"
    "v_fma_mix_f32 v72, v72, v34, 0 op_sel_hi:[0,1,0]\n\t"
    "v_fma_mix_f32 v74, v74, v35, 0 op_sel_hi:[0,1,0]\n\t"
    "v_mfma_f32_16x16x32_bf16 a[108:111], a[228:231], v[144:147], a[108:111]\n\t"
    "ds_read_b64_tr_b16 a[204:205], v16 offset:12288\n\t"
    "v_fma_mix_f32 v73, v73, v34, 0 op_sel:[0,1,0] op_sel_hi:[0,1,0]\n\t"
    "v_fma_mix_f32 v75, v75, v35, 0 op_sel:[0,1,0] op_sel_hi:[0,1,0]\n\t"
    "v_mfma_f32_16x16x32_bf16 a[112:115], a[228:231], v[148:151], a[112:115]\n\t"
    "ds_read_b64_tr_b16 a[206:207], v16 offset:14336\n\t"
    "v_cvt_pk_bf16_f32 v136, v72, v73\n\t"
    "v_cvt_pk_bf16_f32 v137, v74, v75\n\t"
    "v_cvt_pk_f16_f32 v38, v80, v81\n\t"
    "v_mfma_f32_16x16x32_bf16 a[116:119], a[228:231], v[152:155], a[116:119]\n\t"
    "ds_read_b128 v[48:51], v22 offset:128\n\t"
    "v_cvt_pk_f16_f32 v39, v82, v83\n\t"
    "v_pk_fma_f16 v40, v38, v38, s60 neg_lo:[1,0,1] neg_hi:[1,0,1]\n\t"
    "v_mfma_f32_16x16x32_bf16 a[120:123], a[232:235], v[144:147], a[120:123]\n\t"
    "ds_read_b64_tr_b16 a[208:209], v16 offset:16384\n\t"
    "v_pk_fma_f16 v41, v39, v39, s60 neg_lo:[1,0,1] neg_hi:[1,0,1]\n\t"
    "v_pk_mul_f16 v40, v40, v38\n\t"
    "v_pk_mul_f16 v41, v41, v39\n\t"
    "v_mfma_f32_16x16x32_bf16 a[124:127], a[232:235], v[148:151], a[124:127]\n\t"
    "ds_read_b64_tr_b16 a[210:211], v16 offset:18432\n\t"
    "v_exp_f16 v42, v40\n\t"
    "v_mfma_f32_16x16x32_bf16 a[128:131], a[232:235], v[152:155], a[128:131]\n\t"
    "v_exp_f16 v43, v41\n\t"
    "v_exp_f16_sdwa v42, v40 dst_sel:WORD_1 dst_unused:UNUSED_PRESERVE src0_sel:WORD_1\n\t"
    "v_mfma_f32_16x16x32_bf16 a[132:135], a[236:239], v[144:147], a[132:135]\n\t"
    "ds_read_b64_tr_b16 a[212:213], v16 offset:20480\n\t"
    "v_exp_f16_sdwa v43, v41 dst_sel:WORD_1 dst_unused:UNUSED_PRESERVE src0_sel:WORD_1\n\t"
    "v_pk_add_f16 v42, v42, 1.0 op_sel_hi:[1,0]\n\t"
    "v_mfma_f32_16x16x32_bf16 a[136:139], a[236:239], v[148:151], a[136:139]\n\t"
    "ds_read_b64_tr_b16 a[214:215], v16 offset:22528\n\t"
    "v_pk_add_f16 v43, v43, 1.0 op_sel_hi:[1,0]\n\t"
    "v_rcp_f16 v40, v42\n\t"
    "v_mfma_f32_16x16x32_bf16 a[140:143], a[236:239], v[152:155], a[140:143]\n\t"
    "v_rcp_f16 v41, v43\n\t"
    "s_waitcnt lgkmcnt(4)\n\t"
    "v_mfma_f32_16x16x32_bf16 v[96:99], a[192:195], v[160:163], v[48:51]\n\t"
    "ds_read_b64_tr_b16 a[216:217], v17 offset:0\n\t"
    "v_rcp_f16_sdwa v40, v42 dst_sel:WORD_1 dst_unused:UNUSED_PRESERVE src0_sel:WORD_1\n\t"
    "v_mfma_f32_16x16x32_bf16 v[100:103], a[192:195], v[184:187], v[48:51]\n\t"
    "ds_read_b64_tr_b16 a[218:219], v17 offset:2048\n\t"
    "v_rcp_f16_sdwa v41, v43 dst_sel:WORD_1 dst_unused:UNUSED_PRESERVE src0_sel:WORD_1\n\t"
    "v_fma_mix_f32 v80, v80, v40, 0 op_sel_hi:[0,1,0]\n\t"
    "v_mfma_f32_16x16x32_bf16 v[104:107], a[192:195], v[208:211], v[48:51]\n\t"
    "v_fma_mix_f32 v82, v82, v41, 0 op_sel_hi:[0,1,0]\n\t"
    "v_fma_mix_f32 v81, v81, v40, 0 op_sel:[0,1,0] op_sel_hi:[0,1,0]\n\t"
    "v_fma_mix_f32 v83, v83, v41, 0 op_sel:[0,1,0] op_sel_hi:[0,1,0]\n\t"
    "v_cvt_pk_bf16_f32 v130, v80, v81\n\t"
    "v_mfma_f32_16x16x32_bf16 v[96:99], a[196:199], v[164:167], v[96:99]\n\t"
    "ds_read_b64_tr_b16 a[220:221], v17 offset:4096\n\t"
    "v_cvt_pk_bf16_f32 v131, v82, v83\n\t"
    "v_subrev_u32 v18, s30, v18\n\t"
    "v_mfma_f32_16x16x32_bf16 v[100:103], a[196:199], v[188:191], v[100:103]\n\t"
    "ds_read_b64_tr_b16 a[222:223], v17 offset:6144\n\t"
    "v_subrev_u32 v19, s30, v19\n\t"
    "v_subrev_u32 v20, s30, v20\n\t"
    "v_subrev_u32 v21, s30, v21\n\t"
    "v_mfma_f32_16x16x32_bf16 v[104:107], a[196:199], v[212:215], v[104:107]\n\t"
    "v_cvt_pk_f16_f32 v32, v84, v85\n\t"
    "v_cvt_pk_f16_f32 v33, v86, v87\n\t"
    "v_pk_fma_f16 v34, v32, v32, s60 neg_lo:[1,0,1] neg_hi:[1,0,1]\n\t"
    "v_pk_fma_f16 v35, v33, v33, s60 neg_lo:[1,0,1] neg_hi:[1,0,1]\n\t"
    "v_mfma_f32_16x16x32_bf16 v[96:99], a[200:203], v[168:171], v[96:99]\n\t"
    "ds_read_b64_tr_b16 a[224:225], v17 offset:8192\n\t"
    "v_pk_mul_f16 v34, v34, v32\n\t"
    "v_pk_mul_f16 v35, v35, v33\n\t"
    "v_mfma_f32_16x16x32_bf16 v[100:103], a[200:203], v[192:195], v[100:103]\n\t"
    "ds_read_b64_tr_b16 a[226:227], v17 offset:10240\n\t"
    "v_exp_f16 v36, v34\n\t"
    "v_mfma_f32_16x16x32_bf16 v[104:107], a[200:203], v[216:219], v[104:107]\n\t"
    "v_exp_f16 v37, v35\n\t"
    "v_exp_f16_sdwa v36, v34 dst_sel:WORD_1 dst_unused:UNUSED_PRESERVE src0_sel:WORD_1\n\t"
    "v_mfma_f32_16x16x32_bf16 v[96:99], a[204:207], v[172:175], v[96:99]\n\t"
    "ds_read_b64_tr_b16 a[228:229], v17 offset:12288\n\t"
    "v_exp_f16_sdwa v37, v35 dst_sel:WORD_1 dst_unused:UNUSED_PRESERVE src0_sel:WORD_1\n\t"
    "v_pk_add_f16 v36, v36, 1.0 op_sel_hi:[1,0]\n\t"
    "v_mfma_f32_16x16x32_bf16 v[100:103], a[204:207], v[196:199], v[100:103]\n\t"
    "ds_read_b64_tr_b16 a[230:231], v17 offset:14336\n\t"
    "v_pk_add_f16 v37, v37, 1.0 op_sel_hi:[1,0]\n\t"
    "v_rcp_f16 v34, v36\n\t"
    "v_mfma_f32_16x16x32_bf16 v[104:107], a[204:207], v[220:223], v[104:107]\n\t"
    "v_rcp_f16 v35, v37\n\t"
    "s_waitcnt lgkmcnt(4)\n\t"
    "v_mfma_f32_16x16x32_bf16 v[96:99], a[208:211], v[176:179], v[96:99]\n\t"
    "ds_read_b64_tr_b16 a[232:233], v17 offset:16384\n\t"
    "v_rcp_f16_sdwa v34, v36 dst_sel:WORD_1 dst_unused:UNUSED_PRESERVE src0_sel:WORD_1\n\t"
    "v_mfma_f32_16x16x32_bf16 v[100:103], a[208:211], v[200:203], v[100:103]\n\t"
    "ds_read_b64_tr_b16 a[234:235], v17 offset:18432\n\t"
    "v_rcp_f16_sdwa v35, v37 dst_sel:WORD_1 dst_unused:UNUSED_PRESERVE src0_sel:WORD_1\n\t"
    "v_fma_mix_f32 v84, v84, v34, 0 op_sel_hi:[0,1,0]\n\t"
    "v_fma_mix_f32 v86, v86, v35, 0 op_sel_hi:[0,1,0]\n\t"
    "v_mfma_f32_16x16x32_bf16 v[104:107], a[208:211], v[224:227], v[104:107]\n\t"
    "v_fma_mix_f32 v85, v85, v34, 0 op_sel:[0,1,0] op_sel_hi:[0,1,0]\n\t"
    "v_fma_mix_f32 v87, v87, v35, 0 op_sel:[0,1,0] op_sel_hi:[0,1,0]\n\t"
    "v_cvt_pk_bf16_f32 v134, v84, v85\n\t"
    "v_mfma_f32_16x16x32_bf16 v[96:99], a[212:215], v[180:183], v[96:99]\n\t"
    "ds_read_b64_tr_b16 a[236:237], v17 offset:20480\n\t"
    "v_cvt_pk_bf16_f32 v135, v86, v87\n\t"
    "v_cvt_pk_f16_f32 v38, v88, v89\n\t"
    "v_cvt_pk_f16_f32 v39, v90, v91\n\t"
    "v_mfma_f32_16x16x32_bf16 v[100:103], a[212:215], v[204:207], v[100:103]\n\t"
    "ds_read_b64_tr_b16 a[238:239], v17 offset:22528\n\t"
    "v_pk_fma_f16 v40, v38, v38, s60 neg_lo:[1,0,1] neg_hi:[1,0,1]\n\t"
    "v_pk_fma_f16 v41, v39, v39, s60 neg_lo:[1,0,1] neg_hi:[1,0,1]\n\t"
    "v_mfma_f32_16x16x32_bf16 v[104:107], a[212:215], v[228:231], v[104:107]\n\t"
    "v_pk_mul_f16 v40, v40, v38\n\t"
    "v_pk_mul_f16 v41, v41, v39\n\t"
    "v_exp_f16 v42, v40\n\t"
    "v_mfma_f32_16x16x32_bf16 v[112:115], a[216:219], v[160:163], v[52:55]\n\t"
    "ds_read_b64_tr_b16 a[192:193], v18 offset:0\n\t"
    "v_exp_f16 v43, v41\n\t"
    "v_mfma_f32_16x16x32_bf16 v[116:119], a[216:219], v[184:187], v[52:55]\n\t"
    "ds_read_b64_tr_b16 a[194:195], v18 offset:6144\n\t"
    "v_exp_f16_sdwa v42, v40 dst_sel:WORD_1 dst_unused:UNUSED_PRESERVE src0_sel:WORD_1\n\t"
    "v_exp_f16_sdwa v43, v41 dst_sel:WORD_1 dst_unused:UNUSED_PRESERVE src0_sel:WORD_1\n\t"
    "v_mfma_f32_16x16x32_bf16 v[120:123], a[216:219], v[208:211], v[52:55]\n\t"
    "v_pk_add_f16 v42, v42, 1.0 op_sel_hi:[1,0]\n\t"
    "v_pk_add_f16 v43, v43, 1.0 op_sel_hi:[1,0]\n\t"
    "v_mfma_f32_16x16x32_bf16 v[112:115], a[220:223], v[164:167], v[112:115]\n\t"
    "ds_read_b64_tr_b16 a[196:197], v19 offset:0\n\t"
    "v_rcp_f16 v40, v42\n\t"
    "v_rcp_f16 v41, v43\n\t"
    "v_mfma_f32_16x16x32_bf16 v[116:119], a[220:223], v[188:191], v[116:119]\n\t"
    "ds_read_b64_tr_b16 a[198:199], v19 offset:6144\n\t"
    "v_rcp_f16_sdwa v40, v42 dst_sel:WORD_1 dst_unused:UNUSED_PRESERVE src0_sel:WORD_1\n\t"
    "v_mfma_f32_16x16x32_bf16 v[120:123], a[220:223], v[212:215], v[120:123]\n\t"
    "v_rcp_f16_sdwa v41, v43 dst_sel:WORD_1 dst_unused:UNUSED_PRESERVE src0_sel:WORD_1\n\t"
    "v_fma_mix_f32 v88, v88, v40, 0 op_sel_hi:[0,1,0]\n\t"
    "s_waitcnt lgkmcnt(4)\n\t"
    "v_mfma_f32_16x16x32_bf16 v[112:115], a[224:227], v[168:171], v[112:115]\n\t"
    "ds_read_b64_tr_b16 a[200:201], v20 offset:0\n\t"
    "v_fma_mix_f32 v90, v90, v41, 0 op_sel_hi:[0,1,0]\n\t"
    "v_fma_mix_f32 v89, v89, v40, 0 op_sel:[0,1,0] op_sel_hi:[0,1,0]\n\t"
    "v_mfma_f32_16x16x32_bf16 v[116:119], a[224:227], v[192:195], v[116:119]\n\t"
    "ds_read_b64_tr_b16 a[202:203], v20 offset:6144\n\t"
    "v_fma_mix_f32 v91, v91, v41, 0 op_sel:[0,1,0] op_sel_hi:[0,1,0]\n\t"
    "v_cvt_pk_bf16_f32 v138, v88, v89\n\t"
    "v_cvt_pk_bf16_f32 v139, v90, v91\n\t"
    "v_mfma_f32_16x16x32_bf16 v[120:123], a[224:227], v[216:219], v[120:123]\n\t"
    "v_cvt_pk_f16_f32 v32, v96, v97\n\t"
    "v_cvt_pk_f16_f32 v33, v98, v99\n\t"
    "v_pk_fma_f16 v34, v32, v32, s60 neg_lo:[1,0,1] neg_hi:[1,0,1]\n\t"
    "v_pk_fma_f16 v35, v33, v33, s60 neg_lo:[1,0,1] neg_hi:[1,0,1]\n\t"
    "v_mfma_f32_16x16x32_bf16 v[112:115], a[228:231], v[172:175], v[112:115]\n\t"
    "ds_read_b64_tr_b16 a[204:205], v21 offset:0\n\t"
    "v_pk_mul_f16 v34, v34, v32\n\t"
    "v_pk_mul_f16 v35, v35, v33\n\t"
    "v_mfma_f32_16x16x32_bf16 v[116:119], a[228:231], v[196:199], v[116:119]\n\t"
    "ds_read_b64_tr_b16 a[206:207], v21 offset:6144\n\t"
    "v_exp_f16 v36, v34\n\t"
    "v_mfma_f32_16x16x32_bf16 v[120:123], a[228:231], v[220:223], v[120:123]\n\t"
    "v_exp_f16 v37, v35\n\t"
    "v_exp_f16_sdwa v36, v34 dst_sel:WORD_1 dst_unused:UNUSED_PRESERVE src0_sel:WORD_1\n\t"
    "v_mfma_f32_16x16x32_bf16 v[112:115], a[232:235], v[176:179], v[112:115]\n\t"
    "ds_read_b64_tr_b16 a[208:209], v18 offset:1024\n\t"
    "v_exp_f16_sdwa v37, v35 dst_sel:WORD_1 dst_unused:UNUSED_PRESERVE src0_sel:WORD_1\n\t"
    "v_pk_add_f16 v36, v36, 1.0 op_sel_hi:[1,0]\n\t"
    "v_mfma_f32_16x16x32_bf16 v[116:119], a[232:235], v[200:203], v[116:119]\n\t"
    "ds_read_b64_tr_b16 a[210:211], v18 offset:7168\n\t"
    "v_pk_add_f16 v37, v37, 1.0 op_sel_hi:[1,0]\n\t"
    "v_rcp_f16 v34, v36\n\t"
    "v_mfma_f32_16x16x32_bf16 v[120:123], a[232:235], v[224:227], v[120:123]\n\t"
    "v_rcp_f16 v35, v37\n\t"
    "v_rcp_f16_sdwa v34, v36 dst_sel:WORD_1 dst_unused:UNUSED_PRESERVE src0_sel:WORD_1\n\t"
    "v_mfma_f32_16x16x32_bf16 v[112:115], a[236:239], v[180:183], v[112:115]\n\t"
    "ds_read_b64_tr_b16 a[212:213], v19 offset:1024\n\t"
    "v_rcp_f16_sdwa v35, v37 dst_sel:WORD_1 dst_unused:UNUSED_PRESERVE src0_sel:WORD_1\n\t"
    "v_mfma_f32_16x16x32_bf16 v[116:119], a[236:239], v[204:207], v[116:119]\n\t"
    "ds_read_b64_tr_b16 a[214:215], v19 offset:7168\n\t"
    "v_fma_mix_f32 v96, v96, v34, 0 op_sel_hi:[0,1,0]\n\t"
    "v_fma_mix_f32 v98, v98, v35, 0 op_sel_hi:[0,1,0]\n\t"
    "v_mfma_f32_16x16x32_bf16 v[120:123], a[236:239], v[228:231], v[120:123]\n\t"
    "v_fma_mix_f32 v97, v97, v34, 0 op_sel:[0,1,0] op_sel_hi:[0,1,0]\n\t"
    "v_fma_mix_f32 v99, v99, v35, 0 op_sel:[0,1,0] op_sel_hi:[0,1,0]\n\t"
    "v_cvt_pk_bf16_f32 v144, v96, v97\n\t"
    "v_cvt_pk_bf16_f32 v145, v98, v99\n\t"
    "P2_b_%=:\n\t"
    "s_waitcnt lgkmcnt(4)\n\t"
    "v_mfma_f32_16x16x32_bf16 a[0:3], a[192:195], v[128:131], a[0:3]\n\t"
    "ds_read_b64_tr_b16 a[216:217], v20 offset:1024\n\t"
    "v_cvt_pk_f16_f32 v38, v100, v101\n\t"
    "v_cvt_pk_f16_f32 v39, v102, v103\n\t"
    "v_pk_fma_f16 v40, v38, v38, s60 neg_lo:[1,0,1] neg_hi:[1,0,1]\n\t"
    "v_mfma_f32_16x16x32_bf16 a[4:7], a[192:195], v[132:135], a[4:7]\n\t"
    "ds_read_b64_tr_b16 a[218:219], v20 offset:7168\n\t"
    "v_pk_fma_f16 v41, v39, v39, s60 neg_lo:[1,0,1] neg_hi:[1,0,1]\n\t"
    "v_pk_mul_f16 v40, v40, v38\n\t"
    "v_mfma_f32_16x16x32_bf16 a[8:11], a[192:195], v[136:139], a[8:11]\n\t"
    "v_pk_mul_f16 v41, v41, v39\n\t"
    "v_exp_f16 v42, v40\n\t"
    "v_mfma_f32_16x16x32_bf16 a[12:15], a[196:199], v[128:131], a[12:15]\n\t"
    "ds_read_b64_tr_b16 a[220:221], v21 offset:1024\n\t"
    "v_exp_f16 v43, v41\n\t"
    "v_exp_f16_sdwa v42, v40 dst_sel:WORD_1 dst_unused:UNUSED_PRESERVE src0_sel:WORD_1\n\t"
    "v_mfma_f32_16x16x32_bf16 a[16:19], a[196:199], v[132:135], a[16:19]\n\t"
    "ds_read_b64_tr_b16 a[222:223], v21 offset:7168\n\t"
    "v_exp_f16_sdwa v43, v41 dst_sel:WORD_1 dst_unused:UNUSED_PRESERVE src0_sel:WORD_1\n\t"
    "v_mfma_f32_16x16x32_bf16 a[20:23], a[196:199], v[136:139], a[20:23]\n\t"
    "v_pk_add_f16 v42, v42, 1.0 op_sel_hi:[1,0]\n\t"
    "v_pk_add_f16 v43, v43, 1.0 op_sel_hi:[1,0]\n\t"
    "v_rcp_f16 v40, v42\n\t"
    "v_mfma_f32_16x16x32_bf16 a[24:27], a[200:203], v[128:131], a[24:27]\n\t"
    "ds_read_b64_tr_b16 a[224:225], v18 offset:2048\n\t"
    "v_rcp_f16 v41, v43\n\t"
    "v_mfma_f32_16x16x32_bf16 a[28:31], a[200:203], v[132:135], a[28:31]\n\t"
    "ds_read_b64_tr_b16 a[226:227], v18 offset:8192\n\t"
    "v_rcp_f16_sdwa v40, v42 dst_sel:WORD_1 dst_unused:UNUSED_PRESERVE src0_sel:WORD_1\n\t"
    "v_mfma_f32_16x16x32_bf16 a[32:35], a[200:203], v[136:139], a[32:35]\n\t"
    "v_rcp_f16_sdwa v41, v43 dst_sel:WORD_1 dst_unused:UNUSED_PRESERVE src0_sel:WORD_1\n\t"
    "v_fma_mix_f32 v100, v100, v40, 0 op_sel_hi:[0,1,0]\n\t"
    "v_fma_mix_f32 v102, v102, v41, 0 op_sel_hi:[0,1,0]\n\t"
    "v_mfma_f32_16x16x32_bf16 a[36:39], a[204:207], v[128:131], a[36:39]\n\t"
    "ds_read_b64_tr_b16 a[228:229], v19 offset:2048\n\t"
    "v_fma_mix_f32 v101, v101, v40, 0 op_sel:[0,1,0] op_sel_hi:[0,1,0]\n\t"
    "v_fma_mix_f32 v103, v103, v41, 0 op_sel:[0,1,0] op_sel_hi:[0,1,0]\n\t"
    "v_cvt_pk_bf16_f32 v148, v100, v101\n\t"
    "v_mfma_f32_16x16x32_bf16 a[40:43], a[204:207], v[132:135], a[40:43]\n\t"
    "ds_read_b64_tr_b16 a[230:231], v19 offset:8192\n\t"
    "v_cvt_pk_bf16_f32 v149, v102, v103\n\t"
    "v_cvt_pk_f16_f32 v32, v104, v105\n\t"
    "v_mfma_f32_16x16x32_bf16 a[44:47], a[204:207], v[136:139], a[44:47]\n\t"
    "v_cvt_pk_f16_f32 v33, v106, v107\n\t"
    "s_waitcnt vmcnt(0)\n\t"
    "s_barrier\n\t"
    "s_waitcnt lgkmcnt(4)\n\t"
    "v_mfma_f32_16x16x32_bf16 a[48:51], a[208:211], v[128:131], a[48:51]\n\t"
    "ds_read_b64_tr_b16 a[232:233], v20 offset:2048\n\t"
    "v_pk_fma_f16 v34, v32, v32, s60 neg_lo:[1,0,1] neg_hi:[1,0,1]\n\t"
    "v_pk_fma_f16 v35, v33, v33, s60 neg_lo:[1,0,1] neg_hi:[1,0,1]\n\t"
    "v_mfma_f32_16x16x32_bf16 a[52:55], a[208:211], v[132:135], a[52:55]\n\t"
    "ds_read_b64_tr_b16 a[234:235], v20 offset:8192\n\t"
    "v_pk_mul_f16 v34, v34, v32\n\t"
    "s_add_u32 m0, s67, 0\n\t"
    "s_add_u32 s63, s65, 0\n\t"
    "v_mfma_f32_16x16x32_bf16 a[56:59], a[208:211], v[136:139], a[56:59]\n\t"
    "buffer_load_dwordx4 v26, s[40:43], s63 offen offset:0 lds\n\t"
    "v_pk_mul_f16 v35, v35, v33\n\t"
    "v_mfma_f32_16x16x32_bf16 a[60:63], a[212:215], v[128:131], a[60:63]\n\t"
    "ds_read_b64_tr_b16 a[236:237], v21 offset:2048\n\t"
    "v_exp_f16 v36, v34\n\t"
    "v_exp_f16 v37, v35\n\t"
    "v_mfma_f32_16x16x32_bf16 a[64:67], a[212:215], v[132:135], a[64:67]\n\t"
    "ds_read_b64_tr_b16 a[238:239], v21 offset:8192\n\t"
    "v_exp_f16_sdwa v36, v34 dst_sel:WORD_1 dst_unused:UNUSED_PRESERVE src0_sel:WORD_1\n\t"
    "v_mfma_f32_16x16x32_bf16 a[68:71], a[212:215], v[136:139], a[68:71]\n\t"
    "v_exp_f16_sdwa v37, v35 dst_sel:WORD_1 dst_unused:UNUSED_PRESERVE src0_sel:WORD_1\n\t"
    "v_pk_add_f16 v36, v36, 1.0 op_sel_hi:[1,0]\n\t"
    "v_mfma_f32_16x16x32_bf16 a[72:75], a[216:219], v[128:131], a[72:75]\n\t"
    "ds_read_b64_tr_b16 a[192:193], v14 offset:24576\n\t"
    "v_pk_add_f16 v37, v37, 1.0 op_sel_hi:[1,0]\n\t"
    "v_rcp_f16 v34, v36\n\t"
    "v_mfma_f32_16x16x32_bf16 a[76:79], a[216:219], v[132:135], a[76:79]\n\t"
    "ds_read_b64_tr_b16 a[194:195], v14 offset:26624\n\t"
    "v_rcp_f16 v35, v37\n\t"
    "v_mfma_f32_16x16x32_bf16 a[80:83], a[216:219], v[136:139], a[80:83]\n\t"
    "buffer_load_dwordx4 v27, s[40:43], s63 offen offset:1024 lds\n\t"
    "v_rcp_f16_sdwa v34, v36 dst_sel:WORD_1 dst_unused:UNUSED_PRESERVE src0_sel:WORD_1\n\t"
    "v_mfma_f32_16x16x32_bf16 a[84:87], a[220:223], v[128:131], a[84:87]\n\t"
    "ds_read_b64_tr_b16 a[196:197], v14 offset:28672\n\t"
    "v_rcp_f16_sdwa v35, v37 dst_sel:WORD_1 dst_unused:UNUSED_PRESERVE src0_sel:WORD_1\n\t"
    "v_fma_mix_f32 v104, v104, v34, 0 op_sel_hi:[0,1,0]\n\t"
    "v_mfma_f32_16x16x32_bf16 a[88:91], a[220:223], v[132:135], a[88:91]\n\t"
    "ds_read_b64_tr_b16 a[198:199], v14 offset:30720\n\t"
    "v_fma_mix_f32 v106, v106, v35, 0 op_sel_hi:[0,1,0]\n\t"
    "v_fma_mix_f32 v105, v105, v34, 0 op_sel:[0,1,0] op_sel_hi:[0,1,0]\n\t"
    "v_mfma_f32_16x16x32_bf16 a[92:95], a[220:223], v[136:139], a[92:95]\n\t"
    "ds_read_b128 v[52:55], v22 offset:320\n\t"
    "v_fma_mix_f32 v107, v107, v35, 0 op_sel:[0,1,0] op_sel_hi:[0,1,0]\n\t"
    "v_cvt_pk_bf16_f32 v152, v104, v105\n\t"
    "s_waitcnt lgkmcnt(5)\n\t"
    "v_mfma_f32_16x16x32_bf16 a[96:99], a[224:227], v[128:131], a[96:99]\n\t"
    "ds_read_b64_tr_b16 a[200:201], v14 offset:32768\n\t"
    "v_cvt_pk_bf16_f32 v153, v106, v107\n\t"
    "v_cvt_pk_f16_f32 v38, v112, v113\n\t"
    "v_mfma_f32_16x16x32_bf16 a[100:103], a[224:227], v[132:135], a[100:103]\n\t"
    "ds_read_b64_tr_b16 a[202:203], v14 offset:34816\n\t"
    "v_cvt_pk_f16_f32 v39, v114, v115\n\t"
    "v_pk_fma_f16 v40, v38, v38, s60 neg_lo:[1,0,1] neg_hi:[1,0,1]\n\t"
    "v_pk_fma_f16 v41, v39, v39, s60 neg_lo:[1,0,1] neg_hi:[1,0,1]\n\t"
    "v_mfma_f32_16x16x32_bf16 a[104:107], a[224:227], v[136:139], a[104:107]\n\t"
    "buffer_load_dwordx4 v28, s[40:43], s63 offen offset:2048 lds\n\t"
    "v_pk_mul_f16 v40, v40, v38\n\t"
    "v_mfma_f32_16x16x32_bf16 a[108:111], a[228:231], v[128:131], a[108:111]\n\t"
    "ds_read_b64_tr_b16 a[204:205], v14 offset:36864\n\t"
    "v_pk_mul_f16 v41, v41, v39\n\t"
    "v_exp_f16 v42, v40\n\t"
    "v_mfma_f32_16x16x32_bf16 a[112:115], a[228:231], v[132:135], a[112:115]\n\t"
    "ds_read_b64_tr_b16 a[206:207], v14 offset:38912\n\t"
    "v_exp_f16 v43, v41\n\t"
    "v_mfma_f32_16x16x32_bf16 a[116:119], a[228:231], v[136:139], a[116:119]\n\t"
    "ds_read_b128 v[48:51], v22 offset:256\n\t"
    "v_exp_f16_sdwa v42, v40 dst_sel:WORD_1 dst_unused:UNUSED_PRESERVE src0_sel:WORD_1\n\t"
    "v_exp_f16_sdwa v43, v41 dst_sel:WORD_1 dst_unused:UNUSED_PRESERVE src0_sel:WORD_1\n\t"
    "v_mfma_f32_16x16x32_bf16 a[120:123], a[232:235], v[128:131], a[120:123]\n\t"
    "ds_read_b64_tr_b16 a[208:209], v14 offset:40960\n\t"
    "v_pk_add_f16 v42, v42, 1.0 op_sel_hi:[1,0]\n\t"
    "v_pk_add_f16 v43, v43, 1.0 op_sel_hi:[1,0]\n\t"
    "v_mfma_f32_16x16x32_bf16 a[124:127], a[232:235], v[132:135], a[124:127]\n\t"
    "ds_read_b64_tr_b16 a[210:211], v14 offset:43008\n\t"
    "v_rcp_f16 v40, v42\n\t"
    "v_mfma_f32_16x16x32_bf16 a[128:131], a[232:235], v[136:139], a[128:131]\n\t"
    "buffer_load_dwordx4 v58, s[40:43], s63 offen offset:3072 lds\n\t"
    "v_rcp_f16 v41, v43\n\t"
    "v_mfma_f32_16x16x32_bf16 a[132:135], a[236:239], v[128:131], a[132:135]\n\t"
    "ds_read_b64_tr_b16 a[212:213], v14 offset:45056\n\t"
    "v_rcp_f16_sdwa v40, v42 dst_sel:WORD_1 dst_unused:UNUSED_PRESERVE src0_sel:WORD_1\n\t"
    "v_mfma_f32_16x16x32_bf16 a[136:139], a[236:239], v[132:135], a[136:139]\n\t"
    "ds_read_b64_tr_b16 a[214:215], v14 offset:47104\n\t"
    "v_rcp_f16_sdwa v41, v43 dst_sel:WORD_1 dst_unused:UNUSED_PRESERVE src0_sel:WORD_1\n\t"
    "v_fma_mix_f32 v112, v112, v40, 0 op_sel_hi:[0,1,0]\n\t"
    "v_mfma_f32_16x16x32_bf16 a[140:143], a[236:239], v[136:139], a[140:143]\n\t"
    "v_fma_mix_f32 v114, v114, v41, 0 op_sel_hi:[0,1,0]\n\t"
    "v_fma_mix_f32 v113, v113, v40, 0 op_sel:[0,1,0] op_sel_hi:[0,1,0]\n\t"
    "v_fma_mix_f32 v115, v115, v41, 0 op_sel:[0,1,0] op_sel_hi:[0,1,0]\n\t"
    "s_waitcnt lgkmcnt(4)\n\t"
    "v_mfma_f32_16x16x32_bf16 v[64:67], a[192:195], v[160:163], v[48:51]\n\t"
    "ds_read_b64_tr_b16 a[216:217], v15 offset:24576\n\t"
    "v_cvt_pk_bf16_f32 v146, v112, v113\n\t"
    "v_cvt_pk_bf16_f32 v147, v114, v115\n\t"
    "v_mfma_f32_16x16x32_bf16 v[68:71], a[192:195], v[184:187], v[48:51]\n\t"
    "ds_read_b64_tr_b16 a[218:219], v15 offset:26624\n\t"
    "v_cvt_pk_f16_f32 v32, v116, v117\n\t"
    "v_cvt_pk_f16_f32 v33, v118, v119\n\t"
    "s_add_u32 m0, s68, 36864\n\t"
    "v_mfma_f32_16x16x32_bf16 v[72:75], a[192:195], v[208:211], v[48:51]\n\t"
    "buffer_load_dwordx4 v29, s[44:47], s66 offen offset:0 lds\n\t"
    "v_pk_fma_f16 v34, v32, v32, s60 neg_lo:[1,0,1] neg_hi:[1,0,1]\n\t"
    "v_mfma_f32_16x16x32_bf16 v[64:67], a[196:199], v[164:167], v[64:67]\n\t"
    "ds_read_b64_tr_b16 a[220:221], v15 offset:28672\n\t"
    "v_pk_fma_f16 v35, v33, v33, s60 neg_lo:[1,0,1] neg_hi:[1,0,1]\n\t"
    "v_pk_mul_f16 v34, v34, v32\n\t"
    "v_pk_mul_f16 v35, v35, v33\n\t"
    "v_mfma_f32_16x16x32_bf16 v[68:71], a[196:199], v[188:191], v[68:71]\n\t"
    "ds_read_b64_tr_b16 a[222:223], v15 offset:30720\n\t"
    "v_exp_f16 v36, v34\n\t"
    "v_mfma_f32_16x16x32_bf16 v[72:75], a[196:199], v[212:215], v[72:75]\n\t"
    "v_exp_f16 v37, v35\n\t"
    "v_exp_f16_sdwa v36, v34 dst_sel:WORD_1 dst_unused:UNUSED_PRESERVE src0_sel:WORD_1\n\t"
    "v_mfma_f32_16x16x32_bf16 v[64:67], a[200:203], v[168:171], v[64:67]\n\t"
    "ds_read_b64_tr_b16 a[224:225], v15 offset:32768\n\t"
    "v_exp_f16_sdwa v37, v35 dst_sel:WORD_1 dst_unused:UNUSED_PRESERVE src0_sel:WORD_1\n\t"
    "v_mfma_f32_16x16x32_bf16 v[68:71], a[200:203], v[192:195], v[68:71]\n\t"
    "ds_read_b64_tr_b16 a[226:227], v15 offset:34816\n\t"
    "v_pk_add_f16 v36, v36, 1.0 op_sel_hi:[1,0]\n\t"
    "v_pk_add_f16 v37, v37, 1.0 op_sel_hi:[1,0]\n\t"
    "v_rcp_f16 v34, v36\n\t"
    "v_mfma_f32_16x16x32_bf16 v[72:75], a[200:203], v[216:219], v[72:75]\n\t"
    "buffer_load_dwordx4 v30, s[44:47], s66 offen offset:1024 lds\n\t"
    "v_mfma_f32_16x16x32_bf16 v[64:67], a[204:207], v[172:175], v[64:67]\n\t"
    "ds_read_b64_tr_b16 a[228:229], v15 offset:36864\n\t"
    "v_rcp_f16 v35, v37\n\t"
    "v_rcp_f16_sdwa v34, v36 dst_sel:WORD_1 dst_unused:UNUSED_PRESERVE src0_sel:WORD_1\n\t"
    "v_mfma_f32_16x16x32_bf16 v[68:71], a[204:207], v[196:199], v[68:71]\n\t"
    "ds_read_b64_tr_b16 a[230:231], v15 offset:38912\n\t"
    "v_rcp_f16_sdwa v35, v37 dst_sel:WORD_1 dst_unused:UNUSED_PRESERVE src0_sel:WORD_1\n\t"
    "v_mfma_f32_16x16x32_bf16 v[72:75], a[204:207], v[220:223], v[72:75]\n\t"
    "v_fma_mix_f32 v116, v116, v34, 0 op_sel_hi:[0,1,0]\n\t"
    "v_fma_mix_f32 v118, v118, v35, 0 op_sel_hi:[0,1,0]\n\t"
    "s_waitcnt lgkmcnt(4)\n\t"
    "v_mfma_f32_16x16x32_bf16 v[64:67], a[208:211], v[176:179], v[64:67]\n\t"
    "ds_read_b64_tr_b16 a[232:233], v15 offset:40960\n\t"
    "v_fma_mix_f32 v117, v117, v34, 0 op_sel:[0,1,0] op_sel_hi:[0,1,0]\n\t"
    "v_fma_mix_f32 v119, v119, v35, 0 op_sel:[0,1,0] op_sel_hi:[0,1,0]\n\t"
    "v_cvt_pk_bf16_f32 v150, v116, v117\n\t"
    "v_mfma_f32_16x16x32_bf16 v[68:71], a[208:211], v[200:203], v[68:71]\n\t"
    "ds_read_b64_tr_b16 a[234:235], v15 offset:43008\n\t"
    "v_cvt_pk_bf16_f32 v151, v118, v119\n\t"
    "v_cvt_pk_f16_f32 v38, v120, v121\n\t"
    "v_mfma_f32_16x16x32_bf16 v[72:75], a[208:211], v[224:227], v[72:75]\n\t"
    "buffer_load_dwordx4 v31, s[44:47], s66 offen offset:2048 lds\n\t"
    "v_cvt_pk_f16_f32 v39, v122, v123\n\t"
    "v_pk_fma_f16 v40, v38, v38, s60 neg_lo:[1,0,1] neg_hi:[1,0,1]\n\t"
    "v_mfma_f32_16x16x32_bf16 v[64:67], a[212:215], v[180:183], v[64:67]\n\t"
    "ds_read_b64_tr_b16 a[236:237], v15 offset:45056\n\t"
    "v_pk_fma_f16 v41, v39, v39, s60 neg_lo:[1,0,1] neg_hi:[1,0,1]\n\t"
    "v_pk_mul_f16 v40, v40, v38\n\t"
    "v_pk_mul_f16 v41, v41, v39\n\t"
    "v_mfma_f32_16x16x32_bf16 v[68:71], a[212:215], v[204:207], v[68:71]\n\t"
    "ds_read_b64_tr_b16 a[238:239], v15 offset:47104\n\t"
    "v_exp_f16 v42, v40\n\t"
    "v_mfma_f32_16x16x32_bf16 v[72:75], a[212:215], v[228:231], v[72:75]\n\t"
    "v_exp_f16 v43, v41\n\t"
    "v_exp_f16_sdwa v42, v40 dst_sel:WORD_1 dst_unused:UNUSED_PRESERVE src0_sel:WORD_1\n\t"
    "v_mfma_f32_16x16x32_bf16 v[80:83], a[216:219], v[160:163], v[52:55]\n\t"
    "ds_read_b64_tr_b16 a[192:193], v18 offset:12288\n\t"
    "v_exp_f16_sdwa v43, v41 dst_sel:WORD_1 dst_unused:UNUSED_PRESERVE src0_sel:WORD_1\n\t"
    "v_mfma_f32_16x16x32_bf16 v[84:87], a[216:219], v[184:187], v[52:55]\n\t"
    "ds_read_b64_tr_b16 a[194:195], v18 offset:18432\n\t"
    "v_pk_add_f16 v42, v42, 1.0 op_sel_hi:[1,0]\n\t"
    "v_pk_add_f16 v43, v43, 1.0 op_sel_hi:[1,0]\n\t"
    "v_mfma_f32_16x16x32_bf16 v[88:91], a[216:219], v[208:211], v[52:55]\n\t"
    "buffer_load_dwordx4 v59, s[44:47], s66 offen offset:3072 lds\n\t"
    "v_rcp_f16 v40, v42\n\t"
    "v_mfma_f32_16x16x32_bf16 v[80:83], a[220:223], v[164:167], v[80:83]\n\t"
    "ds_read_b64_tr_b16 a[196:197], v19 offset:12288\n\t"
    "v_rcp_f16 v41, v43\n\t"
    "v_rcp_f16_sdwa v40, v42 dst_sel:WORD_1 dst_unused:UNUSED_PRESERVE src0_sel:WORD_1\n\t"
    "v_mfma_f32_16x16x32_bf16 v[84:87], a[220:223], v[188:191], v[84:87]\n\t"
    "ds_read_b64_tr_b16 a[198:199], v19 offset:18432\n\t"
    "v_rcp_f16_sdwa v41, v43 dst_sel:WORD_1 dst_unused:UNUSED_PRESERVE src0_sel:WORD_1\n\t"
    "v_mfma_f32_16x16x32_bf16 v[88:91], a[220:223], v[212:215], v[88:91]\n\t"
    "v_fma_mix_f32 v120, v120, v40, 0 op_sel_hi:[0,1,0]\n\t"
    "v_fma_mix_f32 v122, v122, v41, 0 op_sel_hi:[0,1,0]\n\t"
    "s_waitcnt lgkmcnt(4)\n\t"
    "v_mfma_f32_16x16x32_bf16 v[80:83], a[224:227], v[168:171], v[80:83]\n\t"
    "ds_read_b64_tr_b16 a[200:201], v20 offset:12288\n\t"
    "v_fma_mix_f32 v121, v121, v40, 0 op_sel:[0,1,0] op_sel_hi:[0,1,0]\n\t"
    "v_fma_mix_f32 v123, v123, v41, 0 op_sel:[0,1,0] op_sel_hi:[0,1,0]\n\t"
    "v_cvt_pk_bf16_f32 v154, v120, v121\n\t"
    "v_mfma_f32_16x16x32_bf16 v[84:87], a[224:227], v[192:195], v[84:87]\n\t"
    "ds_read_b64_tr_b16 a[202:203], v20 offset:18432\n\t"
    "v_cvt_pk_bf16_f32 v155, v122, v123\n\t"
    "s_add_u32 s66, s66, 12288\n\t"
    "v_mfma_f32_16x16x32_bf16 v[88:91], a[224:227], v[216:219], v[88:91]\n\t"
    "v_cvt_pk_f16_f32 v32, v64, v65\n\t"
    "v_cvt_pk_f16_f32 v33, v66, v67\n\t"
    "v_pk_fma_f16 v34, v32, v32, s60 neg_lo:[1,0,1] neg_hi:[1,0,1]\n\t"
    "v_pk_fma_f16 v35, v33, v33, s60 neg_lo:[1,0,1] neg_hi:[1,0,1]\n\t"
    "v_mfma_f32_16x16x32_bf16 v[80:83], a[228:231], v[172:175], v[80:83]\n\t"
    "ds_read_b64_tr_b16 a[204:205], v21 offset:12288\n\t"
    "v_pk_mul_f16 v34, v34, v32\n\t"
    "v_pk_mul_f16 v35, v35, v33\n\t"
    "v_mfma_f32_16x16x32_bf16 v[84:87], a[228:231], v[196:199], v[84:87]\n\t"
    "ds_read_b64_tr_b16 a[206:207], v21 offset:18432\n\t"
    "v_exp_f16 v36, v34\n\t"
    "v_exp_f16 v37, v35\n\t"
    "v_mfma_f32_16x16x32_bf16 v[88:91], a[228:231], v[220:223], v[88:91]\n\t"
    "v_exp_f16_sdwa v36, v34 dst_sel:WORD_1 dst_unused:UNUSED_PRESERVE src0_sel:WORD_1\n\t"
    "v_mfma_f32_16x16x32_bf16 v[80:83], a[232:235], v[176:179], v[80:83]\n\t"
    "ds_read_b64_tr_b16 a[208:209], v18 offset:13312\n\t"
    "v_exp_f16_sdwa v37, v35 dst_sel:WORD_1 dst_unused:UNUSED_PRESERVE src0_sel:WORD_1\n\t"
    "v_pk_add_f16 v36, v36, 1.0 op_sel_hi:[1,0]\n\t"
    "v_mfma_f32_16x16x32_bf16 v[84:87], a[232:235], v[200:203], v[84:87]\n\t"
    "ds_read_b64_tr_b16 a[210:211], v18 offset:19456\n\t"
    "v_pk_add_f16 v37, v37, 1.0 op_sel_hi:[1,0]\n\t"
    "v_rcp_f16 v34, v36\n\t"
    "v_mfma_f32_16x16x32_bf16 v[88:91], a[232:235], v[224:227], v[88:91]\n\t"
    "v_rcp_f16 v35, v37\n\t"
    "v_rcp_f16_sdwa v34, v36 dst_sel:WORD_1 dst_unused:UNUSED_PRESERVE src0_sel:WORD_1\n\t"
    "v_mfma_f32_16x16x32_bf16 v[80:83], a[236:239], v[180:183], v[80:83]\n\t"
    "ds_read_b64_tr_b16 a[212:213], v19 offset:13312\n\t"
    "v_rcp_f16_sdwa v35, v37 dst_sel:WORD_1 dst_unused:UNUSED_PRESERVE src0_sel:WORD_1\n\t"
    "v_mfma_f32_16x16x32_bf16 v[84:87], a[236:239], v[204:207], v[84:87]\n\t"
    "ds_read_b64_tr_b16 a[214:215], v19 offset:19456\n\t"
    "v_fma_mix_f32 v64, v64, v34, 0 op_sel_hi:[0,1,0]\n\t"
    "v_fma_mix_f32 v66, v66, v35, 0 op_sel_hi:[0,1,0]\n\t"
    "v_mfma_f32_16x16x32_bf16 v[88:91], a[236:239], v[228:231], v[88:91]\n\t"
    "v_fma_mix_f32 v65, v65, v34, 0 op_sel:[0,1,0] op_sel_hi:[0,1,0]\n\t"
    "v_fma_mix_f32 v67, v67, v35, 0 op_sel:[0,1,0] op_sel_hi:[0,1,0]\n\t"
    "v_cvt_pk_bf16_f32 v128, v64, v65\n\t"
    "v_cvt_pk_bf16_f32 v129, v66, v67\n\t"
    "s_waitcnt lgkmcnt(4)\n\t"
    "v_mfma_f32_16x16x32_bf16 a[0:3], a[192:195], v[144:147], a[0:3]\n\t"
    "ds_read_b64_tr_b16 a[216:217], v20 offset:13312\n\t"
    "v_cvt_pk_f16_f32 v38, v68, v69\n\t"
    "v_cvt_pk_f16_f32 v39, v70, v71\n\t"
    "v_pk_fma_f16 v40, v38, v38, s60 neg_lo:[1,0,1] neg_hi:[1,0,1]\n\t"
    "v_mfma_f32_16x16x32_bf16 a[4:7], a[192:195], v[148:151], a[4:7]\n\t"
    "ds_read_b64_tr_b16 a[218:219], v20 offset:19456\n\t"
    "s_add_u32 m0, s67, 12288\n\t"
    "s_add_u32 s63, s65, 147456\n\t"
    "v_mfma_f32_16x16x32_bf16 a[8:11], a[192:195], v[152:155], a[8:11]\n\t"
    "buffer_load_dwordx4 v26, s[40:43], s63 offen offset:0 lds\n\t"
    "v_pk_fma_f16 v41, v39, v39, s60 neg_lo:[1,0,1] neg_hi:[1,0,1]\n\t"
    "v_pk_mul_f16 v40, v40, v38\n\t"
    "v_mfma_f32_16x16x32_bf16 a[12:15], a[196:199], v[144:147], a[12:15]\n\t"
    "ds_read_b64_tr_b16 a[220:221], v21 offset:13312\n\t"
    "v_pk_mul_f16 v41, v41, v39\n\t"
    "v_exp_f16 v42, v40\n\t"
    "v_mfma_f32_16x16x32_bf16 a[16:19], a[196:199], v[148:151], a[16:19]\n\t"
    "ds_read_b64_tr_b16 a[222:223], v21 offset:19456\n\t"
    "v_exp_f16 v43, v41\n\t"
    "v_mfma_f32_16x16x32_bf16 a[20:23], a[196:199], v[152:155], a[20:23]\n\t"
    "buffer_load_dwordx4 v27, s[40:43], s63 offen offset:1024 lds\n\t"
    "v_exp_f16_sdwa v42, v40 dst_sel:WORD_1 dst_unused:UNUSED_PRESERVE src0_sel:WORD_1\n\t"
    "v_mfma_f32_16x16x32_bf16 a[24:27], a[200:203], v[144:147], a[24:27]\n\t"
    "ds_read_b64_tr_b16 a[224:225], v18 offset:14336\n\t"
    "v_exp_f16_sdwa v43, v41 dst_sel:WORD_1 dst_unused:UNUSED_PRESERVE src0_sel:WORD_1\n\t"
    "v_mfma_f32_16x16x32_bf16 a[28:31], a[200:203], v[148:151], a[28:31]\n\t"
    "ds_read_b64_tr_b16 a[226:227], v18 offset:20480\n\t"
    "v_pk_add_f16 v42, v42, 1.0 op_sel_hi:[1,0]\n\t"
    "v_pk_add_f16 v43, v43, 1.0 op_sel_hi:[1,0]\n\t"
    "v_mfma_f32_16x16x32_bf16 a[32:35], a[200:203], v[152:155], a[32:35]\n\t"
    "buffer_load_dwordx4 v28, s[40:43], s63 offen offset:2048 lds\n\t"
    "v_rcp_f16 v40, v42\n\t"
    "v_mfma_f32_16x16x32_bf16 a[36:39], a[204:207], v[144:147], a[36:39]\n\t"
    "ds_read_b64_tr_b16 a[228:229], v19 offset:14336\n\t"
    "v_rcp_f16 v41, v43\n\t"
    "v_mfma_f32_16x16x32_bf16 a[40:43], a[204:207], v[148:151], a[40:43]\n\t"
    "ds_read_b64_tr_b16 a[230:231], v19 offset:20480\n\t"
    "v_rcp_f16_sdwa v40, v42 dst_sel:WORD_1 dst_unused:UNUSED_PRESERVE src0_sel:WORD_1\n\t"
    "v_rcp_f16_sdwa v41, v43 dst_sel:WORD_1 dst_unused:UNUSED_PRESERVE src0_sel:WORD_1\n\t"
    "v_mfma_f32_16x16x32_bf16 a[44:47], a[204:207], v[152:155], a[44:47]\n\t"
    "buffer_load_dwordx4 v58, s[40:43], s63 offen offset:3072 lds\n\t"
    "s_waitcnt lgkmcnt(4)\n\t"
    "v_mfma_f32_16x16x32_bf16 a[48:51], a[208:211], v[144:147], a[48:51]\n\t"
    "ds_read_b64_tr_b16 a[232:233], v20 offset:14336\n\t"
    "v_fma_mix_f32 v68, v68, v40, 0 op_sel_hi:[0,1,0]\n\t"
    "v_fma_mix_f32 v70, v70, v41, 0 op_sel_hi:[0,1,0]\n\t"
    "v_mfma_f32_16x16x32_bf16 a[52:55], a[208:211], v[148:151], a[52:55]\n\t"
    "ds_read_b64_tr_b16 a[234:235], v20 offset:20480\n\t"
    "s_add_u32 s65, s65, 128\n\t"
    "s_add_u32 s68, s68, s30\n\t"
    "s_add_u32 m0, s68, 0\n\t"
    "v_mfma_f32_16x16x32_bf16 a[56:59], a[208:211], v[152:155], a[56:59]\n\t"
    "buffer_load_dwordx4 v29, s[44:47], s66 offen offset:0 lds\n\t"
    "v_fma_mix_f32 v69, v69, v40, 0 op_sel:[0,1,0] op_sel_hi:[0,1,0]\n\t"
    "v_fma_mix_f32 v71, v71, v41, 0 op_sel:[0,1,0] op_sel_hi:[0,1,0]\n\t"
    "v_mfma_f32_16x16x32_bf16 a[60:63], a[212:215], v[144:147], a[60:63]\n\t"
    "ds_read_b64_tr_b16 a[236:237], v21 offset:14336\n\t"
    "v_cvt_pk_bf16_f32 v132, v68, v69\n\t"
    "v_cvt_pk_bf16_f32 v133, v70, v71\n\t"
    "v_mfma_f32_16x16x32_bf16 a[64:67], a[212:215], v[148:151], a[64:67]\n\t"
    "ds_read_b64_tr_b16 a[238:239], v21 offset:20480\n\t"
    "v_cvt_pk_f16_f32 v32, v72, v73\n\t"
    "v_cvt_pk_f16_f32 v33, v74, v75\n\t"
    "v_pk_fma_f16 v34, v32, v32, s60 neg_lo:[1,0,1] neg_hi:[1,0,1]\n\t"
    "v_mfma_f32_16x16x32_bf16 a[68:71], a[212:215], v[152:155], a[68:71]\n\t"
    "buffer_load_dwordx4 v30, s[44:47], s66 offen offset:1024 lds\n\t"
    "v_pk_fma_f16 v35, v33, v33, s60 neg_lo:[1,0,1] neg_hi:[1,0,1]\n\t"
    "v_mfma_f32_16x16x32_bf16 a[72:75], a[216:219], v[144:147], a[72:75]\n\t"
    "ds_read_b64_tr_b16 a[192:193], v16 offset:24576\n\t"
    "v_pk_mul_f16 v34, v34, v32\n\t"
    "v_pk_mul_f16 v35, v35, v33\n\t"
    "v_mfma_f32_16x16x32_bf16 a[76:79], a[216:219], v[148:151], a[76:79]\n\t"
    "ds_read_b64_tr_b16 a[194:195], v16 offset:26624\n\t"
    "v_exp_f16 v36, v34\n\t"
    "v_exp_f16 v37, v35\n\t"
    "v_mfma_f32_16x16x32_bf16 a[80:83], a[216:219], v[152:155], a[80:83]\n\t"
    "buffer_load_dwordx4 v31, s[44:47], s66 offen offset:2048 lds\n\t"
    "v_mfma_f32_16x16x32_bf16 a[84:87], a[220:223], v[144:147], a[84:87]\n\t"
    "ds_read_b64_tr_b16 a[196:197], v16 offset:28672\n\t"
    "v_exp_f16_sdwa v36, v34 dst_sel:WORD_1 dst_unused:UNUSED_PRESERVE src0_sel:WORD_1\n\t"
    "v_exp_f16_sdwa v37, v35 dst_sel:WORD_1 dst_unused:UNUSED_PRESERVE src0_sel:WORD_1\n\t"
    "v_mfma_f32_16x16x32_bf16 a[88:91], a[220:223], v[148:151], a[88:91]\n\t"
    "ds_read_b64_tr_b16 a[198:199], v16 offset:30720\n\t"
    "v_pk_add_f16 v36, v36, 1.0 op_sel_hi:[1,0]\n\t"
    "v_pk_add_f16 v37, v37, 1.0 op_sel_hi:[1,0]\n\t"
    "v_mfma_f32_16x16x32_bf16 a[92:95], a[220:223], v[152:155], a[92:95]\n\t"
    "ds_read_b128 v[52:55], v22 offset:448\n\t"
    "buffer_load_dwordx4 v59, s[44:47], s66 offen offset:3072 lds\n\t"
    "s_waitcnt lgkmcnt(5)\n\t"
    "v_mfma_f32_16x16x32_bf16 a[96:99], a[224:227], v[144:147], a[96:99]\n\t"
    "ds_read_b64_tr_b16 a[200:201], v16 offset:32768\n\t"
    "v_rcp_f16 v34, v36\n\t"
    "v_mfma_f32_16x16x32_bf16 a[100:103], a[224:227], v[148:151], a[100:103]\n\t"
    "ds_read_b64_tr_b16 a[202:203], v16 offset:34816\n\t"
    "v_rcp_f16 v35, v37\n\t"
    "s_add_u32 s66, s66, 12288\n\t"
    "v_mfma_f32_16x16x32_bf16 a[104:107], a[224:227], v[152:155], a[104:107]\n\t"
    "v_rcp_f16_sdwa v34, v36 dst_sel:WORD_1 dst_unused:UNUSED_PRESERVE src0_sel:WORD_1\n\t"
    "v_rcp_f16_sdwa v35, v37 dst_sel:WORD_1 dst_unused:UNUSED_PRESERVE src0_sel:WORD_1\n\t"
    "v_mfma_f32_16x16x32_bf16 a[108:111], a[228:231], v[144:147], a[108:111]\n\t"
    "ds_read_b64_tr_b16 a[204:205], v16 offset:36864\n\t"
    "v_fma_mix_f32 v72, v72, v34, 0 op_sel_hi:[0,1,0]\n\t"
    "v_fma_mix_f32 v74, v74, v35, 0 op_sel_hi:[0,1,0]\n\t"
    "v_mfma_f32_16x16x32_bf16 a[112:115], a[228:231], v[148:151], a[112:115]\n\t"
    "ds_read_b64_tr_b16 a[206:207], v16 offset:38912\n\t"
    "v_fma_mix_f32 v73, v73, v34, 0 op_sel:[0,1,0] op_sel_hi:[0,1,0]\n\t"
    "v_fma_mix_f32 v75, v75, v35, 0 op_sel:[0,1,0] op_sel_hi:[0,1,0]\n\t"
    "v_cvt_pk_bf16_f32 v136, v72, v73\n\t"
    "v_mfma_f32_16x16x32_bf16 a[116:119], a[228:231], v[152:155], a[116:119]\n\t"
    "ds_read_b128 v[48:51], v22 offset:384\n\t"
    "v_cvt_pk_bf16_f32 v137, v74, v75\n\t"
    "v_cvt_pk_f16_f32 v38, v80, v81\n\t"
    "v_mfma_f32_16x16x32_bf16 a[120:123], a[232:235], v[144:147], a[120:123]\n\t"
    "ds_read_b64_tr_b16 a[208:209], v16 offset:40960\n\t"
    "v_cvt_pk_f16_f32 v39, v82, v83\n\t"
    "v_pk_fma_f16 v40, v38, v38, s60 neg_lo:[1,0,1] neg_hi:[1,0,1]\n\t"
    "v_pk_fma_f16 v41, v39, v39, s60 neg_lo:[1,0,1] neg_hi:[1,0,1]\n\t"
    "v_mfma_f32_16x16x32_bf16 a[124:127], a[232:235], v[148:151], a[124:127]\n\t"
    "ds_read_b64_tr_b16 a[210:211], v16 offset:43008\n\t"
    "v_pk_mul_f16 v40, v40, v38\n\t"
    "v_pk_mul_f16 v41, v41, v39\n\t"
    "v_mfma_f32_16x16x32_bf16 a[128:131], a[232:235], v[152:155], a[128:131]\n\t"
    "v_exp_f16 v42, v40\n\t"
    "v_exp_f16 v43, v41\n\t"
    "v_mfma_f32_16x16x32_bf16 a[132:135], a[236:239], v[144:147], a[132:135]\n\t"
    "ds_read_b64_tr_b16 a[212:213], v16 offset:45056\n\t"
    "v_exp_f16_sdwa v42, v40 dst_sel:WORD_1 dst_unused:UNUSED_PRESERVE src0_sel:WORD_1\n\t"
    "v_mfma_f32_16x16x32_bf16 a[136:139], a[236:239], v[148:151], a[136:139]\n\t"
    "ds_read_b64_tr_b16 a[214:215], v16 offset:47104\n\t"
    "v_exp_f16_sdwa v43, v41 dst_sel:WORD_1 dst_unused:UNUSED_PRESERVE src0_sel:WORD_1\n\t"
    "v_pk_add_f16 v42, v42, 1.0 op_sel_hi:[1,0]\n\t"
    "v_mfma_f32_16x16x32_bf16 a[140:143], a[236:239], v[152:155], a[140:143]\n\t"
    "v_pk_add_f16 v43, v43, 1.0 op_sel_hi:[1,0]\n\t"
    "v_rcp_f16 v40, v42\n\t"
    "s_waitcnt lgkmcnt(4)\n\t"
    "v_mfma_f32_16x16x32_bf16 v[96:99], a[192:195], v[160:163], v[48:51]\n\t"
    "ds_read_b64_tr_b16 a[216:217], v17 offset:24576\n\t"
    "v_rcp_f16 v41, v43\n\t"
    "v_mfma_f32_16x16x32_bf16 v[100:103], a[192:195], v[184:187], v[48:51]\n\t"
    "ds_read_b64_tr_b16 a[218:219], v17 offset:26624\n\t"
    "v_rcp_f16_sdwa v40, v42 dst_sel:WORD_1 dst_unused:UNUSED_PRESERVE src0_sel:WORD_1\n\t"
    "v_mfma_f32_16x16x32_bf16 v[104:107], a[192:195], v[208:211], v[48:51]\n\t"
    "v_rcp_f16_sdwa v41, v43 dst_sel:WORD_1 dst_unused:UNUSED_PRESERVE src0_sel:WORD_1\n\t"
    "v_fma_mix_f32 v80, v80, v40, 0 op_sel_hi:[0,1,0]\n\t"
    "v_fma_mix_f32 v82, v82, v41, 0 op_sel_hi:[0,1,0]\n\t"
    "v_mfma_f32_16x16x32_bf16 v[96:99], a[196:199], v[164:167], v[96:99]\n\t"
    "ds_read_b64_tr_b16 a[220:221], v17 offset:28672\n\t"
    "v_fma_mix_f32 v81, v81, v40, 0 op_sel:[0,1,0] op_sel_hi:[0,1,0]\n\t"
    "v_fma_mix_f32 v83, v83, v41, 0 op_sel:[0,1,0] op_sel_hi:[0,1,0]\n\t"
    "v_cvt_pk_bf16_f32 v130, v80, v81\n\t"
    "v_mfma_f32_16x16x32_bf16 v[100:103], a[196:199], v[188:191], v[100:103]\n\t"
    "ds_read_b64_tr_b16 a[222:223], v17 offset:30720\n\t"
    "v_cvt_pk_bf16_f32 v131, v82, v83\n\t"
    "v_cvt_pk_f16_f32 v32, v84, v85\n\t"
    "v_mfma_f32_16x16x32_bf16 v[104:107], a[196:199], v[212:215], v[104:107]\n\t"
    "v_cvt_pk_f16_f32 v33, v86, v87\n\t"
    "v_pk_fma_f16 v34, v32, v32, s60 neg_lo:[1,0,1] neg_hi:[1,0,1]\n\t"
    "v_pk_fma_f16 v35, v33, v33, s60 neg_lo:[1,0,1] neg_hi:[1,0,1]\n\t"
    "v_pk_mul_f16 v34, v34, v32\n\t"
    "v_mfma_f32_16x16x32_bf16 v[96:99], a[200:203], v[168:171], v[96:99]\n\t"
    "ds_read_b64_tr_b16 a[224:225], v17 offset:32768\n\t"
    "v_pk_mul_f16 v35, v35, v33\n\t"
    "v_exp_f16 v36, v34\n\t"
    "v_mfma_f32_16x16x32_bf16 v[100:103], a[200:203], v[192:195], v[100:103]\n\t"
    "ds_read_b64_tr_b16 a[226:227], v17 offset:34816\n\t"
    "v_exp_f16 v37, v35\n\t"
    "v_mfma_f32_16x16x32_bf16 v[104:107], a[200:203], v[216:219], v[104:107]\n\t"
    "v_exp_f16_sdwa v36, v34 dst_sel:WORD_1 dst_unused:UNUSED_PRESERVE src0_sel:WORD_1\n\t"
    "v_exp_f16_sdwa v37, v35 dst_sel:WORD_1 dst_unused:UNUSED_PRESERVE src0_sel:WORD_1\n\t"
    "v_mfma_f32_16x16x32_bf16 v[96:99], a[204:207], v[172:175], v[96:99]\n\t"
    "ds_read_b64_tr_b16 a[228:229], v17 offset:36864\n\t"
    "v_pk_add_f16 v36, v36, 1.0 op_sel_hi:[1,0]\n\t"
    "v_pk_add_f16 v37, v37, 1.0 op_sel_hi:[1,0]\n\t"
    "v_mfma_f32_16x16x32_bf16 v[100:103], a[204:207], v[196:199], v[100:103]\n\t"
    "ds_read_b64_tr_b16 a[230:231], v17 offset:38912\n\t"
    "v_rcp_f16 v34, v36\n\t"
    "v_mfma_f32_16x16x32_bf16 v[104:107], a[204:207], v[220:223], v[104:107]\n\t"
    "v_rcp_f16 v35, v37\n\t"
    "v_rcp_f16_sdwa v34, v36 dst_sel:WORD_1 dst_unused:UNUSED_PRESERVE src0_sel:WORD_1\n\t"
    "s_waitcnt lgkmcnt(4)\n\t"
    "v_mfma_f32_16x16x32_bf16 v[96:99], a[208:211], v[176:179], v[96:99]\n\t"
    "ds_read_b64_tr_b16 a[232:233], v17 offset:40960\n\t"
    "v_rcp_f16_sdwa v35, v37 dst_sel:WORD_1 dst_unused:UNUSED_PRESERVE src0_sel:WORD_1\n\t"
    "v_mfma_f32_16x16x32_bf16 v[100:103], a[208:211], v[200:203], v[100:103]\n\t"
    "ds_read_b64_tr_b16 a[234:235], v17 offset:43008\n\t"
    "v_fma_mix_f32 v84, v84, v34, 0 op_sel_hi:[0,1,0]\n\t"
    "v_fma_mix_f32 v86, v86, v35, 0 op_sel_hi:[0,1,0]\n\t"
    "v_mfma_f32_16x16x32_bf16 v[104:107], a[208:211], v[224:227], v[104:107]\n\t"
    "v_fma_mix_f32 v85, v85, v34, 0 op_sel:[0,1,0] op_sel_hi:[0,1,0]\n\t"
    "v_fma_mix_f32 v87, v87, v35, 0 op_sel:[0,1,0] op_sel_hi:[0,1,0]\n\t"
    "v_cvt_pk_bf16_f32 v134, v84, v85\n\t"
    "v_cvt_pk_bf16_f32 v135, v86, v87\n\t"
    "v_mfma_f32_16x16x32_bf16 v[96:99], a[212:215], v[180:183], v[96:99]\n\t"
    "ds_read_b64_tr_b16 a[236:237], v17 offset:45056\n\t"
    "v_cvt_pk_f16_f32 v38, v88, v89\n\t"
    "v_cvt_pk_f16_f32 v39, v90, v91\n\t"
    "v_pk_fma_f16 v40, v38, v38, s60 neg_lo:[1,0,1] neg_hi:[1,0,1]\n\t"
    "v_mfma_f32_16x16x32_bf16 v[100:103], a[212:215], v[204:207], v[100:103]\n\t"
    "ds_read_b64_tr_b16 a[238:239], v17 offset:47104\n\t"
    "v_pk_fma_f16 v41, v39, v39, s60 neg_lo:[1,0,1] neg_hi:[1,0,1]\n\t"
    "v_pk_mul_f16 v40, v40, v38\n\t"
    "v_mfma_f32_16x16x32_bf16 v[104:107], a[212:215], v[228:231], v[104:107]\n\t"
    "v_pk_mul_f16 v41, v41, v39\n\t"
    "v_exp_f16 v42, v40\n\t"
    "v_mfma_f32_16x16x32_bf16 v[112:115], a[216:219], v[160:163], v[52:55]\n\t"
    "ds_read_b64_tr_b16 a[192:193], v18 offset:24576\n\t"
    "v_exp_f16 v43, v41\n\t"
    "v_exp_f16_sdwa v42, v40 dst_sel:WORD_1 dst_unused:UNUSED_PRESERVE src0_sel:WORD_1\n\t"
    "v_mfma_f32_16x16x32_bf16 v[116:119], a[216:219], v[184:187], v[52:55]\n\t"
    "ds_read_b64_tr_b16 a[194:195], v18 offset:30720\n\t"
    "v_exp_f16_sdwa v43, v41 dst_sel:WORD_1 dst_unused:UNUSED_PRESERVE src0_sel:WORD_1\n\t"
    "v_mfma_f32_16x16x32_bf16 v[120:123], a[216:219], v[208:211], v[52:55]\n\t"
    "v_pk_add_f16 v42, v42, 1.0 op_sel_hi:[1,0]\n\t"
    "v_pk_add_f16 v43, v43, 1.0 op_sel_hi:[1,0]\n\t"
    "v_rcp_f16 v40, v42\n\t"
    "v_mfma_f32_16x16x32_bf16 v[112:115], a[220:223], v[164:167], v[112:115]\n\t"
    "ds_read_b64_tr_b16 a[196:197], v19 offset:24576\n\t"
    "v_rcp_f16 v41, v43\n\t"
    "v_mfma_f32_16x16x32_bf16 v[116:119], a[220:223], v[188:191], v[116:119]\n\t"
    "ds_read_b64_tr_b16 a[198:199], v19 offset:30720\n\t"
    "v_rcp_f16_sdwa v40, v42 dst_sel:WORD_1 dst_unused:UNUSED_PRESERVE src0_sel:WORD_1\n\t"
    "v_mfma_f32_16x16x32_bf16 v[120:123], a[220:223], v[212:215], v[120:123]\n\t"
    "v_rcp_f16_sdwa v41, v43 dst_sel:WORD_1 dst_unused:UNUSED_PRESERVE src0_sel:WORD_1\n\t"
    "v_fma_mix_f32 v88, v88, v40, 0 op_sel_hi:[0,1,0]\n\t"
    "s_waitcnt lgkmcnt(4)\n\t"
    "v_mfma_f32_16x16x32_bf16 v[112:115], a[224:227], v[168:171], v[112:115]\n\t"
    "ds_read_b64_tr_b16 a[200:201], v20 offset:24576\n\t"
    "v_fma_mix_f32 v90, v90, v41, 0 op_sel_hi:[0,1,0]\n\t"
    "v_fma_mix_f32 v89, v89, v40, 0 op_sel:[0,1,0] op_sel_hi:[0,1,0]\n\t"
    "v_fma_mix_f32 v91, v91, v41, 0 op_sel:[0,1,0] op_sel_hi:[0,1,0]\n\t"
    "v_mfma_f32_16x16x32_bf16 v[116:119], a[224:227], v[192:195], v[116:119]\n\t"
    "ds_read_b64_tr_b16 a[202:203], v20 offset:30720\n\t"
    "v_cvt_pk_bf16_f32 v138, v88, v89\n\t"
    "v_cvt_pk_bf16_f32 v139, v90, v91\n\t"
    "v_mfma_f32_16x16x32_bf16 v[120:123], a[224:227], v[216:219], v[120:123]\n\t"
    "v_cvt_pk_f16_f32 v32, v96, v97\n\t"
    "v_cvt_pk_f16_f32 v33, v98, v99\n\t"
    "v_pk_fma_f16 v34, v32, v32, s60 neg_lo:[1,0,1] neg_hi:[1,0,1]\n\t"
    "v_pk_fma_f16 v35, v33, v33, s60 neg_lo:[1,0,1] neg_hi:[1,0,1]\n\t"
    "v_mfma_f32_16x16x32_bf16 v[112:115], a[228:231], v[172:175], v[112:115]\n\t"
    "ds_read_b64_tr_b16 a[204:205], v21 offset:24576\n\t"
    "v_pk_mul_f16 v34, v34, v32\n\t"
    "v_pk_mul_f16 v35, v35, v33\n\t"
    "v_mfma_f32_16x16x32_bf16 v[116:119], a[228:231], v[196:199], v[116:119]\n\t"
    "ds_read_b64_tr_b16 a[206:207], v21 offset:30720\n\t"
    "v_exp_f16 v36, v34\n\t"
    "v_exp_f16 v37, v35\n\t"
    "v_mfma_f32_16x16x32_bf16 v[120:123], a[228:231], v[220:223], v[120:123]\n\t"
    "v_exp_f16_sdwa v36, v34 dst_sel:WORD_1 dst_unused:UNUSED_PRESERVE src0_sel:WORD_1\n\t"
    "v_mfma_f32_16x16x32_bf16 v[112:115], a[232:235], v[176:179], v[112:115]\n\t"
    "ds_read_b64_tr_b16 a[208:209], v18 offset:25600\n\t"
    "v_exp_f16_sdwa v37, v35 dst_sel:WORD_1 dst_unused:UNUSED_PRESERVE src0_sel:WORD_1\n\t"
    "v_pk_add_f16 v36, v36, 1.0 op_sel_hi:[1,0]\n\t"
    "v_mfma_f32_16x16x32_bf16 v[116:119], a[232:235], v[200:203], v[116:119]\n\t"
    "ds_read_b64_tr_b16 a[210:211], v18 offset:31744\n\t"
    "v_pk_add_f16 v37, v37, 1.0 op_sel_hi:[1,0]\n\t"
    "v_rcp_f16 v34, v36\n\t"
    "v_mfma_f32_16x16x32_bf16 v[120:123], a[232:235], v[224:227], v[120:123]\n\t"
    "v_rcp_f16 v35, v37\n\t"
    "v_rcp_f16_sdwa v34, v36 dst_sel:WORD_1 dst_unused:UNUSED_PRESERVE src0_sel:WORD_1\n\t"
    "v_mfma_f32_16x16x32_bf16 v[112:115], a[236:239], v[180:183], v[112:115]\n\t"
    "ds_read_b64_tr_b16 a[212:213], v19 offset:25600\n\t"
    "v_rcp_f16_sdwa v35, v37 dst_sel:WORD_1 dst_unused:UNUSED_PRESERVE src0_sel:WORD_1\n\t"
    "v_mfma_f32_16x16x32_bf16 v[116:119], a[236:239], v[204:207], v[116:119]\n\t"
    "ds_read_b64_tr_b16 a[214:215], v19 offset:31744\n\t"
    "v_fma_mix_f32 v96, v96, v34, 0 op_sel_hi:[0,1,0]\n\t"
    "v_fma_mix_f32 v98, v98, v35, 0 op_sel_hi:[0,1,0]\n\t"
    "v_mfma_f32_16x16x32_bf16 v[120:123], a[236:239], v[228:231], v[120:123]\n\t"
    "v_fma_mix_f32 v97, v97, v34, 0 op_sel:[0,1,0] op_sel_hi:[0,1,0]\n\t"
    "v_fma_mix_f32 v99, v99, v35, 0 op_sel:[0,1,0] op_sel_hi:[0,1,0]\n\t"
    "v_cvt_pk_bf16_f32 v144, v96, v97\n\t"
    "v_cvt_pk_bf16_f32 v145, v98, v99\n\t"
    "v_add_u32 v22, 512, v22\n\t"
    "s_sub_u32 s30, 0, s30\n\t"
    "s_sub_u32 s69, s69, 1\n\t"
    "s_cmp_lg_u32 s69, 0\n\t"
    "s_cbranch_scc1 loop_b_%=\n\t"
    "s_add_u32 s61, s75, s57\n\t"
    "s_mul_i32 s61, s61, 224\n\t"
    "s_add_u32 s61, s61, s58\n\t"
    "s_mul_i32 s70, s61, 384\n\t"
    "s_add_u32 s71, s70, 86016\n\t"
    "s_add_u32 s72, s70, 172032\n\t"
    "v_lshlrev_b32 v26, 4, v0\n\t"
    "v_add_u32 v27, 4096, v26\n\t"
    "s_mov_b32 s64, 0xffff0000\n\t"
    "s_add_u32 s61, s57, 2\n\t"
    "s_mul_i32 s61, s61, 224\n\t"
    "s_add_u32 s61, s61, s58\n\t"
    "s_mul_i32 s61, s61, 384\n\t"
    "s_lshl_b32 s62, s79, 10\n\t"
    "s_add_u32 s73, s61, s62\n\t"
    "s_add_u32 s74, s73, 86016\n\t"
    "s_waitcnt lgkmcnt(0)\n\t"
    "v_mfma_f32_16x16x32_bf16 a[0:3], a[192:195], v[128:131], a[0:3]\n\t"
    "ds_read_b64_tr_b16 a[216:217], v20 offset:25600\n\t"
    "ds_read_b64_tr_b16 a[218:219], v20 offset:31744\n\t"
    "v_cvt_pk_f16_f32 v38, v112, v113\n\t"
    "v_cvt_pk_f16_f32 v39, v114, v115\n\t"
    "v_pk_fma_f16 v40, v38, v38, s60 neg_lo:[1,0,1] neg_hi:[1,0,1]\n\t"
    "v_pk_fma_f16 v41, v39, v39, s60 neg_lo:[1,0,1] neg_hi:[1,0,1]\n\t"
    "v_pk_mul_f16 v40, v40, v38\n\t"
    "v_mfma_f32_16x16x32_bf16 a[12:15], a[196:199], v[128:131], a[12:15]\n\t"
    "ds_read_b64_tr_b16 a[220:221], v21 offset:25600\n\t"
    "ds_read_b64_tr_b16 a[222:223], v21 offset:31744\n\t"
    "v_pk_mul_f16 v41, v41, v39\n\t"
    "v_exp_f16 v42, v40\n\t"
    "v_exp_f16 v43, v41\n\t"
    "v_mfma_f32_16x16x32_bf16 a[24:27], a[200:203], v[128:131], a[24:27]\n\t"
    "ds_read_b64_tr_b16 a[224:225], v18 offset:26624\n\t"
    "ds_read_b64_tr_b16 a[226:227], v18 offset:32768\n\t"
    "v_exp_f16_sdwa v42, v40 dst_sel:WORD_1 dst_unused:UNUSED_PRESERVE src0_sel:WORD_1\n\t"
    "v_exp_f16_sdwa v43, v41 dst_sel:WORD_1 dst_unused:UNUSED_PRESERVE src0_sel:WORD_1\n\t"
    "v_pk_add_f16 v42, v42, 1.0 op_sel_hi:[1,0]\n\t"
    "s_waitcnt vmcnt(0)\n\t"
    "s_barrier\n\t"
    "v_mfma_f32_16x16x32_bf16 a[36:39], a[204:207], v[128:131], a[36:39]\n\t"
    "ds_read_b64_tr_b16 a[228:229], v19 offset:26624\n\t"
    "ds_read_b64_tr_b16 a[230:231], v19 offset:32768\n\t"
    "buffer_load_dwordx4 v[160:163], v26, s[36:39], s70 offen offset:0\n\t"
    "v_pk_add_f16 v43, v43, 1.0 op_sel_hi:[1,0]\n\t"
    "v_rcp_f16 v40, v42\n\t"
    "v_rcp_f16 v41, v43\n\t"
    "v_mfma_f32_16x16x32_bf16 a[48:51], a[208:211], v[128:131], a[48:51]\n\t"
    "ds_read_b64_tr_b16 a[232:233], v20 offset:26624\n\t"
    "ds_read_b64_tr_b16 a[234:235], v20 offset:32768\n\t"
    "v_rcp_f16_sdwa v40, v42 dst_sel:WORD_1 dst_unused:UNUSED_PRESERVE src0_sel:WORD_1\n\t"
    "v_rcp_f16_sdwa v41, v43 dst_sel:WORD_1 dst_unused:UNUSED_PRESERVE src0_sel:WORD_1\n\t"
    "v_fma_mix_f32 v112, v112, v40, 0 op_sel_hi:[0,1,0]\n\t"
    "v_mfma_f32_16x16x32_bf16 a[60:63], a[212:215], v[128:131], a[60:63]\n\t"
    "ds_read_b64_tr_b16 a[236:237], v21 offset:26624\n\t"
    "ds_read_b64_tr_b16 a[238:239], v21 offset:32768\n\t"
    "buffer_load_dwordx4 v[164:167], v26, s[36:39], s70 offen offset:1024\n\t"
    "v_fma_mix_f32 v114, v114, v41, 0 op_sel_hi:[0,1,0]\n\t"
    "v_fma_mix_f32 v113, v113, v40, 0 op_sel:[0,1,0] op_sel_hi:[0,1,0]\n\t"
    "v_fma_mix_f32 v115, v115, v41, 0 op_sel:[0,1,0] op_sel_hi:[0,1,0]\n\t"
    "v_cvt_pk_bf16_f32 v146, v112, v113\n\t"
    "v_cvt_pk_bf16_f32 v147, v114, v115\n\t"
    "s_waitcnt lgkmcnt(6)\n\t"
    "v_mfma_f32_16x16x32_bf16 a[72:75], a[216:219], v[128:131], a[72:75]\n\t"
    "ds_read_b64_tr_b16 a[144:145], v18 offset:36864\n\t"
    "ds_read_b64_tr_b16 a[146:147], v18 offset:43008\n\t"
    "s_mul_i32 s61, s79, 25088\n\t"
    "v_and_b32 v40, 15, v0\n\t"
    "v_mul_u32_u24 v40, 784, v40\n\t"
    "v_bfe_u32 v41, v0, 4, 1\n\t"
    "v_mul_u32_u24 v41, 384, v41\n\t"
    "v_mfma_f32_16x16x32_bf16 a[84:87], a[220:223], v[128:131], a[84:87]\n\t"
    "ds_read_b64_tr_b16 a[148:149], v19 offset:36864\n\t"
    "ds_read_b64_tr_b16 a[150:151], v19 offset:43008\n\t"
    "buffer_load_dwordx4 v[168:171], v26, s[36:39], s70 offen offset:2048\n\t"
    "v_add3_u32 v40, v40, v41, s61\n\t"
    "v_lshrrev_b32 v41, 5, v0\n\t"
    "v_lshl_add_u32 v29, v41, 4, v40\n\t"
    "v_add_u32 v40, 0, v0\n\t"
    "v_mul_u32_u24 v41, 2731, v40\n\t"
    "v_mfma_f32_16x16x32_bf16 a[96:99], a[224:227], v[128:131], a[96:99]\n\t"
    "ds_read_b64_tr_b16 a[152:153], v20 offset:36864\n\t"
    "ds_read_b64_tr_b16 a[154:155], v20 offset:43008\n\t"
    "v_lshrrev_b32 v41, 16, v41\n\t"
    "v_mul_u32_u24 v42, 24, v41\n\t"
    "v_sub_u32 v40, v40, v42\n\t"
    "v_mul_u32_u24 v41, 784, v41\n\t"
    "v_lshl_add_u32 v41, v40, 4, v41\n\t"
    "s_waitcnt lgkmcnt(6)\n\t"
    "v_mfma_f32_16x16x32_bf16 a[108:111], a[228:231], v[128:131], a[108:111]\n\t"
    "ds_read_b64_tr_b16 a[156:157], v21 offset:36864\n\t"
    "ds_read_b64_tr_b16 a[158:159], v21 offset:43008\n\t"
    "v_add_u32 v14, s61, v41\n\t"
    "v_lshlrev_b32 v40, 5, v40\n\t"
    "v_add_u32 v44, 157440, v40\n\t"
    "v_add_u32 v40, 64, v0\n\t"
    "v_mul_u32_u24 v41, 2731, v40\n\t"
    "v_mfma_f32_16x16x32_bf16 a[120:123], a[232:235], v[128:131], a[120:123]\n\t"
    "ds_read_b64_tr_b16 a[160:161], v18 offset:37888\n\t"
    "ds_read_b64_tr_b16 a[162:163], v18 offset:44032\n\t"
    "v_lshrrev_b32 v41, 16, v41\n\t"
    "v_mul_u32_u24 v42, 24, v41\n\t"
    "v_sub_u32 v40, v40, v42\n\t"
    "v_mul_u32_u24 v41, 784, v41\n\t"
    "v_lshl_add_u32 v41, v40, 4, v41\n\t"
    "v_mfma_f32_16x16x32_bf16 a[132:135], a[236:239], v[128:131], a[132:135]\n\t"
    "ds_read_b64_tr_b16 a[164:165], v19 offset:37888\n\t"
    "ds_read_b64_tr_b16 a[166:167], v19 offset:44032\n\t"
    "v_add_u32 v15, s61, v41\n\t"
    "v_lshlrev_b32 v40, 5, v40\n\t"
    "v_add_u32 v45, 157440, v40\n\t"
    "v_add_u32 v40, 128, v0\n\t"
    "v_mul_u32_u24 v41, 2731, v40\n\t"
    "s_waitcnt lgkmcnt(6)\n\t"
    "v_mfma_f32_16x16x32_bf16 a[0:3], a[144:147], v[144:147], a[0:3]\n\t"
    "ds_read_b64_tr_b16 a[168:169], v20 offset:37888\n\t"
    "ds_read_b64_tr_b16 a[170:171], v20 offset:44032\n\t"
    "v_lshrrev_b32 v41, 16, v41\n\t"
    "v_mul_u32_u24 v42, 24, v41\n\t"
    "v_sub_u32 v40, v40, v42\n\t"
    "v_mul_u32_u24 v41, 784, v41\n\t"
    "v_lshl_add_u32 v41, v40, 4, v41\n\t"
    "v_mfma_f32_16x16x32_bf16 a[12:15], a[148:151], v[144:147], a[12:15]\n\t"
    "ds_read_b64_tr_b16 a[172:173], v21 offset:37888\n\t"
    "ds_read_b64_tr_b16 a[174:175], v21 offset:44032\n\t"
    "v_add_u32 v16, s61, v41\n\t"
    "v_lshlrev_b32 v40, 5, v40\n\t"
    "v_add_u32 v46, 157440, v40\n\t"
    "s_lshl_b32 s62, s79, 6\n\t"
    "v_add_u32 v10, s62, v0\n\t"
    "v_mfma_f32_16x16x32_bf16 a[24:27], a[152:155], v[144:147], a[24:27]\n\t"
    "ds_read_b64_tr_b16 a[176:177], v18 offset:38912\n\t"
    "ds_read_b64_tr_b16 a[178:179], v18 offset:45056\n\t"
    "v_mul_u32_u24 v11, 2731, v10\n\t"
    "v_lshrrev_b32 v11, 16, v11\n\t"
    "v_mul_u32_u24 v42, 24, v11\n\t"
    "v_sub_u32 v42, v10, v42\n\t"
    "v_mul_u32_u24 v10, 784, v11\n\t"
    "s_waitcnt lgkmcnt(6)\n\t"
    "v_mfma_f32_16x16x32_bf16 a[36:39], a[156:159], v[144:147], a[36:39]\n\t"
    "ds_read_b64_tr_b16 a[180:181], v19 offset:38912\n\t"
    "ds_read_b64_tr_b16 a[182:183], v19 offset:45056\n\t"
    "v_lshl_add_u32 v10, v42, 4, v10\n\t"
    "v_add_u32 v10, 75264, v10\n\t"
    "v_lshlrev_b32 v11, 5, v42\n\t"
    "v_add_u32 v11, 157440, v11\n\t"
    "v_mfma_f32_16x16x32_bf16 a[48:51], a[160:163], v[144:147], a[48:51]\n\t"
    "ds_read_b64_tr_b16 a[184:185], v20 offset:38912\n\t"
    "ds_read_b64_tr_b16 a[186:187], v20 offset:45056\n\t"
    "ds_write_b128 v29, a[0:3] offset:0\n\t"
    "v_mfma_f32_16x16x32_bf16 a[60:63], a[164:167], v[144:147], a[60:63]\n\t"
    "ds_read_b64_tr_b16 a[188:189], v21 offset:38912\n\t"
    "ds_read_b64_tr_b16 a[190:191], v21 offset:45056\n\t"
    "ds_write_b128 v29, a[12:15] offset:32\n\t"
    "s_waitcnt lgkmcnt(8)\n\t"
    "v_mfma_f32_16x16x32_bf16 a[72:75], a[168:171], v[144:147], a[72:75]\n\t"
    "ds_write_b128 v29, a[24:27] offset:64\n\t"
    "v_mfma_f32_16x16x32_bf16 a[84:87], a[172:175], v[144:147], a[84:87]\n\t"
    "ds_write_b128 v29, a[36:39] offset:96\n\t"
    "v_mfma_f32_16x16x32_bf16 a[96:99], a[176:179], v[144:147], a[96:99]\n\t"
    "ds_write_b128 v29, a[48:51] offset:128\n\t"
    "s_waitcnt lgkmcnt(4)\n\t"
    "v_mfma_f32_16x16x32_bf16 a[108:111], a[180:183], v[144:147], a[108:111]\n\t"
    "ds_write_b128 v29, a[60:63] offset:160\n\t"
    "v_mfma_f32_16x16x32_bf16 a[120:123], a[184:187], v[144:147], a[120:123]\n\t"
    "ds_write_b128 v29, a[72:75] offset:192\n\t"
    "ds_read_b128 v[64:67], v44\n\t"
    "v_mfma_f32_16x16x32_bf16 a[132:135], a[188:191], v[144:147], a[132:135]\n\t"
    "ds_write_b128 v29, a[84:87] offset:224\n\t"
    "ds_read_b128 v[68:71], v44 offset:16\n\t"
    "v_mfma_f32_16x16x32_bf16 a[4:7], a[192:195], v[132:135], a[4:7]\n\t"
    "ds_write_b128 v29, a[96:99] offset:256\n\t"
    "ds_read_b128 v[72:75], v45\n\t"
    "v_mfma_f32_16x16x32_bf16 a[16:19], a[196:199], v[132:135], a[16:19]\n\t"
    "ds_write_b128 v29, a[108:111] offset:288\n\t"
    "ds_read_b128 v[76:79], v45 offset:16\n\t"
    "v_mfma_f32_16x16x32_bf16 a[28:31], a[200:203], v[132:135], a[28:31]\n\t"
    "ds_write_b128 v29, a[120:123] offset:320\n\t"
    "ds_read_b128 v[80:83], v46\n\t"
    "v_mfma_f32_16x16x32_bf16 a[40:43], a[204:207], v[132:135], a[40:43]\n\t"
    "s_waitcnt lgkmcnt(11)\n\t"
    "ds_write_b128 v29, a[132:135] offset:352\n\t"
    "ds_read_b128 v[84:87], v46 offset:16\n\t"
    "v_mfma_f32_16x16x32_bf16 a[52:55], a[208:211], v[132:135], a[52:55]\n\t"
    "v_mfma_f32_16x16x32_bf16 a[64:67], a[212:215], v[132:135], a[64:67]\n\t"
    "buffer_load_dwordx4 v[172:175], v26, s[36:39], s70 offen offset:3072\n\t"
    "ds_read_b128 v[88:91], v14 offset:0\n\t"
    "ds_read_b128 v[92:95], v14 offset:384\n\t"
    "s_waitcnt vmcnt(3)\n\t"
    "v_lshlrev_b32 v108, 16, v160\n\t"
    "v_and_b32 v109, s64, v160\n\t"
    "v_lshlrev_b32 v110, 16, v161\n\t"
    "v_and_b32 v111, s64, v161\n\t"
    "v_lshlrev_b32 v112, 16, v162\n\t"
    "v_and_b32 v113, s64, v162\n\t"
    "v_lshlrev_b32 v114, 16, v163\n\t"
    "v_and_b32 v115, s64, v163\n\t"
    "v_cvt_pk_f16_f32 v38, v100, v101\n\t"
    "v_cvt_pk_f16_f32 v39, v102, v103\n\t"
    "v_pk_fma_f16 v40, v38, v38, s60 neg_lo:[1,0,1] neg_hi:[1,0,1]\n\t"
    "v_mfma_f32_16x16x32_bf16 a[76:79], a[216:219], v[132:135], a[76:79]\n\t"
    "v_pk_fma_f16 v41, v39, v39, s60 neg_lo:[1,0,1] neg_hi:[1,0,1]\n\t"
    "v_pk_mul_f16 v40, v40, v38\n\t"
    "v_pk_mul_f16 v41, v41, v39\n\t"
    "v_exp_f16 v42, v40\n\t"
    "v_exp_f16 v43, v41\n\t"
    "v_exp_f16_sdwa v42, v40 dst_sel:WORD_1 dst_unused:UNUSED_PRESERVE src0_sel:WORD_1\n\t"
    "v_exp_f16_sdwa v43, v41 dst_sel:WORD_1 dst_unused:UNUSED_PRESERVE src0_sel:WORD_1\n\t"
    "v_pk_add_f16 v42, v42, 1.0 op_sel_hi:[1,0]\n\t"
    "v_mfma_f32_16x16x32_bf16 a[88:91], a[220:223], v[132:135], a[88:91]\n\t"
    "buffer_load_dwordx4 v[176:179], v27, s[36:39], s70 offen offset:0\n\t"
    "v_pk_add_f16 v43, v43, 1.0 op_sel_hi:[1,0]\n\t"
    "v_rcp_f16 v40, v42\n\t"
    "v_rcp_f16 v41, v43\n\t"
    "v_rcp_f16_sdwa v40, v42 dst_sel:WORD_1 dst_unused:UNUSED_PRESERVE src0_sel:WORD_1\n\t"
    "v_rcp_f16_sdwa v41, v43 dst_sel:WORD_1 dst_unused:UNUSED_PRESERVE src0_sel:WORD_1\n\t"
    "v_fma_mix_f32 v100, v100, v40, 0 op_sel_hi:[0,1,0]\n\t"
    "v_fma_mix_f32 v102, v102, v41, 0 op_sel_hi:[0,1,0]\n\t"
    "v_fma_mix_f32 v101, v101, v40, 0 op_sel:[0,1,0] op_sel_hi:[0,1,0]\n\t"
    "v_mfma_f32_16x16x32_bf16 a[100:103], a[224:227], v[132:135], a[100:103]\n\t"
    "v_fma_mix_f32 v103, v103, v41, 0 op_sel:[0,1,0] op_sel_hi:[0,1,0]\n\t"
    "v_cvt_pk_bf16_f32 v148, v100, v101\n\t"
    "v_cvt_pk_bf16_f32 v149, v102, v103\n\t"
    "v_cvt_pk_f16_f32 v32, v116, v117\n\t"
    "v_cvt_pk_f16_f32 v33, v118, v119\n\t"
    "v_pk_fma_f16 v34, v32, v32, s60 neg_lo:[1,0,1] neg_hi:[1,0,1]\n\t"
    "v_pk_fma_f16 v35, v33, v33, s60 neg_lo:[1,0,1] neg_hi:[1,0,1]\n\t"
    "v_pk_mul_f16 v34, v34, v32\n\t"
    "v_pk_mul_f16 v35, v35, v33\n\t"
    "v_exp_f16 v36, v34\n\t"
    "v_mfma_f32_16x16x32_bf16 a[112:115], a[228:231], v[132:135], a[112:115]\n\t"
    "buffer_load_dwordx4 v[180:183], v27, s[36:39], s70 offen offset:1024\n\t"
    "v_exp_f16 v37, v35\n\t"
    "v_exp_f16_sdwa v36, v34 dst_sel:WORD_1 dst_unused:UNUSED_PRESERVE src0_sel:WORD_1\n\t"
    "v_exp_f16_sdwa v37, v35 dst_sel:WORD_1 dst_unused:UNUSED_PRESERVE src0_sel:WORD_1\n\t"
    "v_pk_add_f16 v36, v36, 1.0 op_sel_hi:[1,0]\n\t"
    "v_pk_add_f16 v37, v37, 1.0 op_sel_hi:[1,0]\n\t"
    "v_rcp_f16 v34, v36\n\t"
    "v_rcp_f16 v35, v37\n\t"
    "v_mfma_f32_16x16x32_bf16 a[124:127], a[232:235], v[132:135], a[124:127]\n\t"
    "v_rcp_f16_sdwa v34, v36 dst_sel:WORD_1 dst_unused:UNUSED_PRESERVE src0_sel:WORD_1\n\t"
    "v_rcp_f16_sdwa v35, v37 dst_sel:WORD_1 dst_unused:UNUSED_PRESERVE src0_sel:WORD_1\n\t"
    "v_fma_mix_f32 v116, v116, v34, 0 op_sel_hi:[0,1,0]\n\t"
    "v_fma_mix_f32 v118, v118, v35, 0 op_sel_hi:[0,1,0]\n\t"
    "v_fma_mix_f32 v117, v117, v34, 0 op_sel:[0,1,0] op_sel_hi:[0,1,0]\n\t"
    "v_fma_mix_f32 v119, v119, v35, 0 op_sel:[0,1,0] op_sel_hi:[0,1,0]\n\t"
    "v_cvt_pk_bf16_f32 v150, v116, v117\n\t"
    "v_cvt_pk_bf16_f32 v151, v118, v119\n\t"
    "s_waitcnt lgkmcnt(0)\n\t"
    "v_fmac_f32 v108, v88, v64\n\t"
    "v_mfma_f32_16x16x32_bf16 a[136:139], a[236:239], v[132:135], a[136:139]\n\t"
    "buffer_load_dwordx4 v[184:187], v26, s[36:39], s71 offen offset:0\n\t"
    "v_fmac_f32 v109, v89, v65\n\t"
    "v_fmac_f32 v110, v90, v66\n\t"
    "v_fmac_f32 v111, v91, v67\n\t"
    "v_fmac_f32 v112, v92, v68\n\t"
    "v_fmac_f32 v113, v93, v69\n\t"
    "v_fmac_f32 v114, v94, v70\n\t"
    "v_fmac_f32 v115, v95, v71\n\t"
    "v_cvt_pk_bf16_f32 v116, v108, v109\n\t"
    "v_cvt_pk_bf16_f32 v117, v110, v111\n\t"
    "v_cvt_pk_bf16_f32 v118, v112, v113\n\t"
    "v_cvt_pk_bf16_f32 v119, v114, v115\n\t"
    "v_mfma_f32_16x16x32_bf16 a[4:7], a[144:147], v[148:151], a[4:7]\n\t"
    "buffer_store_dwordx4 v[116:119], v26, s[48:51], s70 offen offset:0 sc1 nt\n\t"
    "v_mfma_f32_16x16x32_bf16 a[16:19], a[148:151], v[148:151], a[16:19]\n\t"
    "buffer_load_dwordx4 v[188:191], v26, s[36:39], s71 offen offset:1024\n\t"
    "v_mfma_f32_16x16x32_bf16 a[28:31], a[152:155], v[148:151], a[28:31]\n\t"
    "v_mfma_f32_16x16x32_bf16 a[40:43], a[156:159], v[148:151], a[40:43]\n\t"
    "buffer_load_dwordx4 v[192:195], v26, s[36:39], s71 offen offset:2048\n\t"
    "ds_read_b128 v[96:99], v15 offset:0\n\t"
    "ds_read_b128 v[100:103], v15 offset:384\n\t"
    "s_waitcnt vmcnt(8)\n\t"
    "v_lshlrev_b32 v108, 16, v164\n\t"
    "v_and_b32 v109, s64, v164\n\t"
    "v_lshlrev_b32 v110, 16, v165\n\t"
    "v_and_b32 v111, s64, v165\n\t"
    "v_lshlrev_b32 v112, 16, v166\n\t"
    "v_mfma_f32_16x16x32_bf16 a[52:55], a[160:163], v[148:151], a[52:55]\n\t"
    "v_and_b32 v113, s64, v166\n\t"
    "v_lshlrev_b32 v114, 16, v167\n\t"
    "v_and_b32 v115, s64, v167\n\t"
    "v_mfma_f32_16x16x32_bf16 a[64:67], a[164:167], v[148:151], a[64:67]\n\t"
    "buffer_load_dwordx4 v[196:199], v26, s[36:39], s71 offen offset:3072\n\t"
    "v_mfma_f32_16x16x32_bf16 a[76:79], a[168:171], v[148:151], a[76:79]\n\t"
    "ds_write_b128 v29, a[4:7] offset:12544\n\t"
    "s_waitcnt lgkmcnt(1)\n\t"
    "v_fmac_f32 v108, v96, v72\n\t"
    "v_fmac_f32 v109, v97, v73\n\t"
    "v_fmac_f32 v110, v98, v74\n\t"
    "v_fmac_f32 v111, v99, v75\n\t"
    "v_fmac_f32 v112, v100, v76\n\t"
    "v_mfma_f32_16x16x32_bf16 a[88:91], a[172:175], v[148:151], a[88:91]\n\t"
    "buffer_load_dwordx4 v[200:203], v27, s[36:39], s71 offen offset:0\n\t"
    "v_fmac_f32 v113, v101, v77\n\t"
    "v_fmac_f32 v114, v102, v78\n\t"
    "v_fmac_f32 v115, v103, v79\n\t"
    "v_cvt_pk_bf16_f32 v124, v108, v109\n\t"
    "v_cvt_pk_bf16_f32 v125, v110, v111\n\t"
    "v_cvt_pk_bf16_f32 v126, v112, v113\n\t"
    "v_mfma_f32_16x16x32_bf16 a[100:103], a[176:179], v[148:151], a[100:103]\n\t"
    "ds_write_b128 v29, a[16:19] offset:12576\n\t"
    "v_cvt_pk_bf16_f32 v127, v114, v115\n\t"
    "v_mfma_f32_16x16x32_bf16 a[112:115], a[180:183], v[148:151], a[112:115]\n\t"
    "buffer_load_dwordx4 v[204:207], v27, s[36:39], s71 offen offset:1024\n\t"
    "v_mfma_f32_16x16x32_bf16 a[124:127], a[184:187], v[148:151], a[124:127]\n\t"
    "ds_write_b128 v29, a[28:31] offset:12608\n\t"
    "buffer_store_dwordx4 v[124:127], v26, s[48:51], s70 offen offset:1024 sc1 nt\n\t"
    "v_mfma_f32_16x16x32_bf16 a[136:139], a[188:191], v[148:151], a[136:139]\n\t"
    "buffer_load_dwordx4 v[208:211], v26, s[36:39], s72 offen offset:0\n\t"
    "v_mfma_f32_16x16x32_bf16 a[8:11], a[192:195], v[136:139], a[8:11]\n\t"
    "ds_write_b128 v29, a[40:43] offset:12640\n\t"
    "v_mfma_f32_16x16x32_bf16 a[20:23], a[196:199], v[136:139], a[20:23]\n\t"
    "buffer_load_dwordx4 v[212:215], v26, s[36:39], s72 offen offset:1024\n\t"
    "ds_read_b128 v[88:91], v16 offset:0\n\t"
    "ds_read_b128 v[92:95], v16 offset:384\n\t"
    "s_waitcnt vmcnt(13)\n\t"
    "v_lshlrev_b32 v108, 16, v168\n\t"
    "v_and_b32 v109, s64, v168\n\t"
    "v_lshlrev_b32 v110, 16, v169\n\t"
    "v_and_b32 v111, s64, v169\n\t"
    "v_lshlrev_b32 v112, 16, v170\n\t"
    "v_and_b32 v113, s64, v170\n\t"
    "v_lshlrev_b32 v114, 16, v171\n\t"
    "v_and_b32 v115, s64, v171\n\t"
    "v_mfma_f32_16x16x32_bf16 a[32:35], a[200:203], v[136:139], a[32:35]\n\t"
    "ds_write_b128 v29, a[52:55] offset:12672\n\t"
    "v_mfma_f32_16x16x32_bf16 a[44:47], a[204:207], v[136:139], a[44:47]\n\t"
    "buffer_load_dwordx4 v[216:219], v26, s[36:39], s72 offen offset:2048\n\t"
    "v_mfma_f32_16x16x32_bf16 a[56:59], a[208:211], v[136:139], a[56:59]\n\t"
    "ds_write_b128 v29, a[64:67] offset:12704\n\t"
    "s_waitcnt lgkmcnt(2)\n\t"
    "v_fmac_f32 v108, v88, v80\n\t"
    "v_fmac_f32 v109, v89, v81\n\t"
    "v_fmac_f32 v110, v90, v82\n\t"
    "v_fmac_f32 v111, v91, v83\n\t"
    "v_fmac_f32 v112, v92, v84\n\t"
    "v_fmac_f32 v113, v93, v85\n\t"
    "v_fmac_f32 v114, v94, v86\n\t"
    "v_fmac_f32 v115, v95, v87\n\t"
    "v_cvt_pk_bf16_f32 v116, v108, v109\n\t"
    "v_cvt_pk_bf16_f32 v117, v110, v111\n\t"
    "v_mfma_f32_16x16x32_bf16 a[68:71], a[212:215], v[136:139], a[68:71]\n\t"
    "buffer_load_dwordx4 v[220:223], v26, s[36:39], s72 offen offset:3072\n\t"
    "v_cvt_pk_bf16_f32 v118, v112, v113\n\t"
    "v_cvt_pk_bf16_f32 v119, v114, v115\n\t"
    "v_cvt_pk_f16_f32 v32, v104, v105\n\t"
    "v_cvt_pk_f16_f32 v33, v106, v107\n\t"
    "v_pk_fma_f16 v34, v32, v32, s60 neg_lo:[1,0,1] neg_hi:[1,0,1]\n\t"
    "v_pk_fma_f16 v35, v33, v33, s60 neg_lo:[1,0,1] neg_hi:[1,0,1]\n\t"
    "v_pk_mul_f16 v34, v34, v32\n\t"
    "v_pk_mul_f16 v35, v35, v33\n\t"
    "v_exp_f16 v36, v34\n\t"
    "v_mfma_f32_16x16x32_bf16 a[80:83], a[216:219], v[136:139], a[80:83]\n\t"
    "v_exp_f16 v37, v35\n\t"
    "v_exp_f16_sdwa v36, v34 dst_sel:WORD_1 dst_unused:UNUSED_PRESERVE src0_sel:WORD_1\n\t"
    "v_exp_f16_sdwa v37, v35 dst_sel:WORD_1 dst_unused:UNUSED_PRESERVE src0_sel:WORD_1\n\t"
    "v_pk_add_f16 v36, v36, 1.0 op_sel_hi:[1,0]\n\t"
    "v_pk_add_f16 v37, v37, 1.0 op_sel_hi:[1,0]\n\t"
    "v_rcp_f16 v34, v36\n\t"
    "v_mfma_f32_16x16x32_bf16 a[92:95], a[220:223], v[136:139], a[92:95]\n\t"
    "ds_write_b128 v29, a[76:79] offset:12736\n\t"
    "v_rcp_f16 v35, v37\n\t"
    "v_rcp_f16_sdwa v34, v36 dst_sel:WORD_1 dst_unused:UNUSED_PRESERVE src0_sel:WORD_1\n\t"
    "v_rcp_f16_sdwa v35, v37 dst_sel:WORD_1 dst_unused:UNUSED_PRESERVE src0_sel:WORD_1\n\t"
    "v_fma_mix_f32 v104, v104, v34, 0 op_sel_hi:[0,1,0]\n\t"
    "v_fma_mix_f32 v106, v106, v35, 0 op_sel_hi:[0,1,0]\n\t"
    "v_fma_mix_f32 v105, v105, v34, 0 op_sel:[0,1,0] op_sel_hi:[0,1,0]\n\t"
    "v_fma_mix_f32 v107, v107, v35, 0 op_sel:[0,1,0] op_sel_hi:[0,1,0]\n\t"
    "v_cvt_pk_bf16_f32 v152, v104, v105\n\t"
    "v_mfma_f32_16x16x32_bf16 a[104:107], a[224:227], v[136:139], a[104:107]\n\t"
    "buffer_store_dwordx4 v[116:119], v26, s[48:51], s70 offen offset:2048 sc1 nt\n\t"
    "v_cvt_pk_bf16_f32 v153, v106, v107\n\t"
    "v_cvt_pk_f16_f32 v38, v120, v121\n\t"
    "v_cvt_pk_f16_f32 v39, v122, v123\n\t"
    "v_pk_fma_f16 v40, v38, v38, s60 neg_lo:[1,0,1] neg_hi:[1,0,1]\n\t"
    "v_pk_fma_f16 v41, v39, v39, s60 neg_lo:[1,0,1] neg_hi:[1,0,1]\n\t"
    "v_pk_mul_f16 v40, v40, v38\n\t"
    "v_pk_mul_f16 v41, v41, v39\n\t"
    "v_exp_f16 v42, v40\n\t"
    "v_exp_f16 v43, v41\n\t"
    "v_mfma_f32_16x16x32_bf16 a[116:119], a[228:231], v[136:139], a[116:119]\n\t"
    "ds_write_b128 v29, a[88:91] offset:12768\n\t"
    "v_exp_f16_sdwa v42, v40 dst_sel:WORD_1 dst_unused:UNUSED_PRESERVE src0_sel:WORD_1\n\t"
    "v_exp_f16_sdwa v43, v41 dst_sel:WORD_1 dst_unused:UNUSED_PRESERVE src0_sel:WORD_1\n\t"
    "v_pk_add_f16 v42, v42, 1.0 op_sel_hi:[1,0]\n\t"
    "v_pk_add_f16 v43, v43, 1.0 op_sel_hi:[1,0]\n\t"
    "v_rcp_f16 v40, v42\n\t"
    "v_rcp_f16 v41, v43\n\t"
    "v_mfma_f32_16x16x32_bf16 a[128:131], a[232:235], v[136:139], a[128:131]\n\t"
    "v_rcp_f16_sdwa v40, v42 dst_sel:WORD_1 dst_unused:UNUSED_PRESERVE src0_sel:WORD_1\n\t"
    "v_rcp_f16_sdwa v41, v43 dst_sel:WORD_1 dst_unused:UNUSED_PRESERVE src0_sel:WORD_1\n\t"
    "v_fma_mix_f32 v120, v120, v40, 0 op_sel_hi:[0,1,0]\n\t"
    "v_fma_mix_f32 v122, v122, v41, 0 op_sel_hi:[0,1,0]\n\t"
    "v_fma_mix_f32 v121, v121, v40, 0 op_sel:[0,1,0] op_sel_hi:[0,1,0]\n\t"
    "v_fma_mix_f32 v123, v123, v41, 0 op_sel:[0,1,0] op_sel_hi:[0,1,0]\n\t"
    "v_cvt_pk_bf16_f32 v154, v120, v121\n\t"
    "v_cvt_pk_bf16_f32 v155, v122, v123\n\t"
    "v_mfma_f32_16x16x32_bf16 a[140:143], a[236:239], v[136:139], a[140:143]\n\t"
    "ds_write_b128 v29, a[100:103] offset:12800\n\t"
    "ds_read_b128 v[96:99], v14 offset:6272\n\t"
    "ds_read_b128 v[100:103], v14 offset:6656\n\t"
    "s_waitcnt vmcnt(15)\n\t"
    "v_lshlrev_b32 v108, 16, v172\n\t"
    "v_and_b32 v109, s64, v172\n\t"
    "v_lshlrev_b32 v110, 16, v173\n\t"
    "v_mfma_f32_16x16x32_bf16 a[8:11], a[144:147], v[152:155], a[8:11]\n\t"
    "v_and_b32 v111, s64, v173\n\t"
    "v_lshlrev_b32 v112, 16, v174\n\t"
    "v_and_b32 v113, s64, v174\n\t"
    "v_mfma_f32_16x16x32_bf16 a[20:23], a[148:151], v[152:155], a[20:23]\n\t"
    "ds_write_b128 v29, a[112:115] offset:12832\n\t"
    "v_lshlrev_b32 v114, 16, v175\n\t"
    "v_and_b32 v115, s64, v175\n\t"
    "v_mfma_f32_16x16x32_bf16 a[32:35], a[152:155], v[152:155], a[32:35]\n\t"
    "s_waitcnt lgkmcnt(1)\n\t"
    "v_fmac_f32 v108, v96, v64\n\t"
    "v_fmac_f32 v109, v97, v65\n\t"
    "v_fmac_f32 v110, v98, v66\n\t"
    "v_mfma_f32_16x16x32_bf16 a[44:47], a[156:159], v[152:155], a[44:47]\n\t"
    "ds_write_b128 v29, a[124:127] offset:12864\n\t"
    "v_fmac_f32 v111, v99, v67\n\t"
    "v_fmac_f32 v112, v100, v68\n\t"
    "v_fmac_f32 v113, v101, v69\n\t"
    "v_mfma_f32_16x16x32_bf16 a[56:59], a[160:163], v[152:155], a[56:59]\n\t"
    "v_fmac_f32 v114, v102, v70\n\t"
    "v_fmac_f32 v115, v103, v71\n\t"
    "v_cvt_pk_bf16_f32 v124, v108, v109\n\t"
    "v_mfma_f32_16x16x32_bf16 a[68:71], a[164:167], v[152:155], a[68:71]\n\t"
    "ds_write_b128 v29, a[136:139] offset:12896\n\t"
    "v_cvt_pk_bf16_f32 v125, v110, v111\n\t"
    "v_cvt_pk_bf16_f32 v126, v112, v113\n\t"
    "v_cvt_pk_bf16_f32 v127, v114, v115\n\t"
    "v_mfma_f32_16x16x32_bf16 a[80:83], a[168:171], v[152:155], a[80:83]\n\t"
    "buffer_store_dwordx4 v[124:127], v26, s[48:51], s70 offen offset:3072 sc1 nt\n\t"
    "v_mfma_f32_16x16x32_bf16 a[92:95], a[172:175], v[152:155], a[92:95]\n\t"
    "buffer_load_dwordx4 v[224:227], v27, s[36:39], s72 offen offset:0\n\t"
    "v_mfma_f32_16x16x32_bf16 a[104:107], a[176:179], v[152:155], a[104:107]\n\t"
    "v_mfma_f32_16x16x32_bf16 a[116:119], a[180:183], v[152:155], a[116:119]\n\t"
    "buffer_load_dwordx4 v[228:231], v27, s[36:39], s72 offen offset:1024\n\t"
    "v_mfma_f32_16x16x32_bf16 a[128:131], a[184:187], v[152:155], a[128:131]\n\t"
    "v_mfma_f32_16x16x32_bf16 a[140:143], a[188:191], v[152:155], a[140:143]\n\t"
    "buffer_load_dwordx4 v[232:235], v26, s[36:39], s74 offen offset:0\n\t"
    "ds_read_b128 v[48:51], v11\n\t"
    "ds_read_b128 v[52:55], v11 offset:16\n\t"
    "ds_read_b128 v[96:99], v15 offset:6272\n\t"
    "ds_read_b128 v[100:103], v15 offset:6656\n\t"
    "ds_read_b128 v[104:107], v16 offset:6272\n\t"
    "ds_read_b128 v[108:111], v16 offset:6656\n\t"
    "ds_read_b128 v[88:91], v14 offset:12544\n\t"
    "ds_read_b128 v[92:95], v14 offset:12928\n\t"
    "buffer_load_dwordx4 v[236:239], v26, s[36:39], s74 offen offset:3072\n\t"
    "ds_write_b128 v29, a[8:11] offset:0\n\t"
    "s_waitcnt vmcnt(19)\n\t"
    "v_lshlrev_b32 v128, 16, v176\n\t"
    "v_and_b32 v129, s64, v176\n\t"
    "v_lshlrev_b32 v130, 16, v177\n\t"
    "v_and_b32 v131, s64, v177\n\t"
    "v_lshlrev_b32 v132, 16, v178\n\t"
    "v_and_b32 v133, s64, v178\n\t"
    "v_lshlrev_b32 v134, 16, v179\n\t"
    "v_and_b32 v135, s64, v179\n\t"
    "ds_write_b128 v29, a[20:23] offset:32\n\t"
    "s_waitcnt lgkmcnt(6)\n\t"
    "v_pk_fma_f32 v[128:129], v[96:97], v[72:73], v[128:129]\n\t"
    "v_pk_fma_f32 v[130:131], v[98:99], v[74:75], v[130:131]\n\t"
    "v_pk_fma_f32 v[132:133], v[100:101], v[76:77], v[132:133]\n\t"
    "v_pk_fma_f32 v[134:135], v[102:103], v[78:79], v[134:135]\n\t"
    "v_cvt_pk_bf16_f32 v112, v128, v129\n\t"
    "v_cvt_pk_bf16_f32 v113, v130, v131\n\t"
    "v_cvt_pk_bf16_f32 v114, v132, v133\n\t"
    "v_cvt_pk_bf16_f32 v115, v134, v135\n\t"
    "buffer_store_dwordx4 v[112:115], v27, s[48:51], s70 offen offset:0 sc1 nt\n\t"
    "ds_read_b128 v[96:99], v15 offset:12544\n\t"
    "ds_read_b128 v[100:103], v15 offset:12928\n\t"
    "ds_write_b128 v29, a[32:35] offset:64\n\t"
    "s_waitcnt vmcnt(19)\n\t"
    "v_lshlrev_b32 v136, 16, v180\n\t"
    "v_and_b32 v137, s64, v180\n\t"
    "v_lshlrev_b32 v138, 16, v181\n\t"
    "v_and_b32 v139, s64, v181\n\t"
    "v_lshlrev_b32 v140, 16, v182\n\t"
    "v_and_b32 v141, s64, v182\n\t"
    "v_lshlrev_b32 v142, 16, v183\n\t"
    "v_and_b32 v143, s64, v183\n\t"
    "ds_write_b128 v29, a[44:47] offset:96\n\t"
    "s_waitcnt lgkmcnt(8)\n\t"
    "v_pk_fma_f32 v[136:137], v[104:105], v[80:81], v[136:137]\n\t"
    "v_pk_fma_f32 v[138:139], v[106:107], v[82:83], v[138:139]\n\t"
    "v_pk_fma_f32 v[140:141], v[108:109], v[84:85], v[140:141]\n\t"
    "v_pk_fma_f32 v[142:143], v[110:111], v[86:87], v[142:143]\n\t"
    "v_cvt_pk_bf16_f32 v116, v136, v137\n\t"
    "v_cvt_pk_bf16_f32 v117, v138, v139\n\t"
    "v_cvt_pk_bf16_f32 v118, v140, v141\n\t"
    "v_cvt_pk_bf16_f32 v119, v142, v143\n\t"
    "buffer_store_dwordx4 v[116:119], v27, s[48:51], s70 offen offset:1024 sc1 nt\n\t"
    "ds_read_b128 v[104:107], v16 offset:12544\n\t"
    "ds_read_b128 v[108:111], v16 offset:12928\n\t"
    "ds_write_b128 v29, a[56:59] offset:128\n\t"
    "s_waitcnt vmcnt(16)\n\t"
    "v_lshlrev_b32 v128, 16, v184\n\t"
    "v_and_b32 v129, s64, v184\n\t"
    "v_lshlrev_b32 v130, 16, v185\n\t"
    "v_and_b32 v131, s64, v185\n\t"
    "v_lshlrev_b32 v132, 16, v186\n\t"
    "v_and_b32 v133, s64, v186\n\t"
    "v_lshlrev_b32 v134, 16, v187\n\t"
    "v_and_b32 v135, s64, v187\n\t"
    "ds_write_b128 v29, a[68:71] offset:160\n\t"
    "s_waitcnt lgkmcnt(10)\n\t"
    "v_pk_fma_f32 v[128:129], v[88:89], v[64:65], v[128:129]\n\t"
    "v_pk_fma_f32 v[130:131], v[90:91], v[66:67], v[130:131]\n\t"
    "v_pk_fma_f32 v[132:133], v[92:93], v[68:69], v[132:133]\n\t"
    "v_pk_fma_f32 v[134:135], v[94:95], v[70:71], v[134:135]\n\t"
    "v_cvt_pk_bf16_f32 v112, v128, v129\n\t"
    "v_cvt_pk_bf16_f32 v113, v130, v131\n\t"
    "v_cvt_pk_bf16_f32 v114, v132, v133\n\t"
    "v_cvt_pk_bf16_f32 v115, v134, v135\n\t"
    "buffer_store_dwordx4 v[112:115], v26, s[48:51], s71 offen offset:0 sc1 nt\n\t"
    "ds_read_b128 v[88:91], v14 offset:18816\n\t"
    "ds_read_b128 v[92:95], v14 offset:19200\n\t"
    "ds_write_b128 v29, a[80:83] offset:192\n\t"
    "v_lshlrev_b32 v136, 16, v188\n\t"
    "v_and_b32 v137, s64, v188\n\t"
    "v_lshlrev_b32 v138, 16, v189\n\t"
    "v_and_b32 v139, s64, v189\n\t"
    "v_lshlrev_b32 v140, 16, v190\n\t"
    "v_and_b32 v141, s64, v190\n\t"
    "v_lshlrev_b32 v142, 16, v191\n\t"
    "v_and_b32 v143, s64, v191\n\t"
    "ds_write_b128 v29, a[92:95] offset:224\n\t"
    "s_waitcnt lgkmcnt(10)\n\t"
    "v_pk_fma_f32 v[136:137], v[96:97], v[72:73], v[136:137]\n\t"
    "v_pk_fma_f32 v[138:139], v[98:99], v[74:75], v[138:139]\n\t"
    "v_pk_fma_f32 v[140:141], v[100:101], v[76:77], v[140:141]\n\t"
    "v_pk_fma_f32 v[142:143], v[102:103], v[78:79], v[142:143]\n\t"
    "v_cvt_pk_bf16_f32 v116, v136, v137\n\t"
    "v_cvt_pk_bf16_f32 v117, v138, v139\n\t"
    "v_cvt_pk_bf16_f32 v118, v140, v141\n\t"
    "v_cvt_pk_bf16_f32 v119, v142, v143\n\t"
    "buffer_store_dwordx4 v[116:119], v26, s[48:51], s71 offen offset:1024 sc1 nt\n\t"
    "ds_read_b128 v[96:99], v15 offset:18816\n\t"
    "ds_read_b128 v[100:103], v15 offset:19200\n\t"
    "ds_write_b128 v29, a[104:107] offset:256\n\t"
    "v_lshlrev_b32 v128, 16, v192\n\t"
    "v_and_b32 v129, s64, v192\n\t"
    "v_lshlrev_b32 v130, 16, v193\n\t"
    "v_and_b32 v131, s64, v193\n\t"
    "v_lshlrev_b32 v132, 16, v194\n\t"
    "v_and_b32 v133, s64, v194\n\t"
    "v_lshlrev_b32 v134, 16, v195\n\t"
    "v_and_b32 v135, s64, v195\n\t"
    "ds_write_b128 v29, a[116:119] offset:288\n\t"
    "s_waitcnt lgkmcnt(10)\n\t"
    "v_pk_fma_f32 v[128:129], v[104:105], v[80:81], v[128:129]\n\t"
    "v_pk_fma_f32 v[130:131], v[106:107], v[82:83], v[130:131]\n\t"
    "v_pk_fma_f32 v[132:133], v[108:109], v[84:85], v[132:133]\n\t"
    "v_pk_fma_f32 v[134:135], v[110:111], v[86:87], v[134:135]\n\t"
    "v_cvt_pk_bf16_f32 v112, v128, v129\n\t"
    "v_cvt_pk_bf16_f32 v113, v130, v131\n\t"
    "v_cvt_pk_bf16_f32 v114, v132, v133\n\t"
    "v_cvt_pk_bf16_f32 v115, v134, v135\n\t"
    "buffer_store_dwordx4 v[112:115], v26, s[48:51], s71 offen offset:2048 sc1 nt\n\t"
    "ds_read_b128 v[104:107], v16 offset:18816\n\t"
    "ds_read_b128 v[108:111], v16 offset:19200\n\t"
    "ds_write_b128 v29, a[128:131] offset:320\n\t"
    "s_waitcnt vmcnt(16)\n\t"
    "v_lshlrev_b32 v136, 16, v196\n\t"
    "v_and_b32 v137, s64, v196\n\t"
    "v_lshlrev_b32 v138, 16, v197\n\t"
    "v_and_b32 v139, s64, v197\n\t"
    "v_lshlrev_b32 v140, 16, v198\n\t"
    "v_and_b32 v141, s64, v198\n\t"
    "v_lshlrev_b32 v142, 16, v199\n\t"
    "v_and_b32 v143, s64, v199\n\t"
    "ds_write_b128 v29, a[140:143] offset:352\n\t"
    "s_waitcnt lgkmcnt(10)\n\t"
    "v_pk_fma_f32 v[136:137], v[88:89], v[64:65], v[136:137]\n\t"
    "v_pk_fma_f32 v[138:139], v[90:91], v[66:67], v[138:139]\n\t"
    "v_pk_fma_f32 v[140:141], v[92:93], v[68:69], v[140:141]\n\t"
    "v_pk_fma_f32 v[142:143], v[94:95], v[70:71], v[142:143]\n\t"
    "v_cvt_pk_bf16_f32 v116, v136, v137\n\t"
    "v_cvt_pk_bf16_f32 v117, v138, v139\n\t"
    "v_cvt_pk_bf16_f32 v118, v140, v141\n\t"
    "v_cvt_pk_bf16_f32 v119, v142, v143\n\t"
    "buffer_store_dwordx4 v[116:119], v26, s[48:51], s71 offen offset:3072 sc1 nt\n\t"
    "ds_read_b128 v[88:91], v14 offset:0\n\t"
    "ds_read_b128 v[92:95], v14 offset:384\n\t"
    "v_lshlrev_b32 v128, 16, v200\n\t"
    "v_and_b32 v129, s64, v200\n\t"
    "v_lshlrev_b32 v130, 16, v201\n\t"
    "v_and_b32 v131, s64, v201\n\t"
    "v_lshlrev_b32 v132, 16, v202\n\t"
    "v_and_b32 v133, s64, v202\n\t"
    "v_lshlrev_b32 v134, 16, v203\n\t"
    "v_and_b32 v135, s64, v203\n\t"
    "s_waitcnt lgkmcnt(8)\n\t"
    "v_pk_fma_f32 v[128:129], v[96:97], v[72:73], v[128:129]\n\t"
    "v_pk_fma_f32 v[130:131], v[98:99], v[74:75], v[130:131]\n\t"
    "v_pk_fma_f32 v[132:133], v[100:101], v[76:77], v[132:133]\n\t"
    "v_pk_fma_f32 v[134:135], v[102:103], v[78:79], v[134:135]\n\t"
    "v_cvt_pk_bf16_f32 v112, v128, v129\n\t"
    "v_cvt_pk_bf16_f32 v113, v130, v131\n\t"
    "v_cvt_pk_bf16_f32 v114, v132, v133\n\t"
    "v_cvt_pk_bf16_f32 v115, v134, v135\n\t"
    "buffer_store_dwordx4 v[112:115], v27, s[48:51], s71 offen offset:0 sc1 nt\n\t"
    "ds_read_b128 v[96:99], v15 offset:0\n\t"
    "ds_read_b128 v[100:103], v15 offset:384\n\t"
    "v_lshlrev_b32 v136, 16, v204\n\t"
    "v_and_b32 v137, s64, v204\n\t"
    "v_lshlrev_b32 v138, 16, v205\n\t"
    "v_and_b32 v139, s64, v205\n\t"
    "v_lshlrev_b32 v140, 16, v206\n\t"
    "v_and_b32 v141, s64, v206\n\t"
    "v_lshlrev_b32 v142, 16, v207\n\t"
    "v_and_b32 v143, s64, v207\n\t"
    "s_waitcnt lgkmcnt(6)\n\t"
    "v_pk_fma_f32 v[136:137], v[104:105], v[80:81], v[136:137]\n\t"
    "v_pk_fma_f32 v[138:139], v[106:107], v[82:83], v[138:139]\n\t"
    "v_pk_fma_f32 v[140:141], v[108:109], v[84:85], v[140:141]\n\t"
    "v_pk_fma_f32 v[142:143], v[110:111], v[86:87], v[142:143]\n\t"
    "v_cvt_pk_bf16_f32 v116, v136, v137\n\t"
    "v_cvt_pk_bf16_f32 v117, v138, v139\n\t"
    "v_cvt_pk_bf16_f32 v118, v140, v141\n\t"
    "v_cvt_pk_bf16_f32 v119, v142, v143\n\t"
    "buffer_store_dwordx4 v[116:119], v27, s[48:51], s71 offen offset:1024 sc1 nt\n\t"
    "s_barrier\n\t"
    "ds_read_b128 v[144:147], v10 offset:37632\n\t"
    "ds_read_b128 v[148:151], v10 offset:38016\n\t"
    "ds_read_b128 v[152:155], v10 offset:43904\n\t"
    "ds_read_b128 v[156:159], v10 offset:44288\n\t"
    "ds_read_b128 v[104:107], v16 offset:0\n\t"
    "ds_read_b128 v[108:111], v16 offset:384\n\t"
    "s_waitcnt vmcnt(15)\n\t"
    "v_lshlrev_b32 v128, 16, v208\n\t"
    "v_and_b32 v129, s64, v208\n\t"
    "v_lshlrev_b32 v130, 16, v209\n\t"
    "v_and_b32 v131, s64, v209\n\t"
    "v_lshlrev_b32 v132, 16, v210\n\t"
    "v_and_b32 v133, s64, v210\n\t"
    "v_lshlrev_b32 v134, 16, v211\n\t"
    "v_and_b32 v135, s64, v211\n\t"
    "s_waitcnt lgkmcnt(8)\n\t"
    "v_pk_fma_f32 v[128:129], v[88:89], v[64:65], v[128:129]\n\t"
    "v_pk_fma_f32 v[130:131], v[90:91], v[66:67], v[130:131]\n\t"
    "v_pk_fma_f32 v[132:133], v[92:93], v[68:69], v[132:133]\n\t"
    "v_pk_fma_f32 v[134:135], v[94:95], v[70:71], v[134:135]\n\t"
    "v_cvt_pk_bf16_f32 v112, v128, v129\n\t"
    "v_cvt_pk_bf16_f32 v113, v130, v131\n\t"
    "v_cvt_pk_bf16_f32 v114, v132, v133\n\t"
    "v_cvt_pk_bf16_f32 v115, v134, v135\n\t"
    "buffer_store_dwordx4 v[112:115], v26, s[48:51], s72 offen offset:0 sc1 nt\n\t"
    "ds_read_b128 v[88:91], v14 offset:6272\n\t"
    "ds_read_b128 v[92:95], v14 offset:6656\n\t"
    "v_lshlrev_b32 v136, 16, v212\n\t"
    "v_and_b32 v137, s64, v212\n\t"
    "v_lshlrev_b32 v138, 16, v213\n\t"
    "v_and_b32 v139, s64, v213\n\t"
    "v_lshlrev_b32 v140, 16, v214\n\t"
    "v_and_b32 v141, s64, v214\n\t"
    "v_lshlrev_b32 v142, 16, v215\n\t"
    "v_and_b32 v143, s64, v215\n\t"
    "s_waitcnt lgkmcnt(8)\n\t"
    "v_pk_fma_f32 v[136:137], v[96:97], v[72:73], v[136:137]\n\t"
    "v_pk_fma_f32 v[138:139], v[98:99], v[74:75], v[138:139]\n\t"
    "v_pk_fma_f32 v[140:141], v[100:101], v[76:77], v[140:141]\n\t"
    "v_pk_fma_f32 v[142:143], v[102:103], v[78:79], v[142:143]\n\t"
    "v_cvt_pk_bf16_f32 v116, v136, v137\n\t"
    "v_cvt_pk_bf16_f32 v117, v138, v139\n\t"
    "v_cvt_pk_bf16_f32 v118, v140, v141\n\t"
    "v_cvt_pk_bf16_f32 v119, v142, v143\n\t"
    "buffer_store_dwordx4 v[116:119], v26, s[48:51], s72 offen offset:1024 sc1 nt\n\t"
    "ds_read_b128 v[96:99], v15 offset:6272\n\t"
    "ds_read_b128 v[100:103], v15 offset:6656\n\t"
    "v_lshlrev_b32 v128, 16, v216\n\t"
    "v_and_b32 v129, s64, v216\n\t"
    "v_lshlrev_b32 v130, 16, v217\n\t"
    "v_and_b32 v131, s64, v217\n\t"
    "v_lshlrev_b32 v132, 16, v218\n\t"
    "v_and_b32 v133, s64, v218\n\t"
    "v_lshlrev_b32 v134, 16, v219\n\t"
    "v_and_b32 v135, s64, v219\n\t"
    "s_waitcnt lgkmcnt(4)\n\t"
    "v_pk_fma_f32 v[128:129], v[104:105], v[80:81], v[128:129]\n\t"
    "v_pk_fma_f32 v[130:131], v[106:107], v[82:83], v[130:131]\n\t"
    "v_pk_fma_f32 v[132:133], v[108:109], v[84:85], v[132:133]\n\t"
    "v_pk_fma_f32 v[134:135], v[110:111], v[86:87], v[134:135]\n\t"
    "v_cvt_pk_bf16_f32 v112, v128, v129\n\t"
    "v_cvt_pk_bf16_f32 v113, v130, v131\n\t"
    "v_cvt_pk_bf16_f32 v114, v132, v133\n\t"
    "v_cvt_pk_bf16_f32 v115, v134, v135\n\t"
    "buffer_store_dwordx4 v[112:115], v26, s[48:51], s72 offen offset:2048 sc1 nt\n\t"
    "ds_read_b128 v[104:107], v16 offset:6272\n\t"
    "ds_read_b128 v[108:111], v16 offset:6656\n\t"
    "s_waitcnt vmcnt(13)\n\t"
    "v_lshlrev_b32 v136, 16, v220\n\t"
    "v_and_b32 v137, s64, v220\n\t"
    "v_lshlrev_b32 v138, 16, v221\n\t"
    "v_and_b32 v139, s64, v221\n\t"
    "v_lshlrev_b32 v140, 16, v222\n\t"
    "v_and_b32 v141, s64, v222\n\t"
    "v_lshlrev_b32 v142, 16, v223\n\t"
    "v_and_b32 v143, s64, v223\n\t"
    "s_waitcnt lgkmcnt(4)\n\t"
    "v_pk_fma_f32 v[136:137], v[88:89], v[64:65], v[136:137]\n\t"
    "v_pk_fma_f32 v[138:139], v[90:91], v[66:67], v[138:139]\n\t"
    "v_pk_fma_f32 v[140:141], v[92:93], v[68:69], v[140:141]\n\t"
    "v_pk_fma_f32 v[142:143], v[94:95], v[70:71], v[142:143]\n\t"
    "v_cvt_pk_bf16_f32 v116, v136, v137\n\t"
    "v_cvt_pk_bf16_f32 v117, v138, v139\n\t"
    "v_cvt_pk_bf16_f32 v118, v140, v141\n\t"
    "v_cvt_pk_bf16_f32 v119, v142, v143\n\t"
    "buffer_store_dwordx4 v[116:119], v26, s[48:51], s72 offen offset:3072 sc1 nt\n\t"
    "v_lshlrev_b32 v128, 16, v224\n\t"
    "v_and_b32 v129, s64, v224\n\t"
    "v_lshlrev_b32 v130, 16, v225\n\t"
    "v_and_b32 v131, s64, v225\n\t"
    "v_lshlrev_b32 v132, 16, v226\n\t"
    "v_and_b32 v133, s64, v226\n\t"
    "v_lshlrev_b32 v134, 16, v227\n\t"
    "v_and_b32 v135, s64, v227\n\t"
    "s_waitcnt lgkmcnt(2)\n\t"
    "v_pk_fma_f32 v[128:129], v[96:97], v[72:73], v[128:129]\n\t"
    "v_pk_fma_f32 v[130:131], v[98:99], v[74:75], v[130:131]\n\t"
    "v_pk_fma_f32 v[132:133], v[100:101], v[76:77], v[132:133]\n\t"
    "v_pk_fma_f32 v[134:135], v[102:103], v[78:79], v[134:135]\n\t"
    "v_cvt_pk_bf16_f32 v112, v128, v129\n\t"
    "v_cvt_pk_bf16_f32 v113, v130, v131\n\t"
    "v_cvt_pk_bf16_f32 v114, v132, v133\n\t"
    "v_cvt_pk_bf16_f32 v115, v134, v135\n\t"
    "buffer_store_dwordx4 v[112:115], v27, s[48:51], s72 offen offset:0 sc1 nt\n\t"
    "v_lshlrev_b32 v136, 16, v228\n\t"
    "v_and_b32 v137, s64, v228\n\t"
    "v_lshlrev_b32 v138, 16, v229\n\t"
    "v_and_b32 v139, s64, v229\n\t"
    "v_lshlrev_b32 v140, 16, v230\n\t"
    "v_and_b32 v141, s64, v230\n\t"
    "v_lshlrev_b32 v142, 16, v231\n\t"
    "v_and_b32 v143, s64, v231\n\t"
    "s_waitcnt lgkmcnt(0)\n\t"
    "v_pk_fma_f32 v[136:137], v[104:105], v[80:81], v[136:137]\n\t"
    "v_pk_fma_f32 v[138:139], v[106:107], v[82:83], v[138:139]\n\t"
    "v_pk_fma_f32 v[140:141], v[108:109], v[84:85], v[140:141]\n\t"
    "v_pk_fma_f32 v[142:143], v[110:111], v[86:87], v[142:143]\n\t"
    "v_cvt_pk_bf16_f32 v116, v136, v137\n\t"
    "v_cvt_pk_bf16_f32 v117, v138, v139\n\t"
    "v_cvt_pk_bf16_f32 v118, v140, v141\n\t"
    "v_cvt_pk_bf16_f32 v119, v142, v143\n\t"
    "buffer_store_dwordx4 v[116:119], v27, s[48:51], s72 offen offset:1024 sc1 nt\n\t"
    "s_waitcnt vmcnt(15)\n\t"
    "v_lshlrev_b32 v2, 16, v232\n\t"
    "v_and_b32 v3, s64, v232\n\t"
    "v_lshlrev_b32 v4, 16, v233\n\t"
    "v_and_b32 v5, s64, v233\n\t"
    "v_lshlrev_b32 v6, 16, v234\n\t"
    "v_and_b32 v7, s64, v234\n\t"
    "v_lshlrev_b32 v8, 16, v235\n\t"
    "v_and_b32 v9, s64, v235\n\t"
    "v_pk_fma_f32 v[2:3], v[144:145], v[48:49], v[2:3]\n\t"
    "v_pk_fma_f32 v[4:5], v[146:147], v[50:51], v[4:5]\n\t"
    "v_pk_fma_f32 v[6:7], v[148:149], v[52:53], v[6:7]\n\t"
    "v_pk_fma_f32 v[8:9], v[150:151], v[54:55], v[8:9]\n\t"
    "v_cvt_pk_bf16_f32 v244, v2, v3\n\t"
    "v_cvt_pk_bf16_f32 v245, v4, v5\n\t"
    "v_cvt_pk_bf16_f32 v246, v6, v7\n\t"
    "v_cvt_pk_bf16_f32 v247, v8, v9\n\t"
    "buffer_store_dwordx4 v[244:247], v26, s[48:51], s74 offen offset:0 sc1 nt\n\t"
    "s_waitcnt vmcnt(15)\n\t"
    "v_lshlrev_b32 v2, 16, v236\n\t"
    "v_and_b32 v3, s64, v236\n\t"
    "v_lshlrev_b32 v4, 16, v237\n\t"
    "v_and_b32 v5, s64, v237\n\t"
    "v_lshlrev_b32 v6, 16, v238\n\t"
    "v_and_b32 v7, s64, v238\n\t"
    "v_lshlrev_b32 v8, 16, v239\n\t"
    "v_and_b32 v9, s64, v239\n\t"
    "v_pk_fma_f32 v[2:3], v[152:153], v[48:49], v[2:3]\n\t"
    "v_pk_fma_f32 v[4:5], v[154:155], v[50:51], v[4:5]\n\t"
    "v_pk_fma_f32 v[6:7], v[156:157], v[52:53], v[6:7]\n\t"
    "v_pk_fma_f32 v[8:9], v[158:159], v[54:55], v[8:9]\n\t"
    "v_cvt_pk_bf16_f32 v248, v2, v3\n\t"
    "v_cvt_pk_bf16_f32 v249, v4, v5\n\t"
    "v_cvt_pk_bf16_f32 v250, v6, v7\n\t"
    "v_cvt_pk_bf16_f32 v251, v8, v9\n\t"
    "buffer_store_dwordx4 v[248:251], v26, s[48:51], s74 offen offset:3072 sc1 nt\n\t"
    "end_b_%=:\n\t"
    "s_endpgm\n\t"
    "prog_c_%=:\n\t"
    "v_and_b32 v61, 15, v0\n\t"
    "v_lshrrev_b32 v62, 4, v0\n\t"
    "v_and_b32 v53, 3, v0\n\t"
    "v_mul_u32_u24 v9, 0x5555, v53\n\t"
    "v_and_b32 v53, 1, v0\n\t"
    "v_lshlrev_b32 v53, 4, v53\n\t"
    "v_mov_b32 v54, 0xffff\n\t"
    "v_lshlrev_b32 v53, v53, v54\n\t"
    "v_lshrrev_b32 v54, 2, v61\n\t"
    "v_cmp_eq_u32 vcc, 0, v54\n\t"
    "s_nop 1\n\t"
    "v_cndmask_b32 v1, 0, v53, vcc\n\t"
    "v_cmp_eq_u32 vcc, 1, v54\n\t"
    "s_nop 1\n\t"
    "v_cndmask_b32 v2, 0, v53, vcc\n\t"
    "v_cmp_eq_u32 vcc, 2, v54\n\t"
    "s_nop 1\n\t"
    "v_cndmask_b32 v3, 0, v53, vcc\n\t"
    "v_cmp_eq_u32 vcc, 3, v54\n\t"
    "s_nop 1\n\t"
    "v_cndmask_b32 v4, 0, v53, vcc\n\t"
    "v_lshrrev_b32 v54, 3, v61\n\t"
    "v_and_b32 v55, 1, v62\n\t"
    "v_cmp_eq_u32 vcc, v54, v55\n\t"
    "v_bfe_u32 v54, v61, 1, 2\n\t"
    "s_nop 0\n\t"
    "v_cndmask_b32 v53, 0, v53, vcc\n\t"
    "v_cmp_eq_u32 vcc, 0, v54\n\t"
    "s_nop 1\n\t"
    "v_cndmask_b32 v5, 0, v53, vcc\n\t"
    "v_cmp_eq_u32 vcc, 1, v54\n\t"
    "s_nop 1\n\t"
    "v_cndmask_b32 v6, 0, v53, vcc\n\t"
    "v_cmp_eq_u32 vcc, 2, v54\n\t"
    "s_nop 1\n\t"
    "v_cndmask_b32 v7, 0, v53, vcc\n\t"
    "v_cmp_eq_u32 vcc, 3, v54\n\t"
    "s_nop 1\n\t"
    "v_cndmask_b32 v8, 0, v53, vcc\n\t"
    "v_lshrrev_b32 v53, 1, v62\n\t"
    "v_and_b32 v54, 14, v61\n\t"
    "v_lshlrev_b32 v54, 1, v54\n\t"
    "v_add_u32 v54, 158208, v54\n\t"
    "v_mul_u32_u24 v55, 384, v53\n\t"
    "v_add_u32 v10, v54, v55\n\t"
    "v_add_u32 v10, 2304, v10\n\t"
    "v_mul_u32_u24 v55, 3, v53\n\t"
    "v_and_b32 v53, 1, v62\n\t"
    "v_add_u32 v55, v55, v53\n\t"
    "v_mul_u32_u24 v55, 384, v55\n\t"
    "v_add_u32 v11, v54, v55\n\t"
    "v_mul_u32_u24 v55, 1152, v62\n\t"
    "v_add_u32 v53, 768, v55\n\t"
    "v_min_u32 v53, 3456, v53\n\t"
    "v_add_u32 v12, v54, v53\n\t"
    "v_add_u32 v53, -384, v55\n\t"
    "v_min_u32 v53, 3456, v53\n\t"
    "v_add_u32 v13, v54, v53\n\t"
    "s_waitcnt vmcnt(6)\n\t"
    "s_barrier\n\t"
    "ds_read2_b32 v[112:113], v10 offset0:48 offset1:56\n\t"
    "ds_read2_b32 v[114:115], v11 offset0:48 offset1:56\n\t"
    "ds_read2_b32 v[116:117], v12 offset0:48 offset1:56\n\t"
    "ds_read2_b32 v[118:119], v13 offset0:48 offset1:56\n\t"
    "ds_read2_b32 v[120:121], v10 offset0:64 offset1:72\n\t"
    "ds_read2_b32 v[122:123], v11 offset0:64 offset1:72\n\t"
    "ds_read2_b32 v[124:125], v12 offset0:64 offset1:72\n\t"
    "ds_read2_b32 v[126:127], v13 offset0:64 offset1:72\n\t"
    "ds_read2_b32 v[128:129], v10 offset0:80 offset1:88\n\t"
    "ds_read2_b32 v[130:131], v11 offset0:80 offset1:88\n\t"
    "ds_read2_b32 v[132:133], v12 offset0:80 offset1:88\n\t"
    "ds_read2_b32 v[134:135], v13 offset0:80 offset1:88\n\t"
    "s_waitcnt lgkmcnt(8)\n\t"
    "v_and_b32 v32, v5, v112\n\t"
    "v_and_b32 v33, v6, v112\n\t"
    "v_and_b32 v34, v7, v112\n\t"
    "v_and_b32 v35, v8, v112\n\t"
    "v_accvgpr_write_b32 a112, v32\n\t"
    "v_accvgpr_write_b32 a113, v33\n\t"
    "v_accvgpr_write_b32 a114, v34\n\t"
    "v_accvgpr_write_b32 a115, v35\n\t"
    "v_and_b32 v36, v1, v114\n\t"
    "v_and_b32 v37, v2, v114\n\t"
    "v_and_b32 v38, v3, v114\n\t"
    "v_and_b32 v39, v4, v114\n\t"
    "v_accvgpr_write_b32 a116, v36\n\t"
    "v_accvgpr_write_b32 a117, v37\n\t"
    "v_accvgpr_write_b32 a118, v38\n\t"
    "v_accvgpr_write_b32 a119, v39\n\t"
    "v_and_b32 v32, v1, v116\n\t"
    "v_and_b32 v33, v2, v116\n\t"
    "v_and_b32 v34, v3, v116\n\t"
    "v_and_b32 v35, v4, v116\n\t"
    "v_accvgpr_write_b32 a120, v32\n\t"
    "v_accvgpr_write_b32 a121, v33\n\t"
    "v_accvgpr_write_b32 a122, v34\n\t"
    "v_accvgpr_write_b32 a123, v35\n\t"
    "v_and_b32 v36, v1, v118\n\t"
    "v_and_b32 v37, v2, v118\n\t"
    "v_and_b32 v38, v3, v118\n\t"
    "v_and_b32 v39, v4, v118\n\t"
    "v_accvgpr_write_b32 a124, v36\n\t"
    "v_accvgpr_write_b32 a125, v37\n\t"
    "v_accvgpr_write_b32 a126, v38\n\t"
    "v_accvgpr_write_b32 a127, v39\n\t"
    "v_and_b32 v32, v5, v113\n\t"
    "v_and_b32 v33, v6, v113\n\t"
    "v_and_b32 v34, v7, v113\n\t"
    "v_and_b32 v35, v8, v113\n\t"
    "v_accvgpr_write_b32 a128, v32\n\t"
    "v_accvgpr_write_b32 a129, v33\n\t"
    "v_accvgpr_write_b32 a130, v34\n\t"
    "v_accvgpr_write_b32 a131, v35\n\t"
    "v_and_b32 v36, v1, v115\n\t"
    "v_and_b32 v37, v2, v115\n\t"
    "v_and_b32 v38, v3, v115\n\t"
    "v_and_b32 v39, v4, v115\n\t"
    "v_accvgpr_write_b32 a132, v36\n\t"
    "v_accvgpr_write_b32 a133, v37\n\t"
    "v_accvgpr_write_b32 a134, v38\n\t"
    "v_accvgpr_write_b32 a135, v39\n\t"
    "v_and_b32 v32, v1, v117\n\t"
    "v_and_b32 v33, v2, v117\n\t"
    "v_and_b32 v34, v3, v117\n\t"
    "v_and_b32 v35, v4, v117\n\t"
    "v_accvgpr_write_b32 a136, v32\n\t"
    "v_accvgpr_write_b32 a137, v33\n\t"
    "v_accvgpr_write_b32 a138, v34\n\t"
    "v_accvgpr_write_b32 a139, v35\n\t"
    "v_and_b32 v36, v1, v119\n\t"
    "v_and_b32 v37, v2, v119\n\t"
    "v_and_b32 v38, v3, v119\n\t"
    "v_and_b32 v39, v4, v119\n\t"
    "v_accvgpr_write_b32 a140, v36\n\t"
    "v_accvgpr_write_b32 a141, v37\n\t"
    "v_accvgpr_write_b32 a142, v38\n\t"
    "v_accvgpr_write_b32 a143, v39\n\t"
    "s_waitcnt lgkmcnt(4)\n\t"
    "v_and_b32 v32, v5, v120\n\t"
    "v_and_b32 v33, v6, v120\n\t"
    "v_and_b32 v34, v7, v120\n\t"
    "v_and_b32 v35, v8, v120\n\t"
    "v_accvgpr_write_b32 a144, v32\n\t"
    "v_accvgpr_write_b32 a145, v33\n\t"
    "v_accvgpr_write_b32 a146, v34\n\t"
    "v_accvgpr_write_b32 a147, v35\n\t"
    "v_and_b32 v36, v1, v122\n\t"
    "v_and_b32 v37, v2, v122\n\t"
    "v_and_b32 v38, v3, v122\n\t"
    "v_and_b32 v39, v4, v122\n\t"
    "v_accvgpr_write_b32 a148, v36\n\t"
    "v_accvgpr_write_b32 a149, v37\n\t"
    "v_accvgpr_write_b32 a150, v38\n\t"
    "v_accvgpr_write_b32 a151, v39\n\t"
    "v_and_b32 v32, v1, v124\n\t"
    "v_and_b32 v33, v2, v124\n\t"
    "v_and_b32 v34, v3, v124\n\t"
    "v_and_b32 v35, v4, v124\n\t"
    "v_accvgpr_write_b32 a152, v32\n\t"
    "v_accvgpr_write_b32 a153, v33\n\t"
    "v_accvgpr_write_b32 a154, v34\n\t"
    "v_accvgpr_write_b32 a155, v35\n\t"
    "v_and_b32 v36, v1, v126\n\t"
    "v_and_b32 v37, v2, v126\n\t"
    "v_and_b32 v38, v3, v126\n\t"
    "v_and_b32 v39, v4, v126\n\t"
    "v_accvgpr_write_b32 a156, v36\n\t"
    "v_accvgpr_write_b32 a157, v37\n\t"
    "v_accvgpr_write_b32 a158, v38\n\t"
    "v_accvgpr_write_b32 a159, v39\n\t"
    "v_and_b32 v32, v5, v121\n\t"
    "v_and_b32 v33, v6, v121\n\t"
    "v_and_b32 v34, v7, v121\n\t"
    "v_and_b32 v35, v8, v121\n\t"
    "v_accvgpr_write_b32 a160, v32\n\t"
    "v_accvgpr_write_b32 a161, v33\n\t"
    "v_accvgpr_write_b32 a162, v34\n\t"
    "v_accvgpr_write_b32 a163, v35\n\t"
    "v_and_b32 v36, v1, v123\n\t"
    "v_and_b32 v37, v2, v123\n\t"
    "v_and_b32 v38, v3, v123\n\t"
    "v_and_b32 v39, v4, v123\n\t"
    "v_accvgpr_write_b32 a164, v36\n\t"
    "v_accvgpr_write_b32 a165, v37\n\t"
    "v_accvgpr_write_b32 a166, v38\n\t"
    "v_accvgpr_write_b32 a167, v39\n\t"
    "v_and_b32 v32, v1, v125\n\t"
    "v_and_b32 v33, v2, v125\n\t"
    "v_and_b32 v34, v3, v125\n\t"
    "v_and_b32 v35, v4, v125\n\t"
    "v_accvgpr_write_b32 a168, v32\n\t"
    "v_accvgpr_write_b32 a169, v33\n\t"
    "v_accvgpr_write_b32 a170, v34\n\t"
    "v_accvgpr_write_b32 a171, v35\n\t"
    "v_and_b32 v36, v1, v127\n\t"
    "v_and_b32 v37, v2, v127\n\t"
    "v_and_b32 v38, v3, v127\n\t"
    "v_and_b32 v39, v4, v127\n\t"
    "v_accvgpr_write_b32 a172, v36\n\t"
    "v_accvgpr_write_b32 a173, v37\n\t"
    "v_accvgpr_write_b32 a174, v38\n\t"
    "v_accvgpr_write_b32 a175, v39\n\t"
    "s_waitcnt lgkmcnt(0)\n\t"
    "v_and_b32 v32, v5, v128\n\t"
    "v_and_b32 v33, v6, v128\n\t"
    "v_and_b32 v34, v7, v128\n\t"
    "v_and_b32 v35, v8, v128\n\t"
    "v_accvgpr_write_b32 a176, v32\n\t"
    "v_accvgpr_write_b32 a177, v33\n\t"
    "v_accvgpr_write_b32 a178, v34\n\t"
    "v_accvgpr_write_b32 a179, v35\n\t"
    "v_and_b32 v36, v1, v130\n\t"
    "v_and_b32 v37, v2, v130\n\t"
    "v_and_b32 v38, v3, v130\n\t"
    "v_and_b32 v39, v4, v130\n\t"
    "v_accvgpr_write_b32 a180, v36\n\t"
    "v_accvgpr_write_b32 a181, v37\n\t"
    "v_accvgpr_write_b32 a182, v38\n\t"
    "v_accvgpr_write_b32 a183, v39\n\t"
    "v_and_b32 v32, v1, v132\n\t"
    "v_and_b32 v33, v2, v132\n\t"
    "v_and_b32 v34, v3, v132\n\t"
    "v_and_b32 v35, v4, v132\n\t"
    "v_accvgpr_write_b32 a184, v32\n\t"
    "v_accvgpr_write_b32 a185, v33\n\t"
    "v_accvgpr_write_b32 a186, v34\n\t"
    "v_accvgpr_write_b32 a187, v35\n\t"
    "v_and_b32 v36, v1, v134\n\t"
    "v_and_b32 v37, v2, v134\n\t"
    "v_and_b32 v38, v3, v134\n\t"
    "v_and_b32 v39, v4, v134\n\t"
    "v_accvgpr_write_b32 a188, v36\n\t"
    "v_accvgpr_write_b32 a189, v37\n\t"
    "v_accvgpr_write_b32 a190, v38\n\t"
    "v_accvgpr_write_b32 a191, v39\n\t"
    "v_and_b32 v32, v5, v129\n\t"
    "v_and_b32 v33, v6, v129\n\t"
    "v_and_b32 v34, v7, v129\n\t"
    "v_and_b32 v35, v8, v129\n\t"
    "v_accvgpr_write_b32 a192, v32\n\t"
    "v_accvgpr_write_b32 a193, v33\n\t"
    "v_accvgpr_write_b32 a194, v34\n\t"
    "v_accvgpr_write_b32 a195, v35\n\t"
    "v_and_b32 v36, v1, v131\n\t"
    "v_and_b32 v37, v2, v131\n\t"
    "v_and_b32 v38, v3, v131\n\t"
    "v_and_b32 v39, v4, v131\n\t"
    "v_accvgpr_write_b32 a196, v36\n\t"
    "v_accvgpr_write_b32 a197, v37\n\t"
    "v_accvgpr_write_b32 a198, v38\n\t"
    "v_accvgpr_write_b32 a199, v39\n\t"
    "v_and_b32 v32, v1, v133\n\t"
    "v_and_b32 v33, v2, v133\n\t"
    "v_and_b32 v34, v3, v133\n\t"
    "v_and_b32 v35, v4, v133\n\t"
    "v_accvgpr_write_b32 a200, v32\n\t"
    "v_accvgpr_write_b32 a201, v33\n\t"
    "v_accvgpr_write_b32 a202, v34\n\t"
    "v_accvgpr_write_b32 a203, v35\n\t"
    "v_and_b32 v36, v1, v135\n\t"
    "v_and_b32 v37, v2, v135\n\t"
    "v_and_b32 v38, v3, v135\n\t"
    "v_and_b32 v39, v4, v135\n\t"
    "v_accvgpr_write_b32 a204, v36\n\t"
    "v_accvgpr_write_b32 a205, v37\n\t"
    "v_accvgpr_write_b32 a206, v38\n\t"
    "v_accvgpr_write_b32 a207, v39\n\t"
    "v_lshrrev_b32 v53, 1, v62\n\t"
    "s_mul_i32 s61, s75, 2304\n\t"
    "s_add_u32 s61, s61, 24576\n\t"
    "v_add_u32 v54, v53, v61\n\t"
    "v_lshlrev_b32 v14, 7, v54\n\t"
    "v_and_b32 v54, 6, v54\n\t"
    "v_and_or_b32 v54, v62, 1, v54\n\t"
    "v_lshl_add_u32 v14, v54, 4, v14\n\t"
    "v_add_u32 v14, s61, v14\n\t"
    "v_add_u32 v54, 2, v61\n\t"
    "v_lshlrev_b32 v18, 7, v54\n\t"
    "v_and_b32 v54, 6, v54\n\t"
    "v_and_or_b32 v54, v62, 1, v54\n\t"
    "v_lshl_add_u32 v18, v54, 4, v18\n\t"
    "v_add_u32 v18, s61, v18\n\t"
    "v_mul_u32_u24 v54, 2304, v53\n\t"
    "v_add_u32 v18, v18, v54\n\t"
    "v_xor_b32 v15, 32, v14\n\t"
    "v_xor_b32 v16, 64, v14\n\t"
    "v_xor_b32 v17, 96, v14\n\t"
    "v_xor_b32 v19, 32, v18\n\t"
    "v_xor_b32 v20, 64, v18\n\t"
    "v_xor_b32 v21, 96, v18\n\t"
    "s_mov_b64 s[48:49], s[98:99]\n\t"
    "s_mov_b32 s50, 19267584\n\t"
    "s_mov_b32 s51, 0x20000\n\t"
    "s_mov_b32 s60, 0x44ea44ea\n\t"
    "v_lshlrev_b32 v53, 2, v57\n\t"
    "v_min_u32 v54, 764, v53\n\t"
    "v_add_u32 v53, 0x20000, v53\n\t"
    "v_add_u32 v54, 0x20000, v54\n\t"
    "s_mov_b32 s61, 0x3eefe414\n\t"
    "s_mov_b32 s62, 0x4008986d\n\t"
    "v_lshlrev_b32 v48, 16, v48\n\t"
    "v_lshlrev_b32 v49, 16, v49\n\t"
    "v_lshlrev_b32 v50, 16, v50\n\t"
    "v_lshlrev_b32 v51, 16, v51\n\t"
    "v_lshlrev_b32 v52, 16, v52\n\t"
    "v_mul_f32 v48, s61, v48\n\t"
    "v_mul_f32 v49, s61, v49\n\t"
    "v_mul_f32 v50, s61, v50\n\t"
    "v_mul_f32 v51, s61, v51\n\t"
    "v_mul_f32 v52, s62, v52\n\t"
    "ds_write_b32 v53, v48 offset:22528\n\t"
    "ds_write_b32 v53, v49 offset:23552\n\t"
    "ds_write_b32 v53, v50 offset:24576\n\t"
    "ds_write_b32 v54, v51 offset:25600\n\t"
    "ds_write_b32 v54, v52 offset:26368\n\t"
    "s_waitcnt vmcnt(0) lgkmcnt(0)\n\t"
    "s_barrier\n\t"
    "ds_read2_b32 v[48:49], v10 offset0:0 offset1:8\n\t"
    "ds_read2_b32 v[50:51], v11 offset0:0 offset1:8\n\t"
    "ds_read2_b32 v[52:53], v12 offset0:0 offset1:8\n\t"
    "ds_read2_b32 v[54:55], v13 offset0:0 offset1:8\n\t"
    "ds_read_b128 a[8:11], v14 offset:4608\n\t"
    "ds_read_b128 a[12:15], v14 offset:6912\n\t"
    "ds_read_b128 a[16:19], v14 offset:9216\n\t"
    "ds_read_b128 a[20:23], v14 offset:11520\n\t"
    "ds_read_b128 a[0:3], v14 offset:0\n\t"
    "ds_read_b128 a[4:7], v14 offset:2304\n\t"
    "ds_read_b128 a[24:27], v18 offset:0\n\t"
    "ds_read_b128 a[28:31], v18 offset:4608\n\t"
    "ds_read_b128 a[32:35], v18 offset:9216\n\t"
    "s_waitcnt lgkmcnt(9)\n\t"
    "v_and_b32 v32, v5, v48\n\t"
    "v_and_b32 v33, v6, v48\n\t"
    "v_and_b32 v34, v7, v48\n\t"
    "v_and_b32 v35, v8, v48\n\t"
    "v_and_b32 v36, v1, v50\n\t"
    "v_and_b32 v37, v2, v50\n\t"
    "v_and_b32 v38, v3, v50\n\t"
    "v_and_b32 v39, v4, v50\n\t"
    "v_and_b32 v40, v1, v52\n\t"
    "v_and_b32 v41, v2, v52\n\t"
    "v_and_b32 v42, v3, v52\n\t"
    "v_and_b32 v43, v4, v52\n\t"
    "v_and_b32 v44, v1, v54\n\t"
    "v_and_b32 v45, v2, v54\n\t"
    "v_and_b32 v46, v3, v54\n\t"
    "v_and_b32 v47, v4, v54\n\t"
    "s_waitcnt lgkmcnt(5)\n\t"
    "v_mfma_f32_16x16x32_bf16 v[64:67], v[32:35], a[8:11], 0\n\t"
    "ds_read_b128 a[44:47], v15 offset:4608\n\t"
    "v_mfma_f32_16x16x32_bf16 v[112:115], v[32:35], a[12:15], 0\n\t"
    "ds_read_b128 a[48:51], v15 offset:6912\n\t"
    "v_mfma_f32_16x16x32_bf16 v[160:163], v[32:35], a[16:19], 0\n\t"
    "ds_read_b128 a[52:55], v15 offset:9216\n\t"
    "v_mfma_f32_16x16x32_bf16 v[208:211], v[32:35], a[20:23], 0\n\t"
    "ds_read_b128 a[56:59], v15 offset:11520\n\t"
    "s_waitcnt lgkmcnt(7)\n\t"
    "v_smfmac_f32_16x16x64_bf16 v[64:67], v[36:39], a[0:7], v9\n\t"
    "ds_read_b128 a[36:39], v15 offset:0\n\t"
    "v_and_b32 v32, v5, v49\n\t"
    "v_smfmac_f32_16x16x64_bf16 v[112:115], v[36:39], a[4:11], v9\n\t"
    "ds_read_b128 a[40:43], v15 offset:2304\n\t"
    "v_and_b32 v33, v6, v49\n\t"
    "v_smfmac_f32_16x16x64_bf16 v[160:163], v[36:39], a[8:15], v9\n\t"
    "ds_read_b128 a[60:63], v19 offset:0\n\t"
    "v_and_b32 v34, v7, v49\n\t"
    "v_smfmac_f32_16x16x64_bf16 v[208:211], v[36:39], a[12:19], v9\n\t"
    "ds_read_b128 a[64:67], v19 offset:4608\n\t"
    "v_and_b32 v35, v8, v49\n\t"
    "s_waitcnt lgkmcnt(8)\n\t"
    "v_smfmac_f32_16x16x64_bf16 v[64:67], v[40:43], a[24:31], v9\n\t"
    "ds_read_b128 a[68:71], v19 offset:9216\n\t"
    "ds_read2_b32 v[48:49], v10 offset0:16 offset1:24\n\t"
    "v_and_b32 v36, v1, v51\n\t"
    "v_smfmac_f32_16x16x64_bf16 v[160:163], v[40:43], a[28:35], v9\n\t"
    "v_and_b32 v37, v2, v51\n\t"
    "v_smfmac_f32_16x16x64_bf16 v[112:115], v[44:47], a[24:31], v9\n\t"
    "v_and_b32 v38, v3, v51\n\t"
    "v_smfmac_f32_16x16x64_bf16 v[208:211], v[44:47], a[28:35], v9\n\t"
    "v_and_b32 v39, v4, v51\n\t"
    "v_and_b32 v40, v1, v53\n\t"
    "v_and_b32 v41, v2, v53\n\t"
    "v_and_b32 v42, v3, v53\n\t"
    "v_and_b32 v43, v4, v53\n\t"
    "v_and_b32 v44, v1, v55\n\t"
    "v_and_b32 v45, v2, v55\n\t"
    "v_and_b32 v46, v3, v55\n\t"
    "v_and_b32 v47, v4, v55\n\t"
    "v_sub_f32 v144, 0, v64\n\t"
    "v_sub_f32 v146, 0, v112\n\t"
    "v_sub_f32 v148, 0, v160\n\t"
    "v_sub_f32 v150, 0, v208\n\t"
    "v_permlane16_swap_b32 v144, v146\n\t"
    "s_nop 0\n\t"
    "v_permlane16_swap_b32 v148, v150\n\t"
    "s_nop 1\n\t"
    "v_permlane32_swap_b32 v144, v148\n\t"
    "v_mov_b32 v148, v144\n\t"
    "s_nop 1\n\t"
    "v_permlane32_swap_b32 v144, v148\n\t"
    "v_mov_b32 v146, v144\n\t"
    "v_mov_b32 v150, v148\n\t"
    "s_nop 0\n\t"
    "v_permlane16_swap_b32 v144, v146\n\t"
    "v_permlane16_swap_b32 v148, v150\n\t"
    "v_mov_b32 v22, v144\n\t"
    "v_mov_b32 v23, v144\n\t"
    "v_mov_b32 v24, v144\n\t"
    "v_mov_b32 v25, v144\n\t"
    "v_mov_b32 v26, v146\n\t"
    "v_mov_b32 v27, v146\n\t"
    "v_mov_b32 v28, v146\n\t"
    "v_mov_b32 v29, v146\n\t"
    "v_mov_b32 v56, v148\n\t"
    "v_mov_b32 v57, v148\n\t"
    "v_mov_b32 v58, v148\n\t"
    "v_mov_b32 v59, v148\n\t"
    "v_mov_b32 v60, v150\n\t"
    "v_mov_b32 v61, v150\n\t"
    "v_mov_b32 v62, v150\n\t"
    "v_mov_b32 v63, v150\n\t"
    "v_add_f32 v64, v64, v144\n\t"
    "v_add_f32 v65, v65, v144\n\t"
    "v_add_f32 v66, v66, v144\n\t"
    "v_add_f32 v67, v67, v144\n\t"
    "v_add_f32 v112, v112, v146\n\t"
    "v_add_f32 v113, v113, v146\n\t"
    "v_add_f32 v114, v114, v146\n\t"
    "v_add_f32 v115, v115, v146\n\t"
    "v_add_f32 v160, v160, v148\n\t"
    "v_add_f32 v161, v161, v148\n\t"
    "v_add_f32 v162, v162, v148\n\t"
    "v_add_f32 v163, v163, v148\n\t"
    "v_add_f32 v208, v208, v150\n\t"
    "v_add_f32 v209, v209, v150\n\t"
    "v_add_f32 v210, v210, v150\n\t"
    "v_add_f32 v211, v211, v150\n\t"
    "s_waitcnt lgkmcnt(6)\n\t"
    "v_mfma_f32_16x16x32_bf16 v[68:71], v[32:35], a[44:47], v[22:25]\n\t"
    "ds_read_b128 a[80:83], v16 offset:4608\n\t"
    "ds_read2_b32 v[50:51], v11 offset0:16 offset1:24\n\t"
    "v_mfma_f32_16x16x32_bf16 v[116:119], v[32:35], a[48:51], v[26:29]\n\t"
    "ds_read_b128 a[84:87], v16 offset:6912\n\t"
    "ds_read2_b32 v[52:53], v12 offset0:16 offset1:24\n\t"
    "v_mfma_f32_16x16x32_bf16 v[164:167], v[32:35], a[52:55], v[56:59]\n\t"
    "ds_read_b128 a[88:91], v16 offset:9216\n\t"
    "ds_read2_b32 v[54:55], v13 offset0:16 offset1:24\n\t"
    "v_mfma_f32_16x16x32_bf16 v[212:215], v[32:35], a[56:59], v[60:63]\n\t"
    "ds_read_b128 a[92:95], v16 offset:11520\n\t"
    "s_waitcnt lgkmcnt(7)\n\t"
    "v_smfmac_f32_16x16x64_bf16 v[68:71], v[36:39], a[36:43], v9\n\t"
    "ds_read_b128 a[72:75], v16 offset:0\n\t"
    "v_and_b32 v32, v5, v48\n\t"
    "v_smfmac_f32_16x16x64_bf16 v[116:119], v[36:39], a[40:47], v9\n\t"
    "ds_read_b128 a[76:79], v16 offset:2304\n\t"
    "v_and_b32 v33, v6, v48\n\t"
    "v_smfmac_f32_16x16x64_bf16 v[164:167], v[36:39], a[44:51], v9\n\t"
    "ds_read_b128 a[96:99], v20 offset:0\n\t"
    "v_and_b32 v34, v7, v48\n\t"
    "v_smfmac_f32_16x16x64_bf16 v[212:215], v[36:39], a[48:55], v9\n\t"
    "ds_read_b128 a[100:103], v20 offset:4608\n\t"
    "v_and_b32 v35, v8, v48\n\t"
    "s_waitcnt lgkmcnt(9)\n\t"
    "v_smfmac_f32_16x16x64_bf16 v[68:71], v[40:43], a[60:67], v9\n\t"
    "ds_read_b128 a[104:107], v20 offset:9216\n\t"
    "v_and_b32 v36, v1, v50\n\t"
    "v_smfmac_f32_16x16x64_bf16 v[164:167], v[40:43], a[64:71], v9\n\t"
    "v_and_b32 v37, v2, v50\n\t"
    "v_smfmac_f32_16x16x64_bf16 v[116:119], v[44:47], a[60:67], v9\n\t"
    "v_and_b32 v38, v3, v50\n\t"
    "v_smfmac_f32_16x16x64_bf16 v[212:215], v[44:47], a[64:71], v9\n\t"
    "v_and_b32 v39, v4, v50\n\t"
    "s_waitcnt lgkmcnt(6)\n\t"
    "v_and_b32 v40, v1, v52\n\t"
    "v_and_b32 v41, v2, v52\n\t"
    "v_and_b32 v42, v3, v52\n\t"
    "v_and_b32 v43, v4, v52\n\t"
    "v_and_b32 v44, v1, v54\n\t"
    "v_and_b32 v45, v2, v54\n\t"
    "v_and_b32 v46, v3, v54\n\t"
    "v_and_b32 v47, v4, v54\n\t"
    "s_waitcnt lgkmcnt(5)\n\t"
    "v_mfma_f32_16x16x32_bf16 v[72:75], v[32:35], a[80:83], v[22:25]\n\t"
    "ds_read_b128 a[8:11], v17 offset:4608\n\t"
    "v_mfma_f32_16x16x32_bf16 v[120:123], v[32:35], a[84:87], v[26:29]\n\t"
    "ds_read_b128 a[12:15], v17 offset:6912\n\t"
    "v_mfma_f32_16x16x32_bf16 v[168:171], v[32:35], a[88:91], v[56:59]\n\t"
    "ds_read_b128 a[16:19], v17 offset:9216\n\t"
    "v_mfma_f32_16x16x32_bf16 v[216:219], v[32:35], a[92:95], v[60:63]\n\t"
    "ds_read_b128 a[20:23], v17 offset:11520\n\t"
    "s_waitcnt lgkmcnt(7)\n\t"
    "v_smfmac_f32_16x16x64_bf16 v[72:75], v[36:39], a[72:79], v9\n\t"
    "ds_read_b128 a[0:3], v17 offset:0\n\t"
    "v_and_b32 v32, v5, v49\n\t"
    "v_smfmac_f32_16x16x64_bf16 v[120:123], v[36:39], a[76:83], v9\n\t"
    "ds_read_b128 a[4:7], v17 offset:2304\n\t"
    "v_and_b32 v33, v6, v49\n\t"
    "v_smfmac_f32_16x16x64_bf16 v[168:171], v[36:39], a[80:87], v9\n\t"
    "ds_read_b128 a[24:27], v21 offset:0\n\t"
    "v_and_b32 v34, v7, v49\n\t"
    "v_smfmac_f32_16x16x64_bf16 v[216:219], v[36:39], a[84:91], v9\n\t"
    "ds_read_b128 a[28:31], v21 offset:4608\n\t"
    "v_and_b32 v35, v8, v49\n\t"
    "s_waitcnt lgkmcnt(8)\n\t"
    "v_smfmac_f32_16x16x64_bf16 v[72:75], v[40:43], a[96:103], v9\n\t"
    "ds_read_b128 a[32:35], v21 offset:9216\n\t"
    "ds_read2_b32 v[48:49], v10 offset0:32 offset1:40\n\t"
    "v_and_b32 v36, v1, v51\n\t"
    "v_smfmac_f32_16x16x64_bf16 v[168:171], v[40:43], a[100:107], v9\n\t"
    "v_and_b32 v37, v2, v51\n\t"
    "v_smfmac_f32_16x16x64_bf16 v[120:123], v[44:47], a[96:103], v9\n\t"
    "v_and_b32 v38, v3, v51\n\t"
    "v_smfmac_f32_16x16x64_bf16 v[216:219], v[44:47], a[100:107], v9\n\t"
    "v_and_b32 v39, v4, v51\n\t"
    "v_and_b32 v40, v1, v53\n\t"
    "v_and_b32 v41, v2, v53\n\t"
    "v_and_b32 v42, v3, v53\n\t"
    "v_and_b32 v43, v4, v53\n\t"
    "v_and_b32 v44, v1, v55\n\t"
    "v_and_b32 v45, v2, v55\n\t"
    "v_and_b32 v46, v3, v55\n\t"
    "v_and_b32 v47, v4, v55\n\t"
    "s_waitcnt lgkmcnt(6)\n\t"
    "v_mfma_f32_16x16x32_bf16 v[76:79], v[32:35], a[8:11], v[22:25]\n\t"
    "ds_read2_b32 v[50:51], v11 offset0:32 offset1:40\n\t"
    "v_mfma_f32_16x16x32_bf16 v[124:127], v[32:35], a[12:15], v[26:29]\n\t"
    "ds_read2_b32 v[52:53], v12 offset0:32 offset1:40\n\t"
    "v_mfma_f32_16x16x32_bf16 v[172:175], v[32:35], a[16:19], v[56:59]\n\t"
    "ds_read2_b32 v[54:55], v13 offset0:32 offset1:40\n\t"
    "v_mfma_f32_16x16x32_bf16 v[220:223], v[32:35], a[20:23], v[60:63]\n\t"
    "s_waitcnt lgkmcnt(3)\n\t"
    "v_smfmac_f32_16x16x64_bf16 v[76:79], v[36:39], a[0:7], v9\n\t"
    "v_smfmac_f32_16x16x64_bf16 v[124:127], v[36:39], a[4:11], v9\n\t"
    "v_smfmac_f32_16x16x64_bf16 v[172:175], v[36:39], a[8:15], v9\n\t"
    "v_smfmac_f32_16x16x64_bf16 v[220:223], v[36:39], a[12:19], v9\n\t"
    "s_waitcnt lgkmcnt(2)\n\t"
    "v_smfmac_f32_16x16x64_bf16 v[76:79], v[40:43], a[24:31], v9\n\t"
    "v_smfmac_f32_16x16x64_bf16 v[172:175], v[40:43], a[28:35], v9\n\t"
    "v_smfmac_f32_16x16x64_bf16 v[124:127], v[44:47], a[24:31], v9\n\t"
    "v_smfmac_f32_16x16x64_bf16 v[220:223], v[44:47], a[28:35], v9\n\t"
    "s_waitcnt lgkmcnt(0)\n\t"
    "v_pk_mul_f32 v[34:35], v[64:65], v[64:65]\n\t"
    "v_pk_mul_f32 v[38:39], v[112:113], v[112:113]\n\t"
    "v_pk_mul_f32 v[42:43], v[160:161], v[160:161]\n\t"
    "v_pk_mul_f32 v[46:47], v[208:209], v[208:209]\n\t"
    "v_pk_fma_f32 v[34:35], v[66:67], v[66:67], v[34:35]\n\t"
    "v_pk_fma_f32 v[38:39], v[114:115], v[114:115], v[38:39]\n\t"
    "v_pk_fma_f32 v[42:43], v[162:163], v[162:163], v[42:43]\n\t"
    "v_pk_fma_f32 v[46:47], v[210:211], v[210:211], v[46:47]\n\t"
    "v_pk_add_f32 v[32:33], v[64:65], v[66:67]\n\t"
    "v_pk_add_f32 v[36:37], v[112:113], v[114:115]\n\t"
    "v_pk_add_f32 v[40:41], v[160:161], v[162:163]\n\t"
    "v_pk_add_f32 v[44:45], v[208:209], v[210:211]\n\t"
    "v_pk_fma_f32 v[34:35], v[68:69], v[68:69], v[34:35]\n\t"
    "v_pk_fma_f32 v[38:39], v[116:117], v[116:117], v[38:39]\n\t"
    "v_pk_fma_f32 v[42:43], v[164:165], v[164:165], v[42:43]\n\t"
    "v_pk_fma_f32 v[46:47], v[212:213], v[212:213], v[46:47]\n\t"
    "v_pk_add_f32 v[32:33], v[32:33], v[68:69]\n\t"
    "v_pk_add_f32 v[36:37], v[36:37], v[116:117]\n\t"
    "v_pk_add_f32 v[40:41], v[40:41], v[164:165]\n\t"
    "v_pk_add_f32 v[44:45], v[44:45], v[212:213]\n\t"
    "v_pk_fma_f32 v[34:35], v[70:71], v[70:71], v[34:35]\n\t"
    "v_pk_fma_f32 v[38:39], v[118:119], v[118:119], v[38:39]\n\t"
    "v_pk_fma_f32 v[42:43], v[166:167], v[166:167], v[42:43]\n\t"
    "v_pk_fma_f32 v[46:47], v[214:215], v[214:215], v[46:47]\n\t"
    "v_pk_add_f32 v[32:33], v[32:33], v[70:71]\n\t"
    "v_pk_add_f32 v[36:37], v[36:37], v[118:119]\n\t"
    "v_pk_add_f32 v[40:41], v[40:41], v[166:167]\n\t"
    "v_pk_add_f32 v[44:45], v[44:45], v[214:215]\n\t"
    "v_pk_fma_f32 v[34:35], v[72:73], v[72:73], v[34:35]\n\t"
    "v_pk_fma_f32 v[38:39], v[120:121], v[120:121], v[38:39]\n\t"
    "v_pk_fma_f32 v[42:43], v[168:169], v[168:169], v[42:43]\n\t"
    "v_pk_fma_f32 v[46:47], v[216:217], v[216:217], v[46:47]\n\t"
    "v_pk_add_f32 v[32:33], v[32:33], v[72:73]\n\t"
    "v_pk_add_f32 v[36:37], v[36:37], v[120:121]\n\t"
    "v_pk_add_f32 v[40:41], v[40:41], v[168:169]\n\t"
    "v_pk_add_f32 v[44:45], v[44:45], v[216:217]\n\t"
    "v_pk_fma_f32 v[34:35], v[74:75], v[74:75], v[34:35]\n\t"
    "v_pk_fma_f32 v[38:39], v[122:123], v[122:123], v[38:39]\n\t"
    "v_pk_fma_f32 v[42:43], v[170:171], v[170:171], v[42:43]\n\t"
    "v_pk_fma_f32 v[46:47], v[218:219], v[218:219], v[46:47]\n\t"
    "v_pk_add_f32 v[32:33], v[32:33], v[74:75]\n\t"
    "v_pk_add_f32 v[36:37], v[36:37], v[122:123]\n\t"
    "v_pk_add_f32 v[40:41], v[40:41], v[170:171]\n\t"
    "v_pk_add_f32 v[44:45], v[44:45], v[218:219]\n\t"
    "v_pk_fma_f32 v[34:35], v[76:77], v[76:77], v[34:35]\n\t"
    "v_pk_fma_f32 v[38:39], v[124:125], v[124:125], v[38:39]\n\t"
    "v_pk_fma_f32 v[42:43], v[172:173], v[172:173], v[42:43]\n\t"
    "v_pk_fma_f32 v[46:47], v[220:221], v[220:221], v[46:47]\n\t"
    "v_pk_add_f32 v[32:33], v[32:33], v[76:77]\n\t"
    "v_pk_add_f32 v[36:37], v[36:37], v[124:125]\n\t"
    "v_pk_add_f32 v[40:41], v[40:41], v[172:173]\n\t"
    "v_pk_add_f32 v[44:45], v[44:45], v[220:221]\n\t"
    "s_barrier\n\t"
    "ds_read_b128 a[44:47], v14 offset:39424\n\t"
    "ds_read_b128 a[48:51], v14 offset:41728\n\t"
    "ds_read_b128 a[52:55], v14 offset:44032\n\t"
    "ds_read_b128 a[56:59], v14 offset:46336\n\t"
    "ds_read_b128 a[36:39], v14 offset:34816\n\t"
    "ds_read_b128 a[40:43], v14 offset:37120\n\t"
    "ds_read_b128 a[60:63], v18 offset:34816\n\t"
    "ds_read_b128 a[64:67], v18 offset:39424\n\t"
    "ds_read_b128 a[68:71], v18 offset:44032\n\t"
    "v_pk_fma_f32 v[34:35], v[78:79], v[78:79], v[34:35]\n\t"
    "v_pk_fma_f32 v[38:39], v[126:127], v[126:127], v[38:39]\n\t"
    "v_pk_fma_f32 v[42:43], v[174:175], v[174:175], v[42:43]\n\t"
    "v_pk_fma_f32 v[46:47], v[222:223], v[222:223], v[46:47]\n\t"
    "v_pk_add_f32 v[32:33], v[32:33], v[78:79]\n\t"
    "v_pk_add_f32 v[36:37], v[36:37], v[126:127]\n\t"
    "v_pk_add_f32 v[40:41], v[40:41], v[174:175]\n\t"
    "v_pk_add_f32 v[44:45], v[44:45], v[222:223]\n\t"
    "v_add_f32 v32, v32, v33\n\t"
    "v_add_f32 v34, v34, v35\n\t"
    "v_add_f32 v36, v36, v37\n\t"
    "v_add_f32 v38, v38, v39\n\t"
    "v_add_f32 v40, v40, v41\n\t"
    "v_add_f32 v42, v42, v43\n\t"
    "v_add_f32 v44, v44, v45\n\t"
    "v_add_f32 v46, v46, v47\n\t"
    "v_permlane16_swap_b32 v32, v36\n\t"
    "v_permlane16_swap_b32 v34, v38\n\t"
    "v_permlane16_swap_b32 v40, v44\n\t"
    "v_permlane16_swap_b32 v42, v46\n\t"
    "v_add_f32 v32, v32, v36\n\t"
    "v_add_f32 v34, v34, v38\n\t"
    "v_add_f32 v40, v40, v44\n\t"
    "v_add_f32 v42, v42, v46\n\t"
    "s_nop 0\n\t"
    "v_permlane32_swap_b32 v32, v40\n\t"
    "v_permlane32_swap_b32 v34, v42\n\t"
    "v_add_f32 v30, v32, v40\n\t"
    "v_add_f32 v31, v34, v42\n\t"
    "v_and_b32 v32, v5, v48\n\t"
    "v_and_b32 v33, v6, v48\n\t"
    "v_and_b32 v34, v7, v48\n\t"
    "v_and_b32 v35, v8, v48\n\t"
    "v_and_b32 v36, v1, v50\n\t"
    "v_and_b32 v37, v2, v50\n\t"
    "v_and_b32 v38, v3, v50\n\t"
    "v_and_b32 v39, v4, v50\n\t"
    "v_and_b32 v40, v1, v52\n\t"
    "v_and_b32 v41, v2, v52\n\t"
    "v_and_b32 v42, v3, v52\n\t"
    "v_and_b32 v43, v4, v52\n\t"
    "v_and_b32 v44, v1, v54\n\t"
    "v_and_b32 v45, v2, v54\n\t"
    "v_and_b32 v46, v3, v54\n\t"
    "v_and_b32 v47, v4, v54\n\t"
    "s_waitcnt lgkmcnt(5)\n\t"
    "v_mfma_f32_16x16x32_bf16 v[80:83], v[32:35], a[44:47], v[22:25]\n\t"
    "ds_read_b128 a[80:83], v15 offset:39424\n\t"
    "v_mfma_f32_16x16x32_bf16 v[128:131], v[32:35], a[48:51], v[26:29]\n\t"
    "ds_read_b128 a[84:87], v15 offset:41728\n\t"
    "v_mfma_f32_16x16x32_bf16 v[176:179], v[32:35], a[52:55], v[56:59]\n\t"
    "ds_read_b128 a[88:91], v15 offset:44032\n\t"
    "v_mfma_f32_16x16x32_bf16 v[224:227], v[32:35], a[56:59], v[60:63]\n\t"
    "ds_read_b128 a[92:95], v15 offset:46336\n\t"
    "s_waitcnt lgkmcnt(7)\n\t"
    "v_smfmac_f32_16x16x64_bf16 v[80:83], v[36:39], a[36:43], v9\n\t"
    "ds_read_b128 a[72:75], v15 offset:34816\n\t"
    "v_and_b32 v32, v5, v49\n\t"
    "v_smfmac_f32_16x16x64_bf16 v[128:131], v[36:39], a[40:47], v9\n\t"
    "ds_read_b128 a[76:79], v15 offset:37120\n\t"
    "v_and_b32 v33, v6, v49\n\t"
    "v_smfmac_f32_16x16x64_bf16 v[176:179], v[36:39], a[44:51], v9\n\t"
    "ds_read_b128 a[96:99], v19 offset:34816\n\t"
    "v_and_b32 v34, v7, v49\n\t"
    "v_smfmac_f32_16x16x64_bf16 v[224:227], v[36:39], a[48:55], v9\n\t"
    "ds_read_b128 a[100:103], v19 offset:39424\n\t"
    "v_and_b32 v35, v8, v49\n\t"
    "s_waitcnt lgkmcnt(8)\n\t"
    "v_smfmac_f32_16x16x64_bf16 v[80:83], v[40:43], a[60:67], v9\n\t"
    "ds_read_b128 a[104:107], v19 offset:44032\n\t"
    "v_and_b32 v36, v1, v51\n\t"
    "v_smfmac_f32_16x16x64_bf16 v[176:179], v[40:43], a[64:71], v9\n\t"
    "v_and_b32 v37, v2, v51\n\t"
    "v_smfmac_f32_16x16x64_bf16 v[128:131], v[44:47], a[60:67], v9\n\t"
    "v_and_b32 v38, v3, v51\n\t"
    "v_smfmac_f32_16x16x64_bf16 v[224:227], v[44:47], a[64:71], v9\n\t"
    "v_and_b32 v39, v4, v51\n\t"
    "v_and_b32 v40, v1, v53\n\t"
    "v_and_b32 v41, v2, v53\n\t"
    "v_and_b32 v42, v3, v53\n\t"
    "v_and_b32 v43, v4, v53\n\t"
    "v_and_b32 v44, v1, v55\n\t"
    "v_and_b32 v45, v2, v55\n\t"
    "v_and_b32 v46, v3, v55\n\t"
    "v_and_b32 v47, v4, v55\n\t"
    "s_waitcnt lgkmcnt(5)\n\t"
    "v_mfma_f32_16x16x32_bf16 v[84:87], v[32:35], a[80:83], v[22:25]\n\t"
    "ds_read_b128 a[8:11], v16 offset:39424\n\t"
    "v_mfma_f32_16x16x32_bf16 v[132:135], v[32:35], a[84:87], v[26:29]\n\t"
    "ds_read_b128 a[12:15], v16 offset:41728\n\t"
    "v_mfma_f32_16x16x32_bf16 v[180:183], v[32:35], a[88:91], v[56:59]\n\t"
    "ds_read_b128 a[16:19], v16 offset:44032\n\t"
    "v_mfma_f32_16x16x32_bf16 v[228:231], v[32:35], a[92:95], v[60:63]\n\t"
    "ds_read_b128 a[20:23], v16 offset:46336\n\t"
    "s_waitcnt lgkmcnt(7)\n\t"
    "v_smfmac_f32_16x16x64_bf16 v[84:87], v[36:39], a[72:79], v9\n\t"
    "ds_read_b128 a[0:3], v16 offset:34816\n\t"
    "v_smfmac_f32_16x16x64_bf16 v[132:135], v[36:39], a[76:83], v9\n\t"
    "ds_read_b128 a[4:7], v16 offset:37120\n\t"
    "v_smfmac_f32_16x16x64_bf16 v[180:183], v[36:39], a[80:87], v9\n\t"
    "ds_read_b128 a[24:27], v20 offset:34816\n\t"
    "v_smfmac_f32_16x16x64_bf16 v[228:231], v[36:39], a[84:91], v9\n\t"
    "ds_read_b128 a[28:31], v20 offset:39424\n\t"
    "s_waitcnt lgkmcnt(8)\n\t"
    "v_smfmac_f32_16x16x64_bf16 v[84:87], v[40:43], a[96:103], v9\n\t"
    "ds_read_b128 a[32:35], v20 offset:44032\n\t"
    "v_smfmac_f32_16x16x64_bf16 v[180:183], v[40:43], a[100:107], v9\n\t"
    "v_smfmac_f32_16x16x64_bf16 v[132:135], v[44:47], a[96:103], v9\n\t"
    "v_smfmac_f32_16x16x64_bf16 v[228:231], v[44:47], a[100:107], v9\n\t"
    "s_waitcnt lgkmcnt(5)\n\t"
    "v_mfma_f32_16x16x32_bf16 v[88:91], a[112:115], a[8:11], v[22:25]\n\t"
    "ds_read_b128 a[44:47], v17 offset:39424\n\t"
    "v_mfma_f32_16x16x32_bf16 v[136:139], a[112:115], a[12:15], v[26:29]\n\t"
    "ds_read_b128 a[48:51], v17 offset:41728\n\t"
    "v_mfma_f32_16x16x32_bf16 v[184:187], a[112:115], a[16:19], v[56:59]\n\t"
    "ds_read_b128 a[52:55], v17 offset:44032\n\t"
    "v_mfma_f32_16x16x32_bf16 v[232:235], a[112:115], a[20:23], v[60:63]\n\t"
    "ds_read_b128 a[56:59], v17 offset:46336\n\t"
    "s_waitcnt lgkmcnt(7)\n\t"
    "v_smfmac_f32_16x16x64_bf16 v[88:91], a[116:119], a[0:7], v9\n\t"
    "ds_read_b128 a[36:39], v17 offset:34816\n\t"
    "v_smfmac_f32_16x16x64_bf16 v[136:139], a[116:119], a[4:11], v9\n\t"
    "ds_read_b128 a[40:43], v17 offset:37120\n\t"
    "v_smfmac_f32_16x16x64_bf16 v[184:187], a[116:119], a[8:15], v9\n\t"
    "ds_read_b128 a[60:63], v21 offset:34816\n\t"
    "v_smfmac_f32_16x16x64_bf16 v[232:235], a[116:119], a[12:19], v9\n\t"
    "ds_read_b128 a[64:67], v21 offset:39424\n\t"
    "s_waitcnt lgkmcnt(8)\n\t"
    "v_smfmac_f32_16x16x64_bf16 v[88:91], a[120:123], a[24:31], v9\n\t"
    "ds_read_b128 a[68:71], v21 offset:44032\n\t"
    "v_smfmac_f32_16x16x64_bf16 v[184:187], a[120:123], a[28:35], v9\n\t"
    "v_smfmac_f32_16x16x64_bf16 v[136:139], a[124:127], a[24:31], v9\n\t"
    "v_smfmac_f32_16x16x64_bf16 v[232:235], a[124:127], a[28:35], v9\n\t"
    "s_waitcnt lgkmcnt(5)\n\t"
    "v_mfma_f32_16x16x32_bf16 v[92:95], a[128:131], a[44:47], v[22:25]\n\t"
    "v_mfma_f32_16x16x32_bf16 v[140:143], a[128:131], a[48:51], v[26:29]\n\t"
    "v_mfma_f32_16x16x32_bf16 v[188:191], a[128:131], a[52:55], v[56:59]\n\t"
    "v_mfma_f32_16x16x32_bf16 v[236:239], a[128:131], a[56:59], v[60:63]\n\t"
    "s_waitcnt lgkmcnt(3)\n\t"
    "v_smfmac_f32_16x16x64_bf16 v[92:95], a[132:135], a[36:43], v9\n\t"
    "v_smfmac_f32_16x16x64_bf16 v[140:143], a[132:135], a[40:47], v9\n\t"
    "v_smfmac_f32_16x16x64_bf16 v[188:191], a[132:135], a[44:51], v9\n\t"
    "v_smfmac_f32_16x16x64_bf16 v[236:239], a[132:135], a[48:55], v9\n\t"
    "s_waitcnt lgkmcnt(0)\n\t"
    "v_smfmac_f32_16x16x64_bf16 v[92:95], a[136:139], a[60:67], v9\n\t"
    "v_smfmac_f32_16x16x64_bf16 v[188:191], a[136:139], a[64:71], v9\n\t"
    "v_smfmac_f32_16x16x64_bf16 v[140:143], a[140:143], a[60:67], v9\n\t"
    "v_smfmac_f32_16x16x64_bf16 v[236:239], a[140:143], a[64:71], v9\n\t"
    "v_pk_mul_f32 v[34:35], v[80:81], v[80:81]\n\t"
    "v_pk_mul_f32 v[38:39], v[128:129], v[128:129]\n\t"
    "v_pk_mul_f32 v[42:43], v[176:177], v[176:177]\n\t"
    "v_pk_mul_f32 v[46:47], v[224:225], v[224:225]\n\t"
    "v_pk_fma_f32 v[34:35], v[82:83], v[82:83], v[34:35]\n\t"
    "v_pk_fma_f32 v[38:39], v[130:131], v[130:131], v[38:39]\n\t"
    "v_pk_fma_f32 v[42:43], v[178:179], v[178:179], v[42:43]\n\t"
    "v_pk_fma_f32 v[46:47], v[226:227], v[226:227], v[46:47]\n\t"
    "v_pk_add_f32 v[32:33], v[80:81], v[82:83]\n\t"
    "v_pk_add_f32 v[36:37], v[128:129], v[130:131]\n\t"
    "v_pk_add_f32 v[40:41], v[176:177], v[178:179]\n\t"
    "v_pk_add_f32 v[44:45], v[224:225], v[226:227]\n\t"
    "v_pk_fma_f32 v[34:35], v[84:85], v[84:85], v[34:35]\n\t"
    "v_pk_fma_f32 v[38:39], v[132:133], v[132:133], v[38:39]\n\t"
    "v_pk_fma_f32 v[42:43], v[180:181], v[180:181], v[42:43]\n\t"
    "v_pk_fma_f32 v[46:47], v[228:229], v[228:229], v[46:47]\n\t"
    "v_pk_add_f32 v[32:33], v[32:33], v[84:85]\n\t"
    "v_pk_add_f32 v[36:37], v[36:37], v[132:133]\n\t"
    "v_pk_add_f32 v[40:41], v[40:41], v[180:181]\n\t"
    "v_pk_add_f32 v[44:45], v[44:45], v[228:229]\n\t"
    "v_pk_fma_f32 v[34:35], v[86:87], v[86:87], v[34:35]\n\t"
    "v_pk_fma_f32 v[38:39], v[134:135], v[134:135], v[38:39]\n\t"
    "v_pk_fma_f32 v[42:43], v[182:183], v[182:183], v[42:43]\n\t"
    "v_pk_fma_f32 v[46:47], v[230:231], v[230:231], v[46:47]\n\t"
    "v_pk_add_f32 v[32:33], v[32:33], v[86:87]\n\t"
    "v_pk_add_f32 v[36:37], v[36:37], v[134:135]\n\t"
    "v_pk_add_f32 v[40:41], v[40:41], v[182:183]\n\t"
    "v_pk_add_f32 v[44:45], v[44:45], v[230:231]\n\t"
    "v_pk_fma_f32 v[34:35], v[88:89], v[88:89], v[34:35]\n\t"
    "v_pk_fma_f32 v[38:39], v[136:137], v[136:137], v[38:39]\n\t"
    "v_pk_fma_f32 v[42:43], v[184:185], v[184:185], v[42:43]\n\t"
    "v_pk_fma_f32 v[46:47], v[232:233], v[232:233], v[46:47]\n\t"
    "v_pk_add_f32 v[32:33], v[32:33], v[88:89]\n\t"
    "v_pk_add_f32 v[36:37], v[36:37], v[136:137]\n\t"
    "v_pk_add_f32 v[40:41], v[40:41], v[184:185]\n\t"
    "v_pk_add_f32 v[44:45], v[44:45], v[232:233]\n\t"
    "v_pk_fma_f32 v[34:35], v[90:91], v[90:91], v[34:35]\n\t"
    "v_pk_fma_f32 v[38:39], v[138:139], v[138:139], v[38:39]\n\t"
    "v_pk_fma_f32 v[42:43], v[186:187], v[186:187], v[42:43]\n\t"
    "v_pk_fma_f32 v[46:47], v[234:235], v[234:235], v[46:47]\n\t"
    "v_pk_add_f32 v[32:33], v[32:33], v[90:91]\n\t"
    "v_pk_add_f32 v[36:37], v[36:37], v[138:139]\n\t"
    "v_pk_add_f32 v[40:41], v[40:41], v[186:187]\n\t"
    "v_pk_add_f32 v[44:45], v[44:45], v[234:235]\n\t"
    "v_pk_fma_f32 v[34:35], v[92:93], v[92:93], v[34:35]\n\t"
    "v_pk_fma_f32 v[38:39], v[140:141], v[140:141], v[38:39]\n\t"
    "v_pk_fma_f32 v[42:43], v[188:189], v[188:189], v[42:43]\n\t"
    "v_pk_fma_f32 v[46:47], v[236:237], v[236:237], v[46:47]\n\t"
    "v_pk_add_f32 v[32:33], v[32:33], v[92:93]\n\t"
    "v_pk_add_f32 v[36:37], v[36:37], v[140:141]\n\t"
    "v_pk_add_f32 v[40:41], v[40:41], v[188:189]\n\t"
    "v_pk_add_f32 v[44:45], v[44:45], v[236:237]\n\t"
    "v_add_u32 v14, 69632, v14\n\t"
    "v_add_u32 v15, 69632, v15\n\t"
    "v_add_u32 v16, 69632, v16\n\t"
    "v_add_u32 v17, 69632, v17\n\t"
    "v_add_u32 v18, 69632, v18\n\t"
    "v_add_u32 v19, 69632, v19\n\t"
    "v_add_u32 v20, 69632, v20\n\t"
    "v_add_u32 v21, 69632, v21\n\t"
    "s_barrier\n\t"
    "ds_read_b128 a[80:83], v14 offset:4608\n\t"
    "ds_read_b128 a[84:87], v14 offset:6912\n\t"
    "ds_read_b128 a[88:91], v14 offset:9216\n\t"
    "ds_read_b128 a[92:95], v14 offset:11520\n\t"
    "ds_read_b128 a[72:75], v14 offset:0\n\t"
    "ds_read_b128 a[76:79], v14 offset:2304\n\t"
    "ds_read_b128 a[96:99], v18 offset:0\n\t"
    "ds_read_b128 a[100:103], v18 offset:4608\n\t"
    "ds_read_b128 a[104:107], v18 offset:9216\n\t"
    "v_pk_fma_f32 v[34:35], v[94:95], v[94:95], v[34:35]\n\t"
    "v_pk_fma_f32 v[38:39], v[142:143], v[142:143], v[38:39]\n\t"
    "v_pk_fma_f32 v[42:43], v[190:191], v[190:191], v[42:43]\n\t"
    "v_pk_fma_f32 v[46:47], v[238:239], v[238:239], v[46:47]\n\t"
    "v_pk_add_f32 v[32:33], v[32:33], v[94:95]\n\t"
    "v_pk_add_f32 v[36:37], v[36:37], v[142:143]\n\t"
    "v_pk_add_f32 v[40:41], v[40:41], v[190:191]\n\t"
    "v_pk_add_f32 v[44:45], v[44:45], v[238:239]\n\t"
    "v_add_f32 v32, v32, v33\n\t"
    "v_add_f32 v34, v34, v35\n\t"
    "v_add_f32 v36, v36, v37\n\t"
    "v_add_f32 v38, v38, v39\n\t"
    "v_add_f32 v40, v40, v41\n\t"
    "v_add_f32 v42, v42, v43\n\t"
    "v_add_f32 v44, v44, v45\n\t"
    "v_add_f32 v46, v46, v47\n\t"
    "v_permlane16_swap_b32 v32, v36\n\t"
    "v_permlane16_swap_b32 v34, v38\n\t"
    "v_permlane16_swap_b32 v40, v44\n\t"
    "v_permlane16_swap_b32 v42, v46\n\t"
    "v_add_f32 v32, v32, v36\n\t"
    "v_add_f32 v34, v34, v38\n\t"
    "v_add_f32 v40, v40, v44\n\t"
    "v_add_f32 v42, v42, v46\n\t"
    "s_nop 0\n\t"
    "v_permlane32_swap_b32 v32, v40\n\t"
    "v_permlane32_swap_b32 v34, v42\n\t"
    "v_add_f32 v32, v32, v40\n\t"
    "v_add_f32 v34, v34, v42\n\t"
    "v_add_f32 v30, v30, v32\n\t"
    "v_add_f32 v31, v31, v34\n\t"
    "s_waitcnt lgkmcnt(5)\n\t"
    "v_mfma_f32_16x16x32_bf16 v[96:99], a[144:147], a[80:83], v[22:25]\n\t"
    "ds_read_b128 a[8:11], v15 offset:4608\n\t"
    "v_mfma_f32_16x16x32_bf16 v[144:147], a[144:147], a[84:87], v[26:29]\n\t"
    "ds_read_b128 a[12:15], v15 offset:6912\n\t"
    "v_mfma_f32_16x16x32_bf16 v[192:195], a[144:147], a[88:91], v[56:59]\n\t"
    "ds_read_b128 a[16:19], v15 offset:9216\n\t"
    "v_mfma_f32_16x16x32_bf16 v[240:243], a[144:147], a[92:95], v[60:63]\n\t"
    "ds_read_b128 a[20:23], v15 offset:11520\n\t"
    "s_waitcnt lgkmcnt(7)\n\t"
    "v_smfmac_f32_16x16x64_bf16 v[96:99], a[148:151], a[72:79], v9\n\t"
    "ds_read_b128 a[0:3], v15 offset:0\n\t"
    "v_smfmac_f32_16x16x64_bf16 v[144:147], a[148:151], a[76:83], v9\n\t"
    "ds_read_b128 a[4:7], v15 offset:2304\n\t"
    "v_smfmac_f32_16x16x64_bf16 v[192:195], a[148:151], a[80:87], v9\n\t"
    "ds_read_b128 a[24:27], v19 offset:0\n\t"
    "v_smfmac_f32_16x16x64_bf16 v[240:243], a[148:151], a[84:91], v9\n\t"
    "ds_read_b128 a[28:31], v19 offset:4608\n\t"
    "s_waitcnt lgkmcnt(8)\n\t"
    "v_smfmac_f32_16x16x64_bf16 v[96:99], a[152:155], a[96:103], v9\n\t"
    "ds_read_b128 a[32:35], v19 offset:9216\n\t"
    "v_smfmac_f32_16x16x64_bf16 v[192:195], a[152:155], a[100:107], v9\n\t"
    "v_smfmac_f32_16x16x64_bf16 v[144:147], a[156:159], a[96:103], v9\n\t"
    "v_smfmac_f32_16x16x64_bf16 v[240:243], a[156:159], a[100:107], v9\n\t"
    "s_waitcnt lgkmcnt(5)\n\t"
    "v_mfma_f32_16x16x32_bf16 v[100:103], a[160:163], a[8:11], v[22:25]\n\t"
    "ds_read_b128 a[44:47], v16 offset:4608\n\t"
    "v_mfma_f32_16x16x32_bf16 v[148:151], a[160:163], a[12:15], v[26:29]\n\t"
    "ds_read_b128 a[48:51], v16 offset:6912\n\t"
    "v_mfma_f32_16x16x32_bf16 v[196:199], a[160:163], a[16:19], v[56:59]\n\t"
    "ds_read_b128 a[52:55], v16 offset:9216\n\t"
    "v_mfma_f32_16x16x32_bf16 v[244:247], a[160:163], a[20:23], v[60:63]\n\t"
    "ds_read_b128 a[56:59], v16 offset:11520\n\t"
    "s_waitcnt lgkmcnt(7)\n\t"
    "v_smfmac_f32_16x16x64_bf16 v[100:103], a[164:167], a[0:7], v9\n\t"
    "ds_read_b128 a[36:39], v16 offset:0\n\t"
    "v_smfmac_f32_16x16x64_bf16 v[148:151], a[164:167], a[4:11], v9\n\t"
    "ds_read_b128 a[40:43], v16 offset:2304\n\t"
    "v_smfmac_f32_16x16x64_bf16 v[196:199], a[164:167], a[8:15], v9\n\t"
    "ds_read_b128 a[60:63], v20 offset:0\n\t"
    "v_smfmac_f32_16x16x64_bf16 v[244:247], a[164:167], a[12:19], v9\n\t"
    "ds_read_b128 a[64:67], v20 offset:4608\n\t"
    "s_waitcnt lgkmcnt(8)\n\t"
    "v_smfmac_f32_16x16x64_bf16 v[100:103], a[168:171], a[24:31], v9\n\t"
    "ds_read_b128 a[68:71], v20 offset:9216\n\t"
    "v_smfmac_f32_16x16x64_bf16 v[196:199], a[168:171], a[28:35], v9\n\t"
    "v_smfmac_f32_16x16x64_bf16 v[148:151], a[172:175], a[24:31], v9\n\t"
    "v_smfmac_f32_16x16x64_bf16 v[244:247], a[172:175], a[28:35], v9\n\t"
    "s_waitcnt lgkmcnt(5)\n\t"
    "v_mfma_f32_16x16x32_bf16 v[104:107], a[176:179], a[44:47], v[22:25]\n\t"
    "ds_read_b128 a[80:83], v17 offset:4608\n\t"
    "v_mfma_f32_16x16x32_bf16 v[152:155], a[176:179], a[48:51], v[26:29]\n\t"
    "ds_read_b128 a[84:87], v17 offset:6912\n\t"
    "v_mfma_f32_16x16x32_bf16 v[200:203], a[176:179], a[52:55], v[56:59]\n\t"
    "ds_read_b128 a[88:91], v17 offset:9216\n\t"
    "v_mfma_f32_16x16x32_bf16 v[248:251], a[176:179], a[56:59], v[60:63]\n\t"
    "ds_read_b128 a[92:95], v17 offset:11520\n\t"
    "s_waitcnt lgkmcnt(7)\n\t"
    "v_smfmac_f32_16x16x64_bf16 v[104:107], a[180:183], a[36:43], v9\n\t"
    "ds_read_b128 a[72:75], v17 offset:0\n\t"
    "v_smfmac_f32_16x16x64_bf16 v[152:155], a[180:183], a[40:47], v9\n\t"
    "ds_read_b128 a[76:79], v17 offset:2304\n\t"
    "v_smfmac_f32_16x16x64_bf16 v[200:203], a[180:183], a[44:51], v9\n\t"
    "ds_read_b128 a[96:99], v21 offset:0\n\t"
    "v_smfmac_f32_16x16x64_bf16 v[248:251], a[180:183], a[48:55], v9\n\t"
    "ds_read_b128 a[100:103], v21 offset:4608\n\t"
    "s_waitcnt lgkmcnt(8)\n\t"
    "v_smfmac_f32_16x16x64_bf16 v[104:107], a[184:187], a[60:67], v9\n\t"
    "ds_read_b128 a[104:107], v21 offset:9216\n\t"
    "v_smfmac_f32_16x16x64_bf16 v[200:203], a[184:187], a[64:71], v9\n\t"
    "v_smfmac_f32_16x16x64_bf16 v[152:155], a[188:191], a[60:67], v9\n\t"
    "v_smfmac_f32_16x16x64_bf16 v[248:251], a[188:191], a[64:71], v9\n\t"
    "s_waitcnt lgkmcnt(5)\n\t"
    "v_mfma_f32_16x16x32_bf16 v[108:111], a[192:195], a[80:83], v[22:25]\n\t"
    "v_mfma_f32_16x16x32_bf16 v[156:159], a[192:195], a[84:87], v[26:29]\n\t"
    "v_mfma_f32_16x16x32_bf16 v[204:207], a[192:195], a[88:91], v[56:59]\n\t"
    "v_mfma_f32_16x16x32_bf16 v[252:255], a[192:195], a[92:95], v[60:63]\n\t"
    "s_waitcnt lgkmcnt(3)\n\t"
    "v_smfmac_f32_16x16x64_bf16 v[108:111], a[196:199], a[72:79], v9\n\t"
    "v_smfmac_f32_16x16x64_bf16 v[156:159], a[196:199], a[76:83], v9\n\t"
    "v_smfmac_f32_16x16x64_bf16 v[204:207], a[196:199], a[80:87], v9\n\t"
    "v_smfmac_f32_16x16x64_bf16 v[252:255], a[196:199], a[84:91], v9\n\t"
    "s_waitcnt lgkmcnt(0)\n\t"
    "v_smfmac_f32_16x16x64_bf16 v[108:111], a[200:203], a[96:103], v9\n\t"
    "v_smfmac_f32_16x16x64_bf16 v[204:207], a[200:203], a[100:107], v9\n\t"
    "v_smfmac_f32_16x16x64_bf16 v[156:159], a[204:207], a[96:103], v9\n\t"
    "v_smfmac_f32_16x16x64_bf16 v[252:255], a[204:207], a[100:107], v9\n\t"
    "v_pk_mul_f32 v[34:35], v[96:97], v[96:97]\n\t"
    "v_pk_mul_f32 v[38:39], v[144:145], v[144:145]\n\t"
    "v_pk_mul_f32 v[42:43], v[192:193], v[192:193]\n\t"
    "v_pk_mul_f32 v[46:47], v[240:241], v[240:241]\n\t"
    "v_pk_fma_f32 v[34:35], v[98:99], v[98:99], v[34:35]\n\t"
    "v_pk_fma_f32 v[38:39], v[146:147], v[146:147], v[38:39]\n\t"
    "v_pk_fma_f32 v[42:43], v[194:195], v[194:195], v[42:43]\n\t"
    "v_pk_fma_f32 v[46:47], v[242:243], v[242:243], v[46:47]\n\t"
    "v_pk_add_f32 v[32:33], v[96:97], v[98:99]\n\t"
    "v_pk_add_f32 v[36:37], v[144:145], v[146:147]\n\t"
    "v_pk_add_f32 v[40:41], v[192:193], v[194:195]\n\t"
    "v_pk_add_f32 v[44:45], v[240:241], v[242:243]\n\t"
    "v_pk_fma_f32 v[34:35], v[100:101], v[100:101], v[34:35]\n\t"
    "v_pk_fma_f32 v[38:39], v[148:149], v[148:149], v[38:39]\n\t"
    "v_pk_fma_f32 v[42:43], v[196:197], v[196:197], v[42:43]\n\t"
    "v_pk_fma_f32 v[46:47], v[244:245], v[244:245], v[46:47]\n\t"
    "v_pk_add_f32 v[32:33], v[32:33], v[100:101]\n\t"
    "v_pk_add_f32 v[36:37], v[36:37], v[148:149]\n\t"
    "v_pk_add_f32 v[40:41], v[40:41], v[196:197]\n\t"
    "v_pk_add_f32 v[44:45], v[44:45], v[244:245]\n\t"
    "v_pk_fma_f32 v[34:35], v[102:103], v[102:103], v[34:35]\n\t"
    "v_pk_fma_f32 v[38:39], v[150:151], v[150:151], v[38:39]\n\t"
    "v_pk_fma_f32 v[42:43], v[198:199], v[198:199], v[42:43]\n\t"
    "v_pk_fma_f32 v[46:47], v[246:247], v[246:247], v[46:47]\n\t"
    "v_pk_add_f32 v[32:33], v[32:33], v[102:103]\n\t"
    "v_pk_add_f32 v[36:37], v[36:37], v[150:151]\n\t"
    "v_pk_add_f32 v[40:41], v[40:41], v[198:199]\n\t"
    "v_pk_add_f32 v[44:45], v[44:45], v[246:247]\n\t"
    "v_pk_fma_f32 v[34:35], v[104:105], v[104:105], v[34:35]\n\t"
    "v_pk_fma_f32 v[38:39], v[152:153], v[152:153], v[38:39]\n\t"
    "v_pk_fma_f32 v[42:43], v[200:201], v[200:201], v[42:43]\n\t"
    "v_pk_fma_f32 v[46:47], v[248:249], v[248:249], v[46:47]\n\t"
    "v_pk_add_f32 v[32:33], v[32:33], v[104:105]\n\t"
    "v_pk_add_f32 v[36:37], v[36:37], v[152:153]\n\t"
    "v_pk_add_f32 v[40:41], v[40:41], v[200:201]\n\t"
    "v_pk_add_f32 v[44:45], v[44:45], v[248:249]\n\t"
    "v_pk_fma_f32 v[34:35], v[106:107], v[106:107], v[34:35]\n\t"
    "v_pk_fma_f32 v[38:39], v[154:155], v[154:155], v[38:39]\n\t"
    "v_pk_fma_f32 v[42:43], v[202:203], v[202:203], v[42:43]\n\t"
    "v_pk_fma_f32 v[46:47], v[250:251], v[250:251], v[46:47]\n\t"
    "v_pk_add_f32 v[32:33], v[32:33], v[106:107]\n\t"
    "v_pk_add_f32 v[36:37], v[36:37], v[154:155]\n\t"
    "v_pk_add_f32 v[40:41], v[40:41], v[202:203]\n\t"
    "v_pk_add_f32 v[44:45], v[44:45], v[250:251]\n\t"
    "v_pk_fma_f32 v[34:35], v[108:109], v[108:109], v[34:35]\n\t"
    "v_pk_fma_f32 v[38:39], v[156:157], v[156:157], v[38:39]\n\t"
    "v_pk_fma_f32 v[42:43], v[204:205], v[204:205], v[42:43]\n\t"
    "v_pk_fma_f32 v[46:47], v[252:253], v[252:253], v[46:47]\n\t"
    "v_pk_add_f32 v[32:33], v[32:33], v[108:109]\n\t"
    "v_pk_add_f32 v[36:37], v[36:37], v[156:157]\n\t"
    "v_pk_add_f32 v[40:41], v[40:41], v[204:205]\n\t"
    "v_pk_add_f32 v[44:45], v[44:45], v[252:253]\n\t"
    "v_pk_fma_f32 v[34:35], v[110:111], v[110:111], v[34:35]\n\t"
    "v_pk_fma_f32 v[38:39], v[158:159], v[158:159], v[38:39]\n\t"
    "v_pk_fma_f32 v[42:43], v[206:207], v[206:207], v[42:43]\n\t"
    "v_pk_fma_f32 v[46:47], v[254:255], v[254:255], v[46:47]\n\t"
    "v_pk_add_f32 v[32:33], v[32:33], v[110:111]\n\t"
    "v_pk_add_f32 v[36:37], v[36:37], v[158:159]\n\t"
    "v_pk_add_f32 v[40:41], v[40:41], v[206:207]\n\t"
    "v_pk_add_f32 v[44:45], v[44:45], v[254:255]\n\t"
    "v_add_f32 v32, v32, v33\n\t"
    "v_add_f32 v34, v34, v35\n\t"
    "v_add_f32 v36, v36, v37\n\t"
    "v_add_f32 v38, v38, v39\n\t"
    "v_add_f32 v40, v40, v41\n\t"
    "v_add_f32 v42, v42, v43\n\t"
    "v_add_f32 v44, v44, v45\n\t"
    "v_add_f32 v46, v46, v47\n\t"
    "v_permlane16_swap_b32 v32, v36\n\t"
    "v_permlane16_swap_b32 v34, v38\n\t"
    "v_permlane16_swap_b32 v40, v44\n\t"
    "v_permlane16_swap_b32 v42, v46\n\t"
    "v_add_f32 v32, v32, v36\n\t"
    "v_add_f32 v34, v34, v38\n\t"
    "v_add_f32 v40, v40, v44\n\t"
    "v_add_f32 v42, v42, v46\n\t"
    "s_nop 0\n\t"
    "v_permlane32_swap_b32 v32, v40\n\t"
    "v_permlane32_swap_b32 v34, v42\n\t"
    "v_add_f32 v32, v32, v40\n\t"
    "v_add_f32 v34, v34, v42\n\t"
    "v_add_f32 v30, v30, v32\n\t"
    "v_add_f32 v31, v31, v34\n\t"
    "s_barrier\n\t"
    "v_and_b32 v1, 15, v0\n\t"
    "v_lshrrev_b32 v2, 4, v0\n\t"
    "v_bfe_u32 v4, v0, 2, 2\n\t"
    "v_and_b32 v5, 3, v0\n\t"
    "v_lshlrev_b32 v5, 3, v5\n\t"
    "v_lshl_add_u32 v5, v4, 7, v5\n\t"
    "v_and_b32 v4, 2, v4\n\t"
    "v_and_or_b32 v4, v2, 1, v4\n\t"
    "v_lshl_add_u32 v5, v4, 5, v5\n\t"
    "v_lshrrev_b32 v3, 1, v2\n\t"
    "v_lshl_add_u32 v14, v3, 10, v5\n\t"
    "v_lshlrev_b32 v4, 5, v3\n\t"
    "v_xor_b32 v5, v5, v4\n\t"
    "v_lshl_add_u32 v5, v3, 9, v5\n\t"
    "v_add_u32 v18, 55296, v5\n\t"
    "v_xor_b32 v19, 64, v18\n\t"
    "v_xor_b32 v15, 32, v14\n\t"
    "v_xor_b32 v16, 64, v14\n\t"
    "v_xor_b32 v17, 96, v14\n\t"
    "v_lshlrev_b32 v22, 4, v3\n\t"
    "v_add_u32 v22, 153600, v22\n\t"
    "v_mul_f32 v30, 0x3baaaaab, v30\n\t"
    "v_mul_f32 v31, 0x3baaaaab, v31\n\t"
    "v_fma_f32 v31, -v30, v30, v31\n\t"
    "v_max_f32 v31, 0, v31\n\t"
    "v_add_f32 v31, 0x3727c5ac, v31\n\t"
    "v_rsq_f32 v31, v31\n\t"
    "s_nop 0\n\t"
    "v_mul_f32 v32, 0x3eefe414, v31\n\t"
    "v_mul_f32_e64 v33, -v30, v32\n\t"
    "v_mov_b32 v36, v32\n\t"
    "v_mov_b32 v37, v33\n\t"
    "s_nop 0\n\t"
    "v_permlane32_swap_b32 v32, v36\n\t"
    "v_permlane32_swap_b32 v33, v37\n\t"
    "v_mov_b32 v34, v32\n\t"
    "v_mov_b32 v35, v33\n\t"
    "v_mov_b32 v38, v36\n\t"
    "v_mov_b32 v39, v37\n\t"
    "v_permlane16_swap_b32 v32, v34\n\t"
    "v_permlane16_swap_b32 v33, v35\n\t"
    "v_permlane16_swap_b32 v36, v38\n\t"
    "v_permlane16_swap_b32 v37, v39\n\t"
    "v_pk_fma_f32 v[210:211], v[210:211], v[38:39], v[38:39] op_sel:[0,0,1] op_sel_hi:[1,0,1]\n\t"
    "v_pk_fma_f32 v[208:209], v[208:209], v[38:39], v[38:39] op_sel:[0,0,1] op_sel_hi:[1,0,1]\n\t"
    "v_cvt_pk_bf16_f32 v211, v210, v211\n\t"
    "v_pk_fma_f32 v[160:161], v[160:161], v[36:37], v[36:37] op_sel:[0,0,1] op_sel_hi:[1,0,1]\n\t"
    "v_cvt_pk_bf16_f32 v210, v208, v209\n\t"
    "v_pk_fma_f32 v[162:163], v[162:163], v[36:37], v[36:37] op_sel:[0,0,1] op_sel_hi:[1,0,1]\n\t"
    "v_cvt_pk_bf16_f32 v208, v160, v161\n\t"
    "v_pk_fma_f32 v[214:215], v[214:215], v[38:39], v[38:39] op_sel:[0,0,1] op_sel_hi:[1,0,1]\n\t"
    "v_cvt_pk_bf16_f32 v209, v162, v163\n\t"
    "v_pk_fma_f32 v[212:213], v[212:213], v[38:39], v[38:39] op_sel:[0,0,1] op_sel_hi:[1,0,1]\n\t"
    "v_cvt_pk_bf16_f32 v215, v214, v215\n\t"
    "v_pk_fma_f32 v[164:165], v[164:165], v[36:37], v[36:37] op_sel:[0,0,1] op_sel_hi:[1,0,1]\n\t"
    "v_cvt_pk_bf16_f32 v214, v212, v213\n\t"
    "v_pk_fma_f32 v[166:167], v[166:167], v[36:37], v[36:37] op_sel:[0,0,1] op_sel_hi:[1,0,1]\n\t"
    "v_cvt_pk_bf16_f32 v212, v164, v165\n\t"
    "v_pk_fma_f32 v[218:219], v[218:219], v[38:39], v[38:39] op_sel:[0,0,1] op_sel_hi:[1,0,1]\n\t"
    "v_cvt_pk_bf16_f32 v213, v166, v167\n\t"
    "v_pk_fma_f32 v[216:217], v[216:217], v[38:39], v[38:39] op_sel:[0,0,1] op_sel_hi:[1,0,1]\n\t"
    "v_cvt_pk_bf16_f32 v219, v218, v219\n\t"
    "v_pk_fma_f32 v[168:169], v[168:169], v[36:37], v[36:37] op_sel:[0,0,1] op_sel_hi:[1,0,1]\n\t"
    "v_cvt_pk_bf16_f32 v218, v216, v217\n\t"
    "v_pk_fma_f32 v[170:171], v[170:171], v[36:37], v[36:37] op_sel:[0,0,1] op_sel_hi:[1,0,1]\n\t"
    "v_cvt_pk_bf16_f32 v216, v168, v169\n\t"
    "v_pk_fma_f32 v[222:223], v[222:223], v[38:39], v[38:39] op_sel:[0,0,1] op_sel_hi:[1,0,1]\n\t"
    "v_cvt_pk_bf16_f32 v217, v170, v171\n\t"
    "v_pk_fma_f32 v[220:221], v[220:221], v[38:39], v[38:39] op_sel:[0,0,1] op_sel_hi:[1,0,1]\n\t"
    "v_cvt_pk_bf16_f32 v223, v222, v223\n\t"
    "v_pk_fma_f32 v[172:173], v[172:173], v[36:37], v[36:37] op_sel:[0,0,1] op_sel_hi:[1,0,1]\n\t"
    "v_cvt_pk_bf16_f32 v222, v220, v221\n\t"
    "v_pk_fma_f32 v[174:175], v[174:175], v[36:37], v[36:37] op_sel:[0,0,1] op_sel_hi:[1,0,1]\n\t"
    "v_cvt_pk_bf16_f32 v220, v172, v173\n\t"
    "v_pk_fma_f32 v[226:227], v[226:227], v[38:39], v[38:39] op_sel:[0,0,1] op_sel_hi:[1,0,1]\n\t"
    "v_cvt_pk_bf16_f32 v221, v174, v175\n\t"
    "v_pk_fma_f32 v[224:225], v[224:225], v[38:39], v[38:39] op_sel:[0,0,1] op_sel_hi:[1,0,1]\n\t"
    "v_cvt_pk_bf16_f32 v227, v226, v227\n\t"
    "v_pk_fma_f32 v[176:177], v[176:177], v[36:37], v[36:37] op_sel:[0,0,1] op_sel_hi:[1,0,1]\n\t"
    "v_cvt_pk_bf16_f32 v226, v224, v225\n\t"
    "v_pk_fma_f32 v[178:179], v[178:179], v[36:37], v[36:37] op_sel:[0,0,1] op_sel_hi:[1,0,1]\n\t"
    "v_cvt_pk_bf16_f32 v224, v176, v177\n\t"
    "v_pk_fma_f32 v[230:231], v[230:231], v[38:39], v[38:39] op_sel:[0,0,1] op_sel_hi:[1,0,1]\n\t"
    "v_cvt_pk_bf16_f32 v225, v178, v179\n\t"
    "v_pk_fma_f32 v[228:229], v[228:229], v[38:39], v[38:39] op_sel:[0,0,1] op_sel_hi:[1,0,1]\n\t"
    "v_cvt_pk_bf16_f32 v231, v230, v231\n\t"
    "v_pk_fma_f32 v[180:181], v[180:181], v[36:37], v[36:37] op_sel:[0,0,1] op_sel_hi:[1,0,1]\n\t"
    "v_cvt_pk_bf16_f32 v230, v228, v229\n\t"
    "v_pk_fma_f32 v[182:183], v[182:183], v[36:37], v[36:37] op_sel:[0,0,1] op_sel_hi:[1,0,1]\n\t"
    "v_cvt_pk_bf16_f32 v228, v180, v181\n\t"
    "v_pk_fma_f32 v[234:235], v[234:235], v[38:39], v[38:39] op_sel:[0,0,1] op_sel_hi:[1,0,1]\n\t"
    "v_cvt_pk_bf16_f32 v229, v182, v183\n\t"
    "v_pk_fma_f32 v[232:233], v[232:233], v[38:39], v[38:39] op_sel:[0,0,1] op_sel_hi:[1,0,1]\n\t"
    "v_cvt_pk_bf16_f32 v235, v234, v235\n\t"
    "v_pk_fma_f32 v[184:185], v[184:185], v[36:37], v[36:37] op_sel:[0,0,1] op_sel_hi:[1,0,1]\n\t"
    "v_cvt_pk_bf16_f32 v234, v232, v233\n\t"
    "v_pk_fma_f32 v[186:187], v[186:187], v[36:37], v[36:37] op_sel:[0,0,1] op_sel_hi:[1,0,1]\n\t"
    "v_cvt_pk_bf16_f32 v232, v184, v185\n\t"
    "v_pk_fma_f32 v[238:239], v[238:239], v[38:39], v[38:39] op_sel:[0,0,1] op_sel_hi:[1,0,1]\n\t"
    "v_cvt_pk_bf16_f32 v233, v186, v187\n\t"
    "v_pk_fma_f32 v[236:237], v[236:237], v[38:39], v[38:39] op_sel:[0,0,1] op_sel_hi:[1,0,1]\n\t"
    "v_cvt_pk_bf16_f32 v239, v238, v239\n\t"
    "v_pk_fma_f32 v[188:189], v[188:189], v[36:37], v[36:37] op_sel:[0,0,1] op_sel_hi:[1,0,1]\n\t"
    "v_cvt_pk_bf16_f32 v238, v236, v237\n\t"
    "v_pk_fma_f32 v[190:191], v[190:191], v[36:37], v[36:37] op_sel:[0,0,1] op_sel_hi:[1,0,1]\n\t"
    "v_cvt_pk_bf16_f32 v236, v188, v189\n\t"
    "v_pk_fma_f32 v[242:243], v[242:243], v[38:39], v[38:39] op_sel:[0,0,1] op_sel_hi:[1,0,1]\n\t"
    "v_cvt_pk_bf16_f32 v237, v190, v191\n\t"
    "v_pk_fma_f32 v[240:241], v[240:241], v[38:39], v[38:39] op_sel:[0,0,1] op_sel_hi:[1,0,1]\n\t"
    "v_cvt_pk_bf16_f32 v243, v242, v243\n\t"
    "v_pk_fma_f32 v[192:193], v[192:193], v[36:37], v[36:37] op_sel:[0,0,1] op_sel_hi:[1,0,1]\n\t"
    "v_cvt_pk_bf16_f32 v242, v240, v241\n\t"
    "v_pk_fma_f32 v[194:195], v[194:195], v[36:37], v[36:37] op_sel:[0,0,1] op_sel_hi:[1,0,1]\n\t"
    "v_cvt_pk_bf16_f32 v240, v192, v193\n\t"
    "v_pk_fma_f32 v[246:247], v[246:247], v[38:39], v[38:39] op_sel:[0,0,1] op_sel_hi:[1,0,1]\n\t"
    "v_cvt_pk_bf16_f32 v241, v194, v195\n\t"
    "v_pk_fma_f32 v[244:245], v[244:245], v[38:39], v[38:39] op_sel:[0,0,1] op_sel_hi:[1,0,1]\n\t"
    "v_cvt_pk_bf16_f32 v247, v246, v247\n\t"
    "v_pk_fma_f32 v[196:197], v[196:197], v[36:37], v[36:37] op_sel:[0,0,1] op_sel_hi:[1,0,1]\n\t"
    "v_cvt_pk_bf16_f32 v246, v244, v245\n\t"
    "v_pk_fma_f32 v[198:199], v[198:199], v[36:37], v[36:37] op_sel:[0,0,1] op_sel_hi:[1,0,1]\n\t"
    "v_cvt_pk_bf16_f32 v244, v196, v197\n\t"
    "v_pk_fma_f32 v[250:251], v[250:251], v[38:39], v[38:39] op_sel:[0,0,1] op_sel_hi:[1,0,1]\n\t"
    "v_cvt_pk_bf16_f32 v245, v198, v199\n\t"
    "v_pk_fma_f32 v[248:249], v[248:249], v[38:39], v[38:39] op_sel:[0,0,1] op_sel_hi:[1,0,1]\n\t"
    "v_cvt_pk_bf16_f32 v251, v250, v251\n\t"
    "v_pk_fma_f32 v[200:201], v[200:201], v[36:37], v[36:37] op_sel:[0,0,1] op_sel_hi:[1,0,1]\n\t"
    "v_cvt_pk_bf16_f32 v250, v248, v249\n\t"
    "v_pk_fma_f32 v[202:203], v[202:203], v[36:37], v[36:37] op_sel:[0,0,1] op_sel_hi:[1,0,1]\n\t"
    "v_cvt_pk_bf16_f32 v248, v200, v201\n\t"
    "v_pk_fma_f32 v[254:255], v[254:255], v[38:39], v[38:39] op_sel:[0,0,1] op_sel_hi:[1,0,1]\n\t"
    "v_cvt_pk_bf16_f32 v249, v202, v203\n\t"
    "v_pk_fma_f32 v[252:253], v[252:253], v[38:39], v[38:39] op_sel:[0,0,1] op_sel_hi:[1,0,1]\n\t"
    "v_cvt_pk_bf16_f32 v255, v254, v255\n\t"
    "v_pk_fma_f32 v[204:205], v[204:205], v[36:37], v[36:37] op_sel:[0,0,1] op_sel_hi:[1,0,1]\n\t"
    "v_cvt_pk_bf16_f32 v254, v252, v253\n\t"
    "v_pk_fma_f32 v[206:207], v[206:207], v[36:37], v[36:37] op_sel:[0,0,1] op_sel_hi:[1,0,1]\n\t"
    "v_cvt_pk_bf16_f32 v252, v204, v205\n\t"
    "v_pk_fma_f32 v[64:65], v[64:65], v[32:33], v[32:33] op_sel:[0,0,1] op_sel_hi:[1,0,1]\n\t"
    "v_cvt_pk_bf16_f32 v253, v206, v207\n\t"
    "v_pk_fma_f32 v[112:113], v[112:113], v[34:35], v[34:35] op_sel:[0,0,1] op_sel_hi:[1,0,1]\n\t"
    "v_cvt_pk_bf16_f32 v160, v64, v65\n\t"
    "v_pk_fma_f32 v[66:67], v[66:67], v[32:33], v[32:33] op_sel:[0,0,1] op_sel_hi:[1,0,1]\n\t"
    "v_cvt_pk_bf16_f32 v162, v112, v113\n\t"
    "v_pk_fma_f32 v[114:115], v[114:115], v[34:35], v[34:35] op_sel:[0,0,1] op_sel_hi:[1,0,1]\n\t"
    "v_cvt_pk_bf16_f32 v161, v66, v67\n\t"
    "v_pk_fma_f32 v[68:69], v[68:69], v[32:33], v[32:33] op_sel:[0,0,1] op_sel_hi:[1,0,1]\n\t"
    "v_cvt_pk_bf16_f32 v163, v114, v115\n\t"
    "v_pk_fma_f32 v[116:117], v[116:117], v[34:35], v[34:35] op_sel:[0,0,1] op_sel_hi:[1,0,1]\n\t"
    "v_cvt_pk_bf16_f32 v164, v68, v69\n\t"
    "v_pk_fma_f32 v[70:71], v[70:71], v[32:33], v[32:33] op_sel:[0,0,1] op_sel_hi:[1,0,1]\n\t"
    "v_cvt_pk_bf16_f32 v166, v116, v117\n\t"
    "v_pk_fma_f32 v[118:119], v[118:119], v[34:35], v[34:35] op_sel:[0,0,1] op_sel_hi:[1,0,1]\n\t"
    "v_cvt_pk_bf16_f32 v165, v70, v71\n\t"
    "v_pk_fma_f32 v[72:73], v[72:73], v[32:33], v[32:33] op_sel:[0,0,1] op_sel_hi:[1,0,1]\n\t"
    "v_cvt_pk_bf16_f32 v167, v118, v119\n\t"
    "v_pk_fma_f32 v[120:121], v[120:121], v[34:35], v[34:35] op_sel:[0,0,1] op_sel_hi:[1,0,1]\n\t"
    "v_cvt_pk_bf16_f32 v168, v72, v73\n\t"
    "v_pk_fma_f32 v[74:75], v[74:75], v[32:33], v[32:33] op_sel:[0,0,1] op_sel_hi:[1,0,1]\n\t"
    "v_cvt_pk_bf16_f32 v170, v120, v121\n\t"
    "v_pk_fma_f32 v[122:123], v[122:123], v[34:35], v[34:35] op_sel:[0,0,1] op_sel_hi:[1,0,1]\n\t"
    "v_cvt_pk_bf16_f32 v169, v74, v75\n\t"
    "v_pk_fma_f32 v[76:77], v[76:77], v[32:33], v[32:33] op_sel:[0,0,1] op_sel_hi:[1,0,1]\n\t"
    "v_cvt_pk_bf16_f32 v171, v122, v123\n\t"
    "v_pk_fma_f32 v[124:125], v[124:125], v[34:35], v[34:35] op_sel:[0,0,1] op_sel_hi:[1,0,1]\n\t"
    "v_cvt_pk_bf16_f32 v172, v76, v77\n\t"
    "v_pk_fma_f32 v[78:79], v[78:79], v[32:33], v[32:33] op_sel:[0,0,1] op_sel_hi:[1,0,1]\n\t"
    "v_cvt_pk_bf16_f32 v174, v124, v125\n\t"
    "v_pk_fma_f32 v[126:127], v[126:127], v[34:35], v[34:35] op_sel:[0,0,1] op_sel_hi:[1,0,1]\n\t"
    "v_cvt_pk_bf16_f32 v173, v78, v79\n\t"
    "v_pk_fma_f32 v[80:81], v[80:81], v[32:33], v[32:33] op_sel:[0,0,1] op_sel_hi:[1,0,1]\n\t"
    "v_cvt_pk_bf16_f32 v175, v126, v127\n\t"
    "v_pk_fma_f32 v[128:129], v[128:129], v[34:35], v[34:35] op_sel:[0,0,1] op_sel_hi:[1,0,1]\n\t"
    "v_cvt_pk_bf16_f32 v176, v80, v81\n\t"
    "v_pk_fma_f32 v[82:83], v[82:83], v[32:33], v[32:33] op_sel:[0,0,1] op_sel_hi:[1,0,1]\n\t"
    "v_cvt_pk_bf16_f32 v178, v128, v129\n\t"
    "v_pk_fma_f32 v[130:131], v[130:131], v[34:35], v[34:35] op_sel:[0,0,1] op_sel_hi:[1,0,1]\n\t"
    "v_cvt_pk_bf16_f32 v177, v82, v83\n\t"
    "v_pk_fma_f32 v[84:85], v[84:85], v[32:33], v[32:33] op_sel:[0,0,1] op_sel_hi:[1,0,1]\n\t"
    "v_cvt_pk_bf16_f32 v179, v130, v131\n\t"
    "v_pk_fma_f32 v[132:133], v[132:133], v[34:35], v[34:35] op_sel:[0,0,1] op_sel_hi:[1,0,1]\n\t"
    "v_cvt_pk_bf16_f32 v180, v84, v85\n\t"
    "v_pk_fma_f32 v[86:87], v[86:87], v[32:33], v[32:33] op_sel:[0,0,1] op_sel_hi:[1,0,1]\n\t"
    "v_cvt_pk_bf16_f32 v182, v132, v133\n\t"
    "v_pk_fma_f32 v[134:135], v[134:135], v[34:35], v[34:35] op_sel:[0,0,1] op_sel_hi:[1,0,1]\n\t"
    "v_cvt_pk_bf16_f32 v181, v86, v87\n\t"
    "ds_read_b64_tr_b16 a[192:193], v14 offset:0\n\t"
    "ds_read_b64_tr_b16 a[194:195], v15 offset:512\n\t"
    "ds_read_b64_tr_b16 a[196:197], v14 offset:2048\n\t"
    "ds_read_b64_tr_b16 a[198:199], v15 offset:2560\n\t"
    "ds_read_b64_tr_b16 a[200:201], v14 offset:4096\n\t"
    "ds_read_b64_tr_b16 a[202:203], v15 offset:4608\n\t"
    "ds_read_b64_tr_b16 a[204:205], v14 offset:6144\n\t"
    "ds_read_b64_tr_b16 a[206:207], v15 offset:6656\n\t"
    "v_pk_fma_f32 v[88:89], v[88:89], v[32:33], v[32:33] op_sel:[0,0,1] op_sel_hi:[1,0,1]\n\t"
    "v_cvt_pk_bf16_f32 v183, v134, v135\n\t"
    "v_pk_fma_f32 v[136:137], v[136:137], v[34:35], v[34:35] op_sel:[0,0,1] op_sel_hi:[1,0,1]\n\t"
    "v_cvt_pk_bf16_f32 v184, v88, v89\n\t"
    "v_pk_fma_f32 v[90:91], v[90:91], v[32:33], v[32:33] op_sel:[0,0,1] op_sel_hi:[1,0,1]\n\t"
    "v_cvt_pk_bf16_f32 v186, v136, v137\n\t"
    "v_pk_fma_f32 v[138:139], v[138:139], v[34:35], v[34:35] op_sel:[0,0,1] op_sel_hi:[1,0,1]\n\t"
    "v_cvt_pk_bf16_f32 v185, v90, v91\n\t"
    "v_pk_fma_f32 v[92:93], v[92:93], v[32:33], v[32:33] op_sel:[0,0,1] op_sel_hi:[1,0,1]\n\t"
    "v_cvt_pk_bf16_f32 v187, v138, v139\n\t"
    "v_pk_fma_f32 v[140:141], v[140:141], v[34:35], v[34:35] op_sel:[0,0,1] op_sel_hi:[1,0,1]\n\t"
    "v_cvt_pk_bf16_f32 v188, v92, v93\n\t"
    "v_pk_fma_f32 v[94:95], v[94:95], v[32:33], v[32:33] op_sel:[0,0,1] op_sel_hi:[1,0,1]\n\t"
    "v_cvt_pk_bf16_f32 v190, v140, v141\n\t"
    "v_pk_fma_f32 v[142:143], v[142:143], v[34:35], v[34:35] op_sel:[0,0,1] op_sel_hi:[1,0,1]\n\t"
    "v_cvt_pk_bf16_f32 v189, v94, v95\n\t"
    "v_pk_fma_f32 v[96:97], v[96:97], v[32:33], v[32:33] op_sel:[0,0,1] op_sel_hi:[1,0,1]\n\t"
    "v_cvt_pk_bf16_f32 v191, v142, v143\n\t"
    "v_pk_fma_f32 v[144:145], v[144:145], v[34:35], v[34:35] op_sel:[0,0,1] op_sel_hi:[1,0,1]\n\t"
    "v_cvt_pk_bf16_f32 v192, v96, v97\n\t"
    "v_pk_fma_f32 v[98:99], v[98:99], v[32:33], v[32:33] op_sel:[0,0,1] op_sel_hi:[1,0,1]\n\t"
    "v_cvt_pk_bf16_f32 v194, v144, v145\n\t"
    "v_pk_fma_f32 v[146:147], v[146:147], v[34:35], v[34:35] op_sel:[0,0,1] op_sel_hi:[1,0,1]\n\t"
    "v_cvt_pk_bf16_f32 v193, v98, v99\n\t"
    "ds_read_b64_tr_b16 a[208:209], v14 offset:8192\n\t"
    "ds_read_b64_tr_b16 a[210:211], v15 offset:8704\n\t"
    "ds_read_b64_tr_b16 a[212:213], v14 offset:10240\n\t"
    "ds_read_b64_tr_b16 a[214:215], v15 offset:10752\n\t"
    "ds_read_b128 v[48:51], v22 offset:0\n\t"
    "ds_read_b128 v[52:55], v22 offset:32\n\t"
    "ds_read_b128 v[56:59], v22 offset:64\n\t"
    "s_waitcnt lgkmcnt(11)\n\t"
    "ds_read_b128 v[60:63], v22 offset:96\n\t"
    "v_pk_fma_f32 v[100:101], v[100:101], v[32:33], v[32:33] op_sel:[0,0,1] op_sel_hi:[1,0,1]\n\t"
    "v_cvt_pk_bf16_f32 v195, v146, v147\n\t"
    "v_pk_fma_f32 v[148:149], v[148:149], v[34:35], v[34:35] op_sel:[0,0,1] op_sel_hi:[1,0,1]\n\t"
    "v_cvt_pk_bf16_f32 v196, v100, v101\n\t"
    "v_pk_fma_f32 v[102:103], v[102:103], v[32:33], v[32:33] op_sel:[0,0,1] op_sel_hi:[1,0,1]\n\t"
    "v_cvt_pk_bf16_f32 v198, v148, v149\n\t"
    "v_pk_fma_f32 v[150:151], v[150:151], v[34:35], v[34:35] op_sel:[0,0,1] op_sel_hi:[1,0,1]\n\t"
    "v_cvt_pk_bf16_f32 v197, v102, v103\n\t"
    "v_pk_fma_f32 v[104:105], v[104:105], v[32:33], v[32:33] op_sel:[0,0,1] op_sel_hi:[1,0,1]\n\t"
    "v_cvt_pk_bf16_f32 v199, v150, v151\n\t"
    "v_pk_fma_f32 v[152:153], v[152:153], v[34:35], v[34:35] op_sel:[0,0,1] op_sel_hi:[1,0,1]\n\t"
    "v_cvt_pk_bf16_f32 v200, v104, v105\n\t"
    "v_pk_fma_f32 v[106:107], v[106:107], v[32:33], v[32:33] op_sel:[0,0,1] op_sel_hi:[1,0,1]\n\t"
    "v_cvt_pk_bf16_f32 v202, v152, v153\n\t"
    "v_pk_fma_f32 v[154:155], v[154:155], v[34:35], v[34:35] op_sel:[0,0,1] op_sel_hi:[1,0,1]\n\t"
    "v_cvt_pk_bf16_f32 v201, v106, v107\n\t"
    "v_pk_fma_f32 v[108:109], v[108:109], v[32:33], v[32:33] op_sel:[0,0,1] op_sel_hi:[1,0,1]\n\t"
    "v_cvt_pk_bf16_f32 v203, v154, v155\n\t"
    "v_pk_fma_f32 v[156:157], v[156:157], v[34:35], v[34:35] op_sel:[0,0,1] op_sel_hi:[1,0,1]\n\t"
    "v_cvt_pk_bf16_f32 v204, v108, v109\n\t"
    "v_pk_fma_f32 v[110:111], v[110:111], v[32:33], v[32:33] op_sel:[0,0,1] op_sel_hi:[1,0,1]\n\t"
    "v_cvt_pk_bf16_f32 v206, v156, v157\n\t"
    "v_pk_fma_f32 v[158:159], v[158:159], v[34:35], v[34:35] op_sel:[0,0,1] op_sel_hi:[1,0,1]\n\t"
    "v_cvt_pk_bf16_f32 v205, v110, v111\n\t"
    "v_cvt_pk_bf16_f32 v207, v158, v159\n\t"
    "v_permlane16_swap_b32 v208, v210\n\t"
    "v_permlane16_swap_b32 v209, v211\n\t"
    "v_permlane16_swap_b32 v212, v214\n\t"
    "v_permlane16_swap_b32 v213, v215\n\t"
    "v_permlane16_swap_b32 v216, v218\n\t"
    "v_permlane16_swap_b32 v217, v219\n\t"
    "v_permlane16_swap_b32 v220, v222\n\t"
    "v_permlane16_swap_b32 v221, v223\n\t"
    "v_permlane16_swap_b32 v224, v226\n\t"
    "v_permlane16_swap_b32 v225, v227\n\t"
    "v_permlane16_swap_b32 v228, v230\n\t"
    "v_permlane16_swap_b32 v229, v231\n\t"
    "v_permlane16_swap_b32 v232, v234\n\t"
    "v_permlane16_swap_b32 v233, v235\n\t"
    "v_permlane16_swap_b32 v236, v238\n\t"
    "v_permlane16_swap_b32 v237, v239\n\t"
    "v_permlane16_swap_b32 v240, v242\n\t"
    "v_permlane16_swap_b32 v241, v243\n\t"
    "v_permlane16_swap_b32 v244, v246\n\t"
    "v_permlane16_swap_b32 v245, v247\n\t"
    "v_permlane16_swap_b32 v248, v250\n\t"
    "v_permlane16_swap_b32 v249, v251\n\t"
    "v_permlane16_swap_b32 v252, v254\n\t"
    "v_permlane16_swap_b32 v253, v255\n\t"
    "v_permlane16_swap_b32 v160, v162\n\t"
    "v_permlane16_swap_b32 v161, v163\n\t"
    "v_permlane16_swap_b32 v164, v166\n\t"
    "v_permlane16_swap_b32 v165, v167\n\t"
    "v_permlane16_swap_b32 v168, v170\n\t"
    "v_permlane16_swap_b32 v169, v171\n\t"
    "v_permlane16_swap_b32 v172, v174\n\t"
    "v_permlane16_swap_b32 v173, v175\n\t"
    "v_permlane16_swap_b32 v176, v178\n\t"
    "v_permlane16_swap_b32 v177, v179\n\t"
    "v_permlane16_swap_b32 v180, v182\n\t"
    "v_permlane16_swap_b32 v181, v183\n\t"
    "v_permlane16_swap_b32 v184, v186\n\t"
    "v_permlane16_swap_b32 v185, v187\n\t"
    "v_permlane16_swap_b32 v188, v190\n\t"
    "v_permlane16_swap_b32 v189, v191\n\t"
    "v_permlane16_swap_b32 v192, v194\n\t"
    "v_permlane16_swap_b32 v193, v195\n\t"
    "v_permlane16_swap_b32 v196, v198\n\t"
    "v_permlane16_swap_b32 v197, v199\n\t"
    "v_permlane16_swap_b32 v200, v202\n\t"
    "v_permlane16_swap_b32 v201, v203\n\t"
    "v_permlane16_swap_b32 v204, v206\n\t"
    "v_permlane16_swap_b32 v205, v207\n\t"
    "v_add_u32 v23, 3072, v22\n\t"
    "s_waitcnt lgkmcnt(0)\n\t"
    "v_mfma_f32_32x32x16_bf16 v[64:79], a[192:195], v[160:163], v[48:63]\n\t"
    "ds_read_b64_tr_b16 a[216:217], v14 offset:12288\n\t"
    "ds_read_b128 a[0:3], v23 offset:0\n\t"
    "v_mfma_f32_32x32x16_bf16 v[80:95], a[192:195], v[208:211], v[48:63]\n\t"
    "ds_read_b64_tr_b16 a[218:219], v15 offset:12800\n\t"
    "ds_read_b128 a[4:7], v23 offset:32\n\t"
    "v_mfma_f32_32x32x16_bf16 v[64:79], a[196:199], v[164:167], v[64:79]\n\t"
    "ds_read_b64_tr_b16 a[220:221], v14 offset:14336\n\t"
    "ds_read_b128 a[8:11], v23 offset:64\n\t"
    "v_mfma_f32_32x32x16_bf16 v[80:95], a[196:199], v[212:215], v[80:95]\n\t"
    "ds_read_b64_tr_b16 a[222:223], v15 offset:14848\n\t"
    "ds_read_b128 a[12:15], v23 offset:96\n\t"
    "v_mfma_f32_32x32x16_bf16 v[64:79], a[200:203], v[168:171], v[64:79]\n\t"
    "ds_read_b64_tr_b16 a[224:225], v14 offset:16384\n\t"
    "ds_read_b128 a[16:19], v23 offset:0\n\t"
    "v_mfma_f32_32x32x16_bf16 v[80:95], a[200:203], v[216:219], v[80:95]\n\t"
    "ds_read_b64_tr_b16 a[226:227], v15 offset:16896\n\t"
    "ds_read_b128 a[20:23], v23 offset:32\n\t"
    "s_waitcnt lgkmcnt(9)\n\t"
    "v_mfma_f32_32x32x16_bf16 v[64:79], a[204:207], v[172:175], v[64:79]\n\t"
    "ds_read_b64_tr_b16 a[228:229], v14 offset:18432\n\t"
    "ds_read_b128 a[24:27], v23 offset:64\n\t"
    "v_mfma_f32_32x32x16_bf16 v[80:95], a[204:207], v[220:223], v[80:95]\n\t"
    "ds_read_b64_tr_b16 a[230:231], v15 offset:18944\n\t"
    "ds_read_b128 v[48:51], v22 offset:128\n\t"
    "ds_read_b128 a[28:31], v23 offset:96\n\t"
    "s_waitcnt lgkmcnt(10)\n\t"
    "v_mfma_f32_32x32x16_bf16 v[64:79], a[208:211], v[176:179], v[64:79]\n\t"
    "ds_read_b64_tr_b16 a[232:233], v14 offset:20480\n\t"
    "ds_read_b128 a[32:35], v23 offset:128\n\t"
    "v_mfma_f32_32x32x16_bf16 v[80:95], a[208:211], v[224:227], v[80:95]\n\t"
    "ds_read_b64_tr_b16 a[234:235], v15 offset:20992\n\t"
    "ds_read_b128 a[36:39], v23 offset:160\n\t"
    "s_waitcnt lgkmcnt(10)\n\t"
    "v_mfma_f32_32x32x16_bf16 v[64:79], a[212:215], v[180:183], v[64:79]\n\t"
    "ds_read_b64_tr_b16 a[236:237], v14 offset:22528\n\t"
    "ds_read_b128 a[40:43], v23 offset:192\n\t"
    "v_mfma_f32_32x32x16_bf16 v[80:95], a[212:215], v[228:231], v[80:95]\n\t"
    "ds_read_b64_tr_b16 a[238:239], v15 offset:23040\n\t"
    "ds_read_b128 v[52:55], v22 offset:160\n\t"
    "ds_read_b128 a[44:47], v23 offset:224\n\t"
    "s_waitcnt lgkmcnt(11)\n\t"
    "v_mfma_f32_32x32x16_bf16 v[64:79], a[216:219], v[184:187], v[64:79]\n\t"
    "ds_read_b64_tr_b16 a[192:193], v16 offset:0\n\t"
    "ds_read_b128 a[48:51], v23 offset:128\n\t"
    "v_mfma_f32_32x32x16_bf16 v[80:95], a[216:219], v[232:235], v[80:95]\n\t"
    "ds_read_b64_tr_b16 a[194:195], v17 offset:512\n\t"
    "ds_read_b128 a[52:55], v23 offset:160\n\t"
    "s_waitcnt lgkmcnt(10)\n\t"
    "v_mfma_f32_32x32x16_bf16 v[64:79], a[220:223], v[188:191], v[64:79]\n\t"
    "ds_read_b64_tr_b16 a[196:197], v16 offset:2048\n\t"
    "ds_read_b128 a[56:59], v23 offset:192\n\t"
    "v_mfma_f32_32x32x16_bf16 v[80:95], a[220:223], v[236:239], v[80:95]\n\t"
    "ds_read_b64_tr_b16 a[198:199], v17 offset:2560\n\t"
    "ds_read_b128 v[56:59], v22 offset:192\n\t"
    "ds_read_b128 a[60:63], v23 offset:224\n\t"
    "s_waitcnt lgkmcnt(11)\n\t"
    "v_mfma_f32_32x32x16_bf16 v[64:79], a[224:227], v[192:195], v[64:79]\n\t"
    "ds_read_b64_tr_b16 a[200:201], v16 offset:4096\n\t"
    "ds_read_b128 a[64:67], v23 offset:256\n\t"
    "v_mfma_f32_32x32x16_bf16 v[80:95], a[224:227], v[240:243], v[80:95]\n\t"
    "ds_read_b64_tr_b16 a[202:203], v17 offset:4608\n\t"
    "ds_read_b128 a[68:71], v23 offset:288\n\t"
    "s_waitcnt lgkmcnt(10)\n\t"
    "v_mfma_f32_32x32x16_bf16 v[64:79], a[228:231], v[196:199], v[64:79]\n\t"
    "ds_read_b64_tr_b16 a[204:205], v16 offset:6144\n\t"
    "ds_read_b128 a[72:75], v23 offset:320\n\t"
    "v_mfma_f32_32x32x16_bf16 v[80:95], a[228:231], v[244:247], v[80:95]\n\t"
    "ds_read_b64_tr_b16 a[206:207], v17 offset:6656\n\t"
    "ds_read_b128 v[60:63], v22 offset:224\n\t"
    "ds_read_b128 a[76:79], v23 offset:352\n\t"
    "s_waitcnt lgkmcnt(11)\n\t"
    "v_mfma_f32_32x32x16_bf16 v[64:79], a[232:235], v[200:203], v[64:79]\n\t"
    "ds_read_b64_tr_b16 a[208:209], v16 offset:8192\n\t"
    "ds_read_b128 a[80:83], v23 offset:256\n\t"
    "v_mfma_f32_32x32x16_bf16 v[80:95], a[232:235], v[248:251], v[80:95]\n\t"
    "ds_read_b64_tr_b16 a[210:211], v17 offset:8704\n\t"
    "ds_read_b128 a[84:87], v23 offset:288\n\t"
    "s_waitcnt lgkmcnt(10)\n\t"
    "v_mfma_f32_32x32x16_bf16 v[64:79], a[236:239], v[204:207], v[64:79]\n\t"
    "ds_read_b64_tr_b16 a[212:213], v16 offset:10240\n\t"
    "ds_read_b128 a[88:91], v23 offset:320\n\t"
    "v_mfma_f32_32x32x16_bf16 v[80:95], a[236:239], v[252:255], v[80:95]\n\t"
    "ds_read_b64_tr_b16 a[214:215], v17 offset:10752\n\t"
    "ds_read_b128 a[92:95], v23 offset:352\n\t"
    "s_waitcnt lgkmcnt(9)\n\t"
    "v_mfma_f32_32x32x16_bf16 v[96:111], a[192:195], v[160:163], v[48:63]\n\t"
    "ds_read_b64_tr_b16 a[216:217], v16 offset:12288\n\t"
    "ds_read_b128 a[96:99], v23 offset:384\n\t"
    "v_mfma_f32_32x32x16_bf16 v[112:127], a[192:195], v[208:211], v[48:63]\n\t"
    "ds_read_b64_tr_b16 a[218:219], v17 offset:12800\n\t"
    "ds_read_b128 a[100:103], v23 offset:416\n\t"
    "s_waitcnt lgkmcnt(9)\n\t"
    "v_mfma_f32_32x32x16_bf16 v[96:111], a[196:199], v[164:167], v[96:111]\n\t"
    "ds_read_b64_tr_b16 a[220:221], v16 offset:14336\n\t"
    "ds_read_b128 a[104:107], v23 offset:448\n\t"
    "v_cvt_pk_f16_f32 v2, v64, v65\n\t"
    "v_cvt_pk_f16_f32 v3, v66, v67\n\t"
    "v_pk_fma_f16 v4, v2, v2, s60 neg_lo:[1,0,1] neg_hi:[1,0,1]\n\t"
    "v_pk_fma_f16 v5, v3, v3, s60 neg_lo:[1,0,1] neg_hi:[1,0,1]\n\t"
    "v_pk_mul_f16 v4, v4, v2\n\t"
    "v_pk_mul_f16 v5, v5, v3\n\t"
    "v_exp_f16 v6, v4\n\t"
    "v_exp_f16 v7, v5\n\t"
    "v_mfma_f32_32x32x16_bf16 v[112:127], a[196:199], v[212:215], v[112:127]\n\t"
    "ds_read_b64_tr_b16 a[222:223], v17 offset:14848\n\t"
    "ds_read_b128 a[108:111], v23 offset:480\n\t"
    "v_exp_f16_sdwa v6, v4 dst_sel:WORD_1 dst_unused:UNUSED_PRESERVE src0_sel:WORD_1\n\t"
    "v_exp_f16_sdwa v7, v5 dst_sel:WORD_1 dst_unused:UNUSED_PRESERVE src0_sel:WORD_1\n\t"
    "v_pk_add_f16 v6, v6, 1.0 op_sel_hi:[1,0]\n\t"
    "v_pk_add_f16 v7, v7, 1.0 op_sel_hi:[1,0]\n\t"
    "v_rcp_f16 v4, v6\n\t"
    "v_rcp_f16 v5, v7\n\t"
    "s_waitcnt lgkmcnt(9)\n\t"
    "v_mfma_f32_32x32x16_bf16 v[96:111], a[200:203], v[168:171], v[96:111]\n\t"
    "ds_read_b64_tr_b16 a[224:225], v16 offset:16384\n\t"
    "ds_read_b128 a[112:115], v23 offset:384\n\t"
    "v_rcp_f16_sdwa v4, v6 dst_sel:WORD_1 dst_unused:UNUSED_PRESERVE src0_sel:WORD_1\n\t"
    "v_rcp_f16_sdwa v5, v7 dst_sel:WORD_1 dst_unused:UNUSED_PRESERVE src0_sel:WORD_1\n\t"
    "v_fma_mix_f32 v64, v64, v4, 0 op_sel_hi:[0,1,0]\n\t"
    "v_fma_mix_f32 v66, v66, v5, 0 op_sel_hi:[0,1,0]\n\t"
    "v_fma_mix_f32 v65, v65, v4, 0 op_sel:[0,1,0] op_sel_hi:[0,1,0]\n\t"
    "v_fma_mix_f32 v67, v67, v5, 0 op_sel:[0,1,0] op_sel_hi:[0,1,0]\n\t"
    "v_cvt_pk_bf16_f32 v128, v64, v65\n\t"
    "v_mfma_f32_32x32x16_bf16 v[112:127], a[200:203], v[216:219], v[112:127]\n\t"
    "ds_read_b64_tr_b16 a[226:227], v17 offset:16896\n\t"
    "ds_read_b128 a[116:119], v23 offset:416\n\t"
    "v_cvt_pk_bf16_f32 v129, v66, v67\n\t"
    "v_cvt_pk_f16_f32 v8, v68, v69\n\t"
    "v_cvt_pk_f16_f32 v9, v70, v71\n\t"
    "v_pk_fma_f16 v10, v8, v8, s60 neg_lo:[1,0,1] neg_hi:[1,0,1]\n\t"
    "v_pk_fma_f16 v11, v9, v9, s60 neg_lo:[1,0,1] neg_hi:[1,0,1]\n\t"
    "v_pk_mul_f16 v10, v10, v8\n\t"
    "v_pk_mul_f16 v11, v11, v9\n\t"
    "v_exp_f16 v12, v10\n\t"
    "s_barrier\n\t"
    "s_waitcnt lgkmcnt(9)\n\t"
    "v_mfma_f32_32x32x16_bf16 v[96:111], a[204:207], v[172:175], v[96:111]\n\t"
    "ds_read_b64_tr_b16 a[228:229], v16 offset:18432\n\t"
    "ds_read_b128 a[120:123], v23 offset:448\n\t"
    "v_exp_f16 v13, v11\n\t"
    "v_exp_f16_sdwa v12, v10 dst_sel:WORD_1 dst_unused:UNUSED_PRESERVE src0_sel:WORD_1\n\t"
    "v_exp_f16_sdwa v13, v11 dst_sel:WORD_1 dst_unused:UNUSED_PRESERVE src0_sel:WORD_1\n\t"
    "v_pk_add_f16 v12, v12, 1.0 op_sel_hi:[1,0]\n\t"
    "v_pk_add_f16 v13, v13, 1.0 op_sel_hi:[1,0]\n\t"
    "v_rcp_f16 v10, v12\n\t"
    "v_mfma_f32_32x32x16_bf16 v[112:127], a[204:207], v[220:223], v[112:127]\n\t"
    "ds_read_b64_tr_b16 a[230:231], v17 offset:18944\n\t"
    "ds_read_b128 a[124:127], v23 offset:480\n\t"
    "v_rcp_f16 v11, v13\n\t"
    "v_rcp_f16_sdwa v10, v12 dst_sel:WORD_1 dst_unused:UNUSED_PRESERVE src0_sel:WORD_1\n\t"
    "v_rcp_f16_sdwa v11, v13 dst_sel:WORD_1 dst_unused:UNUSED_PRESERVE src0_sel:WORD_1\n\t"
    "v_fma_mix_f32 v68, v68, v10, 0 op_sel_hi:[0,1,0]\n\t"
    "v_fma_mix_f32 v70, v70, v11, 0 op_sel_hi:[0,1,0]\n\t"
    "s_waitcnt lgkmcnt(9)\n\t"
    "v_mfma_f32_32x32x16_bf16 v[96:111], a[208:211], v[176:179], v[96:111]\n\t"
    "ds_read_b64_tr_b16 a[232:233], v16 offset:20480\n\t"
    "ds_read_b128 a[128:131], v23 offset:512\n\t"
    "v_fma_mix_f32 v69, v69, v10, 0 op_sel:[0,1,0] op_sel_hi:[0,1,0]\n\t"
    "v_fma_mix_f32 v71, v71, v11, 0 op_sel:[0,1,0] op_sel_hi:[0,1,0]\n\t"
    "v_cvt_pk_bf16_f32 v130, v68, v69\n\t"
    "v_cvt_pk_bf16_f32 v131, v70, v71\n\t"
    "v_cvt_pk_f16_f32 v2, v80, v81\n\t"
    "v_cvt_pk_f16_f32 v3, v82, v83\n\t"
    "v_pk_fma_f16 v4, v2, v2, s60 neg_lo:[1,0,1] neg_hi:[1,0,1]\n\t"
    "v_pk_fma_f16 v5, v3, v3, s60 neg_lo:[1,0,1] neg_hi:[1,0,1]\n\t"
    "v_pk_mul_f16 v4, v4, v2\n\t"
    "v_pk_mul_f16 v5, v5, v3\n\t"
    "v_mfma_f32_32x32x16_bf16 v[112:127], a[208:211], v[224:227], v[112:127]\n\t"
    "ds_read_b64_tr_b16 a[234:235], v17 offset:20992\n\t"
    "ds_read_b128 a[132:135], v23 offset:544\n\t"
    "v_exp_f16 v6, v4\n\t"
    "v_exp_f16 v7, v5\n\t"
    "v_exp_f16_sdwa v6, v4 dst_sel:WORD_1 dst_unused:UNUSED_PRESERVE src0_sel:WORD_1\n\t"
    "v_exp_f16_sdwa v7, v5 dst_sel:WORD_1 dst_unused:UNUSED_PRESERVE src0_sel:WORD_1\n\t"
    "v_pk_add_f16 v6, v6, 1.0 op_sel_hi:[1,0]\n\t"
    "s_waitcnt lgkmcnt(9)\n\t"
    "v_mfma_f32_32x32x16_bf16 v[96:111], a[212:215], v[180:183], v[96:111]\n\t"
    "ds_read_b64_tr_b16 a[236:237], v16 offset:22528\n\t"
    "ds_read_b128 a[136:139], v23 offset:576\n\t"
    "v_pk_add_f16 v7, v7, 1.0 op_sel_hi:[1,0]\n\t"
    "v_rcp_f16 v4, v6\n\t"
    "v_rcp_f16 v5, v7\n\t"
    "v_rcp_f16_sdwa v4, v6 dst_sel:WORD_1 dst_unused:UNUSED_PRESERVE src0_sel:WORD_1\n\t"
    "v_rcp_f16_sdwa v5, v7 dst_sel:WORD_1 dst_unused:UNUSED_PRESERVE src0_sel:WORD_1\n\t"
    "v_fma_mix_f32 v80, v80, v4, 0 op_sel_hi:[0,1,0]\n\t"
    "v_mfma_f32_32x32x16_bf16 v[112:127], a[212:215], v[228:231], v[112:127]\n\t"
    "ds_read_b64_tr_b16 a[238:239], v17 offset:23040\n\t"
    "ds_read_b128 a[140:143], v23 offset:608\n\t"
    "v_fma_mix_f32 v82, v82, v5, 0 op_sel_hi:[0,1,0]\n\t"
    "v_fma_mix_f32 v81, v81, v4, 0 op_sel:[0,1,0] op_sel_hi:[0,1,0]\n\t"
    "v_fma_mix_f32 v83, v83, v5, 0 op_sel:[0,1,0] op_sel_hi:[0,1,0]\n\t"
    "v_cvt_pk_bf16_f32 v136, v80, v81\n\t"
    "v_cvt_pk_bf16_f32 v137, v82, v83\n\t"
    "v_cvt_pk_f16_f32 v8, v84, v85\n\t"
    "v_cvt_pk_f16_f32 v9, v86, v87\n\t"
    "v_pk_fma_f16 v10, v8, v8, s60 neg_lo:[1,0,1] neg_hi:[1,0,1]\n\t"
    "v_pk_fma_f16 v11, v9, v9, s60 neg_lo:[1,0,1] neg_hi:[1,0,1]\n\t"
    "s_waitcnt lgkmcnt(9)\n\t"
    "v_mfma_f32_32x32x16_bf16 v[96:111], a[216:219], v[184:187], v[96:111]\n\t"
    "ds_read_b64_tr_b16 a[192:193], v18 offset:0\n\t"
    "ds_read_b128 a[144:147], v23 offset:512\n\t"
    "v_pk_mul_f16 v10, v10, v8\n\t"
    "v_pk_mul_f16 v11, v11, v9\n\t"
    "v_exp_f16 v12, v10\n\t"
    "v_exp_f16 v13, v11\n\t"
    "v_exp_f16_sdwa v12, v10 dst_sel:WORD_1 dst_unused:UNUSED_PRESERVE src0_sel:WORD_1\n\t"
    "v_exp_f16_sdwa v13, v11 dst_sel:WORD_1 dst_unused:UNUSED_PRESERVE src0_sel:WORD_1\n\t"
    "v_pk_add_f16 v12, v12, 1.0 op_sel_hi:[1,0]\n\t"
    "v_mfma_f32_32x32x16_bf16 v[112:127], a[216:219], v[232:235], v[112:127]\n\t"
    "ds_read_b64_tr_b16 a[194:195], v18 offset:3072\n\t"
    "ds_read_b128 a[148:151], v23 offset:544\n\t"
    "v_pk_add_f16 v13, v13, 1.0 op_sel_hi:[1,0]\n\t"
    "v_rcp_f16 v10, v12\n\t"
    "v_rcp_f16 v11, v13\n\t"
    "v_rcp_f16_sdwa v10, v12 dst_sel:WORD_1 dst_unused:UNUSED_PRESERVE src0_sel:WORD_1\n\t"
    "v_rcp_f16_sdwa v11, v13 dst_sel:WORD_1 dst_unused:UNUSED_PRESERVE src0_sel:WORD_1\n\t"
    "s_waitcnt lgkmcnt(9)\n\t"
    "v_mfma_f32_32x32x16_bf16 v[96:111], a[220:223], v[188:191], v[96:111]\n\t"
    "ds_read_b64_tr_b16 a[196:197], v19 offset:0\n\t"
    "ds_read_b128 a[152:155], v23 offset:576\n\t"
    "v_fma_mix_f32 v84, v84, v10, 0 op_sel_hi:[0,1,0]\n\t"
    "v_fma_mix_f32 v86, v86, v11, 0 op_sel_hi:[0,1,0]\n\t"
    "v_fma_mix_f32 v85, v85, v10, 0 op_sel:[0,1,0] op_sel_hi:[0,1,0]\n\t"
    "v_fma_mix_f32 v87, v87, v11, 0 op_sel:[0,1,0] op_sel_hi:[0,1,0]\n\t"
    "v_cvt_pk_bf16_f32 v138, v84, v85\n\t"
    "v_cvt_pk_bf16_f32 v139, v86, v87\n\t"
    "v_cvt_pk_f16_f32 v2, v72, v73\n\t"
    "v_cvt_pk_f16_f32 v3, v74, v75\n\t"
    "v_pk_fma_f16 v4, v2, v2, s60 neg_lo:[1,0,1] neg_hi:[1,0,1]\n\t"
    "v_pk_fma_f16 v5, v3, v3, s60 neg_lo:[1,0,1] neg_hi:[1,0,1]\n\t"
    "v_mfma_f32_32x32x16_bf16 v[112:127], a[220:223], v[236:239], v[112:127]\n\t"
    "ds_read_b64_tr_b16 a[198:199], v19 offset:3072\n\t"
    "ds_read_b128 a[156:159], v23 offset:608\n\t"
    "v_pk_mul_f16 v4, v4, v2\n\t"
    "v_pk_mul_f16 v5, v5, v3\n\t"
    "v_exp_f16 v6, v4\n\t"
    "v_exp_f16 v7, v5\n\t"
    "v_exp_f16_sdwa v6, v4 dst_sel:WORD_1 dst_unused:UNUSED_PRESERVE src0_sel:WORD_1\n\t"
    "s_waitcnt lgkmcnt(9)\n\t"
    "v_mfma_f32_32x32x16_bf16 v[96:111], a[224:227], v[192:195], v[96:111]\n\t"
    "ds_read_b64_tr_b16 a[200:201], v18 offset:1024\n\t"
    "ds_read_b128 a[160:163], v23 offset:640\n\t"
    "v_exp_f16_sdwa v7, v5 dst_sel:WORD_1 dst_unused:UNUSED_PRESERVE src0_sel:WORD_1\n\t"
    "v_pk_add_f16 v6, v6, 1.0 op_sel_hi:[1,0]\n\t"
    "v_pk_add_f16 v7, v7, 1.0 op_sel_hi:[1,0]\n\t"
    "v_rcp_f16 v4, v6\n\t"
    "v_rcp_f16 v5, v7\n\t"
    "v_rcp_f16_sdwa v4, v6 dst_sel:WORD_1 dst_unused:UNUSED_PRESERVE src0_sel:WORD_1\n\t"
    "v_mfma_f32_32x32x16_bf16 v[112:127], a[224:227], v[240:243], v[112:127]\n\t"
    "ds_read_b64_tr_b16 a[202:203], v18 offset:4096\n\t"
    "ds_read_b128 a[164:167], v23 offset:672\n\t"
    "v_rcp_f16_sdwa v5, v7 dst_sel:WORD_1 dst_unused:UNUSED_PRESERVE src0_sel:WORD_1\n\t"
    "v_fma_mix_f32 v72, v72, v4, 0 op_sel_hi:[0,1,0]\n\t"
    "v_fma_mix_f32 v74, v74, v5, 0 op_sel_hi:[0,1,0]\n\t"
    "v_fma_mix_f32 v73, v73, v4, 0 op_sel:[0,1,0] op_sel_hi:[0,1,0]\n\t"
    "v_fma_mix_f32 v75, v75, v5, 0 op_sel:[0,1,0] op_sel_hi:[0,1,0]\n\t"
    "v_cvt_pk_bf16_f32 v132, v72, v73\n\t"
    "v_cvt_pk_bf16_f32 v133, v74, v75\n\t"
    "v_cvt_pk_f16_f32 v8, v76, v77\n\t"
    "v_cvt_pk_f16_f32 v9, v78, v79\n\t"
    "s_waitcnt lgkmcnt(9)\n\t"
    "v_mfma_f32_32x32x16_bf16 v[96:111], a[228:231], v[196:199], v[96:111]\n\t"
    "ds_read_b64_tr_b16 a[204:205], v19 offset:1024\n\t"
    "ds_read_b128 a[168:171], v23 offset:704\n\t"
    "v_pk_fma_f16 v10, v8, v8, s60 neg_lo:[1,0,1] neg_hi:[1,0,1]\n\t"
    "v_pk_fma_f16 v11, v9, v9, s60 neg_lo:[1,0,1] neg_hi:[1,0,1]\n\t"
    "v_pk_mul_f16 v10, v10, v8\n\t"
    "v_pk_mul_f16 v11, v11, v9\n\t"
    "v_exp_f16 v12, v10\n\t"
    "v_exp_f16 v13, v11\n\t"
    "v_exp_f16_sdwa v12, v10 dst_sel:WORD_1 dst_unused:UNUSED_PRESERVE src0_sel:WORD_1\n\t"
    "v_mfma_f32_32x32x16_bf16 v[112:127], a[228:231], v[244:247], v[112:127]\n\t"
    "ds_read_b64_tr_b16 a[206:207], v19 offset:4096\n\t"
    "ds_read_b128 a[172:175], v23 offset:736\n\t"
    "v_exp_f16_sdwa v13, v11 dst_sel:WORD_1 dst_unused:UNUSED_PRESERVE src0_sel:WORD_1\n\t"
    "v_pk_add_f16 v12, v12, 1.0 op_sel_hi:[1,0]\n\t"
    "v_pk_add_f16 v13, v13, 1.0 op_sel_hi:[1,0]\n\t"
    "v_rcp_f16 v10, v12\n\t"
    "v_rcp_f16 v11, v13\n\t"
    "s_waitcnt lgkmcnt(9)\n\t"
    "v_mfma_f32_32x32x16_bf16 v[96:111], a[232:235], v[200:203], v[96:111]\n\t"
    "ds_read_b64_tr_b16 a[208:209], v18 offset:2048\n\t"
    "ds_read_b128 a[176:179], v23 offset:640\n\t"
    "v_rcp_f16_sdwa v10, v12 dst_sel:WORD_1 dst_unused:UNUSED_PRESERVE src0_sel:WORD_1\n\t"
    "v_rcp_f16_sdwa v11, v13 dst_sel:WORD_1 dst_unused:UNUSED_PRESERVE src0_sel:WORD_1\n\t"
    "v_fma_mix_f32 v76, v76, v10, 0 op_sel_hi:[0,1,0]\n\t"
    "v_fma_mix_f32 v78, v78, v11, 0 op_sel_hi:[0,1,0]\n\t"
    "v_fma_mix_f32 v77, v77, v10, 0 op_sel:[0,1,0] op_sel_hi:[0,1,0]\n\t"
    "v_fma_mix_f32 v79, v79, v11, 0 op_sel:[0,1,0] op_sel_hi:[0,1,0]\n\t"
    "v_cvt_pk_bf16_f32 v134, v76, v77\n\t"
    "v_cvt_pk_bf16_f32 v135, v78, v79\n\t"
    "v_cvt_pk_f16_f32 v2, v88, v89\n\t"
    "v_mfma_f32_32x32x16_bf16 v[112:127], a[232:235], v[248:251], v[112:127]\n\t"
    "ds_read_b64_tr_b16 a[210:211], v18 offset:5120\n\t"
    "ds_read_b128 a[180:183], v23 offset:672\n\t"
    "v_cvt_pk_f16_f32 v3, v90, v91\n\t"
    "v_pk_fma_f16 v4, v2, v2, s60 neg_lo:[1,0,1] neg_hi:[1,0,1]\n\t"
    "v_pk_fma_f16 v5, v3, v3, s60 neg_lo:[1,0,1] neg_hi:[1,0,1]\n\t"
    "v_pk_mul_f16 v4, v4, v2\n\t"
    "v_pk_mul_f16 v5, v5, v3\n\t"
    "v_exp_f16 v6, v4\n\t"
    "v_exp_f16 v7, v5\n\t"
    "s_waitcnt lgkmcnt(9)\n\t"
    "v_mfma_f32_32x32x16_bf16 v[96:111], a[236:239], v[204:207], v[96:111]\n\t"
    "ds_read_b64_tr_b16 a[212:213], v19 offset:2048\n\t"
    "ds_read_b128 a[184:187], v23 offset:704\n\t"
    "v_exp_f16_sdwa v6, v4 dst_sel:WORD_1 dst_unused:UNUSED_PRESERVE src0_sel:WORD_1\n\t"
    "v_exp_f16_sdwa v7, v5 dst_sel:WORD_1 dst_unused:UNUSED_PRESERVE src0_sel:WORD_1\n\t"
    "v_pk_add_f16 v6, v6, 1.0 op_sel_hi:[1,0]\n\t"
    "v_pk_add_f16 v7, v7, 1.0 op_sel_hi:[1,0]\n\t"
    "v_rcp_f16 v4, v6\n\t"
    "v_rcp_f16 v5, v7\n\t"
    "v_mfma_f32_32x32x16_bf16 v[112:127], a[236:239], v[252:255], v[112:127]\n\t"
    "ds_read_b64_tr_b16 a[214:215], v19 offset:5120\n\t"
    "ds_read_b128 a[188:191], v23 offset:736\n\t"
    "v_rcp_f16_sdwa v4, v6 dst_sel:WORD_1 dst_unused:UNUSED_PRESERVE src0_sel:WORD_1\n\t"
    "v_rcp_f16_sdwa v5, v7 dst_sel:WORD_1 dst_unused:UNUSED_PRESERVE src0_sel:WORD_1\n\t"
    "v_fma_mix_f32 v88, v88, v4, 0 op_sel_hi:[0,1,0]\n\t"
    "v_fma_mix_f32 v90, v90, v5, 0 op_sel_hi:[0,1,0]\n\t"
    "v_fma_mix_f32 v89, v89, v4, 0 op_sel:[0,1,0] op_sel_hi:[0,1,0]\n\t"
    "v_fma_mix_f32 v91, v91, v5, 0 op_sel:[0,1,0] op_sel_hi:[0,1,0]\n\t"
    "v_cvt_pk_bf16_f32 v140, v88, v89\n\t"
    "v_cvt_pk_bf16_f32 v141, v90, v91\n\t"
    "s_mov_b32 s69, 6\n\t"
    "s_mov_b32 s30, 49152\n\t"
    "s_branch P2_c_%=\n\t"
    ".p2align 6\n\t"
    "loop_c_%=:\n\t"
    "s_waitcnt lgkmcnt(4)\n\t"
    "v_mfma_f32_32x32x16_bf16 a[0:15], a[192:195], v[128:131], a[0:15]\n\t"
    "ds_read_b64_tr_b16 a[216:217], v18 offset:30720\n\t"
    "v_cvt_pk_f16_f32 v8, v92, v93\n\t"
    "v_cvt_pk_f16_f32 v9, v94, v95\n\t"
    "v_pk_fma_f16 v10, v8, v8, s60 neg_lo:[1,0,1] neg_hi:[1,0,1]\n\t"
    "v_pk_fma_f16 v11, v9, v9, s60 neg_lo:[1,0,1] neg_hi:[1,0,1]\n\t"
    "v_pk_mul_f16 v10, v10, v8\n\t"
    "v_mfma_f32_32x32x16_bf16 a[16:31], a[192:195], v[136:139], a[16:31]\n\t"
    "ds_read_b64_tr_b16 a[218:219], v18 offset:33792\n\t"
    "v_pk_mul_f16 v11, v11, v9\n\t"
    "v_exp_f16 v12, v10\n\t"
    "v_exp_f16 v13, v11\n\t"
    "v_mfma_f32_32x32x16_bf16 a[32:47], a[196:199], v[128:131], a[32:47]\n\t"
    "ds_read_b64_tr_b16 a[220:221], v19 offset:30720\n\t"
    "v_exp_f16_sdwa v12, v10 dst_sel:WORD_1 dst_unused:UNUSED_PRESERVE src0_sel:WORD_1\n\t"
    "v_exp_f16_sdwa v13, v11 dst_sel:WORD_1 dst_unused:UNUSED_PRESERVE src0_sel:WORD_1\n\t"
    "v_pk_add_f16 v12, v12, 1.0 op_sel_hi:[1,0]\n\t"
    "v_pk_add_f16 v13, v13, 1.0 op_sel_hi:[1,0]\n\t"
    "v_mfma_f32_32x32x16_bf16 a[48:63], a[196:199], v[136:139], a[48:63]\n\t"
    "ds_read_b64_tr_b16 a[222:223], v19 offset:33792\n\t"
    "v_rcp_f16 v10, v12\n\t"
    "v_rcp_f16 v11, v13\n\t"
    "v_rcp_f16_sdwa v10, v12 dst_sel:WORD_1 dst_unused:UNUSED_PRESERVE src0_sel:WORD_1\n\t"
    "v_mfma_f32_32x32x16_bf16 a[64:79], a[200:203], v[128:131], a[64:79]\n\t"
    "ds_read_b64_tr_b16 a[224:225], v18 offset:31744\n\t"
    "v_rcp_f16_sdwa v11, v13 dst_sel:WORD_1 dst_unused:UNUSED_PRESERVE src0_sel:WORD_1\n\t"
    "v_fma_mix_f32 v92, v92, v10, 0 op_sel_hi:[0,1,0]\n\t"
    "v_fma_mix_f32 v94, v94, v11, 0 op_sel_hi:[0,1,0]\n\t"
    "v_fma_mix_f32 v93, v93, v10, 0 op_sel:[0,1,0] op_sel_hi:[0,1,0]\n\t"
    "v_mfma_f32_32x32x16_bf16 a[80:95], a[200:203], v[136:139], a[80:95]\n\t"
    "ds_read_b64_tr_b16 a[226:227], v18 offset:34816\n\t"
    "v_fma_mix_f32 v95, v95, v11, 0 op_sel:[0,1,0] op_sel_hi:[0,1,0]\n\t"
    "v_cvt_pk_bf16_f32 v142, v92, v93\n\t"
    "v_cvt_pk_bf16_f32 v143, v94, v95\n\t"
    "v_cvt_pk_f16_f32 v2, v96, v97\n\t"
    "s_barrier\n\t"
    "v_mfma_f32_32x32x16_bf16 a[96:111], a[204:207], v[128:131], a[96:111]\n\t"
    "ds_read_b64_tr_b16 a[228:229], v19 offset:31744\n\t"
    "v_cvt_pk_f16_f32 v3, v98, v99\n\t"
    "v_pk_fma_f16 v4, v2, v2, s60 neg_lo:[1,0,1] neg_hi:[1,0,1]\n\t"
    "v_pk_fma_f16 v5, v3, v3, s60 neg_lo:[1,0,1] neg_hi:[1,0,1]\n\t"
    "v_pk_mul_f16 v4, v4, v2\n\t"
    "v_pk_mul_f16 v5, v5, v3\n\t"
    "v_mfma_f32_32x32x16_bf16 a[112:127], a[204:207], v[136:139], a[112:127]\n\t"
    "ds_read_b64_tr_b16 a[230:231], v19 offset:34816\n\t"
    "ds_read_b128 v[60:63], v22 offset:96\n\t"
    "v_exp_f16 v6, v4\n\t"
    "v_exp_f16 v7, v5\n\t"
    "s_waitcnt lgkmcnt(5)\n\t"
    "v_mfma_f32_32x32x16_bf16 a[128:143], a[208:211], v[128:131], a[128:143]\n\t"
    "ds_read_b64_tr_b16 a[232:233], v18 offset:32768\n\t"
    "v_exp_f16_sdwa v6, v4 dst_sel:WORD_1 dst_unused:UNUSED_PRESERVE src0_sel:WORD_1\n\t"
    "v_exp_f16_sdwa v7, v5 dst_sel:WORD_1 dst_unused:UNUSED_PRESERVE src0_sel:WORD_1\n\t"
    "v_pk_add_f16 v6, v6, 1.0 op_sel_hi:[1,0]\n\t"
    "v_mfma_f32_32x32x16_bf16 a[144:159], a[208:211], v[136:139], a[144:159]\n\t"
    "ds_read_b64_tr_b16 a[234:235], v18 offset:35840\n\t"
    "v_pk_add_f16 v7, v7, 1.0 op_sel_hi:[1,0]\n\t"
    "v_rcp_f16 v4, v6\n\t"
    "v_rcp_f16 v5, v7\n\t"
    "v_mfma_f32_32x32x16_bf16 a[160:175], a[212:215], v[128:131], a[160:175]\n\t"
    "ds_read_b64_tr_b16 a[236:237], v19 offset:32768\n\t"
    "v_rcp_f16_sdwa v4, v6 dst_sel:WORD_1 dst_unused:UNUSED_PRESERVE src0_sel:WORD_1\n\t"
    "v_rcp_f16_sdwa v5, v7 dst_sel:WORD_1 dst_unused:UNUSED_PRESERVE src0_sel:WORD_1\n\t"
    "v_fma_mix_f32 v96, v96, v4, 0 op_sel_hi:[0,1,0]\n\t"
    "v_mfma_f32_32x32x16_bf16 a[176:191], a[212:215], v[136:139], a[176:191]\n\t"
    "ds_read_b64_tr_b16 a[238:239], v19 offset:35840\n\t"
    "ds_read_b128 v[56:59], v22 offset:64\n\t"
    "v_fma_mix_f32 v98, v98, v5, 0 op_sel_hi:[0,1,0]\n\t"
    "v_fma_mix_f32 v97, v97, v4, 0 op_sel:[0,1,0] op_sel_hi:[0,1,0]\n\t"
    "v_fma_mix_f32 v99, v99, v5, 0 op_sel:[0,1,0] op_sel_hi:[0,1,0]\n\t"
    "v_cvt_pk_bf16_f32 v144, v96, v97\n\t"
    "v_mfma_f32_32x32x16_bf16 a[0:15], a[216:219], v[132:135], a[0:15]\n\t"
    "ds_read_b64_tr_b16 a[192:193], v14 offset:0\n\t"
    "v_cvt_pk_bf16_f32 v145, v98, v99\n\t"
    "v_cvt_pk_f16_f32 v8, v100, v101\n\t"
    "v_cvt_pk_f16_f32 v9, v102, v103\n\t"
    "v_pk_fma_f16 v10, v8, v8, s60 neg_lo:[1,0,1] neg_hi:[1,0,1]\n\t"
    "v_pk_fma_f16 v11, v9, v9, s60 neg_lo:[1,0,1] neg_hi:[1,0,1]\n\t"
    "v_mfma_f32_32x32x16_bf16 a[16:31], a[216:219], v[140:143], a[16:31]\n\t"
    "ds_read_b64_tr_b16 a[194:195], v15 offset:512\n\t"
    "v_pk_mul_f16 v10, v10, v8\n\t"
    "v_pk_mul_f16 v11, v11, v9\n\t"
    "v_exp_f16 v12, v10\n\t"
    "v_exp_f16 v13, v11\n\t"
    "v_mfma_f32_32x32x16_bf16 a[32:47], a[220:223], v[132:135], a[32:47]\n\t"
    "ds_read_b64_tr_b16 a[196:197], v14 offset:2048\n\t"
    "v_exp_f16_sdwa v12, v10 dst_sel:WORD_1 dst_unused:UNUSED_PRESERVE src0_sel:WORD_1\n\t"
    "v_exp_f16_sdwa v13, v11 dst_sel:WORD_1 dst_unused:UNUSED_PRESERVE src0_sel:WORD_1\n\t"
    "v_pk_add_f16 v12, v12, 1.0 op_sel_hi:[1,0]\n\t"
    "v_mfma_f32_32x32x16_bf16 a[48:63], a[220:223], v[140:143], a[48:63]\n\t"
    "ds_read_b64_tr_b16 a[198:199], v15 offset:2560\n\t"
    "ds_read_b128 v[52:55], v22 offset:32\n\t"
    "v_pk_add_f16 v13, v13, 1.0 op_sel_hi:[1,0]\n\t"
    "v_rcp_f16 v10, v12\n\t"
    "s_waitcnt lgkmcnt(6)\n\t"
    "v_mfma_f32_32x32x16_bf16 a[64:79], a[224:227], v[132:135], a[64:79]\n\t"
    "ds_read_b64_tr_b16 a[200:201], v14 offset:4096\n\t"
    "v_rcp_f16 v11, v13\n\t"
    "v_rcp_f16_sdwa v10, v12 dst_sel:WORD_1 dst_unused:UNUSED_PRESERVE src0_sel:WORD_1\n\t"
    "v_rcp_f16_sdwa v11, v13 dst_sel:WORD_1 dst_unused:UNUSED_PRESERVE src0_sel:WORD_1\n\t"
    "v_mfma_f32_32x32x16_bf16 a[80:95], a[224:227], v[140:143], a[80:95]\n\t"
    "ds_read_b64_tr_b16 a[202:203], v15 offset:4608\n\t"
    "v_fma_mix_f32 v100, v100, v10, 0 op_sel_hi:[0,1,0]\n\t"
    "v_fma_mix_f32 v102, v102, v11, 0 op_sel_hi:[0,1,0]\n\t"
    "v_fma_mix_f32 v101, v101, v10, 0 op_sel:[0,1,0] op_sel_hi:[0,1,0]\n\t"
    "v_fma_mix_f32 v103, v103, v11, 0 op_sel:[0,1,0] op_sel_hi:[0,1,0]\n\t"
    "v_cvt_pk_bf16_f32 v146, v100, v101\n\t"
    "v_mfma_f32_32x32x16_bf16 a[96:111], a[228:231], v[132:135], a[96:111]\n\t"
    "ds_read_b64_tr_b16 a[204:205], v14 offset:6144\n\t"
    "v_cvt_pk_bf16_f32 v147, v102, v103\n\t"
    "v_cvt_pk_f16_f32 v2, v112, v113\n\t"
    "v_cvt_pk_f16_f32 v3, v114, v115\n\t"
    "v_pk_fma_f16 v4, v2, v2, s60 neg_lo:[1,0,1] neg_hi:[1,0,1]\n\t"
    "v_pk_fma_f16 v5, v3, v3, s60 neg_lo:[1,0,1] neg_hi:[1,0,1]\n\t"
    "v_mfma_f32_32x32x16_bf16 a[112:127], a[228:231], v[140:143], a[112:127]\n\t"
    "ds_read_b64_tr_b16 a[206:207], v15 offset:6656\n\t"
    "ds_read_b128 v[48:51], v22 offset:0\n\t"
    "v_pk_mul_f16 v4, v4, v2\n\t"
    "v_pk_mul_f16 v5, v5, v3\n\t"
    "v_exp_f16 v6, v4\n\t"
    "v_mfma_f32_32x32x16_bf16 a[128:143], a[232:235], v[132:135], a[128:143]\n\t"
    "ds_read_b64_tr_b16 a[208:209], v14 offset:8192\n\t"
    "v_exp_f16 v7, v5\n\t"
    "v_exp_f16_sdwa v6, v4 dst_sel:WORD_1 dst_unused:UNUSED_PRESERVE src0_sel:WORD_1\n\t"
    "v_exp_f16_sdwa v7, v5 dst_sel:WORD_1 dst_unused:UNUSED_PRESERVE src0_sel:WORD_1\n\t"
    "v_mfma_f32_32x32x16_bf16 a[144:159], a[232:235], v[140:143], a[144:159]\n\t"
    "ds_read_b64_tr_b16 a[210:211], v15 offset:8704\n\t"
    "v_pk_add_f16 v6, v6, 1.0 op_sel_hi:[1,0]\n\t"
    "v_pk_add_f16 v7, v7, 1.0 op_sel_hi:[1,0]\n\t"
    "v_rcp_f16 v4, v6\n\t"
    "v_rcp_f16 v5, v7\n\t"
    "v_mfma_f32_32x32x16_bf16 a[160:175], a[236:239], v[132:135], a[160:175]\n\t"
    "ds_read_b64_tr_b16 a[212:213], v14 offset:10240\n\t"
    "v_rcp_f16_sdwa v4, v6 dst_sel:WORD_1 dst_unused:UNUSED_PRESERVE src0_sel:WORD_1\n\t"
    "v_rcp_f16_sdwa v5, v7 dst_sel:WORD_1 dst_unused:UNUSED_PRESERVE src0_sel:WORD_1\n\t"
    "v_fma_mix_f32 v112, v112, v4, 0 op_sel_hi:[0,1,0]\n\t"
    "v_mfma_f32_32x32x16_bf16 a[176:191], a[236:239], v[140:143], a[176:191]\n\t"
    "ds_read_b64_tr_b16 a[214:215], v15 offset:10752\n\t"
    "v_fma_mix_f32 v114, v114, v5, 0 op_sel_hi:[0,1,0]\n\t"
    "v_fma_mix_f32 v113, v113, v4, 0 op_sel:[0,1,0] op_sel_hi:[0,1,0]\n\t"
    "v_fma_mix_f32 v115, v115, v5, 0 op_sel:[0,1,0] op_sel_hi:[0,1,0]\n\t"
    "v_cvt_pk_bf16_f32 v152, v112, v113\n\t"
    "s_waitcnt lgkmcnt(4)\n\t"
    "v_mfma_f32_32x32x16_bf16 v[64:79], a[192:195], v[160:163], v[48:63]\n\t"
    "ds_read_b64_tr_b16 a[216:217], v14 offset:12288\n\t"
    "v_cvt_pk_bf16_f32 v153, v114, v115\n\t"
    "v_cvt_pk_f16_f32 v8, v116, v117\n\t"
    "v_cvt_pk_f16_f32 v9, v118, v119\n\t"
    "v_pk_fma_f16 v10, v8, v8, s60 neg_lo:[1,0,1] neg_hi:[1,0,1]\n\t"
    "v_pk_fma_f16 v11, v9, v9, s60 neg_lo:[1,0,1] neg_hi:[1,0,1]\n\t"
    "v_mfma_f32_32x32x16_bf16 v[80:95], a[192:195], v[208:211], v[48:63]\n\t"
    "ds_read_b64_tr_b16 a[218:219], v15 offset:12800\n\t"
    "v_pk_mul_f16 v10, v10, v8\n\t"
    "v_pk_mul_f16 v11, v11, v9\n\t"
    "v_exp_f16 v12, v10\n\t"
    "v_exp_f16 v13, v11\n\t"
    "v_mfma_f32_32x32x16_bf16 v[64:79], a[196:199], v[164:167], v[64:79]\n\t"
    "ds_read_b64_tr_b16 a[220:221], v14 offset:14336\n\t"
    "v_exp_f16_sdwa v12, v10 dst_sel:WORD_1 dst_unused:UNUSED_PRESERVE src0_sel:WORD_1\n\t"
    "v_exp_f16_sdwa v13, v11 dst_sel:WORD_1 dst_unused:UNUSED_PRESERVE src0_sel:WORD_1\n\t"
    "v_pk_add_f16 v12, v12, 1.0 op_sel_hi:[1,0]\n\t"
    "v_mfma_f32_32x32x16_bf16 v[80:95], a[196:199], v[212:215], v[80:95]\n\t"
    "ds_read_b64_tr_b16 a[222:223], v15 offset:14848\n\t"
    "v_pk_add_f16 v13, v13, 1.0 op_sel_hi:[1,0]\n\t"
    "v_rcp_f16 v10, v12\n\t"
    "v_rcp_f16 v11, v13\n\t"
    "v_mfma_f32_32x32x16_bf16 v[64:79], a[200:203], v[168:171], v[64:79]\n\t"
    "ds_read_b64_tr_b16 a[224:225], v14 offset:16384\n\t"
    "v_rcp_f16_sdwa v10, v12 dst_sel:WORD_1 dst_unused:UNUSED_PRESERVE src0_sel:WORD_1\n\t"
    "v_rcp_f16_sdwa v11, v13 dst_sel:WORD_1 dst_unused:UNUSED_PRESERVE src0_sel:WORD_1\n\t"
    "v_fma_mix_f32 v116, v116, v10, 0 op_sel_hi:[0,1,0]\n\t"
    "v_mfma_f32_32x32x16_bf16 v[80:95], a[200:203], v[216:219], v[80:95]\n\t"
    "ds_read_b64_tr_b16 a[226:227], v15 offset:16896\n\t"
    "v_fma_mix_f32 v118, v118, v11, 0 op_sel_hi:[0,1,0]\n\t"
    "v_fma_mix_f32 v117, v117, v10, 0 op_sel:[0,1,0] op_sel_hi:[0,1,0]\n\t"
    "v_fma_mix_f32 v119, v119, v11, 0 op_sel:[0,1,0] op_sel_hi:[0,1,0]\n\t"
    "v_cvt_pk_bf16_f32 v154, v116, v117\n\t"
    "v_cvt_pk_bf16_f32 v155, v118, v119\n\t"
    "v_mfma_f32_32x32x16_bf16 v[64:79], a[204:207], v[172:175], v[64:79]\n\t"
    "ds_read_b64_tr_b16 a[228:229], v14 offset:18432\n\t"
    "v_cvt_pk_f16_f32 v2, v104, v105\n\t"
    "v_cvt_pk_f16_f32 v3, v106, v107\n\t"
    "v_pk_fma_f16 v4, v2, v2, s60 neg_lo:[1,0,1] neg_hi:[1,0,1]\n\t"
    "v_pk_fma_f16 v5, v3, v3, s60 neg_lo:[1,0,1] neg_hi:[1,0,1]\n\t"
    "v_pk_mul_f16 v4, v4, v2\n\t"
    "v_mfma_f32_32x32x16_bf16 v[80:95], a[204:207], v[220:223], v[80:95]\n\t"
    "ds_read_b64_tr_b16 a[230:231], v15 offset:18944\n\t"
    "v_pk_mul_f16 v5, v5, v3\n\t"
    "v_exp_f16 v6, v4\n\t"
    "v_exp_f16 v7, v5\n\t"
    "s_waitcnt lgkmcnt(4)\n\t"
    "v_mfma_f32_32x32x16_bf16 v[64:79], a[208:211], v[176:179], v[64:79]\n\t"
    "ds_read_b64_tr_b16 a[232:233], v14 offset:20480\n\t"
    "v_exp_f16_sdwa v6, v4 dst_sel:WORD_1 dst_unused:UNUSED_PRESERVE src0_sel:WORD_1\n\t"
    "v_exp_f16_sdwa v7, v5 dst_sel:WORD_1 dst_unused:UNUSED_PRESERVE src0_sel:WORD_1\n\t"
    "v_pk_add_f16 v6, v6, 1.0 op_sel_hi:[1,0]\n\t"
    "v_mfma_f32_32x32x16_bf16 v[80:95], a[208:211], v[224:227], v[80:95]\n\t"
    "ds_read_b64_tr_b16 a[234:235], v15 offset:20992\n\t"
    "v_pk_add_f16 v7, v7, 1.0 op_sel_hi:[1,0]\n\t"
    "v_rcp_f16 v4, v6\n\t"
    "v_rcp_f16 v5, v7\n\t"
    "v_mfma_f32_32x32x16_bf16 v[64:79], a[212:215], v[180:183], v[64:79]\n\t"
    "ds_read_b64_tr_b16 a[236:237], v14 offset:22528\n\t"
    "v_rcp_f16_sdwa v4, v6 dst_sel:WORD_1 dst_unused:UNUSED_PRESERVE src0_sel:WORD_1\n\t"
    "v_rcp_f16_sdwa v5, v7 dst_sel:WORD_1 dst_unused:UNUSED_PRESERVE src0_sel:WORD_1\n\t"
    "v_fma_mix_f32 v104, v104, v4, 0 op_sel_hi:[0,1,0]\n\t"
    "v_mfma_f32_32x32x16_bf16 v[80:95], a[212:215], v[228:231], v[80:95]\n\t"
    "ds_read_b64_tr_b16 a[238:239], v15 offset:23040\n\t"
    "v_fma_mix_f32 v106, v106, v5, 0 op_sel_hi:[0,1,0]\n\t"
    "v_fma_mix_f32 v105, v105, v4, 0 op_sel:[0,1,0] op_sel_hi:[0,1,0]\n\t"
    "v_fma_mix_f32 v107, v107, v5, 0 op_sel:[0,1,0] op_sel_hi:[0,1,0]\n\t"
    "v_cvt_pk_bf16_f32 v148, v104, v105\n\t"
    "v_cvt_pk_bf16_f32 v149, v106, v107\n\t"
    "v_mfma_f32_32x32x16_bf16 v[64:79], a[216:219], v[184:187], v[64:79]\n\t"
    "ds_read_b64_tr_b16 a[192:193], v18 offset:36864\n\t"
    "v_cvt_pk_f16_f32 v8, v108, v109\n\t"
    "v_cvt_pk_f16_f32 v9, v110, v111\n\t"
    "v_pk_fma_f16 v10, v8, v8, s60 neg_lo:[1,0,1] neg_hi:[1,0,1]\n\t"
    "v_pk_fma_f16 v11, v9, v9, s60 neg_lo:[1,0,1] neg_hi:[1,0,1]\n\t"
    "v_pk_mul_f16 v10, v10, v8\n\t"
    "v_mfma_f32_32x32x16_bf16 v[80:95], a[216:219], v[232:235], v[80:95]\n\t"
    "ds_read_b64_tr_b16 a[194:195], v18 offset:39936\n\t"
    "v_pk_mul_f16 v11, v11, v9\n\t"
    "v_exp_f16 v12, v10\n\t"
    "v_exp_f16 v13, v11\n\t"
    "v_mfma_f32_32x32x16_bf16 v[64:79], a[220:223], v[188:191], v[64:79]\n\t"
    "ds_read_b64_tr_b16 a[196:197], v19 offset:36864\n\t"
    "v_exp_f16_sdwa v12, v10 dst_sel:WORD_1 dst_unused:UNUSED_PRESERVE src0_sel:WORD_1\n\t"
    "v_exp_f16_sdwa v13, v11 dst_sel:WORD_1 dst_unused:UNUSED_PRESERVE src0_sel:WORD_1\n\t"
    "v_pk_add_f16 v12, v12, 1.0 op_sel_hi:[1,0]\n\t"
    "v_mfma_f32_32x32x16_bf16 v[80:95], a[220:223], v[236:239], v[80:95]\n\t"
    "ds_read_b64_tr_b16 a[198:199], v19 offset:39936\n\t"
    "v_pk_add_f16 v13, v13, 1.0 op_sel_hi:[1,0]\n\t"
    "v_rcp_f16 v10, v12\n\t"
    "v_rcp_f16 v11, v13\n\t"
    "s_waitcnt lgkmcnt(4)\n\t"
    "v_mfma_f32_32x32x16_bf16 v[64:79], a[224:227], v[192:195], v[64:79]\n\t"
    "ds_read_b64_tr_b16 a[200:201], v18 offset:37888\n\t"
    "v_rcp_f16_sdwa v10, v12 dst_sel:WORD_1 dst_unused:UNUSED_PRESERVE src0_sel:WORD_1\n\t"
    "v_rcp_f16_sdwa v11, v13 dst_sel:WORD_1 dst_unused:UNUSED_PRESERVE src0_sel:WORD_1\n\t"
    "v_fma_mix_f32 v108, v108, v10, 0 op_sel_hi:[0,1,0]\n\t"
    "v_mfma_f32_32x32x16_bf16 v[80:95], a[224:227], v[240:243], v[80:95]\n\t"
    "ds_read_b64_tr_b16 a[202:203], v18 offset:40960\n\t"
    "v_fma_mix_f32 v110, v110, v11, 0 op_sel_hi:[0,1,0]\n\t"
    "v_fma_mix_f32 v109, v109, v10, 0 op_sel:[0,1,0] op_sel_hi:[0,1,0]\n\t"
    "v_fma_mix_f32 v111, v111, v11, 0 op_sel:[0,1,0] op_sel_hi:[0,1,0]\n\t"
    "v_cvt_pk_bf16_f32 v150, v108, v109\n\t"
    "v_cvt_pk_bf16_f32 v151, v110, v111\n\t"
    "v_mfma_f32_32x32x16_bf16 v[64:79], a[228:231], v[196:199], v[64:79]\n\t"
    "ds_read_b64_tr_b16 a[204:205], v19 offset:37888\n\t"
    "v_cvt_pk_f16_f32 v2, v120, v121\n\t"
    "v_cvt_pk_f16_f32 v3, v122, v123\n\t"
    "v_pk_fma_f16 v4, v2, v2, s60 neg_lo:[1,0,1] neg_hi:[1,0,1]\n\t"
    "v_pk_fma_f16 v5, v3, v3, s60 neg_lo:[1,0,1] neg_hi:[1,0,1]\n\t"
    "v_pk_mul_f16 v4, v4, v2\n\t"
    "v_mfma_f32_32x32x16_bf16 v[80:95], a[228:231], v[244:247], v[80:95]\n\t"
    "ds_read_b64_tr_b16 a[206:207], v19 offset:40960\n\t"
    "v_pk_mul_f16 v5, v5, v3\n\t"
    "v_exp_f16 v6, v4\n\t"
    "v_exp_f16 v7, v5\n\t"
    "v_mfma_f32_32x32x16_bf16 v[64:79], a[232:235], v[200:203], v[64:79]\n\t"
    "ds_read_b64_tr_b16 a[208:209], v18 offset:38912\n\t"
    "v_exp_f16_sdwa v6, v4 dst_sel:WORD_1 dst_unused:UNUSED_PRESERVE src0_sel:WORD_1\n\t"
    "v_exp_f16_sdwa v7, v5 dst_sel:WORD_1 dst_unused:UNUSED_PRESERVE src0_sel:WORD_1\n\t"
    "v_pk_add_f16 v6, v6, 1.0 op_sel_hi:[1,0]\n\t"
    "v_mfma_f32_32x32x16_bf16 v[80:95], a[232:235], v[248:251], v[80:95]\n\t"
    "ds_read_b64_tr_b16 a[210:211], v18 offset:41984\n\t"
    "v_pk_add_f16 v7, v7, 1.0 op_sel_hi:[1,0]\n\t"
    "v_rcp_f16 v4, v6\n\t"
    "v_rcp_f16 v5, v7\n\t"
    "v_mfma_f32_32x32x16_bf16 v[64:79], a[236:239], v[204:207], v[64:79]\n\t"
    "ds_read_b64_tr_b16 a[212:213], v19 offset:38912\n\t"
    "v_rcp_f16_sdwa v4, v6 dst_sel:WORD_1 dst_unused:UNUSED_PRESERVE src0_sel:WORD_1\n\t"
    "v_rcp_f16_sdwa v5, v7 dst_sel:WORD_1 dst_unused:UNUSED_PRESERVE src0_sel:WORD_1\n\t"
    "v_fma_mix_f32 v120, v120, v4, 0 op_sel_hi:[0,1,0]\n\t"
    "v_mfma_f32_32x32x16_bf16 v[80:95], a[236:239], v[252:255], v[80:95]\n\t"
    "ds_read_b64_tr_b16 a[214:215], v19 offset:41984\n\t"
    "v_fma_mix_f32 v122, v122, v5, 0 op_sel_hi:[0,1,0]\n\t"
    "v_fma_mix_f32 v121, v121, v4, 0 op_sel:[0,1,0] op_sel_hi:[0,1,0]\n\t"
    "v_fma_mix_f32 v123, v123, v5, 0 op_sel:[0,1,0] op_sel_hi:[0,1,0]\n\t"
    "v_cvt_pk_bf16_f32 v156, v120, v121\n\t"
    "v_cvt_pk_bf16_f32 v157, v122, v123\n\t"
    "s_waitcnt lgkmcnt(4)\n\t"
    "v_mfma_f32_32x32x16_bf16 a[0:15], a[192:195], v[144:147], a[0:15]\n\t"
    "ds_read_b64_tr_b16 a[216:217], v18 offset:43008\n\t"
    "v_cvt_pk_f16_f32 v8, v124, v125\n\t"
    "v_cvt_pk_f16_f32 v9, v126, v127\n\t"
    "v_pk_fma_f16 v10, v8, v8, s60 neg_lo:[1,0,1] neg_hi:[1,0,1]\n\t"
    "v_pk_fma_f16 v11, v9, v9, s60 neg_lo:[1,0,1] neg_hi:[1,0,1]\n\t"
    "v_pk_mul_f16 v10, v10, v8\n\t"
    "v_mfma_f32_32x32x16_bf16 a[16:31], a[192:195], v[152:155], a[16:31]\n\t"
    "ds_read_b64_tr_b16 a[218:219], v18 offset:46080\n\t"
    "v_pk_mul_f16 v11, v11, v9\n\t"
    "v_exp_f16 v12, v10\n\t"
    "v_exp_f16 v13, v11\n\t"
    "v_mfma_f32_32x32x16_bf16 a[32:47], a[196:199], v[144:147], a[32:47]\n\t"
    "ds_read_b64_tr_b16 a[220:221], v19 offset:43008\n\t"
    "v_exp_f16_sdwa v12, v10 dst_sel:WORD_1 dst_unused:UNUSED_PRESERVE src0_sel:WORD_1\n\t"
    "v_exp_f16_sdwa v13, v11 dst_sel:WORD_1 dst_unused:UNUSED_PRESERVE src0_sel:WORD_1\n\t"
    "v_pk_add_f16 v12, v12, 1.0 op_sel_hi:[1,0]\n\t"
    "v_pk_add_f16 v13, v13, 1.0 op_sel_hi:[1,0]\n\t"
    "v_mfma_f32_32x32x16_bf16 a[48:63], a[196:199], v[152:155], a[48:63]\n\t"
    "ds_read_b64_tr_b16 a[222:223], v19 offset:46080\n\t"
    "v_rcp_f16 v10, v12\n\t"
    "v_rcp_f16 v11, v13\n\t"
    "v_rcp_f16_sdwa v10, v12 dst_sel:WORD_1 dst_unused:UNUSED_PRESERVE src0_sel:WORD_1\n\t"
    "v_mfma_f32_32x32x16_bf16 a[64:79], a[200:203], v[144:147], a[64:79]\n\t"
    "ds_read_b64_tr_b16 a[224:225], v18 offset:44032\n\t"
    "v_rcp_f16_sdwa v11, v13 dst_sel:WORD_1 dst_unused:UNUSED_PRESERVE src0_sel:WORD_1\n\t"
    "v_fma_mix_f32 v124, v124, v10, 0 op_sel_hi:[0,1,0]\n\t"
    "v_fma_mix_f32 v126, v126, v11, 0 op_sel_hi:[0,1,0]\n\t"
    "v_fma_mix_f32 v125, v125, v10, 0 op_sel:[0,1,0] op_sel_hi:[0,1,0]\n\t"
    "v_mfma_f32_32x32x16_bf16 a[80:95], a[200:203], v[152:155], a[80:95]\n\t"
    "ds_read_b64_tr_b16 a[226:227], v18 offset:47104\n\t"
    "v_fma_mix_f32 v127, v127, v11, 0 op_sel:[0,1,0] op_sel_hi:[0,1,0]\n\t"
    "v_cvt_pk_bf16_f32 v158, v124, v125\n\t"
    "v_cvt_pk_bf16_f32 v159, v126, v127\n\t"
    "v_cvt_pk_f16_f32 v2, v64, v65\n\t"
    "v_cvt_pk_f16_f32 v3, v66, v67\n\t"
    "v_mfma_f32_32x32x16_bf16 a[96:111], a[204:207], v[144:147], a[96:111]\n\t"
    "ds_read_b64_tr_b16 a[228:229], v19 offset:44032\n\t"
    "v_pk_fma_f16 v4, v2, v2, s60 neg_lo:[1,0,1] neg_hi:[1,0,1]\n\t"
    "v_pk_fma_f16 v5, v3, v3, s60 neg_lo:[1,0,1] neg_hi:[1,0,1]\n\t"
    "v_pk_mul_f16 v4, v4, v2\n\t"
    "v_pk_mul_f16 v5, v5, v3\n\t"
    "v_exp_f16 v6, v4\n\t"
    "v_mfma_f32_32x32x16_bf16 a[112:127], a[204:207], v[152:155], a[112:127]\n\t"
    "ds_read_b64_tr_b16 a[230:231], v19 offset:47104\n\t"
    "ds_read_b128 v[60:63], v22 offset:224\n\t"
    "v_exp_f16 v7, v5\n\t"
    "v_exp_f16_sdwa v6, v4 dst_sel:WORD_1 dst_unused:UNUSED_PRESERVE src0_sel:WORD_1\n\t"
    "s_waitcnt lgkmcnt(5)\n\t"
    "v_mfma_f32_32x32x16_bf16 a[128:143], a[208:211], v[144:147], a[128:143]\n\t"
    "ds_read_b64_tr_b16 a[232:233], v18 offset:45056\n\t"
    "v_exp_f16_sdwa v7, v5 dst_sel:WORD_1 dst_unused:UNUSED_PRESERVE src0_sel:WORD_1\n\t"
    "v_pk_add_f16 v6, v6, 1.0 op_sel_hi:[1,0]\n\t"
    "v_pk_add_f16 v7, v7, 1.0 op_sel_hi:[1,0]\n\t"
    "v_rcp_f16 v4, v6\n\t"
    "v_mfma_f32_32x32x16_bf16 a[144:159], a[208:211], v[152:155], a[144:159]\n\t"
    "ds_read_b64_tr_b16 a[234:235], v18 offset:48128\n\t"
    "v_rcp_f16 v5, v7\n\t"
    "v_rcp_f16_sdwa v4, v6 dst_sel:WORD_1 dst_unused:UNUSED_PRESERVE src0_sel:WORD_1\n\t"
    "v_rcp_f16_sdwa v5, v7 dst_sel:WORD_1 dst_unused:UNUSED_PRESERVE src0_sel:WORD_1\n\t"
    "v_mfma_f32_32x32x16_bf16 a[160:175], a[212:215], v[144:147], a[160:175]\n\t"
    "ds_read_b64_tr_b16 a[236:237], v19 offset:45056\n\t"
    "v_fma_mix_f32 v64, v64, v4, 0 op_sel_hi:[0,1,0]\n\t"
    "v_fma_mix_f32 v66, v66, v5, 0 op_sel_hi:[0,1,0]\n\t"
    "v_fma_mix_f32 v65, v65, v4, 0 op_sel:[0,1,0] op_sel_hi:[0,1,0]\n\t"
    "v_fma_mix_f32 v67, v67, v5, 0 op_sel:[0,1,0] op_sel_hi:[0,1,0]\n\t"
    "v_cvt_pk_bf16_f32 v128, v64, v65\n\t"
    "v_mfma_f32_32x32x16_bf16 a[176:191], a[212:215], v[152:155], a[176:191]\n\t"
    "ds_read_b64_tr_b16 a[238:239], v19 offset:48128\n\t"
    "ds_read_b128 v[56:59], v22 offset:192\n\t"
    "v_cvt_pk_bf16_f32 v129, v66, v67\n\t"
    "v_cvt_pk_f16_f32 v8, v68, v69\n\t"
    "v_cvt_pk_f16_f32 v9, v70, v71\n\t"
    "v_pk_fma_f16 v10, v8, v8, s60 neg_lo:[1,0,1] neg_hi:[1,0,1]\n\t"
    "v_mfma_f32_32x32x16_bf16 a[0:15], a[216:219], v[148:151], a[0:15]\n\t"
    "ds_read_b64_tr_b16 a[192:193], v16 offset:0\n\t"
    "v_pk_fma_f16 v11, v9, v9, s60 neg_lo:[1,0,1] neg_hi:[1,0,1]\n\t"
    "v_pk_mul_f16 v10, v10, v8\n\t"
    "v_pk_mul_f16 v11, v11, v9\n\t"
    "v_exp_f16 v12, v10\n\t"
    "v_mfma_f32_32x32x16_bf16 a[16:31], a[216:219], v[156:159], a[16:31]\n\t"
    "ds_read_b64_tr_b16 a[194:195], v17 offset:512\n\t"
    "v_exp_f16 v13, v11\n\t"
    "v_exp_f16_sdwa v12, v10 dst_sel:WORD_1 dst_unused:UNUSED_PRESERVE src0_sel:WORD_1\n\t"
    "v_exp_f16_sdwa v13, v11 dst_sel:WORD_1 dst_unused:UNUSED_PRESERVE src0_sel:WORD_1\n\t"
    "v_mfma_f32_32x32x16_bf16 a[32:47], a[220:223], v[148:151], a[32:47]\n\t"
    "ds_read_b64_tr_b16 a[196:197], v16 offset:2048\n\t"
    "v_pk_add_f16 v12, v12, 1.0 op_sel_hi:[1,0]\n\t"
    "v_pk_add_f16 v13, v13, 1.0 op_sel_hi:[1,0]\n\t"
    "v_rcp_f16 v10, v12\n\t"
    "v_rcp_f16 v11, v13\n\t"
    "v_mfma_f32_32x32x16_bf16 a[48:63], a[220:223], v[156:159], a[48:63]\n\t"
    "ds_read_b64_tr_b16 a[198:199], v17 offset:2560\n\t"
    "ds_read_b128 v[52:55], v22 offset:160\n\t"
    "v_rcp_f16_sdwa v10, v12 dst_sel:WORD_1 dst_unused:UNUSED_PRESERVE src0_sel:WORD_1\n\t"
    "v_rcp_f16_sdwa v11, v13 dst_sel:WORD_1 dst_unused:UNUSED_PRESERVE src0_sel:WORD_1\n\t"
    "s_waitcnt lgkmcnt(6)\n\t"
    "v_mfma_f32_32x32x16_bf16 a[64:79], a[224:227], v[148:151], a[64:79]\n\t"
    "ds_read_b64_tr_b16 a[200:201], v16 offset:4096\n\t"
    "v_fma_mix_f32 v68, v68, v10, 0 op_sel_hi:[0,1,0]\n\t"
    "v_fma_mix_f32 v70, v70, v11, 0 op_sel_hi:[0,1,0]\n\t"
    "v_fma_mix_f32 v69, v69, v10, 0 op_sel:[0,1,0] op_sel_hi:[0,1,0]\n\t"
    "v_fma_mix_f32 v71, v71, v11, 0 op_sel:[0,1,0] op_sel_hi:[0,1,0]\n\t"
    "v_cvt_pk_bf16_f32 v130, v68, v69\n\t"
    "v_mfma_f32_32x32x16_bf16 a[80:95], a[224:227], v[156:159], a[80:95]\n\t"
    "ds_read_b64_tr_b16 a[202:203], v17 offset:4608\n\t"
    "v_cvt_pk_bf16_f32 v131, v70, v71\n\t"
    "v_cvt_pk_f16_f32 v2, v80, v81\n\t"
    "v_cvt_pk_f16_f32 v3, v82, v83\n\t"
    "v_pk_fma_f16 v4, v2, v2, s60 neg_lo:[1,0,1] neg_hi:[1,0,1]\n\t"
    "v_pk_fma_f16 v5, v3, v3, s60 neg_lo:[1,0,1] neg_hi:[1,0,1]\n\t"
    "v_mfma_f32_32x32x16_bf16 a[96:111], a[228:231], v[148:151], a[96:111]\n\t"
    "ds_read_b64_tr_b16 a[204:205], v16 offset:6144\n\t"
    "v_pk_mul_f16 v4, v4, v2\n\t"
    "v_pk_mul_f16 v5, v5, v3\n\t"
    "v_exp_f16 v6, v4\n\t"
    "v_exp_f16 v7, v5\n\t"
    "v_mfma_f32_32x32x16_bf16 a[112:127], a[228:231], v[156:159], a[112:127]\n\t"
    "ds_read_b64_tr_b16 a[206:207], v17 offset:6656\n\t"
    "ds_read_b128 v[48:51], v22 offset:128\n\t"
    "v_exp_f16_sdwa v6, v4 dst_sel:WORD_1 dst_unused:UNUSED_PRESERVE src0_sel:WORD_1\n\t"
    "v_exp_f16_sdwa v7, v5 dst_sel:WORD_1 dst_unused:UNUSED_PRESERVE src0_sel:WORD_1\n\t"
    "v_mfma_f32_32x32x16_bf16 a[128:143], a[232:235], v[148:151], a[128:143]\n\t"
    "ds_read_b64_tr_b16 a[208:209], v16 offset:8192\n\t"
    "v_pk_add_f16 v6, v6, 1.0 op_sel_hi:[1,0]\n\t"
    "v_pk_add_f16 v7, v7, 1.0 op_sel_hi:[1,0]\n\t"
    "v_rcp_f16 v4, v6\n\t"
    "v_rcp_f16 v5, v7\n\t"
    "v_mfma_f32_32x32x16_bf16 a[144:159], a[232:235], v[156:159], a[144:159]\n\t"
    "ds_read_b64_tr_b16 a[210:211], v17 offset:8704\n\t"
    "v_rcp_f16_sdwa v4, v6 dst_sel:WORD_1 dst_unused:UNUSED_PRESERVE src0_sel:WORD_1\n\t"
    "v_rcp_f16_sdwa v5, v7 dst_sel:WORD_1 dst_unused:UNUSED_PRESERVE src0_sel:WORD_1\n\t"
    "v_fma_mix_f32 v80, v80, v4, 0 op_sel_hi:[0,1,0]\n\t"
    "v_mfma_f32_32x32x16_bf16 a[160:175], a[236:239], v[148:151], a[160:175]\n\t"
    "ds_read_b64_tr_b16 a[212:213], v16 offset:10240\n\t"
    "v_fma_mix_f32 v82, v82, v5, 0 op_sel_hi:[0,1,0]\n\t"
    "v_fma_mix_f32 v81, v81, v4, 0 op_sel:[0,1,0] op_sel_hi:[0,1,0]\n\t"
    "v_fma_mix_f32 v83, v83, v5, 0 op_sel:[0,1,0] op_sel_hi:[0,1,0]\n\t"
    "v_cvt_pk_bf16_f32 v136, v80, v81\n\t"
    "v_cvt_pk_bf16_f32 v137, v82, v83\n\t"
    "v_mfma_f32_32x32x16_bf16 a[176:191], a[236:239], v[156:159], a[176:191]\n\t"
    "ds_read_b64_tr_b16 a[214:215], v17 offset:10752\n\t"
    "v_subrev_u32 v18, s30, v18\n\t"
    "v_subrev_u32 v19, s30, v19\n\t"
    "v_cvt_pk_f16_f32 v8, v84, v85\n\t"
    "v_cvt_pk_f16_f32 v9, v86, v87\n\t"
    "s_waitcnt lgkmcnt(4)\n\t"
    "v_mfma_f32_32x32x16_bf16 v[96:111], a[192:195], v[160:163], v[48:63]\n\t"
    "ds_read_b64_tr_b16 a[216:217], v16 offset:12288\n\t"
    "v_pk_fma_f16 v10, v8, v8, s60 neg_lo:[1,0,1] neg_hi:[1,0,1]\n\t"
    "v_pk_fma_f16 v11, v9, v9, s60 neg_lo:[1,0,1] neg_hi:[1,0,1]\n\t"
    "v_pk_mul_f16 v10, v10, v8\n\t"
    "v_pk_mul_f16 v11, v11, v9\n\t"
    "v_exp_f16 v12, v10\n\t"
    "v_mfma_f32_32x32x16_bf16 v[112:127], a[192:195], v[208:211], v[48:63]\n\t"
    "ds_read_b64_tr_b16 a[218:219], v17 offset:12800\n\t"
    "v_exp_f16 v13, v11\n\t"
    "v_exp_f16_sdwa v12, v10 dst_sel:WORD_1 dst_unused:UNUSED_PRESERVE src0_sel:WORD_1\n\t"
    "v_exp_f16_sdwa v13, v11 dst_sel:WORD_1 dst_unused:UNUSED_PRESERVE src0_sel:WORD_1\n\t"
    "v_mfma_f32_32x32x16_bf16 v[96:111], a[196:199], v[164:167], v[96:111]\n\t"
    "ds_read_b64_tr_b16 a[220:221], v16 offset:14336\n\t"
    "v_pk_add_f16 v12, v12, 1.0 op_sel_hi:[1,0]\n\t"
    "v_pk_add_f16 v13, v13, 1.0 op_sel_hi:[1,0]\n\t"
    "v_rcp_f16 v10, v12\n\t"
    "v_rcp_f16 v11, v13\n\t"
    "v_mfma_f32_32x32x16_bf16 v[112:127], a[196:199], v[212:215], v[112:127]\n\t"
    "ds_read_b64_tr_b16 a[222:223], v17 offset:14848\n\t"
    "v_rcp_f16_sdwa v10, v12 dst_sel:WORD_1 dst_unused:UNUSED_PRESERVE src0_sel:WORD_1\n\t"
    "v_rcp_f16_sdwa v11, v13 dst_sel:WORD_1 dst_unused:UNUSED_PRESERVE src0_sel:WORD_1\n\t"
    "v_fma_mix_f32 v84, v84, v10, 0 op_sel_hi:[0,1,0]\n\t"
    "v_mfma_f32_32x32x16_bf16 v[96:111], a[200:203], v[168:171], v[96:111]\n\t"
    "ds_read_b64_tr_b16 a[224:225], v16 offset:16384\n\t"
    "v_fma_mix_f32 v86, v86, v11, 0 op_sel_hi:[0,1,0]\n\t"
    "v_fma_mix_f32 v85, v85, v10, 0 op_sel:[0,1,0] op_sel_hi:[0,1,0]\n\t"
    "v_fma_mix_f32 v87, v87, v11, 0 op_sel:[0,1,0] op_sel_hi:[0,1,0]\n\t"
    "v_cvt_pk_bf16_f32 v138, v84, v85\n\t"
    "v_cvt_pk_bf16_f32 v139, v86, v87\n\t"
    "v_mfma_f32_32x32x16_bf16 v[112:127], a[200:203], v[216:219], v[112:127]\n\t"
    "ds_read_b64_tr_b16 a[226:227], v17 offset:16896\n\t"
    "v_cvt_pk_f16_f32 v2, v72, v73\n\t"
    "v_cvt_pk_f16_f32 v3, v74, v75\n\t"
    "v_pk_fma_f16 v4, v2, v2, s60 neg_lo:[1,0,1] neg_hi:[1,0,1]\n\t"
    "v_pk_fma_f16 v5, v3, v3, s60 neg_lo:[1,0,1] neg_hi:[1,0,1]\n\t"
    "v_pk_mul_f16 v4, v4, v2\n\t"
    "v_mfma_f32_32x32x16_bf16 v[96:111], a[204:207], v[172:175], v[96:111]\n\t"
    "ds_read_b64_tr_b16 a[228:229], v16 offset:18432\n\t"
    "v_pk_mul_f16 v5, v5, v3\n\t"
    "v_exp_f16 v6, v4\n\t"
    "v_exp_f16 v7, v5\n\t"
    "v_mfma_f32_32x32x16_bf16 v[112:127], a[204:207], v[220:223], v[112:127]\n\t"
    "ds_read_b64_tr_b16 a[230:231], v17 offset:18944\n\t"
    "v_exp_f16_sdwa v6, v4 dst_sel:WORD_1 dst_unused:UNUSED_PRESERVE src0_sel:WORD_1\n\t"
    "v_exp_f16_sdwa v7, v5 dst_sel:WORD_1 dst_unused:UNUSED_PRESERVE src0_sel:WORD_1\n\t"
    "s_waitcnt lgkmcnt(4)\n\t"
    "v_mfma_f32_32x32x16_bf16 v[96:111], a[208:211], v[176:179], v[96:111]\n\t"
    "ds_read_b64_tr_b16 a[232:233], v16 offset:20480\n\t"
    "v_pk_add_f16 v6, v6, 1.0 op_sel_hi:[1,0]\n\t"
    "v_pk_add_f16 v7, v7, 1.0 op_sel_hi:[1,0]\n\t"
    "v_rcp_f16 v4, v6\n\t"
    "v_rcp_f16 v5, v7\n\t"
    "v_mfma_f32_32x32x16_bf16 v[112:127], a[208:211], v[224:227], v[112:127]\n\t"
    "ds_read_b64_tr_b16 a[234:235], v17 offset:20992\n\t"
    "v_rcp_f16_sdwa v4, v6 dst_sel:WORD_1 dst_unused:UNUSED_PRESERVE src0_sel:WORD_1\n\t"
    "v_rcp_f16_sdwa v5, v7 dst_sel:WORD_1 dst_unused:UNUSED_PRESERVE src0_sel:WORD_1\n\t"
    "v_fma_mix_f32 v72, v72, v4, 0 op_sel_hi:[0,1,0]\n\t"
    "v_mfma_f32_32x32x16_bf16 v[96:111], a[212:215], v[180:183], v[96:111]\n\t"
    "ds_read_b64_tr_b16 a[236:237], v16 offset:22528\n\t"
    "v_fma_mix_f32 v74, v74, v5, 0 op_sel_hi:[0,1,0]\n\t"
    "v_fma_mix_f32 v73, v73, v4, 0 op_sel:[0,1,0] op_sel_hi:[0,1,0]\n\t"
    "v_fma_mix_f32 v75, v75, v5, 0 op_sel:[0,1,0] op_sel_hi:[0,1,0]\n\t"
    "v_cvt_pk_bf16_f32 v132, v72, v73\n\t"
    "v_cvt_pk_bf16_f32 v133, v74, v75\n\t"
    "v_mfma_f32_32x32x16_bf16 v[112:127], a[212:215], v[228:231], v[112:127]\n\t"
    "ds_read_b64_tr_b16 a[238:239], v17 offset:23040\n\t"
    "v_cvt_pk_f16_f32 v8, v76, v77\n\t"
    "v_cvt_pk_f16_f32 v9, v78, v79\n\t"
    "v_pk_fma_f16 v10, v8, v8, s60 neg_lo:[1,0,1] neg_hi:[1,0,1]\n\t"
    "v_pk_fma_f16 v11, v9, v9, s60 neg_lo:[1,0,1] neg_hi:[1,0,1]\n\t"
    "v_pk_mul_f16 v10, v10, v8\n\t"
    "v_mfma_f32_32x32x16_bf16 v[96:111], a[216:219], v[184:187], v[96:111]\n\t"
    "ds_read_b64_tr_b16 a[192:193], v18 offset:0\n\t"
    "v_pk_mul_f16 v11, v11, v9\n\t"
    "v_exp_f16 v12, v10\n\t"
    "v_exp_f16 v13, v11\n\t"
    "v_mfma_f32_32x32x16_bf16 v[112:127], a[216:219], v[232:235], v[112:127]\n\t"
    "ds_read_b64_tr_b16 a[194:195], v18 offset:3072\n\t"
    "v_exp_f16_sdwa v12, v10 dst_sel:WORD_1 dst_unused:UNUSED_PRESERVE src0_sel:WORD_1\n\t"
    "v_exp_f16_sdwa v13, v11 dst_sel:WORD_1 dst_unused:UNUSED_PRESERVE src0_sel:WORD_1\n\t"
    "v_pk_add_f16 v12, v12, 1.0 op_sel_hi:[1,0]\n\t"
    "v_mfma_f32_32x32x16_bf16 v[96:111], a[220:223], v[188:191], v[96:111]\n\t"
    "ds_read_b64_tr_b16 a[196:197], v19 offset:0\n\t"
    "v_pk_add_f16 v13, v13, 1.0 op_sel_hi:[1,0]\n\t"
    "v_rcp_f16 v10, v12\n\t"
    "v_rcp_f16 v11, v13\n\t"
    "v_mfma_f32_32x32x16_bf16 v[112:127], a[220:223], v[236:239], v[112:127]\n\t"
    "ds_read_b64_tr_b16 a[198:199], v19 offset:3072\n\t"
    "v_rcp_f16_sdwa v10, v12 dst_sel:WORD_1 dst_unused:UNUSED_PRESERVE src0_sel:WORD_1\n\t"
    "v_rcp_f16_sdwa v11, v13 dst_sel:WORD_1 dst_unused:UNUSED_PRESERVE src0_sel:WORD_1\n\t"
    "s_waitcnt lgkmcnt(4)\n\t"
    "v_mfma_f32_32x32x16_bf16 v[96:111], a[224:227], v[192:195], v[96:111]\n\t"
    "ds_read_b64_tr_b16 a[200:201], v18 offset:1024\n\t"
    "v_fma_mix_f32 v76, v76, v10, 0 op_sel_hi:[0,1,0]\n\t"
    "v_fma_mix_f32 v78, v78, v11, 0 op_sel_hi:[0,1,0]\n\t"
    "v_fma_mix_f32 v77, v77, v10, 0 op_sel:[0,1,0] op_sel_hi:[0,1,0]\n\t"
    "v_fma_mix_f32 v79, v79, v11, 0 op_sel:[0,1,0] op_sel_hi:[0,1,0]\n\t"
    "v_cvt_pk_bf16_f32 v134, v76, v77\n\t"
    "v_mfma_f32_32x32x16_bf16 v[112:127], a[224:227], v[240:243], v[112:127]\n\t"
    "ds_read_b64_tr_b16 a[202:203], v18 offset:4096\n\t"
    "v_cvt_pk_bf16_f32 v135, v78, v79\n\t"
    "v_cvt_pk_f16_f32 v2, v88, v89\n\t"
    "v_cvt_pk_f16_f32 v3, v90, v91\n\t"
    "v_pk_fma_f16 v4, v2, v2, s60 neg_lo:[1,0,1] neg_hi:[1,0,1]\n\t"
    "v_pk_fma_f16 v5, v3, v3, s60 neg_lo:[1,0,1] neg_hi:[1,0,1]\n\t"
    "v_mfma_f32_32x32x16_bf16 v[96:111], a[228:231], v[196:199], v[96:111]\n\t"
    "ds_read_b64_tr_b16 a[204:205], v19 offset:1024\n\t"
    "v_pk_mul_f16 v4, v4, v2\n\t"
    "v_pk_mul_f16 v5, v5, v3\n\t"
    "v_exp_f16 v6, v4\n\t"
    "v_exp_f16 v7, v5\n\t"
    "v_mfma_f32_32x32x16_bf16 v[112:127], a[228:231], v[244:247], v[112:127]\n\t"
    "ds_read_b64_tr_b16 a[206:207], v19 offset:4096\n\t"
    "v_exp_f16_sdwa v6, v4 dst_sel:WORD_1 dst_unused:UNUSED_PRESERVE src0_sel:WORD_1\n\t"
    "v_exp_f16_sdwa v7, v5 dst_sel:WORD_1 dst_unused:UNUSED_PRESERVE src0_sel:WORD_1\n\t"
    "v_pk_add_f16 v6, v6, 1.0 op_sel_hi:[1,0]\n\t"
    "v_mfma_f32_32x32x16_bf16 v[96:111], a[232:235], v[200:203], v[96:111]\n\t"
    "ds_read_b64_tr_b16 a[208:209], v18 offset:2048\n\t"
    "v_pk_add_f16 v7, v7, 1.0 op_sel_hi:[1,0]\n\t"
    "v_rcp_f16 v4, v6\n\t"
    "v_rcp_f16 v5, v7\n\t"
    "v_mfma_f32_32x32x16_bf16 v[112:127], a[232:235], v[248:251], v[112:127]\n\t"
    "ds_read_b64_tr_b16 a[210:211], v18 offset:5120\n\t"
    "v_rcp_f16_sdwa v4, v6 dst_sel:WORD_1 dst_unused:UNUSED_PRESERVE src0_sel:WORD_1\n\t"
    "v_rcp_f16_sdwa v5, v7 dst_sel:WORD_1 dst_unused:UNUSED_PRESERVE src0_sel:WORD_1\n\t"
    "v_fma_mix_f32 v88, v88, v4, 0 op_sel_hi:[0,1,0]\n\t"
    "v_mfma_f32_32x32x16_bf16 v[96:111], a[236:239], v[204:207], v[96:111]\n\t"
    "ds_read_b64_tr_b16 a[212:213], v19 offset:2048\n\t"
    "v_fma_mix_f32 v90, v90, v5, 0 op_sel_hi:[0,1,0]\n\t"
    "v_fma_mix_f32 v89, v89, v4, 0 op_sel:[0,1,0] op_sel_hi:[0,1,0]\n\t"
    "v_fma_mix_f32 v91, v91, v5, 0 op_sel:[0,1,0] op_sel_hi:[0,1,0]\n\t"
    "v_cvt_pk_bf16_f32 v140, v88, v89\n\t"
    "v_cvt_pk_bf16_f32 v141, v90, v91\n\t"
    "v_mfma_f32_32x32x16_bf16 v[112:127], a[236:239], v[252:255], v[112:127]\n\t"
    "ds_read_b64_tr_b16 a[214:215], v19 offset:5120\n\t"
    "P2_c_%=:\n\t"
    "s_waitcnt lgkmcnt(4)\n\t"
    "v_mfma_f32_32x32x16_bf16 a[0:15], a[192:195], v[128:131], a[0:15]\n\t"
    "ds_read_b64_tr_b16 a[216:217], v18 offset:6144\n\t"
    "v_cvt_pk_f16_f32 v8, v92, v93\n\t"
    "v_cvt_pk_f16_f32 v9, v94, v95\n\t"
    "v_pk_fma_f16 v10, v8, v8, s60 neg_lo:[1,0,1] neg_hi:[1,0,1]\n\t"
    "v_pk_fma_f16 v11, v9, v9, s60 neg_lo:[1,0,1] neg_hi:[1,0,1]\n\t"
    "v_pk_mul_f16 v10, v10, v8\n\t"
    "v_mfma_f32_32x32x16_bf16 a[16:31], a[192:195], v[136:139], a[16:31]\n\t"
    "ds_read_b64_tr_b16 a[218:219], v18 offset:9216\n\t"
    "v_pk_mul_f16 v11, v11, v9\n\t"
    "v_exp_f16 v12, v10\n\t"
    "v_exp_f16 v13, v11\n\t"
    "v_mfma_f32_32x32x16_bf16 a[32:47], a[196:199], v[128:131], a[32:47]\n\t"
    "ds_read_b64_tr_b16 a[220:221], v19 offset:6144\n\t"
    "v_exp_f16_sdwa v12, v10 dst_sel:WORD_1 dst_unused:UNUSED_PRESERVE src0_sel:WORD_1\n\t"
    "v_exp_f16_sdwa v13, v11 dst_sel:WORD_1 dst_unused:UNUSED_PRESERVE src0_sel:WORD_1\n\t"
    "v_pk_add_f16 v12, v12, 1.0 op_sel_hi:[1,0]\n\t"
    "v_pk_add_f16 v13, v13, 1.0 op_sel_hi:[1,0]\n\t"
    "v_mfma_f32_32x32x16_bf16 a[48:63], a[196:199], v[136:139], a[48:63]\n\t"
    "ds_read_b64_tr_b16 a[222:223], v19 offset:9216\n\t"
    "v_rcp_f16 v10, v12\n\t"
    "v_rcp_f16 v11, v13\n\t"
    "v_rcp_f16_sdwa v10, v12 dst_sel:WORD_1 dst_unused:UNUSED_PRESERVE src0_sel:WORD_1\n\t"
    "v_mfma_f32_32x32x16_bf16 a[64:79], a[200:203], v[128:131], a[64:79]\n\t"
    "ds_read_b64_tr_b16 a[224:225], v18 offset:7168\n\t"
    "v_rcp_f16_sdwa v11, v13 dst_sel:WORD_1 dst_unused:UNUSED_PRESERVE src0_sel:WORD_1\n\t"
    "v_fma_mix_f32 v92, v92, v10, 0 op_sel_hi:[0,1,0]\n\t"
    "v_fma_mix_f32 v94, v94, v11, 0 op_sel_hi:[0,1,0]\n\t"
    "v_fma_mix_f32 v93, v93, v10, 0 op_sel:[0,1,0] op_sel_hi:[0,1,0]\n\t"
    "v_mfma_f32_32x32x16_bf16 a[80:95], a[200:203], v[136:139], a[80:95]\n\t"
    "ds_read_b64_tr_b16 a[226:227], v18 offset:10240\n\t"
    "v_fma_mix_f32 v95, v95, v11, 0 op_sel:[0,1,0] op_sel_hi:[0,1,0]\n\t"
    "v_cvt_pk_bf16_f32 v142, v92, v93\n\t"
    "v_cvt_pk_bf16_f32 v143, v94, v95\n\t"
    "v_cvt_pk_f16_f32 v2, v96, v97\n\t"
    "s_barrier\n\t"
    "v_mfma_f32_32x32x16_bf16 a[96:111], a[204:207], v[128:131], a[96:111]\n\t"
    "ds_read_b64_tr_b16 a[228:229], v19 offset:7168\n\t"
    "v_cvt_pk_f16_f32 v3, v98, v99\n\t"
    "v_pk_fma_f16 v4, v2, v2, s60 neg_lo:[1,0,1] neg_hi:[1,0,1]\n\t"
    "v_pk_fma_f16 v5, v3, v3, s60 neg_lo:[1,0,1] neg_hi:[1,0,1]\n\t"
    "v_pk_mul_f16 v4, v4, v2\n\t"
    "v_pk_mul_f16 v5, v5, v3\n\t"
    "v_mfma_f32_32x32x16_bf16 a[112:127], a[204:207], v[136:139], a[112:127]\n\t"
    "ds_read_b64_tr_b16 a[230:231], v19 offset:10240\n\t"
    "ds_read_b128 v[60:63], v22 offset:352\n\t"
    "v_exp_f16 v6, v4\n\t"
    "v_exp_f16 v7, v5\n\t"
    "s_waitcnt lgkmcnt(5)\n\t"
    "v_mfma_f32_32x32x16_bf16 a[128:143], a[208:211], v[128:131], a[128:143]\n\t"
    "ds_read_b64_tr_b16 a[232:233], v18 offset:8192\n\t"
    "v_exp_f16_sdwa v6, v4 dst_sel:WORD_1 dst_unused:UNUSED_PRESERVE src0_sel:WORD_1\n\t"
    "v_exp_f16_sdwa v7, v5 dst_sel:WORD_1 dst_unused:UNUSED_PRESERVE src0_sel:WORD_1\n\t"
    "v_pk_add_f16 v6, v6, 1.0 op_sel_hi:[1,0]\n\t"
    "v_mfma_f32_32x32x16_bf16 a[144:159], a[208:211], v[136:139], a[144:159]\n\t"
    "ds_read_b64_tr_b16 a[234:235], v18 offset:11264\n\t"
    "v_pk_add_f16 v7, v7, 1.0 op_sel_hi:[1,0]\n\t"
    "v_rcp_f16 v4, v6\n\t"
    "v_rcp_f16 v5, v7\n\t"
    "v_mfma_f32_32x32x16_bf16 a[160:175], a[212:215], v[128:131], a[160:175]\n\t"
    "ds_read_b64_tr_b16 a[236:237], v19 offset:8192\n\t"
    "v_rcp_f16_sdwa v4, v6 dst_sel:WORD_1 dst_unused:UNUSED_PRESERVE src0_sel:WORD_1\n\t"
    "v_rcp_f16_sdwa v5, v7 dst_sel:WORD_1 dst_unused:UNUSED_PRESERVE src0_sel:WORD_1\n\t"
    "v_fma_mix_f32 v96, v96, v4, 0 op_sel_hi:[0,1,0]\n\t"
    "v_mfma_f32_32x32x16_bf16 a[176:191], a[212:215], v[136:139], a[176:191]\n\t"
    "ds_read_b64_tr_b16 a[238:239], v19 offset:11264\n\t"
    "ds_read_b128 v[56:59], v22 offset:320\n\t"
    "v_fma_mix_f32 v98, v98, v5, 0 op_sel_hi:[0,1,0]\n\t"
    "v_fma_mix_f32 v97, v97, v4, 0 op_sel:[0,1,0] op_sel_hi:[0,1,0]\n\t"
    "v_fma_mix_f32 v99, v99, v5, 0 op_sel:[0,1,0] op_sel_hi:[0,1,0]\n\t"
    "v_cvt_pk_bf16_f32 v144, v96, v97\n\t"
    "v_mfma_f32_32x32x16_bf16 a[0:15], a[216:219], v[132:135], a[0:15]\n\t"
    "ds_read_b64_tr_b16 a[192:193], v14 offset:24576\n\t"
    "v_cvt_pk_bf16_f32 v145, v98, v99\n\t"
    "v_cvt_pk_f16_f32 v8, v100, v101\n\t"
    "v_cvt_pk_f16_f32 v9, v102, v103\n\t"
    "v_pk_fma_f16 v10, v8, v8, s60 neg_lo:[1,0,1] neg_hi:[1,0,1]\n\t"
    "v_pk_fma_f16 v11, v9, v9, s60 neg_lo:[1,0,1] neg_hi:[1,0,1]\n\t"
    "v_mfma_f32_32x32x16_bf16 a[16:31], a[216:219], v[140:143], a[16:31]\n\t"
    "ds_read_b64_tr_b16 a[194:195], v15 offset:25088\n\t"
    "v_pk_mul_f16 v10, v10, v8\n\t"
    "v_pk_mul_f16 v11, v11, v9\n\t"
    "v_exp_f16 v12, v10\n\t"
    "v_exp_f16 v13, v11\n\t"
    "v_mfma_f32_32x32x16_bf16 a[32:47], a[220:223], v[132:135], a[32:47]\n\t"
    "ds_read_b64_tr_b16 a[196:197], v14 offset:26624\n\t"
    "v_exp_f16_sdwa v12, v10 dst_sel:WORD_1 dst_unused:UNUSED_PRESERVE src0_sel:WORD_1\n\t"
    "v_exp_f16_sdwa v13, v11 dst_sel:WORD_1 dst_unused:UNUSED_PRESERVE src0_sel:WORD_1\n\t"
    "v_pk_add_f16 v12, v12, 1.0 op_sel_hi:[1,0]\n\t"
    "v_mfma_f32_32x32x16_bf16 a[48:63], a[220:223], v[140:143], a[48:63]\n\t"
    "ds_read_b64_tr_b16 a[198:199], v15 offset:27136\n\t"
    "ds_read_b128 v[52:55], v22 offset:288\n\t"
    "v_pk_add_f16 v13, v13, 1.0 op_sel_hi:[1,0]\n\t"
    "v_rcp_f16 v10, v12\n\t"
    "s_waitcnt lgkmcnt(6)\n\t"
    "v_mfma_f32_32x32x16_bf16 a[64:79], a[224:227], v[132:135], a[64:79]\n\t"
    "ds_read_b64_tr_b16 a[200:201], v14 offset:28672\n\t"
    "v_rcp_f16 v11, v13\n\t"
    "v_rcp_f16_sdwa v10, v12 dst_sel:WORD_1 dst_unused:UNUSED_PRESERVE src0_sel:WORD_1\n\t"
    "v_rcp_f16_sdwa v11, v13 dst_sel:WORD_1 dst_unused:UNUSED_PRESERVE src0_sel:WORD_1\n\t"
    "v_mfma_f32_32x32x16_bf16 a[80:95], a[224:227], v[140:143], a[80:95]\n\t"
    "ds_read_b64_tr_b16 a[202:203], v15 offset:29184\n\t"
    "v_fma_mix_f32 v100, v100, v10, 0 op_sel_hi:[0,1,0]\n\t"
    "v_fma_mix_f32 v102, v102, v11, 0 op_sel_hi:[0,1,0]\n\t"
    "v_fma_mix_f32 v101, v101, v10, 0 op_sel:[0,1,0] op_sel_hi:[0,1,0]\n\t"
    "v_fma_mix_f32 v103, v103, v11, 0 op_sel:[0,1,0] op_sel_hi:[0,1,0]\n\t"
    "v_cvt_pk_bf16_f32 v146, v100, v101\n\t"
    "v_mfma_f32_32x32x16_bf16 a[96:111], a[228:231], v[132:135], a[96:111]\n\t"
    "ds_read_b64_tr_b16 a[204:205], v14 offset:30720\n\t"
    "v_cvt_pk_bf16_f32 v147, v102, v103\n\t"
    "v_cvt_pk_f16_f32 v2, v112, v113\n\t"
    "v_cvt_pk_f16_f32 v3, v114, v115\n\t"
    "v_pk_fma_f16 v4, v2, v2, s60 neg_lo:[1,0,1] neg_hi:[1,0,1]\n\t"
    "v_pk_fma_f16 v5, v3, v3, s60 neg_lo:[1,0,1] neg_hi:[1,0,1]\n\t"
    "v_mfma_f32_32x32x16_bf16 a[112:127], a[228:231], v[140:143], a[112:127]\n\t"
    "ds_read_b64_tr_b16 a[206:207], v15 offset:31232\n\t"
    "ds_read_b128 v[48:51], v22 offset:256\n\t"
    "v_pk_mul_f16 v4, v4, v2\n\t"
    "v_pk_mul_f16 v5, v5, v3\n\t"
    "v_exp_f16 v6, v4\n\t"
    "v_mfma_f32_32x32x16_bf16 a[128:143], a[232:235], v[132:135], a[128:143]\n\t"
    "ds_read_b64_tr_b16 a[208:209], v14 offset:32768\n\t"
    "v_exp_f16 v7, v5\n\t"
    "v_exp_f16_sdwa v6, v4 dst_sel:WORD_1 dst_unused:UNUSED_PRESERVE src0_sel:WORD_1\n\t"
    "v_exp_f16_sdwa v7, v5 dst_sel:WORD_1 dst_unused:UNUSED_PRESERVE src0_sel:WORD_1\n\t"
    "v_mfma_f32_32x32x16_bf16 a[144:159], a[232:235], v[140:143], a[144:159]\n\t"
    "ds_read_b64_tr_b16 a[210:211], v15 offset:33280\n\t"
    "v_pk_add_f16 v6, v6, 1.0 op_sel_hi:[1,0]\n\t"
    "v_pk_add_f16 v7, v7, 1.0 op_sel_hi:[1,0]\n\t"
    "v_rcp_f16 v4, v6\n\t"
    "v_rcp_f16 v5, v7\n\t"
    "v_mfma_f32_32x32x16_bf16 a[160:175], a[236:239], v[132:135], a[160:175]\n\t"
    "ds_read_b64_tr_b16 a[212:213], v14 offset:34816\n\t"
    "v_rcp_f16_sdwa v4, v6 dst_sel:WORD_1 dst_unused:UNUSED_PRESERVE src0_sel:WORD_1\n\t"
    "v_rcp_f16_sdwa v5, v7 dst_sel:WORD_1 dst_unused:UNUSED_PRESERVE src0_sel:WORD_1\n\t"
    "v_fma_mix_f32 v112, v112, v4, 0 op_sel_hi:[0,1,0]\n\t"
    "v_mfma_f32_32x32x16_bf16 a[176:191], a[236:239], v[140:143], a[176:191]\n\t"
    "ds_read_b64_tr_b16 a[214:215], v15 offset:35328\n\t"
    "v_fma_mix_f32 v114, v114, v5, 0 op_sel_hi:[0,1,0]\n\t"
    "v_fma_mix_f32 v113, v113, v4, 0 op_sel:[0,1,0] op_sel_hi:[0,1,0]\n\t"
    "v_fma_mix_f32 v115, v115, v5, 0 op_sel:[0,1,0] op_sel_hi:[0,1,0]\n\t"
    "v_cvt_pk_bf16_f32 v152, v112, v113\n\t"
    "s_waitcnt lgkmcnt(4)\n\t"
    "v_mfma_f32_32x32x16_bf16 v[64:79], a[192:195], v[160:163], v[48:63]\n\t"
    "ds_read_b64_tr_b16 a[216:217], v14 offset:36864\n\t"
    "v_cvt_pk_bf16_f32 v153, v114, v115\n\t"
    "v_cvt_pk_f16_f32 v8, v116, v117\n\t"
    "v_cvt_pk_f16_f32 v9, v118, v119\n\t"
    "v_pk_fma_f16 v10, v8, v8, s60 neg_lo:[1,0,1] neg_hi:[1,0,1]\n\t"
    "v_pk_fma_f16 v11, v9, v9, s60 neg_lo:[1,0,1] neg_hi:[1,0,1]\n\t"
    "v_mfma_f32_32x32x16_bf16 v[80:95], a[192:195], v[208:211], v[48:63]\n\t"
    "ds_read_b64_tr_b16 a[218:219], v15 offset:37376\n\t"
    "v_pk_mul_f16 v10, v10, v8\n\t"
    "v_pk_mul_f16 v11, v11, v9\n\t"
    "v_exp_f16 v12, v10\n\t"
    "v_exp_f16 v13, v11\n\t"
    "v_mfma_f32_32x32x16_bf16 v[64:79], a[196:199], v[164:167], v[64:79]\n\t"
    "ds_read_b64_tr_b16 a[220:221], v14 offset:38912\n\t"
    "v_exp_f16_sdwa v12, v10 dst_sel:WORD_1 dst_unused:UNUSED_PRESERVE src0_sel:WORD_1\n\t"
    "v_exp_f16_sdwa v13, v11 dst_sel:WORD_1 dst_unused:UNUSED_PRESERVE src0_sel:WORD_1\n\t"
    "v_pk_add_f16 v12, v12, 1.0 op_sel_hi:[1,0]\n\t"
    "v_mfma_f32_32x32x16_bf16 v[80:95], a[196:199], v[212:215], v[80:95]\n\t"
    "ds_read_b64_tr_b16 a[222:223], v15 offset:39424\n\t"
    "v_pk_add_f16 v13, v13, 1.0 op_sel_hi:[1,0]\n\t"
    "v_rcp_f16 v10, v12\n\t"
    "v_rcp_f16 v11, v13\n\t"
    "v_mfma_f32_32x32x16_bf16 v[64:79], a[200:203], v[168:171], v[64:79]\n\t"
    "ds_read_b64_tr_b16 a[224:225], v14 offset:40960\n\t"
    "v_rcp_f16_sdwa v10, v12 dst_sel:WORD_1 dst_unused:UNUSED_PRESERVE src0_sel:WORD_1\n\t"
    "v_rcp_f16_sdwa v11, v13 dst_sel:WORD_1 dst_unused:UNUSED_PRESERVE src0_sel:WORD_1\n\t"
    "v_fma_mix_f32 v116, v116, v10, 0 op_sel_hi:[0,1,0]\n\t"
    "v_mfma_f32_32x32x16_bf16 v[80:95], a[200:203], v[216:219], v[80:95]\n\t"
    "ds_read_b64_tr_b16 a[226:227], v15 offset:41472\n\t"
    "v_fma_mix_f32 v118, v118, v11, 0 op_sel_hi:[0,1,0]\n\t"
    "v_fma_mix_f32 v117, v117, v10, 0 op_sel:[0,1,0] op_sel_hi:[0,1,0]\n\t"
    "v_fma_mix_f32 v119, v119, v11, 0 op_sel:[0,1,0] op_sel_hi:[0,1,0]\n\t"
    "v_cvt_pk_bf16_f32 v154, v116, v117\n\t"
    "v_cvt_pk_bf16_f32 v155, v118, v119\n\t"
    "v_mfma_f32_32x32x16_bf16 v[64:79], a[204:207], v[172:175], v[64:79]\n\t"
    "ds_read_b64_tr_b16 a[228:229], v14 offset:43008\n\t"
    "v_cvt_pk_f16_f32 v2, v104, v105\n\t"
    "v_cvt_pk_f16_f32 v3, v106, v107\n\t"
    "v_pk_fma_f16 v4, v2, v2, s60 neg_lo:[1,0,1] neg_hi:[1,0,1]\n\t"
    "v_pk_fma_f16 v5, v3, v3, s60 neg_lo:[1,0,1] neg_hi:[1,0,1]\n\t"
    "v_pk_mul_f16 v4, v4, v2\n\t"
    "v_mfma_f32_32x32x16_bf16 v[80:95], a[204:207], v[220:223], v[80:95]\n\t"
    "ds_read_b64_tr_b16 a[230:231], v15 offset:43520\n\t"
    "v_pk_mul_f16 v5, v5, v3\n\t"
    "v_exp_f16 v6, v4\n\t"
    "v_exp_f16 v7, v5\n\t"
    "s_waitcnt lgkmcnt(4)\n\t"
    "v_mfma_f32_32x32x16_bf16 v[64:79], a[208:211], v[176:179], v[64:79]\n\t"
    "ds_read_b64_tr_b16 a[232:233], v14 offset:45056\n\t"
    "v_exp_f16_sdwa v6, v4 dst_sel:WORD_1 dst_unused:UNUSED_PRESERVE src0_sel:WORD_1\n\t"
    "v_exp_f16_sdwa v7, v5 dst_sel:WORD_1 dst_unused:UNUSED_PRESERVE src0_sel:WORD_1\n\t"
    "v_pk_add_f16 v6, v6, 1.0 op_sel_hi:[1,0]\n\t"
    "v_mfma_f32_32x32x16_bf16 v[80:95], a[208:211], v[224:227], v[80:95]\n\t"
    "ds_read_b64_tr_b16 a[234:235], v15 offset:45568\n\t"
    "v_pk_add_f16 v7, v7, 1.0 op_sel_hi:[1,0]\n\t"
    "v_rcp_f16 v4, v6\n\t"
    "v_rcp_f16 v5, v7\n\t"
    "v_mfma_f32_32x32x16_bf16 v[64:79], a[212:215], v[180:183], v[64:79]\n\t"
    "ds_read_b64_tr_b16 a[236:237], v14 offset:47104\n\t"
    "v_rcp_f16_sdwa v4, v6 dst_sel:WORD_1 dst_unused:UNUSED_PRESERVE src0_sel:WORD_1\n\t"
    "v_rcp_f16_sdwa v5, v7 dst_sel:WORD_1 dst_unused:UNUSED_PRESERVE src0_sel:WORD_1\n\t"
    "v_fma_mix_f32 v104, v104, v4, 0 op_sel_hi:[0,1,0]\n\t"
    "v_mfma_f32_32x32x16_bf16 v[80:95], a[212:215], v[228:231], v[80:95]\n\t"
    "ds_read_b64_tr_b16 a[238:239], v15 offset:47616\n\t"
    "v_fma_mix_f32 v106, v106, v5, 0 op_sel_hi:[0,1,0]\n\t"
    "v_fma_mix_f32 v105, v105, v4, 0 op_sel:[0,1,0] op_sel_hi:[0,1,0]\n\t"
    "v_fma_mix_f32 v107, v107, v5, 0 op_sel:[0,1,0] op_sel_hi:[0,1,0]\n\t"
    "v_cvt_pk_bf16_f32 v148, v104, v105\n\t"
    "v_cvt_pk_bf16_f32 v149, v106, v107\n\t"
    "v_mfma_f32_32x32x16_bf16 v[64:79], a[216:219], v[184:187], v[64:79]\n\t"
    "ds_read_b64_tr_b16 a[192:193], v18 offset:12288\n\t"
    "v_cvt_pk_f16_f32 v8, v108, v109\n\t"
    "v_cvt_pk_f16_f32 v9, v110, v111\n\t"
    "v_pk_fma_f16 v10, v8, v8, s60 neg_lo:[1,0,1] neg_hi:[1,0,1]\n\t"
    "v_pk_fma_f16 v11, v9, v9, s60 neg_lo:[1,0,1] neg_hi:[1,0,1]\n\t"
    "v_pk_mul_f16 v10, v10, v8\n\t"
    "v_mfma_f32_32x32x16_bf16 v[80:95], a[216:219], v[232:235], v[80:95]\n\t"
    "ds_read_b64_tr_b16 a[194:195], v18 offset:15360\n\t"
    "v_pk_mul_f16 v11, v11, v9\n\t"
    "v_exp_f16 v12, v10\n\t"
    "v_exp_f16 v13, v11\n\t"
    "v_mfma_f32_32x32x16_bf16 v[64:79], a[220:223], v[188:191], v[64:79]\n\t"
    "ds_read_b64_tr_b16 a[196:197], v19 offset:12288\n\t"
    "v_exp_f16_sdwa v12, v10 dst_sel:WORD_1 dst_unused:UNUSED_PRESERVE src0_sel:WORD_1\n\t"
    "v_exp_f16_sdwa v13, v11 dst_sel:WORD_1 dst_unused:UNUSED_PRESERVE src0_sel:WORD_1\n\t"
    "v_pk_add_f16 v12, v12, 1.0 op_sel_hi:[1,0]\n\t"
    "v_mfma_f32_32x32x16_bf16 v[80:95], a[220:223], v[236:239], v[80:95]\n\t"
    "ds_read_b64_tr_b16 a[198:199], v19 offset:15360\n\t"
    "v_pk_add_f16 v13, v13, 1.0 op_sel_hi:[1,0]\n\t"
    "v_rcp_f16 v10, v12\n\t"
    "v_rcp_f16 v11, v13\n\t"
    "s_waitcnt lgkmcnt(4)\n\t"
    "v_mfma_f32_32x32x16_bf16 v[64:79], a[224:227], v[192:195], v[64:79]\n\t"
    "ds_read_b64_tr_b16 a[200:201], v18 offset:13312\n\t"
    "v_rcp_f16_sdwa v10, v12 dst_sel:WORD_1 dst_unused:UNUSED_PRESERVE src0_sel:WORD_1\n\t"
    "v_rcp_f16_sdwa v11, v13 dst_sel:WORD_1 dst_unused:UNUSED_PRESERVE src0_sel:WORD_1\n\t"
    "v_fma_mix_f32 v108, v108, v10, 0 op_sel_hi:[0,1,0]\n\t"
    "v_mfma_f32_32x32x16_bf16 v[80:95], a[224:227], v[240:243], v[80:95]\n\t"
    "ds_read_b64_tr_b16 a[202:203], v18 offset:16384\n\t"
    "v_fma_mix_f32 v110, v110, v11, 0 op_sel_hi:[0,1,0]\n\t"
    "v_fma_mix_f32 v109, v109, v10, 0 op_sel:[0,1,0] op_sel_hi:[0,1,0]\n\t"
    "v_fma_mix_f32 v111, v111, v11, 0 op_sel:[0,1,0] op_sel_hi:[0,1,0]\n\t"
    "v_cvt_pk_bf16_f32 v150, v108, v109\n\t"
    "v_cvt_pk_bf16_f32 v151, v110, v111\n\t"
    "v_mfma_f32_32x32x16_bf16 v[64:79], a[228:231], v[196:199], v[64:79]\n\t"
    "ds_read_b64_tr_b16 a[204:205], v19 offset:13312\n\t"
    "v_cvt_pk_f16_f32 v2, v120, v121\n\t"
    "v_cvt_pk_f16_f32 v3, v122, v123\n\t"
    "v_pk_fma_f16 v4, v2, v2, s60 neg_lo:[1,0,1] neg_hi:[1,0,1]\n\t"
    "v_pk_fma_f16 v5, v3, v3, s60 neg_lo:[1,0,1] neg_hi:[1,0,1]\n\t"
    "v_pk_mul_f16 v4, v4, v2\n\t"
    "v_mfma_f32_32x32x16_bf16 v[80:95], a[228:231], v[244:247], v[80:95]\n\t"
    "ds_read_b64_tr_b16 a[206:207], v19 offset:16384\n\t"
    "v_pk_mul_f16 v5, v5, v3\n\t"
    "v_exp_f16 v6, v4\n\t"
    "v_exp_f16 v7, v5\n\t"
    "v_mfma_f32_32x32x16_bf16 v[64:79], a[232:235], v[200:203], v[64:79]\n\t"
    "ds_read_b64_tr_b16 a[208:209], v18 offset:14336\n\t"
    "v_exp_f16_sdwa v6, v4 dst_sel:WORD_1 dst_unused:UNUSED_PRESERVE src0_sel:WORD_1\n\t"
    "v_exp_f16_sdwa v7, v5 dst_sel:WORD_1 dst_unused:UNUSED_PRESERVE src0_sel:WORD_1\n\t"
    "v_pk_add_f16 v6, v6, 1.0 op_sel_hi:[1,0]\n\t"
    "v_mfma_f32_32x32x16_bf16 v[80:95], a[232:235], v[248:251], v[80:95]\n\t"
    "ds_read_b64_tr_b16 a[210:211], v18 offset:17408\n\t"
    "v_pk_add_f16 v7, v7, 1.0 op_sel_hi:[1,0]\n\t"
    "v_rcp_f16 v4, v6\n\t"
    "v_rcp_f16 v5, v7\n\t"
    "v_mfma_f32_32x32x16_bf16 v[64:79], a[236:239], v[204:207], v[64:79]\n\t"
    "ds_read_b64_tr_b16 a[212:213], v19 offset:14336\n\t"
    "v_rcp_f16_sdwa v4, v6 dst_sel:WORD_1 dst_unused:UNUSED_PRESERVE src0_sel:WORD_1\n\t"
    "v_rcp_f16_sdwa v5, v7 dst_sel:WORD_1 dst_unused:UNUSED_PRESERVE src0_sel:WORD_1\n\t"
    "v_fma_mix_f32 v120, v120, v4, 0 op_sel_hi:[0,1,0]\n\t"
    "v_mfma_f32_32x32x16_bf16 v[80:95], a[236:239], v[252:255], v[80:95]\n\t"
    "ds_read_b64_tr_b16 a[214:215], v19 offset:17408\n\t"
    "v_fma_mix_f32 v122, v122, v5, 0 op_sel_hi:[0,1,0]\n\t"
    "v_fma_mix_f32 v121, v121, v4, 0 op_sel:[0,1,0] op_sel_hi:[0,1,0]\n\t"
    "v_fma_mix_f32 v123, v123, v5, 0 op_sel:[0,1,0] op_sel_hi:[0,1,0]\n\t"
    "v_cvt_pk_bf16_f32 v156, v120, v121\n\t"
    "v_cvt_pk_bf16_f32 v157, v122, v123\n\t"
    "s_waitcnt lgkmcnt(4)\n\t"
    "v_mfma_f32_32x32x16_bf16 a[0:15], a[192:195], v[144:147], a[0:15]\n\t"
    "ds_read_b64_tr_b16 a[216:217], v18 offset:18432\n\t"
    "v_cvt_pk_f16_f32 v8, v124, v125\n\t"
    "v_cvt_pk_f16_f32 v9, v126, v127\n\t"
    "v_pk_fma_f16 v10, v8, v8, s60 neg_lo:[1,0,1] neg_hi:[1,0,1]\n\t"
    "v_pk_fma_f16 v11, v9, v9, s60 neg_lo:[1,0,1] neg_hi:[1,0,1]\n\t"
    "v_pk_mul_f16 v10, v10, v8\n\t"
    "v_mfma_f32_32x32x16_bf16 a[16:31], a[192:195], v[152:155], a[16:31]\n\t"
    "ds_read_b64_tr_b16 a[218:219], v18 offset:21504\n\t"
    "v_pk_mul_f16 v11, v11, v9\n\t"
    "v_exp_f16 v12, v10\n\t"
    "v_exp_f16 v13, v11\n\t"
    "v_mfma_f32_32x32x16_bf16 a[32:47], a[196:199], v[144:147], a[32:47]\n\t"
    "ds_read_b64_tr_b16 a[220:221], v19 offset:18432\n\t"
    "v_exp_f16_sdwa v12, v10 dst_sel:WORD_1 dst_unused:UNUSED_PRESERVE src0_sel:WORD_1\n\t"
    "v_exp_f16_sdwa v13, v11 dst_sel:WORD_1 dst_unused:UNUSED_PRESERVE src0_sel:WORD_1\n\t"
    "v_pk_add_f16 v12, v12, 1.0 op_sel_hi:[1,0]\n\t"
    "v_pk_add_f16 v13, v13, 1.0 op_sel_hi:[1,0]\n\t"
    "v_mfma_f32_32x32x16_bf16 a[48:63], a[196:199], v[152:155], a[48:63]\n\t"
    "ds_read_b64_tr_b16 a[222:223], v19 offset:21504\n\t"
    "v_rcp_f16 v10, v12\n\t"
    "v_rcp_f16 v11, v13\n\t"
    "v_rcp_f16_sdwa v10, v12 dst_sel:WORD_1 dst_unused:UNUSED_PRESERVE src0_sel:WORD_1\n\t"
    "v_mfma_f32_32x32x16_bf16 a[64:79], a[200:203], v[144:147], a[64:79]\n\t"
    "ds_read_b64_tr_b16 a[224:225], v18 offset:19456\n\t"
    "v_rcp_f16_sdwa v11, v13 dst_sel:WORD_1 dst_unused:UNUSED_PRESERVE src0_sel:WORD_1\n\t"
    "v_fma_mix_f32 v124, v124, v10, 0 op_sel_hi:[0,1,0]\n\t"
    "v_fma_mix_f32 v126, v126, v11, 0 op_sel_hi:[0,1,0]\n\t"
    "v_fma_mix_f32 v125, v125, v10, 0 op_sel:[0,1,0] op_sel_hi:[0,1,0]\n\t"
    "v_mfma_f32_32x32x16_bf16 a[80:95], a[200:203], v[152:155], a[80:95]\n\t"
    "ds_read_b64_tr_b16 a[226:227], v18 offset:22528\n\t"
    "v_fma_mix_f32 v127, v127, v11, 0 op_sel:[0,1,0] op_sel_hi:[0,1,0]\n\t"
    "v_cvt_pk_bf16_f32 v158, v124, v125\n\t"
    "v_cvt_pk_bf16_f32 v159, v126, v127\n\t"
    "v_cvt_pk_f16_f32 v2, v64, v65\n\t"
    "v_cvt_pk_f16_f32 v3, v66, v67\n\t"
    "v_mfma_f32_32x32x16_bf16 a[96:111], a[204:207], v[144:147], a[96:111]\n\t"
    "ds_read_b64_tr_b16 a[228:229], v19 offset:19456\n\t"
    "v_pk_fma_f16 v4, v2, v2, s60 neg_lo:[1,0,1] neg_hi:[1,0,1]\n\t"
    "v_pk_fma_f16 v5, v3, v3, s60 neg_lo:[1,0,1] neg_hi:[1,0,1]\n\t"
    "v_pk_mul_f16 v4, v4, v2\n\t"
    "v_pk_mul_f16 v5, v5, v3\n\t"
    "v_exp_f16 v6, v4\n\t"
    "v_mfma_f32_32x32x16_bf16 a[112:127], a[204:207], v[152:155], a[112:127]\n\t"
    "ds_read_b64_tr_b16 a[230:231], v19 offset:22528\n\t"
    "ds_read_b128 v[60:63], v22 offset:480\n\t"
    "v_exp_f16 v7, v5\n\t"
    "v_exp_f16_sdwa v6, v4 dst_sel:WORD_1 dst_unused:UNUSED_PRESERVE src0_sel:WORD_1\n\t"
    "s_waitcnt lgkmcnt(5)\n\t"
    "v_mfma_f32_32x32x16_bf16 a[128:143], a[208:211], v[144:147], a[128:143]\n\t"
    "ds_read_b64_tr_b16 a[232:233], v18 offset:20480\n\t"
    "v_exp_f16_sdwa v7, v5 dst_sel:WORD_1 dst_unused:UNUSED_PRESERVE src0_sel:WORD_1\n\t"
    "v_pk_add_f16 v6, v6, 1.0 op_sel_hi:[1,0]\n\t"
    "v_pk_add_f16 v7, v7, 1.0 op_sel_hi:[1,0]\n\t"
    "v_rcp_f16 v4, v6\n\t"
    "v_mfma_f32_32x32x16_bf16 a[144:159], a[208:211], v[152:155], a[144:159]\n\t"
    "ds_read_b64_tr_b16 a[234:235], v18 offset:23552\n\t"
    "v_rcp_f16 v5, v7\n\t"
    "v_rcp_f16_sdwa v4, v6 dst_sel:WORD_1 dst_unused:UNUSED_PRESERVE src0_sel:WORD_1\n\t"
    "v_rcp_f16_sdwa v5, v7 dst_sel:WORD_1 dst_unused:UNUSED_PRESERVE src0_sel:WORD_1\n\t"
    "v_mfma_f32_32x32x16_bf16 a[160:175], a[212:215], v[144:147], a[160:175]\n\t"
    "ds_read_b64_tr_b16 a[236:237], v19 offset:20480\n\t"
    "v_fma_mix_f32 v64, v64, v4, 0 op_sel_hi:[0,1,0]\n\t"
    "v_fma_mix_f32 v66, v66, v5, 0 op_sel_hi:[0,1,0]\n\t"
    "v_fma_mix_f32 v65, v65, v4, 0 op_sel:[0,1,0] op_sel_hi:[0,1,0]\n\t"
    "v_fma_mix_f32 v67, v67, v5, 0 op_sel:[0,1,0] op_sel_hi:[0,1,0]\n\t"
    "v_cvt_pk_bf16_f32 v128, v64, v65\n\t"
    "v_mfma_f32_32x32x16_bf16 a[176:191], a[212:215], v[152:155], a[176:191]\n\t"
    "ds_read_b64_tr_b16 a[238:239], v19 offset:23552\n\t"
    "ds_read_b128 v[56:59], v22 offset:448\n\t"
    "v_cvt_pk_bf16_f32 v129, v66, v67\n\t"
    "v_cvt_pk_f16_f32 v8, v68, v69\n\t"
    "v_cvt_pk_f16_f32 v9, v70, v71\n\t"
    "v_pk_fma_f16 v10, v8, v8, s60 neg_lo:[1,0,1] neg_hi:[1,0,1]\n\t"
    "v_mfma_f32_32x32x16_bf16 a[0:15], a[216:219], v[148:151], a[0:15]\n\t"
    "ds_read_b64_tr_b16 a[192:193], v16 offset:24576\n\t"
    "v_pk_fma_f16 v11, v9, v9, s60 neg_lo:[1,0,1] neg_hi:[1,0,1]\n\t"
    "v_pk_mul_f16 v10, v10, v8\n\t"
    "v_pk_mul_f16 v11, v11, v9\n\t"
    "v_exp_f16 v12, v10\n\t"
    "v_mfma_f32_32x32x16_bf16 a[16:31], a[216:219], v[156:159], a[16:31]\n\t"
    "ds_read_b64_tr_b16 a[194:195], v17 offset:25088\n\t"
    "v_exp_f16 v13, v11\n\t"
    "v_exp_f16_sdwa v12, v10 dst_sel:WORD_1 dst_unused:UNUSED_PRESERVE src0_sel:WORD_1\n\t"
    "v_exp_f16_sdwa v13, v11 dst_sel:WORD_1 dst_unused:UNUSED_PRESERVE src0_sel:WORD_1\n\t"
    "v_mfma_f32_32x32x16_bf16 a[32:47], a[220:223], v[148:151], a[32:47]\n\t"
    "ds_read_b64_tr_b16 a[196:197], v16 offset:26624\n\t"
    "v_pk_add_f16 v12, v12, 1.0 op_sel_hi:[1,0]\n\t"
    "v_pk_add_f16 v13, v13, 1.0 op_sel_hi:[1,0]\n\t"
    "v_rcp_f16 v10, v12\n\t"
    "v_rcp_f16 v11, v13\n\t"
    "v_mfma_f32_32x32x16_bf16 a[48:63], a[220:223], v[156:159], a[48:63]\n\t"
    "ds_read_b64_tr_b16 a[198:199], v17 offset:27136\n\t"
    "ds_read_b128 v[52:55], v22 offset:416\n\t"
    "v_rcp_f16_sdwa v10, v12 dst_sel:WORD_1 dst_unused:UNUSED_PRESERVE src0_sel:WORD_1\n\t"
    "v_rcp_f16_sdwa v11, v13 dst_sel:WORD_1 dst_unused:UNUSED_PRESERVE src0_sel:WORD_1\n\t"
    "s_waitcnt lgkmcnt(6)\n\t"
    "v_mfma_f32_32x32x16_bf16 a[64:79], a[224:227], v[148:151], a[64:79]\n\t"
    "ds_read_b64_tr_b16 a[200:201], v16 offset:28672\n\t"
    "v_fma_mix_f32 v68, v68, v10, 0 op_sel_hi:[0,1,0]\n\t"
    "v_fma_mix_f32 v70, v70, v11, 0 op_sel_hi:[0,1,0]\n\t"
    "v_fma_mix_f32 v69, v69, v10, 0 op_sel:[0,1,0] op_sel_hi:[0,1,0]\n\t"
    "v_fma_mix_f32 v71, v71, v11, 0 op_sel:[0,1,0] op_sel_hi:[0,1,0]\n\t"
    "v_cvt_pk_bf16_f32 v130, v68, v69\n\t"
    "v_mfma_f32_32x32x16_bf16 a[80:95], a[224:227], v[156:159], a[80:95]\n\t"
    "ds_read_b64_tr_b16 a[202:203], v17 offset:29184\n\t"
    "v_cvt_pk_bf16_f32 v131, v70, v71\n\t"
    "v_cvt_pk_f16_f32 v2, v80, v81\n\t"
    "v_cvt_pk_f16_f32 v3, v82, v83\n\t"
    "v_pk_fma_f16 v4, v2, v2, s60 neg_lo:[1,0,1] neg_hi:[1,0,1]\n\t"
    "v_pk_fma_f16 v5, v3, v3, s60 neg_lo:[1,0,1] neg_hi:[1,0,1]\n\t"
    "v_mfma_f32_32x32x16_bf16 a[96:111], a[228:231], v[148:151], a[96:111]\n\t"
    "ds_read_b64_tr_b16 a[204:205], v16 offset:30720\n\t"
    "v_pk_mul_f16 v4, v4, v2\n\t"
    "v_pk_mul_f16 v5, v5, v3\n\t"
    "v_exp_f16 v6, v4\n\t"
    "v_exp_f16 v7, v5\n\t"
    "v_mfma_f32_32x32x16_bf16 a[112:127], a[228:231], v[156:159], a[112:127]\n\t"
    "ds_read_b64_tr_b16 a[206:207], v17 offset:31232\n\t"
    "ds_read_b128 v[48:51], v22 offset:384\n\t"
    "v_exp_f16_sdwa v6, v4 dst_sel:WORD_1 dst_unused:UNUSED_PRESERVE src0_sel:WORD_1\n\t"
    "v_exp_f16_sdwa v7, v5 dst_sel:WORD_1 dst_unused:UNUSED_PRESERVE src0_sel:WORD_1\n\t"
    "v_mfma_f32_32x32x16_bf16 a[128:143], a[232:235], v[148:151], a[128:143]\n\t"
    "ds_read_b64_tr_b16 a[208:209], v16 offset:32768\n\t"
    "v_pk_add_f16 v6, v6, 1.0 op_sel_hi:[1,0]\n\t"
    "v_pk_add_f16 v7, v7, 1.0 op_sel_hi:[1,0]\n\t"
    "v_rcp_f16 v4, v6\n\t"
    "v_rcp_f16 v5, v7\n\t"
    "v_mfma_f32_32x32x16_bf16 a[144:159], a[232:235], v[156:159], a[144:159]\n\t"
    "ds_read_b64_tr_b16 a[210:211], v17 offset:33280\n\t"
    "v_rcp_f16_sdwa v4, v6 dst_sel:WORD_1 dst_unused:UNUSED_PRESERVE src0_sel:WORD_1\n\t"
    "v_rcp_f16_sdwa v5, v7 dst_sel:WORD_1 dst_unused:UNUSED_PRESERVE src0_sel:WORD_1\n\t"
    "v_fma_mix_f32 v80, v80, v4, 0 op_sel_hi:[0,1,0]\n\t"
    "v_mfma_f32_32x32x16_bf16 a[160:175], a[236:239], v[148:151], a[160:175]\n\t"
    "ds_read_b64_tr_b16 a[212:213], v16 offset:34816\n\t"
    "v_fma_mix_f32 v82, v82, v5, 0 op_sel_hi:[0,1,0]\n\t"
    "v_fma_mix_f32 v81, v81, v4, 0 op_sel:[0,1,0] op_sel_hi:[0,1,0]\n\t"
    "v_fma_mix_f32 v83, v83, v5, 0 op_sel:[0,1,0] op_sel_hi:[0,1,0]\n\t"
    "v_cvt_pk_bf16_f32 v136, v80, v81\n\t"
    "v_cvt_pk_bf16_f32 v137, v82, v83\n\t"
    "v_mfma_f32_32x32x16_bf16 a[176:191], a[236:239], v[156:159], a[176:191]\n\t"
    "ds_read_b64_tr_b16 a[214:215], v17 offset:35328\n\t"
    "v_cvt_pk_f16_f32 v8, v84, v85\n\t"
    "v_cvt_pk_f16_f32 v9, v86, v87\n\t"
    "v_pk_fma_f16 v10, v8, v8, s60 neg_lo:[1,0,1] neg_hi:[1,0,1]\n\t"
    "v_pk_fma_f16 v11, v9, v9, s60 neg_lo:[1,0,1] neg_hi:[1,0,1]\n\t"
    "s_waitcnt lgkmcnt(4)\n\t"
    "v_mfma_f32_32x32x16_bf16 v[96:111], a[192:195], v[160:163], v[48:63]\n\t"
    "ds_read_b64_tr_b16 a[216:217], v16 offset:36864\n\t"
    "v_pk_mul_f16 v10, v10, v8\n\t"
    "v_pk_mul_f16 v11, v11, v9\n\t"
    "v_exp_f16 v12, v10\n\t"
    "v_exp_f16 v13, v11\n\t"
    "v_mfma_f32_32x32x16_bf16 v[112:127], a[192:195], v[208:211], v[48:63]\n\t"
    "ds_read_b64_tr_b16 a[218:219], v17 offset:37376\n\t"
    "v_exp_f16_sdwa v12, v10 dst_sel:WORD_1 dst_unused:UNUSED_PRESERVE src0_sel:WORD_1\n\t"
    "v_exp_f16_sdwa v13, v11 dst_sel:WORD_1 dst_unused:UNUSED_PRESERVE src0_sel:WORD_1\n\t"
    "v_pk_add_f16 v12, v12, 1.0 op_sel_hi:[1,0]\n\t"
    "v_mfma_f32_32x32x16_bf16 v[96:111], a[196:199], v[164:167], v[96:111]\n\t"
    "ds_read_b64_tr_b16 a[220:221], v16 offset:38912\n\t"
    "v_pk_add_f16 v13, v13, 1.0 op_sel_hi:[1,0]\n\t"
    "v_rcp_f16 v10, v12\n\t"
    "v_rcp_f16 v11, v13\n\t"
    "v_mfma_f32_32x32x16_bf16 v[112:127], a[196:199], v[212:215], v[112:127]\n\t"
    "ds_read_b64_tr_b16 a[222:223], v17 offset:39424\n\t"
    "v_rcp_f16_sdwa v10, v12 dst_sel:WORD_1 dst_unused:UNUSED_PRESERVE src0_sel:WORD_1\n\t"
    "v_rcp_f16_sdwa v11, v13 dst_sel:WORD_1 dst_unused:UNUSED_PRESERVE src0_sel:WORD_1\n\t"
    "v_fma_mix_f32 v84, v84, v10, 0 op_sel_hi:[0,1,0]\n\t"
    "v_mfma_f32_32x32x16_bf16 v[96:111], a[200:203], v[168:171], v[96:111]\n\t"
    "ds_read_b64_tr_b16 a[224:225], v16 offset:40960\n\t"
    "v_fma_mix_f32 v86, v86, v11, 0 op_sel_hi:[0,1,0]\n\t"
    "v_fma_mix_f32 v85, v85, v10, 0 op_sel:[0,1,0] op_sel_hi:[0,1,0]\n\t"
    "v_fma_mix_f32 v87, v87, v11, 0 op_sel:[0,1,0] op_sel_hi:[0,1,0]\n\t"
    "v_cvt_pk_bf16_f32 v138, v84, v85\n\t"
    "v_cvt_pk_bf16_f32 v139, v86, v87\n\t"
    "v_mfma_f32_32x32x16_bf16 v[112:127], a[200:203], v[216:219], v[112:127]\n\t"
    "ds_read_b64_tr_b16 a[226:227], v17 offset:41472\n\t"
    "v_cvt_pk_f16_f32 v2, v72, v73\n\t"
    "v_cvt_pk_f16_f32 v3, v74, v75\n\t"
    "v_pk_fma_f16 v4, v2, v2, s60 neg_lo:[1,0,1] neg_hi:[1,0,1]\n\t"
    "v_pk_fma_f16 v5, v3, v3, s60 neg_lo:[1,0,1] neg_hi:[1,0,1]\n\t"
    "v_pk_mul_f16 v4, v4, v2\n\t"
    "v_mfma_f32_32x32x16_bf16 v[96:111], a[204:207], v[172:175], v[96:111]\n\t"
    "ds_read_b64_tr_b16 a[228:229], v16 offset:43008\n\t"
    "v_pk_mul_f16 v5, v5, v3\n\t"
    "v_exp_f16 v6, v4\n\t"
    "v_exp_f16 v7, v5\n\t"
    "v_mfma_f32_32x32x16_bf16 v[112:127], a[204:207], v[220:223], v[112:127]\n\t"
    "ds_read_b64_tr_b16 a[230:231], v17 offset:43520\n\t"
    "v_exp_f16_sdwa v6, v4 dst_sel:WORD_1 dst_unused:UNUSED_PRESERVE src0_sel:WORD_1\n\t"
    "v_exp_f16_sdwa v7, v5 dst_sel:WORD_1 dst_unused:UNUSED_PRESERVE src0_sel:WORD_1\n\t"
    "s_waitcnt lgkmcnt(4)\n\t"
    "v_mfma_f32_32x32x16_bf16 v[96:111], a[208:211], v[176:179], v[96:111]\n\t"
    "ds_read_b64_tr_b16 a[232:233], v16 offset:45056\n\t"
    "v_pk_add_f16 v6, v6, 1.0 op_sel_hi:[1,0]\n\t"
    "v_pk_add_f16 v7, v7, 1.0 op_sel_hi:[1,0]\n\t"
    "v_rcp_f16 v4, v6\n\t"
    "v_rcp_f16 v5, v7\n\t"
    "v_mfma_f32_32x32x16_bf16 v[112:127], a[208:211], v[224:227], v[112:127]\n\t"
    "ds_read_b64_tr_b16 a[234:235], v17 offset:45568\n\t"
    "v_rcp_f16_sdwa v4, v6 dst_sel:WORD_1 dst_unused:UNUSED_PRESERVE src0_sel:WORD_1\n\t"
    "v_rcp_f16_sdwa v5, v7 dst_sel:WORD_1 dst_unused:UNUSED_PRESERVE src0_sel:WORD_1\n\t"
    "v_fma_mix_f32 v72, v72, v4, 0 op_sel_hi:[0,1,0]\n\t"
    "v_mfma_f32_32x32x16_bf16 v[96:111], a[212:215], v[180:183], v[96:111]\n\t"
    "ds_read_b64_tr_b16 a[236:237], v16 offset:47104\n\t"
    "v_fma_mix_f32 v74, v74, v5, 0 op_sel_hi:[0,1,0]\n\t"
    "v_fma_mix_f32 v73, v73, v4, 0 op_sel:[0,1,0] op_sel_hi:[0,1,0]\n\t"
    "v_fma_mix_f32 v75, v75, v5, 0 op_sel:[0,1,0] op_sel_hi:[0,1,0]\n\t"
    "v_cvt_pk_bf16_f32 v132, v72, v73\n\t"
    "v_cvt_pk_bf16_f32 v133, v74, v75\n\t"
    "v_mfma_f32_32x32x16_bf16 v[112:127], a[212:215], v[228:231], v[112:127]\n\t"
    "ds_read_b64_tr_b16 a[238:239], v17 offset:47616\n\t"
    "v_cvt_pk_f16_f32 v8, v76, v77\n\t"
    "v_cvt_pk_f16_f32 v9, v78, v79\n\t"
    "v_pk_fma_f16 v10, v8, v8, s60 neg_lo:[1,0,1] neg_hi:[1,0,1]\n\t"
    "v_pk_fma_f16 v11, v9, v9, s60 neg_lo:[1,0,1] neg_hi:[1,0,1]\n\t"
    "v_pk_mul_f16 v10, v10, v8\n\t"
    "v_mfma_f32_32x32x16_bf16 v[96:111], a[216:219], v[184:187], v[96:111]\n\t"
    "ds_read_b64_tr_b16 a[192:193], v18 offset:24576\n\t"
    "v_pk_mul_f16 v11, v11, v9\n\t"
    "v_exp_f16 v12, v10\n\t"
    "v_exp_f16 v13, v11\n\t"
    "v_mfma_f32_32x32x16_bf16 v[112:127], a[216:219], v[232:235], v[112:127]\n\t"
    "ds_read_b64_tr_b16 a[194:195], v18 offset:27648\n\t"
    "v_exp_f16_sdwa v12, v10 dst_sel:WORD_1 dst_unused:UNUSED_PRESERVE src0_sel:WORD_1\n\t"
    "v_exp_f16_sdwa v13, v11 dst_sel:WORD_1 dst_unused:UNUSED_PRESERVE src0_sel:WORD_1\n\t"
    "v_pk_add_f16 v12, v12, 1.0 op_sel_hi:[1,0]\n\t"
    "v_mfma_f32_32x32x16_bf16 v[96:111], a[220:223], v[188:191], v[96:111]\n\t"
    "ds_read_b64_tr_b16 a[196:197], v19 offset:24576\n\t"
    "v_pk_add_f16 v13, v13, 1.0 op_sel_hi:[1,0]\n\t"
    "v_rcp_f16 v10, v12\n\t"
    "v_rcp_f16 v11, v13\n\t"
    "v_mfma_f32_32x32x16_bf16 v[112:127], a[220:223], v[236:239], v[112:127]\n\t"
    "ds_read_b64_tr_b16 a[198:199], v19 offset:27648\n\t"
    "v_rcp_f16_sdwa v10, v12 dst_sel:WORD_1 dst_unused:UNUSED_PRESERVE src0_sel:WORD_1\n\t"
    "v_rcp_f16_sdwa v11, v13 dst_sel:WORD_1 dst_unused:UNUSED_PRESERVE src0_sel:WORD_1\n\t"
    "s_waitcnt lgkmcnt(4)\n\t"
    "v_mfma_f32_32x32x16_bf16 v[96:111], a[224:227], v[192:195], v[96:111]\n\t"
    "ds_read_b64_tr_b16 a[200:201], v18 offset:25600\n\t"
    "v_fma_mix_f32 v76, v76, v10, 0 op_sel_hi:[0,1,0]\n\t"
    "v_fma_mix_f32 v78, v78, v11, 0 op_sel_hi:[0,1,0]\n\t"
    "v_fma_mix_f32 v77, v77, v10, 0 op_sel:[0,1,0] op_sel_hi:[0,1,0]\n\t"
    "v_fma_mix_f32 v79, v79, v11, 0 op_sel:[0,1,0] op_sel_hi:[0,1,0]\n\t"
    "v_cvt_pk_bf16_f32 v134, v76, v77\n\t"
    "v_mfma_f32_32x32x16_bf16 v[112:127], a[224:227], v[240:243], v[112:127]\n\t"
    "ds_read_b64_tr_b16 a[202:203], v18 offset:28672\n\t"
    "v_cvt_pk_bf16_f32 v135, v78, v79\n\t"
    "v_cvt_pk_f16_f32 v2, v88, v89\n\t"
    "v_cvt_pk_f16_f32 v3, v90, v91\n\t"
    "v_pk_fma_f16 v4, v2, v2, s60 neg_lo:[1,0,1] neg_hi:[1,0,1]\n\t"
    "v_pk_fma_f16 v5, v3, v3, s60 neg_lo:[1,0,1] neg_hi:[1,0,1]\n\t"
    "v_mfma_f32_32x32x16_bf16 v[96:111], a[228:231], v[196:199], v[96:111]\n\t"
    "ds_read_b64_tr_b16 a[204:205], v19 offset:25600\n\t"
    "v_pk_mul_f16 v4, v4, v2\n\t"
    "v_pk_mul_f16 v5, v5, v3\n\t"
    "v_exp_f16 v6, v4\n\t"
    "v_exp_f16 v7, v5\n\t"
    "v_mfma_f32_32x32x16_bf16 v[112:127], a[228:231], v[244:247], v[112:127]\n\t"
    "ds_read_b64_tr_b16 a[206:207], v19 offset:28672\n\t"
    "v_exp_f16_sdwa v6, v4 dst_sel:WORD_1 dst_unused:UNUSED_PRESERVE src0_sel:WORD_1\n\t"
    "v_exp_f16_sdwa v7, v5 dst_sel:WORD_1 dst_unused:UNUSED_PRESERVE src0_sel:WORD_1\n\t"
    "v_pk_add_f16 v6, v6, 1.0 op_sel_hi:[1,0]\n\t"
    "v_mfma_f32_32x32x16_bf16 v[96:111], a[232:235], v[200:203], v[96:111]\n\t"
    "ds_read_b64_tr_b16 a[208:209], v18 offset:26624\n\t"
    "v_pk_add_f16 v7, v7, 1.0 op_sel_hi:[1,0]\n\t"
    "v_rcp_f16 v4, v6\n\t"
    "v_rcp_f16 v5, v7\n\t"
    "v_mfma_f32_32x32x16_bf16 v[112:127], a[232:235], v[248:251], v[112:127]\n\t"
    "ds_read_b64_tr_b16 a[210:211], v18 offset:29696\n\t"
    "v_rcp_f16_sdwa v4, v6 dst_sel:WORD_1 dst_unused:UNUSED_PRESERVE src0_sel:WORD_1\n\t"
    "v_rcp_f16_sdwa v5, v7 dst_sel:WORD_1 dst_unused:UNUSED_PRESERVE src0_sel:WORD_1\n\t"
    "v_fma_mix_f32 v88, v88, v4, 0 op_sel_hi:[0,1,0]\n\t"
    "v_mfma_f32_32x32x16_bf16 v[96:111], a[236:239], v[204:207], v[96:111]\n\t"
    "ds_read_b64_tr_b16 a[212:213], v19 offset:26624\n\t"
    "v_fma_mix_f32 v90, v90, v5, 0 op_sel_hi:[0,1,0]\n\t"
    "v_fma_mix_f32 v89, v89, v4, 0 op_sel:[0,1,0] op_sel_hi:[0,1,0]\n\t"
    "v_fma_mix_f32 v91, v91, v5, 0 op_sel:[0,1,0] op_sel_hi:[0,1,0]\n\t"
    "v_cvt_pk_bf16_f32 v140, v88, v89\n\t"
    "v_cvt_pk_bf16_f32 v141, v90, v91\n\t"
    "v_mfma_f32_32x32x16_bf16 v[112:127], a[236:239], v[252:255], v[112:127]\n\t"
    "ds_read_b64_tr_b16 a[214:215], v19 offset:29696\n\t"
    "v_add_u32 v22, 512, v22\n\t"
    "s_sub_u32 s30, 0, s30\n\t"
    "s_sub_u32 s69, s69, 1\n\t"
    "s_cmp_lg_u32 s69, 0\n\t"
    "s_cbranch_scc1 loop_c_%=\n\t"
    "s_add_u32 s61, s75, s57\n\t"
    "s_mul_i32 s61, s61, 224\n\t"
    "s_add_u32 s61, s61, s58\n\t"
    "s_mul_i32 s70, s61, 384\n\t"
    "s_add_u32 s71, s70, 86016\n\t"
    "s_add_u32 s72, s70, 172032\n\t"
    "s_add_u32 s73, s70, 258048\n\t"
    "v_lshlrev_b32 v26, 4, v0\n\t"
    "v_add_u32 v27, 4096, v26\n\t"
    "s_mov_b32 s64, 0xffff0000\n\t"
    "s_waitcnt lgkmcnt(0)\n\t"
    "v_mfma_f32_32x32x16_bf16 a[0:15], a[192:195], v[128:131], a[0:15]\n\t"
    "ds_read_b64_tr_b16 a[216:217], v18 offset:30720\n\t"
    "ds_read_b64_tr_b16 a[218:219], v18 offset:33792\n\t"
    "v_cvt_pk_f16_f32 v2, v96, v97\n\t"
    "v_cvt_pk_f16_f32 v3, v98, v99\n\t"
    "v_pk_fma_f16 v4, v2, v2, s60 neg_lo:[1,0,1] neg_hi:[1,0,1]\n\t"
    "v_pk_fma_f16 v5, v3, v3, s60 neg_lo:[1,0,1] neg_hi:[1,0,1]\n\t"
    "v_pk_mul_f16 v4, v4, v2\n\t"
    "v_pk_mul_f16 v5, v5, v3\n\t"
    "v_exp_f16 v6, v4\n\t"
    "v_mfma_f32_32x32x16_bf16 a[32:47], a[196:199], v[128:131], a[32:47]\n\t"
    "ds_read_b64_tr_b16 a[220:221], v19 offset:30720\n\t"
    "ds_read_b64_tr_b16 a[222:223], v19 offset:33792\n\t"
    "v_exp_f16 v7, v5\n\t"
    "v_exp_f16_sdwa v6, v4 dst_sel:WORD_1 dst_unused:UNUSED_PRESERVE src0_sel:WORD_1\n\t"
    "v_exp_f16_sdwa v7, v5 dst_sel:WORD_1 dst_unused:UNUSED_PRESERVE src0_sel:WORD_1\n\t"
    "v_pk_add_f16 v6, v6, 1.0 op_sel_hi:[1,0]\n\t"
    "v_pk_add_f16 v7, v7, 1.0 op_sel_hi:[1,0]\n\t"
    "v_mfma_f32_32x32x16_bf16 a[64:79], a[200:203], v[128:131], a[64:79]\n\t"
    "ds_read_b64_tr_b16 a[224:225], v18 offset:31744\n\t"
    "ds_read_b64_tr_b16 a[226:227], v18 offset:34816\n\t"
    "v_rcp_f16 v4, v6\n\t"
    "v_rcp_f16 v5, v7\n\t"
    "v_rcp_f16_sdwa v4, v6 dst_sel:WORD_1 dst_unused:UNUSED_PRESERVE src0_sel:WORD_1\n\t"
    "v_rcp_f16_sdwa v5, v7 dst_sel:WORD_1 dst_unused:UNUSED_PRESERVE src0_sel:WORD_1\n\t"
    "v_fma_mix_f32 v96, v96, v4, 0 op_sel_hi:[0,1,0]\n\t"
    "s_barrier\n\t"
    "v_mfma_f32_32x32x16_bf16 a[96:111], a[204:207], v[128:131], a[96:111]\n\t"
    "ds_read_b64_tr_b16 a[228:229], v19 offset:31744\n\t"
    "ds_read_b64_tr_b16 a[230:231], v19 offset:34816\n\t"
    "buffer_load_dwordx4 v[160:163], v26, s[36:39], s70 offen offset:0\n\t"
    "v_fma_mix_f32 v98, v98, v5, 0 op_sel_hi:[0,1,0]\n\t"
    "v_fma_mix_f32 v97, v97, v4, 0 op_sel:[0,1,0] op_sel_hi:[0,1,0]\n\t"
    "v_fma_mix_f32 v99, v99, v5, 0 op_sel:[0,1,0] op_sel_hi:[0,1,0]\n\t"
    "v_cvt_pk_bf16_f32 v144, v96, v97\n\t"
    "v_cvt_pk_bf16_f32 v145, v98, v99\n\t"
    "v_cvt_pk_f16_f32 v8, v100, v101\n\t"
    "v_cvt_pk_f16_f32 v9, v102, v103\n\t"
    "v_pk_fma_f16 v10, v8, v8, s60 neg_lo:[1,0,1] neg_hi:[1,0,1]\n\t"
    "v_pk_fma_f16 v11, v9, v9, s60 neg_lo:[1,0,1] neg_hi:[1,0,1]\n\t"
    "v_mfma_f32_32x32x16_bf16 a[128:143], a[208:211], v[128:131], a[128:143]\n\t"
    "ds_read_b64_tr_b16 a[232:233], v18 offset:32768\n\t"
    "ds_read_b64_tr_b16 a[234:235], v18 offset:35840\n\t"
    "v_pk_mul_f16 v10, v10, v8\n\t"
    "v_pk_mul_f16 v11, v11, v9\n\t"
    "v_exp_f16 v12, v10\n\t"
    "v_exp_f16 v13, v11\n\t"
    "v_exp_f16_sdwa v12, v10 dst_sel:WORD_1 dst_unused:UNUSED_PRESERVE src0_sel:WORD_1\n\t"
    "v_mfma_f32_32x32x16_bf16 a[160:175], a[212:215], v[128:131], a[160:175]\n\t"
    "ds_read_b64_tr_b16 a[236:237], v19 offset:32768\n\t"
    "ds_read_b64_tr_b16 a[238:239], v19 offset:35840\n\t"
    "buffer_load_dwordx4 v[164:167], v26, s[36:39], s70 offen offset:1024\n\t"
    "v_exp_f16_sdwa v13, v11 dst_sel:WORD_1 dst_unused:UNUSED_PRESERVE src0_sel:WORD_1\n\t"
    "v_pk_add_f16 v12, v12, 1.0 op_sel_hi:[1,0]\n\t"
    "v_pk_add_f16 v13, v13, 1.0 op_sel_hi:[1,0]\n\t"
    "v_rcp_f16 v10, v12\n\t"
    "v_rcp_f16 v11, v13\n\t"
    "s_waitcnt lgkmcnt(6)\n\t"
    "v_mfma_f32_32x32x16_bf16 a[0:15], a[216:219], v[132:135], a[0:15]\n\t"
    "ds_read_b64_tr_b16 a[240:241], v18 offset:36864\n\t"
    "ds_read_b64_tr_b16 a[242:243], v18 offset:39936\n\t"
    "v_rcp_f16_sdwa v10, v12 dst_sel:WORD_1 dst_unused:UNUSED_PRESERVE src0_sel:WORD_1\n\t"
    "v_rcp_f16_sdwa v11, v13 dst_sel:WORD_1 dst_unused:UNUSED_PRESERVE src0_sel:WORD_1\n\t"
    "v_fma_mix_f32 v100, v100, v10, 0 op_sel_hi:[0,1,0]\n\t"
    "v_fma_mix_f32 v102, v102, v11, 0 op_sel_hi:[0,1,0]\n\t"
    "v_fma_mix_f32 v101, v101, v10, 0 op_sel:[0,1,0] op_sel_hi:[0,1,0]\n\t"
    "v_fma_mix_f32 v103, v103, v11, 0 op_sel:[0,1,0] op_sel_hi:[0,1,0]\n\t"
    "v_mfma_f32_32x32x16_bf16 a[32:47], a[220:223], v[132:135], a[32:47]\n\t"
    "ds_read_b64_tr_b16 a[244:245], v19 offset:36864\n\t"
    "ds_read_b64_tr_b16 a[246:247], v19 offset:39936\n\t"
    "buffer_load_dwordx4 v[168:171], v26, s[36:39], s70 offen offset:2048\n\t"
    "v_cvt_pk_bf16_f32 v146, v100, v101\n\t"
    "v_cvt_pk_bf16_f32 v147, v102, v103\n\t"
    "v_cvt_pk_f16_f32 v2, v104, v105\n\t"
    "v_cvt_pk_f16_f32 v3, v106, v107\n\t"
    "v_pk_fma_f16 v4, v2, v2, s60 neg_lo:[1,0,1] neg_hi:[1,0,1]\n\t"
    "v_pk_fma_f16 v5, v3, v3, s60 neg_lo:[1,0,1] neg_hi:[1,0,1]\n\t"
    "v_pk_mul_f16 v4, v4, v2\n\t"
    "v_pk_mul_f16 v5, v5, v3\n\t"
    "v_mfma_f32_32x32x16_bf16 a[64:79], a[224:227], v[132:135], a[64:79]\n\t"
    "ds_read_b64_tr_b16 a[248:249], v18 offset:37888\n\t"
    "ds_read_b64_tr_b16 a[250:251], v18 offset:40960\n\t"
    "v_exp_f16 v6, v4\n\t"
    "v_exp_f16 v7, v5\n\t"
    "v_exp_f16_sdwa v6, v4 dst_sel:WORD_1 dst_unused:UNUSED_PRESERVE src0_sel:WORD_1\n\t"
    "v_exp_f16_sdwa v7, v5 dst_sel:WORD_1 dst_unused:UNUSED_PRESERVE src0_sel:WORD_1\n\t"
    "s_waitcnt lgkmcnt(6)\n\t"
    "v_mfma_f32_32x32x16_bf16 a[96:111], a[228:231], v[132:135], a[96:111]\n\t"
    "ds_read_b64_tr_b16 a[252:253], v19 offset:37888\n\t"
    "ds_read_b64_tr_b16 a[254:255], v19 offset:40960\n\t"
    "buffer_load_dwordx4 v[172:175], v26, s[36:39], s70 offen offset:3072\n\t"
    "v_pk_add_f16 v6, v6, 1.0 op_sel_hi:[1,0]\n\t"
    "v_pk_add_f16 v7, v7, 1.0 op_sel_hi:[1,0]\n\t"
    "v_rcp_f16 v4, v6\n\t"
    "v_rcp_f16 v5, v7\n\t"
    "v_rcp_f16_sdwa v4, v6 dst_sel:WORD_1 dst_unused:UNUSED_PRESERVE src0_sel:WORD_1\n\t"
    "v_mfma_f32_32x32x16_bf16 a[128:143], a[232:235], v[132:135], a[128:143]\n\t"
    "ds_read_b64_tr_b16 v[232:233], v18 offset:38912\n\t"
    "ds_read_b64_tr_b16 v[234:235], v18 offset:41984\n\t"
    "v_rcp_f16_sdwa v5, v7 dst_sel:WORD_1 dst_unused:UNUSED_PRESERVE src0_sel:WORD_1\n\t"
    "v_fma_mix_f32 v104, v104, v4, 0 op_sel_hi:[0,1,0]\n\t"
    "v_fma_mix_f32 v106, v106, v5, 0 op_sel_hi:[0,1,0]\n\t"
    "v_fma_mix_f32 v105, v105, v4, 0 op_sel:[0,1,0] op_sel_hi:[0,1,0]\n\t"
    "v_fma_mix_f32 v107, v107, v5, 0 op_sel:[0,1,0] op_sel_hi:[0,1,0]\n\t"
    "v_cvt_pk_bf16_f32 v148, v104, v105\n\t"
    "v_cvt_pk_bf16_f32 v149, v106, v107\n\t"
    "v_mfma_f32_32x32x16_bf16 a[160:175], a[236:239], v[132:135], a[160:175]\n\t"
    "ds_read_b64_tr_b16 v[236:237], v19 offset:38912\n\t"
    "ds_read_b64_tr_b16 v[238:239], v19 offset:41984\n\t"
    "buffer_load_dwordx4 v[176:179], v27, s[36:39], s70 offen offset:0\n\t"
    "v_cvt_pk_f16_f32 v8, v108, v109\n\t"
    "v_cvt_pk_f16_f32 v9, v110, v111\n\t"
    "v_pk_fma_f16 v10, v8, v8, s60 neg_lo:[1,0,1] neg_hi:[1,0,1]\n\t"
    "v_pk_fma_f16 v11, v9, v9, s60 neg_lo:[1,0,1] neg_hi:[1,0,1]\n\t"
    "v_pk_mul_f16 v10, v10, v8\n\t"
    "v_pk_mul_f16 v11, v11, v9\n\t"
    "v_exp_f16 v12, v10\n\t"
    "s_waitcnt lgkmcnt(6)\n\t"
    "v_mfma_f32_32x32x16_bf16 a[0:15], a[240:243], v[144:147], a[0:15]\n\t"
    "ds_read_b64_tr_b16 v[240:241], v18 offset:43008\n\t"
    "ds_read_b64_tr_b16 v[242:243], v18 offset:46080\n\t"
    "v_exp_f16 v13, v11\n\t"
    "v_exp_f16_sdwa v12, v10 dst_sel:WORD_1 dst_unused:UNUSED_PRESERVE src0_sel:WORD_1\n\t"
    "v_exp_f16_sdwa v13, v11 dst_sel:WORD_1 dst_unused:UNUSED_PRESERVE src0_sel:WORD_1\n\t"
    "v_pk_add_f16 v12, v12, 1.0 op_sel_hi:[1,0]\n\t"
    "v_pk_add_f16 v13, v13, 1.0 op_sel_hi:[1,0]\n\t"
    "v_mfma_f32_32x32x16_bf16 a[32:47], a[244:247], v[144:147], a[32:47]\n\t"
    "ds_read_b64_tr_b16 v[244:245], v19 offset:43008\n\t"
    "ds_read_b64_tr_b16 v[246:247], v19 offset:46080\n\t"
    "buffer_load_dwordx4 v[180:183], v27, s[36:39], s70 offen offset:1024\n\t"
    "v_rcp_f16 v10, v12\n\t"
    "v_rcp_f16 v11, v13\n\t"
    "v_rcp_f16_sdwa v10, v12 dst_sel:WORD_1 dst_unused:UNUSED_PRESERVE src0_sel:WORD_1\n\t"
    "v_rcp_f16_sdwa v11, v13 dst_sel:WORD_1 dst_unused:UNUSED_PRESERVE src0_sel:WORD_1\n\t"
    "v_mfma_f32_32x32x16_bf16 a[64:79], a[248:251], v[144:147], a[64:79]\n\t"
    "ds_read_b64_tr_b16 v[248:249], v18 offset:44032\n\t"
    "ds_read_b64_tr_b16 v[250:251], v18 offset:47104\n\t"
    "v_fma_mix_f32 v108, v108, v10, 0 op_sel_hi:[0,1,0]\n\t"
    "v_fma_mix_f32 v110, v110, v11, 0 op_sel_hi:[0,1,0]\n\t"
    "v_fma_mix_f32 v109, v109, v10, 0 op_sel:[0,1,0] op_sel_hi:[0,1,0]\n\t"
    "v_fma_mix_f32 v111, v111, v11, 0 op_sel:[0,1,0] op_sel_hi:[0,1,0]\n\t"
    "v_cvt_pk_bf16_f32 v150, v108, v109\n\t"
    "v_cvt_pk_bf16_f32 v151, v110, v111\n\t"
    "s_mov_b32 s61, 75264\n\t"
    "v_and_b32 v40, 31, v0\n\t"
    "s_waitcnt lgkmcnt(6)\n\t"
    "v_mfma_f32_32x32x16_bf16 a[96:111], a[252:255], v[144:147], a[96:111]\n\t"
    "ds_read_b64_tr_b16 v[252:253], v19 offset:44032\n\t"
    "ds_read_b64_tr_b16 v[254:255], v19 offset:47104\n\t"
    "buffer_load_dwordx4 v[184:187], v26, s[36:39], s71 offen offset:0\n\t"
    "v_mul_u32_u24 v40, 784, v40\n\t"
    "v_lshrrev_b32 v41, 5, v0\n\t"
    "v_mul_u32_u24 v41, 384, v41\n\t"
    "v_add3_u32 v29, v40, v41, s61\n\t"
    "v_add_u32 v30, 25088, v29\n\t"
    "v_add_u32 v40, 0, v0\n\t"
    "v_mul_u32_u24 v41, 2731, v40\n\t"
    "v_lshrrev_b32 v41, 16, v41\n\t"
    "v_mfma_f32_32x32x16_bf16 a[128:143], v[232:235], v[144:147], a[128:143]\n\t"
    "ds_read_b64_tr_b16 v[56:57], v18 offset:45056\n\t"
    "ds_read_b64_tr_b16 v[58:59], v18 offset:48128\n\t"
    "v_mul_u32_u24 v42, 24, v41\n\t"
    "v_sub_u32 v40, v40, v42\n\t"
    "v_mul_u32_u24 v41, 784, v41\n\t"
    "v_lshl_add_u32 v41, v40, 4, v41\n\t"
    "v_add_u32 v14, s61, v41\n\t"
    "v_lshlrev_b32 v40, 5, v40\n\t"
    "v_add_u32 v44, 157440, v40\n\t"
    "v_add_u32 v40, 64, v0\n\t"
    "v_mfma_f32_32x32x16_bf16 a[160:175], v[236:239], v[144:147], a[160:175]\n\t"
    "ds_read_b64_tr_b16 v[60:61], v19 offset:45056\n\t"
    "ds_read_b64_tr_b16 v[62:63], v19 offset:48128\n\t"
    "buffer_load_dwordx4 v[188:191], v26, s[36:39], s71 offen offset:1024\n\t"
    "v_mul_u32_u24 v41, 2731, v40\n\t"
    "v_lshrrev_b32 v41, 16, v41\n\t"
    "v_mul_u32_u24 v42, 24, v41\n\t"
    "v_sub_u32 v40, v40, v42\n\t"
    "v_mul_u32_u24 v41, 784, v41\n\t"
    "v_lshl_add_u32 v41, v40, 4, v41\n\t"
    "v_add_u32 v15, s61, v41\n\t"
    "v_lshlrev_b32 v40, 5, v40\n\t"
    "s_waitcnt lgkmcnt(6)\n\t"
    "v_mfma_f32_32x32x16_bf16 a[0:15], v[240:243], v[148:151], a[0:15]\n\t"
    "v_add_u32 v45, 157440, v40\n\t"
    "v_add_u32 v40, 128, v0\n\t"
    "v_mul_u32_u24 v41, 2731, v40\n\t"
    "v_lshrrev_b32 v41, 16, v41\n\t"
    "v_mul_u32_u24 v42, 24, v41\n\t"
    "v_sub_u32 v40, v40, v42\n\t"
    "v_mul_u32_u24 v41, 784, v41\n\t"
    "v_lshl_add_u32 v41, v40, 4, v41\n\t"
    "v_mfma_f32_32x32x16_bf16 a[32:47], v[244:247], v[148:151], a[32:47]\n\t"
    "buffer_load_dwordx4 v[192:195], v26, s[36:39], s71 offen offset:2048\n\t"
    "v_add_u32 v16, s61, v41\n\t"
    "v_lshlrev_b32 v40, 5, v40\n\t"
    "v_add_u32 v46, 157440, v40\n\t"
    "v_cvt_pk_f16_f32 v8, v92, v93\n\t"
    "v_cvt_pk_f16_f32 v9, v94, v95\n\t"
    "v_pk_fma_f16 v10, v8, v8, s60 neg_lo:[1,0,1] neg_hi:[1,0,1]\n\t"
    "v_pk_fma_f16 v11, v9, v9, s60 neg_lo:[1,0,1] neg_hi:[1,0,1]\n\t"
    "v_pk_mul_f16 v10, v10, v8\n\t"
    "v_mfma_f32_32x32x16_bf16 a[64:79], v[248:251], v[148:151], a[64:79]\n\t"
    "ds_write_b128 v29, a[0:3] offset:0\n\t"
    "ds_write_b128 v29, a[4:7] offset:16\n\t"
    "v_pk_mul_f16 v11, v11, v9\n\t"
    "v_exp_f16 v12, v10\n\t"
    "v_exp_f16 v13, v11\n\t"
    "v_exp_f16_sdwa v12, v10 dst_sel:WORD_1 dst_unused:UNUSED_PRESERVE src0_sel:WORD_1\n\t"
    "v_exp_f16_sdwa v13, v11 dst_sel:WORD_1 dst_unused:UNUSED_PRESERVE src0_sel:WORD_1\n\t"
    "s_waitcnt lgkmcnt(2)\n\t"
    "v_mfma_f32_32x32x16_bf16 a[96:111], v[252:255], v[148:151], a[96:111]\n\t"
    "buffer_load_dwordx4 v[196:199], v26, s[36:39], s71 offen offset:3072\n\t"
    "ds_write_b128 v29, a[8:11] offset:32\n\t"
    "ds_write_b128 v29, a[12:15] offset:48\n\t"
    "v_pk_add_f16 v12, v12, 1.0 op_sel_hi:[1,0]\n\t"
    "v_pk_add_f16 v13, v13, 1.0 op_sel_hi:[1,0]\n\t"
    "v_rcp_f16 v10, v12\n\t"
    "v_rcp_f16 v11, v13\n\t"
    "v_rcp_f16_sdwa v10, v12 dst_sel:WORD_1 dst_unused:UNUSED_PRESERVE src0_sel:WORD_1\n\t"
    "v_mfma_f32_32x32x16_bf16 a[128:143], v[56:59], v[148:151], a[128:143]\n\t"
    "ds_write_b128 v29, a[32:35] offset:64\n\t"
    "ds_write_b128 v29, a[36:39] offset:80\n\t"
    "v_rcp_f16_sdwa v11, v13 dst_sel:WORD_1 dst_unused:UNUSED_PRESERVE src0_sel:WORD_1\n\t"
    "v_fma_mix_f32 v92, v92, v10, 0 op_sel_hi:[0,1,0]\n\t"
    "v_fma_mix_f32 v94, v94, v11, 0 op_sel_hi:[0,1,0]\n\t"
    "v_fma_mix_f32 v93, v93, v10, 0 op_sel:[0,1,0] op_sel_hi:[0,1,0]\n\t"
    "v_fma_mix_f32 v95, v95, v11, 0 op_sel:[0,1,0] op_sel_hi:[0,1,0]\n\t"
    "v_cvt_pk_bf16_f32 v142, v92, v93\n\t"
    "v_cvt_pk_bf16_f32 v143, v94, v95\n\t"
    "v_mfma_f32_32x32x16_bf16 a[160:175], v[60:63], v[148:151], a[160:175]\n\t"
    "buffer_load_dwordx4 v[200:203], v27, s[36:39], s71 offen offset:0\n\t"
    "ds_write_b128 v29, a[40:43] offset:96\n\t"
    "ds_write_b128 v29, a[44:47] offset:112\n\t"
    "v_cvt_pk_f16_f32 v2, v112, v113\n\t"
    "v_cvt_pk_f16_f32 v3, v114, v115\n\t"
    "v_pk_fma_f16 v4, v2, v2, s60 neg_lo:[1,0,1] neg_hi:[1,0,1]\n\t"
    "v_pk_fma_f16 v5, v3, v3, s60 neg_lo:[1,0,1] neg_hi:[1,0,1]\n\t"
    "v_pk_mul_f16 v4, v4, v2\n\t"
    "v_pk_mul_f16 v5, v5, v3\n\t"
    "v_exp_f16 v6, v4\n\t"
    "v_mfma_f32_32x32x16_bf16 a[16:31], a[192:195], v[136:139], a[16:31]\n\t"
    "ds_write_b128 v29, a[64:67] offset:128\n\t"
    "ds_write_b128 v29, a[68:71] offset:144\n\t"
    "ds_read_b128 v[64:67], v44\n\t"
    "v_exp_f16 v7, v5\n\t"
    "v_exp_f16_sdwa v6, v4 dst_sel:WORD_1 dst_unused:UNUSED_PRESERVE src0_sel:WORD_1\n\t"
    "v_exp_f16_sdwa v7, v5 dst_sel:WORD_1 dst_unused:UNUSED_PRESERVE src0_sel:WORD_1\n\t"
    "v_pk_add_f16 v6, v6, 1.0 op_sel_hi:[1,0]\n\t"
    "v_pk_add_f16 v7, v7, 1.0 op_sel_hi:[1,0]\n\t"
    "v_mfma_f32_32x32x16_bf16 a[48:63], a[196:199], v[136:139], a[48:63]\n\t"
    "buffer_load_dwordx4 v[204:207], v27, s[36:39], s71 offen offset:1024\n\t"
    "ds_write_b128 v29, a[72:75] offset:160\n\t"
    "ds_write_b128 v29, a[76:79] offset:176\n\t"
    "ds_read_b128 v[68:71], v44 offset:16\n\t"
    "v_rcp_f16 v4, v6\n\t"
    "v_rcp_f16 v5, v7\n\t"
    "v_rcp_f16_sdwa v4, v6 dst_sel:WORD_1 dst_unused:UNUSED_PRESERVE src0_sel:WORD_1\n\t"
    "v_rcp_f16_sdwa v5, v7 dst_sel:WORD_1 dst_unused:UNUSED_PRESERVE src0_sel:WORD_1\n\t"
    "v_mfma_f32_32x32x16_bf16 a[80:95], a[200:203], v[136:139], a[80:95]\n\t"
    "ds_write_b128 v29, a[96:99] offset:192\n\t"
    "s_waitcnt lgkmcnt(11)\n\t"
    "ds_write_b128 v29, a[100:103] offset:208\n\t"
    "ds_read_b128 v[72:75], v45\n\t"
    "v_fma_mix_f32 v112, v112, v4, 0 op_sel_hi:[0,1,0]\n\t"
    "v_fma_mix_f32 v114, v114, v5, 0 op_sel_hi:[0,1,0]\n\t"
    "v_fma_mix_f32 v113, v113, v4, 0 op_sel:[0,1,0] op_sel_hi:[0,1,0]\n\t"
    "v_fma_mix_f32 v115, v115, v5, 0 op_sel:[0,1,0] op_sel_hi:[0,1,0]\n\t"
    "v_cvt_pk_bf16_f32 v152, v112, v113\n\t"
    "v_cvt_pk_bf16_f32 v153, v114, v115\n\t"
    "v_cvt_pk_f16_f32 v8, v116, v117\n\t"
    "v_cvt_pk_f16_f32 v9, v118, v119\n\t"
    "v_mfma_f32_32x32x16_bf16 a[112:127], a[204:207], v[136:139], a[112:127]\n\t"
    "buffer_load_dwordx4 v[208:211], v26, s[36:39], s72 offen offset:0\n\t"
    "ds_write_b128 v29, a[104:107] offset:224\n\t"
    "ds_write_b128 v29, a[108:111] offset:240\n\t"
    "s_waitcnt lgkmcnt(11)\n\t"
    "ds_read_b128 v[76:79], v45 offset:16\n\t"
    "v_pk_fma_f16 v10, v8, v8, s60 neg_lo:[1,0,1] neg_hi:[1,0,1]\n\t"
    "v_pk_fma_f16 v11, v9, v9, s60 neg_lo:[1,0,1] neg_hi:[1,0,1]\n\t"
    "v_pk_mul_f16 v10, v10, v8\n\t"
    "v_pk_mul_f16 v11, v11, v9\n\t"
    "v_exp_f16 v12, v10\n\t"
    "v_exp_f16 v13, v11\n\t"
    "v_mfma_f32_32x32x16_bf16 a[144:159], a[208:211], v[136:139], a[144:159]\n\t"
    "ds_write_b128 v29, a[128:131] offset:256\n\t"
    "ds_write_b128 v29, a[132:135] offset:272\n\t"
    "ds_read_b128 v[80:83], v46\n\t"
    "v_exp_f16_sdwa v12, v10 dst_sel:WORD_1 dst_unused:UNUSED_PRESERVE src0_sel:WORD_1\n\t"
    "v_exp_f16_sdwa v13, v11 dst_sel:WORD_1 dst_unused:UNUSED_PRESERVE src0_sel:WORD_1\n\t"
    "v_pk_add_f16 v12, v12, 1.0 op_sel_hi:[1,0]\n\t"
    "v_pk_add_f16 v13, v13, 1.0 op_sel_hi:[1,0]\n\t"
    "v_rcp_f16 v10, v12\n\t"
    "v_mfma_f32_32x32x16_bf16 a[176:191], a[212:215], v[136:139], a[176:191]\n\t"
    "buffer_load_dwordx4 v[212:215], v26, s[36:39], s72 offen offset:1024\n\t"
    "s_waitcnt lgkmcnt(11)\n\t"
    "ds_write_b128 v29, a[136:139] offset:288\n\t"
    "ds_write_b128 v29, a[140:143] offset:304\n\t"
    "ds_read_b128 v[84:87], v46 offset:16\n\t"
    "v_rcp_f16 v11, v13\n\t"
    "v_rcp_f16_sdwa v10, v12 dst_sel:WORD_1 dst_unused:UNUSED_PRESERVE src0_sel:WORD_1\n\t"
    "v_rcp_f16_sdwa v11, v13 dst_sel:WORD_1 dst_unused:UNUSED_PRESERVE src0_sel:WORD_1\n\t"
    "v_fma_mix_f32 v116, v116, v10, 0 op_sel_hi:[0,1,0]\n\t"
    "v_fma_mix_f32 v118, v118, v11, 0 op_sel_hi:[0,1,0]\n\t"
    "v_mfma_f32_32x32x16_bf16 a[16:31], a[216:219], v[140:143], a[16:31]\n\t"
    "ds_write_b128 v29, a[160:163] offset:320\n\t"
    "s_waitcnt lgkmcnt(11)\n\t"
    "ds_write_b128 v29, a[164:167] offset:336\n\t"
    "v_fma_mix_f32 v117, v117, v10, 0 op_sel:[0,1,0] op_sel_hi:[0,1,0]\n\t"
    "v_fma_mix_f32 v119, v119, v11, 0 op_sel:[0,1,0] op_sel_hi:[0,1,0]\n\t"
    "v_cvt_pk_bf16_f32 v154, v116, v117\n\t"
    "v_cvt_pk_bf16_f32 v155, v118, v119\n\t"
    "v_mfma_f32_32x32x16_bf16 a[48:63], a[220:223], v[140:143], a[48:63]\n\t"
    "buffer_load_dwordx4 v[216:219], v26, s[36:39], s72 offen offset:2048\n\t"
    "ds_write_b128 v29, a[168:171] offset:352\n\t"
    "ds_write_b128 v29, a[172:175] offset:368\n\t"
    "v_mfma_f32_32x32x16_bf16 a[80:95], a[224:227], v[140:143], a[80:95]\n\t"
    "v_mfma_f32_32x32x16_bf16 a[112:127], a[228:231], v[140:143], a[112:127]\n\t"
    "buffer_load_dwordx4 v[220:223], v26, s[36:39], s72 offen offset:3072\n\t"
    "ds_read_b128 v[96:99], v14 offset:0\n\t"
    "s_waitcnt lgkmcnt(11)\n\t"
    "ds_read_b128 v[100:103], v14 offset:384\n\t"
    "s_waitcnt vmcnt(15)\n\t"
    "v_lshlrev_b32 v32, 16, v160\n\t"
    "v_and_b32 v33, s64, v160\n\t"
    "v_lshlrev_b32 v34, 16, v161\n\t"
    "v_and_b32 v35, s64, v161\n\t"
    "v_lshlrev_b32 v36, 16, v162\n\t"
    "v_and_b32 v37, s64, v162\n\t"
    "v_lshlrev_b32 v38, 16, v163\n\t"
    "v_and_b32 v39, s64, v163\n\t"
    "v_mfma_f32_32x32x16_bf16 a[144:159], a[232:235], v[140:143], a[144:159]\n\t"
    "v_mfma_f32_32x32x16_bf16 a[176:191], a[236:239], v[140:143], a[176:191]\n\t"
    "buffer_load_dwordx4 v[224:227], v27, s[36:39], s72 offen offset:0\n\t"
    "v_mfma_f32_32x32x16_bf16 a[16:31], a[240:243], v[152:155], a[16:31]\n\t"
    "s_waitcnt lgkmcnt(0)\n\t"
    "v_fmac_f32 v32, v96, v64\n\t"
    "v_fmac_f32 v33, v97, v65\n\t"
    "v_fmac_f32 v34, v98, v66\n\t"
    "v_fmac_f32 v35, v99, v67\n\t"
    "v_fmac_f32 v36, v100, v68\n\t"
    "v_fmac_f32 v37, v101, v69\n\t"
    "v_fmac_f32 v38, v102, v70\n\t"
    "v_fmac_f32 v39, v103, v71\n\t"
    "v_cvt_pk_bf16_f32 v88, v32, v33\n\t"
    "v_cvt_pk_bf16_f32 v89, v34, v35\n\t"
    "v_cvt_pk_bf16_f32 v90, v36, v37\n\t"
    "v_cvt_pk_bf16_f32 v91, v38, v39\n\t"
    "v_cvt_pk_f16_f32 v2, v120, v121\n\t"
    "v_cvt_pk_f16_f32 v3, v122, v123\n\t"
    "v_mfma_f32_32x32x16_bf16 a[48:63], a[244:247], v[152:155], a[48:63]\n\t"
    "buffer_load_dwordx4 v[228:231], v27, s[36:39], s72 offen offset:1024\n\t"
    "v_pk_fma_f16 v4, v2, v2, s60 neg_lo:[1,0,1] neg_hi:[1,0,1]\n\t"
    "v_pk_fma_f16 v5, v3, v3, s60 neg_lo:[1,0,1] neg_hi:[1,0,1]\n\t"
    "v_pk_mul_f16 v4, v4, v2\n\t"
    "v_pk_mul_f16 v5, v5, v3\n\t"
    "v_exp_f16 v6, v4\n\t"
    "v_exp_f16 v7, v5\n\t"
    "v_exp_f16_sdwa v6, v4 dst_sel:WORD_1 dst_unused:UNUSED_PRESERVE src0_sel:WORD_1\n\t"
    "v_exp_f16_sdwa v7, v5 dst_sel:WORD_1 dst_unused:UNUSED_PRESERVE src0_sel:WORD_1\n\t"
    "v_pk_add_f16 v6, v6, 1.0 op_sel_hi:[1,0]\n\t"
    "v_pk_add_f16 v7, v7, 1.0 op_sel_hi:[1,0]\n\t"
    "v_mfma_f32_32x32x16_bf16 a[80:95], a[248:251], v[152:155], a[80:95]\n\t"
    "v_rcp_f16 v4, v6\n\t"
    "v_rcp_f16 v5, v7\n\t"
    "v_rcp_f16_sdwa v4, v6 dst_sel:WORD_1 dst_unused:UNUSED_PRESERVE src0_sel:WORD_1\n\t"
    "v_rcp_f16_sdwa v5, v7 dst_sel:WORD_1 dst_unused:UNUSED_PRESERVE src0_sel:WORD_1\n\t"
    "v_fma_mix_f32 v120, v120, v4, 0 op_sel_hi:[0,1,0]\n\t"
    "v_fma_mix_f32 v122, v122, v5, 0 op_sel_hi:[0,1,0]\n\t"
    "v_fma_mix_f32 v121, v121, v4, 0 op_sel:[0,1,0] op_sel_hi:[0,1,0]\n\t"
    "v_fma_mix_f32 v123, v123, v5, 0 op_sel:[0,1,0] op_sel_hi:[0,1,0]\n\t"
    "v_cvt_pk_bf16_f32 v156, v120, v121\n\t"
    "v_cvt_pk_bf16_f32 v157, v122, v123\n\t"
    "v_cvt_pk_f16_f32 v8, v124, v125\n\t"
    "v_mfma_f32_32x32x16_bf16 a[112:127], a[252:255], v[152:155], a[112:127]\n\t"
    "ds_read_b128 v[104:107], v15 offset:0\n\t"
    "ds_read_b128 v[108:111], v15 offset:384\n\t"
    "v_cvt_pk_f16_f32 v9, v126, v127\n\t"
    "v_pk_fma_f16 v10, v8, v8, s60 neg_lo:[1,0,1] neg_hi:[1,0,1]\n\t"
    "v_pk_fma_f16 v11, v9, v9, s60 neg_lo:[1,0,1] neg_hi:[1,0,1]\n\t"
    "v_pk_mul_f16 v10, v10, v8\n\t"
    "v_pk_mul_f16 v11, v11, v9\n\t"
    "v_exp_f16 v12, v10\n\t"
    "v_exp_f16 v13, v11\n\t"
    "v_exp_f16_sdwa v12, v10 dst_sel:WORD_1 dst_unused:UNUSED_PRESERVE src0_sel:WORD_1\n\t"
    "v_exp_f16_sdwa v13, v11 dst_sel:WORD_1 dst_unused:UNUSED_PRESERVE src0_sel:WORD_1\n\t"
    "v_pk_add_f16 v12, v12, 1.0 op_sel_hi:[1,0]\n\t"
    "v_pk_add_f16 v13, v13, 1.0 op_sel_hi:[1,0]\n\t"
    "v_mfma_f32_32x32x16_bf16 a[144:159], v[232:235], v[152:155], a[144:159]\n\t"
    "buffer_store_dwordx4 v[88:91], v26, s[48:51], s70 offen offset:0 sc1 nt\n\t"
    "v_rcp_f16 v10, v12\n\t"
    "v_rcp_f16 v11, v13\n\t"
    "v_rcp_f16_sdwa v10, v12 dst_sel:WORD_1 dst_unused:UNUSED_PRESERVE src0_sel:WORD_1\n\t"
    "v_rcp_f16_sdwa v11, v13 dst_sel:WORD_1 dst_unused:UNUSED_PRESERVE src0_sel:WORD_1\n\t"
    "v_fma_mix_f32 v124, v124, v10, 0 op_sel_hi:[0,1,0]\n\t"
    "v_fma_mix_f32 v126, v126, v11, 0 op_sel_hi:[0,1,0]\n\t"
    "v_fma_mix_f32 v125, v125, v10, 0 op_sel:[0,1,0] op_sel_hi:[0,1,0]\n\t"
    "v_fma_mix_f32 v127, v127, v11, 0 op_sel:[0,1,0] op_sel_hi:[0,1,0]\n\t"
    "v_cvt_pk_bf16_f32 v158, v124, v125\n\t"
    "v_cvt_pk_bf16_f32 v159, v126, v127\n\t"
    "v_mfma_f32_32x32x16_bf16 a[176:191], v[236:239], v[152:155], a[176:191]\n\t"
    "s_waitcnt vmcnt(17)\n\t"
    "v_lshlrev_b32 v32, 16, v164\n\t"
    "v_and_b32 v33, s64, v164\n\t"
    "v_lshlrev_b32 v34, 16, v165\n\t"
    "v_and_b32 v35, s64, v165\n\t"
    "v_lshlrev_b32 v36, 16, v166\n\t"
    "v_and_b32 v37, s64, v166\n\t"
    "v_lshlrev_b32 v38, 16, v167\n\t"
    "v_and_b32 v39, s64, v167\n\t"
    "v_mfma_f32_32x32x16_bf16 a[16:31], v[240:243], v[156:159], a[16:31]\n\t"
    "s_waitcnt lgkmcnt(0)\n\t"
    "v_fmac_f32 v32, v104, v72\n\t"
    "v_fmac_f32 v33, v105, v73\n\t"
    "v_fmac_f32 v34, v106, v74\n\t"
    "v_mfma_f32_32x32x16_bf16 a[48:63], v[244:247], v[156:159], a[48:63]\n\t"
    "v_fmac_f32 v35, v107, v75\n\t"
    "v_fmac_f32 v36, v108, v76\n\t"
    "v_fmac_f32 v37, v109, v77\n\t"
    "v_mfma_f32_32x32x16_bf16 a[80:95], v[248:251], v[156:159], a[80:95]\n\t"
    "v_fmac_f32 v38, v110, v78\n\t"
    "v_fmac_f32 v39, v111, v79\n\t"
    "v_cvt_pk_bf16_f32 v128, v32, v33\n\t"
    "v_mfma_f32_32x32x16_bf16 a[112:127], v[252:255], v[156:159], a[112:127]\n\t"
    "v_cvt_pk_bf16_f32 v129, v34, v35\n\t"
    "v_cvt_pk_bf16_f32 v130, v36, v37\n\t"
    "v_cvt_pk_bf16_f32 v131, v38, v39\n\t"
    "v_mfma_f32_32x32x16_bf16 a[144:159], v[56:59], v[156:159], a[144:159]\n\t"
    "buffer_store_dwordx4 v[128:131], v26, s[48:51], s70 offen offset:1024 sc1 nt\n\t"
    "v_mfma_f32_32x32x16_bf16 a[176:191], v[60:63], v[156:159], a[176:191]\n\t"
    "ds_read_b128 v[104:107], v16 offset:0\n\t"
    "ds_read_b128 v[108:111], v16 offset:384\n\t"
    "ds_read_b128 v[88:91], v14 offset:6272\n\t"
    "ds_read_b128 v[92:95], v14 offset:6656\n\t"
    "ds_read_b128 v[96:99], v15 offset:6272\n\t"
    "ds_read_b128 v[100:103], v15 offset:6656\n\t"
    "ds_write_b128 v30, a[16:19] offset:0\n\t"
    "s_waitcnt vmcnt(17)\n\t"
    "v_lshlrev_b32 v144, 16, v168\n\t"
    "v_and_b32 v145, s64, v168\n\t"
    "v_lshlrev_b32 v146, 16, v169\n\t"
    "v_and_b32 v147, s64, v169\n\t"
    "v_lshlrev_b32 v148, 16, v170\n\t"
    "v_and_b32 v149, s64, v170\n\t"
    "v_lshlrev_b32 v150, 16, v171\n\t"
    "v_and_b32 v151, s64, v171\n\t"
    "ds_write_b128 v30, a[20:23] offset:16\n\t"
    "s_waitcnt lgkmcnt(6)\n\t"
    "v_pk_fma_f32 v[144:145], v[104:105], v[80:81], v[144:145]\n\t"
    "v_pk_fma_f32 v[146:147], v[106:107], v[82:83], v[146:147]\n\t"
    "v_pk_fma_f32 v[148:149], v[108:109], v[84:85], v[148:149]\n\t"
    "v_pk_fma_f32 v[150:151], v[110:111], v[86:87], v[150:151]\n\t"
    "v_cvt_pk_bf16_f32 v112, v144, v145\n\t"
    "v_cvt_pk_bf16_f32 v113, v146, v147\n\t"
    "v_cvt_pk_bf16_f32 v114, v148, v149\n\t"
    "v_cvt_pk_bf16_f32 v115, v150, v151\n\t"
    "buffer_store_dwordx4 v[112:115], v26, s[48:51], s70 offen offset:2048 sc1 nt\n\t"
    "ds_write_b128 v30, a[24:27] offset:32\n\t"
    "ds_read_b128 v[104:107], v16 offset:6272\n\t"
    "ds_read_b128 v[108:111], v16 offset:6656\n\t"
    "ds_write_b128 v30, a[28:31] offset:48\n\t"
    "s_waitcnt vmcnt(15)\n\t"
    "v_lshlrev_b32 v152, 16, v172\n\t"
    "v_and_b32 v153, s64, v172\n\t"
    "v_lshlrev_b32 v154, 16, v173\n\t"
    "v_and_b32 v155, s64, v173\n\t"
    "v_lshlrev_b32 v156, 16, v174\n\t"
    "v_and_b32 v157, s64, v174\n\t"
    "v_lshlrev_b32 v158, 16, v175\n\t"
    "v_and_b32 v159, s64, v175\n\t"
    "ds_write_b128 v30, a[48:51] offset:64\n\t"
    "s_waitcnt lgkmcnt(9)\n\t"
    "v_pk_fma_f32 v[152:153], v[88:89], v[64:65], v[152:153]\n\t"
    "v_pk_fma_f32 v[154:155], v[90:91], v[66:67], v[154:155]\n\t"
    "v_pk_fma_f32 v[156:157], v[92:93], v[68:69], v[156:157]\n\t"
    "v_pk_fma_f32 v[158:159], v[94:95], v[70:71], v[158:159]\n\t"
    "v_cvt_pk_bf16_f32 v116, v152, v153\n\t"
    "v_cvt_pk_bf16_f32 v117, v154, v155\n\t"
    "v_cvt_pk_bf16_f32 v118, v156, v157\n\t"
    "v_cvt_pk_bf16_f32 v119, v158, v159\n\t"
    "buffer_store_dwordx4 v[116:119], v26, s[48:51], s70 offen offset:3072 sc1 nt\n\t"
    "ds_write_b128 v30, a[52:55] offset:80\n\t"
    "ds_read_b128 v[88:91], v14 offset:12544\n\t"
    "ds_read_b128 v[92:95], v14 offset:12928\n\t"
    "ds_write_b128 v30, a[56:59] offset:96\n\t"
    "v_lshlrev_b32 v120, 16, v176\n\t"
    "v_and_b32 v121, s64, v176\n\t"
    "v_lshlrev_b32 v122, 16, v177\n\t"
    "v_and_b32 v123, s64, v177\n\t"
    "v_lshlrev_b32 v124, 16, v178\n\t"
    "v_and_b32 v125, s64, v178\n\t"
    "v_lshlrev_b32 v126, 16, v179\n\t"
    "v_and_b32 v127, s64, v179\n\t"
    "ds_write_b128 v30, a[60:63] offset:112\n\t"
    "s_waitcnt lgkmcnt(12)\n\t"
    "v_pk_fma_f32 v[120:121], v[96:97], v[72:73], v[120:121]\n\t"
    "v_pk_fma_f32 v[122:123], v[98:99], v[74:75], v[122:123]\n\t"
    "v_pk_fma_f32 v[124:125], v[100:101], v[76:77], v[124:125]\n\t"
    "v_pk_fma_f32 v[126:127], v[102:103], v[78:79], v[126:127]\n\t"
    "v_cvt_pk_bf16_f32 v112, v120, v121\n\t"
    "v_cvt_pk_bf16_f32 v113, v122, v123\n\t"
    "v_cvt_pk_bf16_f32 v114, v124, v125\n\t"
    "v_cvt_pk_bf16_f32 v115, v126, v127\n\t"
    "buffer_store_dwordx4 v[112:115], v27, s[48:51], s70 offen offset:0 sc1 nt\n\t"
    "ds_write_b128 v30, a[80:83] offset:128\n\t"
    "ds_read_b128 v[96:99], v15 offset:12544\n\t"
    "ds_read_b128 v[100:103], v15 offset:12928\n\t"
    "s_waitcnt lgkmcnt(11)\n\t"
    "ds_write_b128 v30, a[84:87] offset:144\n\t"
    "v_lshlrev_b32 v2, 16, v180\n\t"
    "v_and_b32 v3, s64, v180\n\t"
    "v_lshlrev_b32 v4, 16, v181\n\t"
    "v_and_b32 v5, s64, v181\n\t"
    "v_lshlrev_b32 v6, 16, v182\n\t"
    "v_and_b32 v7, s64, v182\n\t"
    "v_lshlrev_b32 v8, 16, v183\n\t"
    "v_and_b32 v9, s64, v183\n\t"
    "ds_write_b128 v30, a[88:91] offset:160\n\t"
    "s_waitcnt lgkmcnt(12)\n\t"
    "v_pk_fma_f32 v[2:3], v[104:105], v[80:81], v[2:3]\n\t"
    "v_pk_fma_f32 v[4:5], v[106:107], v[82:83], v[4:5]\n\t"
    "v_pk_fma_f32 v[6:7], v[108:109], v[84:85], v[6:7]\n\t"
    "v_pk_fma_f32 v[8:9], v[110:111], v[86:87], v[8:9]\n\t"
    "v_cvt_pk_bf16_f32 v116, v2, v3\n\t"
    "v_cvt_pk_bf16_f32 v117, v4, v5\n\t"
    "v_cvt_pk_bf16_f32 v118, v6, v7\n\t"
    "v_cvt_pk_bf16_f32 v119, v8, v9\n\t"
    "buffer_store_dwordx4 v[116:119], v27, s[48:51], s70 offen offset:1024 sc1 nt\n\t"
    "ds_write_b128 v30, a[92:95] offset:176\n\t"
    "ds_read_b128 v[104:107], v16 offset:12544\n\t"
    "ds_read_b128 v[108:111], v16 offset:12928\n\t"
    "s_waitcnt lgkmcnt(11)\n\t"
    "ds_write_b128 v30, a[112:115] offset:192\n\t"
    "s_waitcnt vmcnt(15)\n\t"
    "v_lshlrev_b32 v48, 16, v184\n\t"
    "v_and_b32 v49, s64, v184\n\t"
    "v_lshlrev_b32 v50, 16, v185\n\t"
    "v_and_b32 v51, s64, v185\n\t"
    "v_lshlrev_b32 v52, 16, v186\n\t"
    "v_and_b32 v53, s64, v186\n\t"
    "v_lshlrev_b32 v54, 16, v187\n\t"
    "v_and_b32 v55, s64, v187\n\t"
    "ds_write_b128 v30, a[116:119] offset:208\n\t"
    "s_waitcnt lgkmcnt(12)\n\t"
    "v_pk_fma_f32 v[48:49], v[88:89], v[64:65], v[48:49]\n\t"
    "v_pk_fma_f32 v[50:51], v[90:91], v[66:67], v[50:51]\n\t"
    "v_pk_fma_f32 v[52:53], v[92:93], v[68:69], v[52:53]\n\t"
    "v_pk_fma_f32 v[54:55], v[94:95], v[70:71], v[54:55]\n\t"
    "v_cvt_pk_bf16_f32 v112, v48, v49\n\t"
    "v_cvt_pk_bf16_f32 v113, v50, v51\n\t"
    "v_cvt_pk_bf16_f32 v114, v52, v53\n\t"
    "v_cvt_pk_bf16_f32 v115, v54, v55\n\t"
    "buffer_store_dwordx4 v[112:115], v26, s[48:51], s71 offen offset:0 sc1 nt\n\t"
    "ds_write_b128 v30, a[120:123] offset:224\n\t"
    "ds_read_b128 v[88:91], v14 offset:18816\n\t"
    "ds_read_b128 v[92:95], v14 offset:19200\n\t"
    "s_waitcnt lgkmcnt(11)\n\t"
    "ds_write_b128 v30, a[124:127] offset:240\n\t"
    "v_lshlrev_b32 v32, 16, v188\n\t"
    "v_and_b32 v33, s64, v188\n\t"
    "v_lshlrev_b32 v34, 16, v189\n\t"
    "v_and_b32 v35, s64, v189\n\t"
    "v_lshlrev_b32 v36, 16, v190\n\t"
    "v_and_b32 v37, s64, v190\n\t"
    "v_lshlrev_b32 v38, 16, v191\n\t"
    "v_and_b32 v39, s64, v191\n\t"
    "ds_write_b128 v30, a[144:147] offset:256\n\t"
    "s_waitcnt lgkmcnt(12)\n\t"
    "v_pk_fma_f32 v[32:33], v[96:97], v[72:73], v[32:33]\n\t"
    "v_pk_fma_f32 v[34:35], v[98:99], v[74:75], v[34:35]\n\t"
    "v_pk_fma_f32 v[36:37], v[100:101], v[76:77], v[36:37]\n\t"
    "v_pk_fma_f32 v[38:39], v[102:103], v[78:79], v[38:39]\n\t"
    "v_cvt_pk_bf16_f32 v116, v32, v33\n\t"
    "v_cvt_pk_bf16_f32 v117, v34, v35\n\t"
    "v_cvt_pk_bf16_f32 v118, v36, v37\n\t"
    "v_cvt_pk_bf16_f32 v119, v38, v39\n\t"
    "buffer_store_dwordx4 v[116:119], v26, s[48:51], s71 offen offset:1024 sc1 nt\n\t"
    "ds_write_b128 v30, a[148:151] offset:272\n\t"
    "ds_read_b128 v[96:99], v15 offset:18816\n\t"
    "ds_read_b128 v[100:103], v15 offset:19200\n\t"
    "s_waitcnt lgkmcnt(11)\n\t"
    "ds_write_b128 v30, a[152:155] offset:288\n\t"
    "v_lshlrev_b32 v128, 16, v192\n\t"
    "v_and_b32 v129, s64, v192\n\t"
    "v_lshlrev_b32 v130, 16, v193\n\t"
    "v_and_b32 v131, s64, v193\n\t"
    "v_lshlrev_b32 v132, 16, v194\n\t"
    "v_and_b32 v133, s64, v194\n\t"
    "v_lshlrev_b32 v134, 16, v195\n\t"
    "v_and_b32 v135, s64, v195\n\t"
    "ds_write_b128 v30, a[156:159] offset:304\n\t"
    "s_waitcnt lgkmcnt(12)\n\t"
    "v_pk_fma_f32 v[128:129], v[104:105], v[80:81], v[128:129]\n\t"
    "v_pk_fma_f32 v[130:131], v[106:107], v[82:83], v[130:131]\n\t"
    "v_pk_fma_f32 v[132:133], v[108:109], v[84:85], v[132:133]\n\t"
    "v_pk_fma_f32 v[134:135], v[110:111], v[86:87], v[134:135]\n\t"
    "v_cvt_pk_bf16_f32 v112, v128, v129\n\t"
    "v_cvt_pk_bf16_f32 v113, v130, v131\n\t"
    "v_cvt_pk_bf16_f32 v114, v132, v133\n\t"
    "v_cvt_pk_bf16_f32 v115, v134, v135\n\t"
    "buffer_store_dwordx4 v[112:115], v26, s[48:51], s71 offen offset:2048 sc1 nt\n\t"
    "ds_write_b128 v30, a[176:179] offset:320\n\t"
    "ds_read_b128 v[104:107], v16 offset:18816\n\t"
    "ds_read_b128 v[108:111], v16 offset:19200\n\t"
    "s_waitcnt lgkmcnt(11)\n\t"
    "ds_write_b128 v30, a[180:183] offset:336\n\t"
    "s_waitcnt vmcnt(15)\n\t"
    "v_lshlrev_b32 v136, 16, v196\n\t"
    "v_and_b32 v137, s64, v196\n\t"
    "v_lshlrev_b32 v138, 16, v197\n\t"
    "v_and_b32 v139, s64, v197\n\t"
    "v_lshlrev_b32 v140, 16, v198\n\t"
    "v_and_b32 v141, s64, v198\n\t"
    "v_lshlrev_b32 v142, 16, v199\n\t"
    "v_and_b32 v143, s64, v199\n\t"
    "ds_write_b128 v30, a[184:187] offset:352\n\t"
    "s_waitcnt lgkmcnt(12)\n\t"
    "v_pk_fma_f32 v[136:137], v[88:89], v[64:65], v[136:137]\n\t"
    "v_pk_fma_f32 v[138:139], v[90:91], v[66:67], v[138:139]\n\t"
    "v_pk_fma_f32 v[140:141], v[92:93], v[68:69], v[140:141]\n\t"
    "v_pk_fma_f32 v[142:143], v[94:95], v[70:71], v[142:143]\n\t"
    "v_cvt_pk_bf16_f32 v116, v136, v137\n\t"
    "v_cvt_pk_bf16_f32 v117, v138, v139\n\t"
    "v_cvt_pk_bf16_f32 v118, v140, v141\n\t"
    "v_cvt_pk_bf16_f32 v119, v142, v143\n\t"
    "buffer_store_dwordx4 v[116:119], v26, s[48:51], s71 offen offset:3072 sc1 nt\n\t"
    "ds_write_b128 v30, a[188:191] offset:368\n\t"
    "s_waitcnt lgkmcnt(0)\n\t"
    "s_barrier\n\t"
    "ds_read_b128 v[88:91], v14 offset:25088\n\t"
    "ds_read_b128 v[92:95], v14 offset:25472\n\t"
    "v_lshlrev_b32 v144, 16, v200\n\t"
    "v_and_b32 v145, s64, v200\n\t"
    "v_lshlrev_b32 v146, 16, v201\n\t"
    "v_and_b32 v147, s64, v201\n\t"
    "v_lshlrev_b32 v148, 16, v202\n\t"
    "v_and_b32 v149, s64, v202\n\t"
    "v_lshlrev_b32 v150, 16, v203\n\t"
    "v_and_b32 v151, s64, v203\n\t"
    "v_pk_fma_f32 v[144:145], v[96:97], v[72:73], v[144:145]\n\t"
    "v_pk_fma_f32 v[146:147], v[98:99], v[74:75], v[146:147]\n\t"
    "v_pk_fma_f32 v[148:149], v[100:101], v[76:77], v[148:149]\n\t"
    "v_pk_fma_f32 v[150:151], v[102:103], v[78:79], v[150:151]\n\t"
    "v_cvt_pk_bf16_f32 v112, v144, v145\n\t"
    "v_cvt_pk_bf16_f32 v113, v146, v147\n\t"
    "v_cvt_pk_bf16_f32 v114, v148, v149\n\t"
    "v_cvt_pk_bf16_f32 v115, v150, v151\n\t"
    "buffer_store_dwordx4 v[112:115], v27, s[48:51], s71 offen offset:0 sc1 nt\n\t"
    "ds_read_b128 v[96:99], v15 offset:25088\n\t"
    "ds_read_b128 v[100:103], v15 offset:25472\n\t"
    "v_lshlrev_b32 v152, 16, v204\n\t"
    "v_and_b32 v153, s64, v204\n\t"
    "v_lshlrev_b32 v154, 16, v205\n\t"
    "v_and_b32 v155, s64, v205\n\t"
    "v_lshlrev_b32 v156, 16, v206\n\t"
    "v_and_b32 v157, s64, v206\n\t"
    "v_lshlrev_b32 v158, 16, v207\n\t"
    "v_and_b32 v159, s64, v207\n\t"
    "v_pk_fma_f32 v[152:153], v[104:105], v[80:81], v[152:153]\n\t"
    "v_pk_fma_f32 v[154:155], v[106:107], v[82:83], v[154:155]\n\t"
    "v_pk_fma_f32 v[156:157], v[108:109], v[84:85], v[156:157]\n\t"
    "v_pk_fma_f32 v[158:159], v[110:111], v[86:87], v[158:159]\n\t"
    "v_cvt_pk_bf16_f32 v116, v152, v153\n\t"
    "v_cvt_pk_bf16_f32 v117, v154, v155\n\t"
    "v_cvt_pk_bf16_f32 v118, v156, v157\n\t"
    "v_cvt_pk_bf16_f32 v119, v158, v159\n\t"
    "buffer_store_dwordx4 v[116:119], v27, s[48:51], s71 offen offset:1024 sc1 nt\n\t"
    "ds_read_b128 v[104:107], v16 offset:25088\n\t"
    "ds_read_b128 v[108:111], v16 offset:25472\n\t"
    "s_waitcnt vmcnt(15)\n\t"
    "v_lshlrev_b32 v120, 16, v208\n\t"
    "v_and_b32 v121, s64, v208\n\t"
    "v_lshlrev_b32 v122, 16, v209\n\t"
    "v_and_b32 v123, s64, v209\n\t"
    "v_lshlrev_b32 v124, 16, v210\n\t"
    "v_and_b32 v125, s64, v210\n\t"
    "v_lshlrev_b32 v126, 16, v211\n\t"
    "v_and_b32 v127, s64, v211\n\t"
    "s_waitcnt lgkmcnt(4)\n\t"
    "v_pk_fma_f32 v[120:121], v[88:89], v[64:65], v[120:121]\n\t"
    "v_pk_fma_f32 v[122:123], v[90:91], v[66:67], v[122:123]\n\t"
    "v_pk_fma_f32 v[124:125], v[92:93], v[68:69], v[124:125]\n\t"
    "v_pk_fma_f32 v[126:127], v[94:95], v[70:71], v[126:127]\n\t"
    "v_cvt_pk_bf16_f32 v112, v120, v121\n\t"
    "v_cvt_pk_bf16_f32 v113, v122, v123\n\t"
    "v_cvt_pk_bf16_f32 v114, v124, v125\n\t"
    "v_cvt_pk_bf16_f32 v115, v126, v127\n\t"
    "buffer_store_dwordx4 v[112:115], v26, s[48:51], s72 offen offset:0 sc1 nt\n\t"
    "ds_read_b128 v[88:91], v14 offset:31360\n\t"
    "ds_read_b128 v[92:95], v14 offset:31744\n\t"
    "v_lshlrev_b32 v2, 16, v212\n\t"
    "v_and_b32 v3, s64, v212\n\t"
    "v_lshlrev_b32 v4, 16, v213\n\t"
    "v_and_b32 v5, s64, v213\n\t"
    "v_lshlrev_b32 v6, 16, v214\n\t"
    "v_and_b32 v7, s64, v214\n\t"
    "v_lshlrev_b32 v8, 16, v215\n\t"
    "v_and_b32 v9, s64, v215\n\t"
    "s_waitcnt lgkmcnt(4)\n\t"
    "v_pk_fma_f32 v[2:3], v[96:97], v[72:73], v[2:3]\n\t"
    "v_pk_fma_f32 v[4:5], v[98:99], v[74:75], v[4:5]\n\t"
    "v_pk_fma_f32 v[6:7], v[100:101], v[76:77], v[6:7]\n\t"
    "v_pk_fma_f32 v[8:9], v[102:103], v[78:79], v[8:9]\n\t"
    "v_cvt_pk_bf16_f32 v116, v2, v3\n\t"
    "v_cvt_pk_bf16_f32 v117, v4, v5\n\t"
    "v_cvt_pk_bf16_f32 v118, v6, v7\n\t"
    "v_cvt_pk_bf16_f32 v119, v8, v9\n\t"
    "buffer_store_dwordx4 v[116:119], v26, s[48:51], s72 offen offset:1024 sc1 nt\n\t"
    "ds_read_b128 v[96:99], v15 offset:31360\n\t"
    "ds_read_b128 v[100:103], v15 offset:31744\n\t"
    "v_lshlrev_b32 v48, 16, v216\n\t"
    "v_and_b32 v49, s64, v216\n\t"
    "v_lshlrev_b32 v50, 16, v217\n\t"
    "v_and_b32 v51, s64, v217\n\t"
    "v_lshlrev_b32 v52, 16, v218\n\t"
    "v_and_b32 v53, s64, v218\n\t"
    "v_lshlrev_b32 v54, 16, v219\n\t"
    "v_and_b32 v55, s64, v219\n\t"
    "s_waitcnt lgkmcnt(4)\n\t"
    "v_pk_fma_f32 v[48:49], v[104:105], v[80:81], v[48:49]\n\t"
    "v_pk_fma_f32 v[50:51], v[106:107], v[82:83], v[50:51]\n\t"
    "v_pk_fma_f32 v[52:53], v[108:109], v[84:85], v[52:53]\n\t"
    "v_pk_fma_f32 v[54:55], v[110:111], v[86:87], v[54:55]\n\t"
    "v_cvt_pk_bf16_f32 v112, v48, v49\n\t"
    "v_cvt_pk_bf16_f32 v113, v50, v51\n\t"
    "v_cvt_pk_bf16_f32 v114, v52, v53\n\t"
    "v_cvt_pk_bf16_f32 v115, v54, v55\n\t"
    "buffer_store_dwordx4 v[112:115], v26, s[48:51], s72 offen offset:2048 sc1 nt\n\t"
    "ds_read_b128 v[104:107], v16 offset:31360\n\t"
    "ds_read_b128 v[108:111], v16 offset:31744\n\t"
    "s_waitcnt vmcnt(15)\n\t"
    "v_lshlrev_b32 v32, 16, v220\n\t"
    "v_and_b32 v33, s64, v220\n\t"
    "v_lshlrev_b32 v34, 16, v221\n\t"
    "v_and_b32 v35, s64, v221\n\t"
    "v_lshlrev_b32 v36, 16, v222\n\t"
    "v_and_b32 v37, s64, v222\n\t"
    "v_lshlrev_b32 v38, 16, v223\n\t"
    "v_and_b32 v39, s64, v223\n\t"
    "s_waitcnt lgkmcnt(4)\n\t"
    "v_pk_fma_f32 v[32:33], v[88:89], v[64:65], v[32:33]\n\t"
    "v_pk_fma_f32 v[34:35], v[90:91], v[66:67], v[34:35]\n\t"
    "v_pk_fma_f32 v[36:37], v[92:93], v[68:69], v[36:37]\n\t"
    "v_pk_fma_f32 v[38:39], v[94:95], v[70:71], v[38:39]\n\t"
    "v_cvt_pk_bf16_f32 v116, v32, v33\n\t"
    "v_cvt_pk_bf16_f32 v117, v34, v35\n\t"
    "v_cvt_pk_bf16_f32 v118, v36, v37\n\t"
    "v_cvt_pk_bf16_f32 v119, v38, v39\n\t"
    "buffer_store_dwordx4 v[116:119], v26, s[48:51], s72 offen offset:3072 sc1 nt\n\t"
    "v_lshlrev_b32 v128, 16, v224\n\t"
    "v_and_b32 v129, s64, v224\n\t"
    "v_lshlrev_b32 v130, 16, v225\n\t"
    "v_and_b32 v131, s64, v225\n\t"
    "v_lshlrev_b32 v132, 16, v226\n\t"
    "v_and_b32 v133, s64, v226\n\t"
    "v_lshlrev_b32 v134, 16, v227\n\t"
    "v_and_b32 v135, s64, v227\n\t"
    "s_waitcnt lgkmcnt(2)\n\t"
    "v_pk_fma_f32 v[128:129], v[96:97], v[72:73], v[128:129]\n\t"
    "v_pk_fma_f32 v[130:131], v[98:99], v[74:75], v[130:131]\n\t"
    "v_pk_fma_f32 v[132:133], v[100:101], v[76:77], v[132:133]\n\t"
    "v_pk_fma_f32 v[134:135], v[102:103], v[78:79], v[134:135]\n\t"
    "v_cvt_pk_bf16_f32 v112, v128, v129\n\t"
    "v_cvt_pk_bf16_f32 v113, v130, v131\n\t"
    "v_cvt_pk_bf16_f32 v114, v132, v133\n\t"
    "v_cvt_pk_bf16_f32 v115, v134, v135\n\t"
    "buffer_store_dwordx4 v[112:115], v27, s[48:51], s72 offen offset:0 sc1 nt\n\t"
    "v_lshlrev_b32 v136, 16, v228\n\t"
    "v_and_b32 v137, s64, v228\n\t"
    "v_lshlrev_b32 v138, 16, v229\n\t"
    "v_and_b32 v139, s64, v229\n\t"
    "v_lshlrev_b32 v140, 16, v230\n\t"
    "v_and_b32 v141, s64, v230\n\t"
    "v_lshlrev_b32 v142, 16, v231\n\t"
    "v_and_b32 v143, s64, v231\n\t"
    "s_waitcnt lgkmcnt(0)\n\t"
    "v_pk_fma_f32 v[136:137], v[104:105], v[80:81], v[136:137]\n\t"
    "v_pk_fma_f32 v[138:139], v[106:107], v[82:83], v[138:139]\n\t"
    "v_pk_fma_f32 v[140:141], v[108:109], v[84:85], v[140:141]\n\t"
    "v_pk_fma_f32 v[142:143], v[110:111], v[86:87], v[142:143]\n\t"
    "v_cvt_pk_bf16_f32 v116, v136, v137\n\t"
    "v_cvt_pk_bf16_f32 v117, v138, v139\n\t"
    "v_cvt_pk_bf16_f32 v118, v140, v141\n\t"
    "v_cvt_pk_bf16_f32 v119, v142, v143\n\t"
    "buffer_store_dwordx4 v[116:119], v27, s[48:51], s72 offen offset:1024 sc1 nt\n\t"
    "end_c_%=:\n\t"
    "s_endpgm\n\t"
    "prog_a_%=:\n\t"
    "s_add_u32 m0, s78, 57344\n\t"
    "s_nop 0\n\t"
    "buffer_load_dwordx4 v96, s[36:39], s71 offen lds\n\t"
    "v_and_b32 v61, 15, v0\n\t"
    "v_lshrrev_b32 v62, 4, v0\n\t"
    "v_and_b32 v53, 3, v0\n\t"
    "v_mul_u32_u24 v9, 0x5555, v53\n\t"
    "v_and_b32 v53, 1, v0\n\t"
    "v_lshlrev_b32 v53, 4, v53\n\t"
    "v_mov_b32 v54, 0xffff\n\t"
    "v_lshlrev_b32 v53, v53, v54\n\t"
    "v_lshrrev_b32 v54, 2, v61\n\t"
    "v_cmp_eq_u32 vcc, 0, v54\n\t"
    "s_nop 1\n\t"
    "v_cndmask_b32 v1, 0, v53, vcc\n\t"
    "v_cmp_eq_u32 vcc, 1, v54\n\t"
    "s_nop 1\n\t"
    "v_cndmask_b32 v2, 0, v53, vcc\n\t"
    "v_cmp_eq_u32 vcc, 2, v54\n\t"
    "s_nop 1\n\t"
    "v_cndmask_b32 v3, 0, v53, vcc\n\t"
    "v_cmp_eq_u32 vcc, 3, v54\n\t"
    "s_nop 1\n\t"
    "v_cndmask_b32 v4, 0, v53, vcc\n\t"
    "v_lshrrev_b32 v54, 3, v61\n\t"
    "v_and_b32 v55, 1, v62\n\t"
    "v_cmp_eq_u32 vcc, v54, v55\n\t"
    "v_bfe_u32 v54, v61, 1, 2\n\t"
    "s_nop 0\n\t"
    "v_cndmask_b32 v53, 0, v53, vcc\n\t"
    "v_cmp_eq_u32 vcc, 0, v54\n\t"
    "s_nop 1\n\t"
    "v_cndmask_b32 v5, 0, v53, vcc\n\t"
    "v_cmp_eq_u32 vcc, 1, v54\n\t"
    "s_nop 1\n\t"
    "v_cndmask_b32 v6, 0, v53, vcc\n\t"
    "v_cmp_eq_u32 vcc, 2, v54\n\t"
    "s_nop 1\n\t"
    "v_cndmask_b32 v7, 0, v53, vcc\n\t"
    "v_cmp_eq_u32 vcc, 3, v54\n\t"
    "s_nop 1\n\t"
    "v_cndmask_b32 v8, 0, v53, vcc\n\t"
    "v_lshrrev_b32 v53, 1, v62\n\t"
    "v_and_b32 v54, 14, v61\n\t"
    "v_lshlrev_b32 v54, 1, v54\n\t"
    "v_add_u32 v54, 158208, v54\n\t"
    "v_mul_u32_u24 v55, 384, v53\n\t"
    "v_add_u32 v10, v54, v55\n\t"
    "v_add_u32 v10, 2304, v10\n\t"
    "v_mul_u32_u24 v55, 3, v53\n\t"
    "v_and_b32 v53, 1, v62\n\t"
    "v_add_u32 v55, v55, v53\n\t"
    "v_mul_u32_u24 v55, 384, v55\n\t"
    "v_add_u32 v11, v54, v55\n\t"
    "v_mul_u32_u24 v55, 1152, v62\n\t"
    "v_add_u32 v53, 768, v55\n\t"
    "v_min_u32 v53, 3456, v53\n\t"
    "v_add_u32 v12, v54, v53\n\t"
    "v_add_u32 v53, -384, v55\n\t"
    "v_min_u32 v53, 3456, v53\n\t"
    "v_add_u32 v13, v54, v53\n\t"
    "v_lshrrev_b32 v53, 1, v62\n\t"
    "s_mul_i32 s61, s75, 2304\n\t"
    "s_add_u32 s61, s61, 24576\n\t"
    "s_add_u32 m0, s78, 61440\n\t"
    "s_nop 0\n\t"
    "buffer_load_dwordx4 v97, s[36:39], s71 offen lds\n\t"
    "v_add_u32 v54, v53, v61\n\t"
    "v_lshlrev_b32 v14, 7, v54\n\t"
    "v_and_b32 v54, 6, v54\n\t"
    "v_and_or_b32 v54, v62, 1, v54\n\t"
    "v_lshl_add_u32 v14, v54, 4, v14\n\t"
    "v_add_u32 v14, s61, v14\n\t"
    "v_add_u32 v54, 2, v61\n\t"
    "v_lshlrev_b32 v18, 7, v54\n\t"
    "v_and_b32 v54, 6, v54\n\t"
    "v_and_or_b32 v54, v62, 1, v54\n\t"
    "v_lshl_add_u32 v18, v54, 4, v18\n\t"
    "v_add_u32 v18, s61, v18\n\t"
    "v_add_u32 v22, 9216, v18\n\t"
    "v_mul_u32_u24 v54, 2304, v53\n\t"
    "v_add_u32 v18, v18, v54\n\t"
    "v_xor_b32 v15, 32, v14\n\t"
    "v_xor_b32 v16, 64, v14\n\t"
    "v_xor_b32 v17, 96, v14\n\t"
    "v_xor_b32 v19, 32, v18\n\t"
    "v_xor_b32 v20, 64, v18\n\t"
    "v_xor_b32 v21, 96, v18\n\t"
    "v_xor_b32 v23, 32, v22\n\t"
    "v_xor_b32 v24, 64, v22\n\t"
    "v_xor_b32 v25, 96, v22\n\t"
    "s_mov_b64 s[48:49], s[98:99]\n\t"
    "s_mov_b32 s50, 19267584\n\t"
    "s_mov_b32 s51, 0x20000\n\t"
    "s_mov_b32 s60, 0x44ea44ea\n\t"
    "s_mov_b64 s[40:41], s[88:89]\n\t"
    "s_mov_b32 s42, 294912\n\t"
    "s_mov_b32 s43, 0x20000\n\t"
    "s_mov_b64 s[44:45], s[92:93]\n\t"
    "s_sub_u32 s44, s44, 4096\n\t"
    "s_subb_u32 s45, s45, 0\n\t"
    "s_mov_b32 s46, 299008\n\t"
    "s_mov_b32 s47, 0x20000\n\t"
    "v_lshrrev_b32 v53, 3, v0\n\t"
    "v_bfe_u32 v54, v0, 4, 1\n\t"
    "v_lshlrev_b32 v54, 2, v54\n\t"
    "v_bfe_u32 v55, v0, 5, 1\n\t"
    "v_lshl_add_u32 v54, v55, 1, v54\n\t"
    "v_and_b32 v55, 7, v0\n\t"
    "v_xor_b32 v54, v54, v55\n\t"
    "v_lshlrev_b32 v54, 4, v54\n\t"
    "v_mul_u32_u24 v55, 1536, v53\n\t"
    "v_add_u32 v26, v55, v54\n\t"
    "v_add_u32 v27, 11264, v26\n\t"
    "v_add_u32 v28, 22528, v26\n\t"
    "v_mul_u32_u24 v55, 384, v53\n\t"
    "v_add_u32 v55, v55, v54\n\t"
    "v_add_u32 v29, 4096, v55\n\t"
    "v_add_u32 v30, 3200, v55\n\t"
    "v_add_u32 v31, 2304, v55\n\t"
    "s_mul_i32 s66, s79, 3072\n\t"
    "s_mul_i32 s67, s79, 3072\n\t"
    "s_mul_i32 s65, s79, 36864\n\t"
    "s_add_u32 s68, s67, 55296\n\t"
    "v_lshlrev_b32 v53, 2, v57\n\t"
    "v_min_u32 v54, 764, v53\n\t"
    "v_add_u32 v53, 0x20000, v53\n\t"
    "v_add_u32 v54, 0x20000, v54\n\t"
    "s_mov_b32 s61, 0x3eefe414\n\t"
    "s_mov_b32 s62, 0x4008986d\n\t"
    "s_add_u32 m0, s78, 65536\n\t"
    "s_nop 0\n\t"
    "buffer_load_dwordx4 v98, s[36:39], s71 offen lds\n\t"
    "s_waitcnt vmcnt(9)\n\t"
    "v_lshlrev_b32 v48, 16, v48\n\t"
    "v_lshlrev_b32 v49, 16, v49\n\t"
    "v_lshlrev_b32 v50, 16, v50\n\t"
    "v_lshlrev_b32 v51, 16, v51\n\t"
    "v_lshlrev_b32 v52, 16, v52\n\t"
    "v_mul_f32 v48, s61, v48\n\t"
    "v_mul_f32 v49, s61, v49\n\t"
    "v_mul_f32 v50, s61, v50\n\t"
    "v_mul_f32 v51, s61, v51\n\t"
    "v_mul_f32 v52, s62, v52\n\t"
    "ds_write_b32 v53, v48 offset:22528\n\t"
    "ds_write_b32 v53, v49 offset:23552\n\t"
    "ds_write_b32 v53, v50 offset:24576\n\t"
    "ds_write_b32 v54, v51 offset:25600\n\t"
    "ds_write_b32 v54, v52 offset:26368\n\t"
    "s_add_u32 m0, s78, 69632\n\t"
    "s_nop 0\n\t"
    "buffer_load_dwordx4 v99, s[36:39], s71 offen lds\n\t"
    "s_add_u32 m0, s78, 73728\n\t"
    "s_nop 0\n\t"
    "buffer_load_dwordx4 v100, s[36:39], s71 offen lds\n\t"
    "s_waitcnt vmcnt(5) lgkmcnt(0)\n\t"
    "s_barrier\n\t"
    "ds_read2_b32 v[48:49], v10 offset0:0 offset1:8\n\t"
    "ds_read2_b32 v[50:51], v11 offset0:0 offset1:8\n\t"
    "ds_read2_b32 v[52:53], v12 offset0:0 offset1:8\n\t"
    "ds_read2_b32 v[54:55], v13 offset0:0 offset1:8\n\t"
    "ds_read_b128 a[8:11], v14 offset:4608\n\t"
    "ds_read_b128 a[12:15], v14 offset:6912\n\t"
    "ds_read_b128 a[16:19], v14 offset:9216\n\t"
    "ds_read_b128 a[0:3], v14 offset:0\n\t"
    "ds_read_b128 a[4:7], v14 offset:2304\n\t"
    "ds_read_b128 a[20:23], v18 offset:0\n\t"
    "ds_read_b128 a[24:27], v18 offset:4608\n\t"
    "ds_read_b128 a[28:31], v22 offset:0\n\t"
    "s_waitcnt lgkmcnt(8)\n\t"
    "v_and_b32 v32, v5, v48\n\t"
    "v_and_b32 v33, v6, v48\n\t"
    "v_and_b32 v34, v7, v48\n\t"
    "v_and_b32 v35, v8, v48\n\t"
    "v_and_b32 v36, v1, v50\n\t"
    "v_and_b32 v37, v2, v50\n\t"
    "v_and_b32 v38, v3, v50\n\t"
    "v_and_b32 v39, v4, v50\n\t"
    "v_and_b32 v40, v1, v52\n\t"
    "v_and_b32 v41, v2, v52\n\t"
    "v_and_b32 v42, v3, v52\n\t"
    "v_and_b32 v43, v4, v52\n\t"
    "v_and_b32 v44, v1, v54\n\t"
    "v_and_b32 v45, v2, v54\n\t"
    "v_and_b32 v46, v3, v54\n\t"
    "v_and_b32 v47, v4, v54\n\t"
    "s_waitcnt lgkmcnt(5)\n\t"
    "v_mfma_f32_16x16x32_bf16 v[64:67], v[32:35], a[8:11], 0\n\t"
    "ds_read_b128 a[40:43], v15 offset:4608\n\t"
    "v_mfma_f32_16x16x32_bf16 v[112:115], v[32:35], a[12:15], 0\n\t"
    "ds_read_b128 a[44:47], v15 offset:6912\n\t"
    "s_add_u32 m0, s78, 77824\n\t"
    "s_nop 0\n\t"
    "buffer_load_dwordx4 v101, s[36:39], s71 offen lds\n\t"
    "v_mfma_f32_16x16x32_bf16 v[208:211], v[32:35], a[16:19], 0\n\t"
    "ds_read_b128 a[48:51], v15 offset:9216\n\t"
    "s_waitcnt lgkmcnt(6)\n\t"
    "v_smfmac_f32_16x16x64_bf16 v[64:67], v[36:39], a[0:7], v9\n\t"
    "ds_read_b128 a[32:35], v15 offset:0\n\t"
    "v_and_b32 v32, v5, v49\n\t"
    "v_and_b32 v33, v6, v49\n\t"
    "v_smfmac_f32_16x16x64_bf16 v[112:115], v[36:39], a[4:11], v9\n\t"
    "ds_read_b128 a[36:39], v15 offset:2304\n\t"
    "s_add_u32 m0, s78, 81920\n\t"
    "s_nop 0\n\t"
    "buffer_load_dwordx4 v102, s[36:39], s71 offen lds\n\t"
    "v_and_b32 v34, v7, v49\n\t"
    "v_smfmac_f32_16x16x64_bf16 v[208:211], v[36:39], a[8:15], v9\n\t"
    "ds_read_b128 a[52:55], v19 offset:0\n\t"
    "v_and_b32 v35, v8, v49\n\t"
    "s_waitcnt lgkmcnt(6)\n\t"
    "v_smfmac_f32_16x16x64_bf16 v[64:67], v[40:43], a[20:27], v9\n\t"
    "ds_read_b128 a[56:59], v19 offset:4608\n\t"
    "ds_read2_b32 v[48:49], v10 offset0:16 offset1:24\n\t"
    "v_and_b32 v36, v1, v51\n\t"
    "v_and_b32 v37, v2, v51\n\t"
    "v_smfmac_f32_16x16x64_bf16 v[208:211], v[40:43], a[24:31], v9\n\t"
    "ds_read_b128 a[60:63], v23 offset:0\n\t"
    "s_add_u32 m0, s78, 86016\n\t"
    "s_nop 0\n\t"
    "buffer_load_dwordx4 v103, s[36:39], s71 offen lds\n\t"
    "v_and_b32 v38, v3, v51\n\t"
    "v_smfmac_f32_16x16x64_bf16 v[112:115], v[44:47], a[20:27], v9\n\t"
    "v_and_b32 v39, v4, v51\n\t"
    "v_and_b32 v40, v1, v53\n\t"
    "v_and_b32 v41, v2, v53\n\t"
    "v_and_b32 v42, v3, v53\n\t"
    "v_and_b32 v43, v4, v53\n\t"
    "v_and_b32 v44, v1, v55\n\t"
    "v_and_b32 v45, v2, v55\n\t"
    "v_and_b32 v46, v3, v55\n\t"
    "v_and_b32 v47, v4, v55\n\t"
    "v_sub_f32 v180, 0, v64\n\t"
    "v_sub_f32 v182, 0, v112\n\t"
    "v_sub_f32 v184, 0, v208\n\t"
    "v_sub_f32 v186, 0, v208\n\t"
    "v_permlane16_swap_b32 v180, v182\n\t"
    "s_nop 0\n\t"
    "v_permlane16_swap_b32 v184, v186\n\t"
    "s_nop 1\n\t"
    "v_permlane32_swap_b32 v180, v184\n\t"
    "v_mov_b32 v184, v180\n\t"
    "s_nop 1\n\t"
    "v_permlane32_swap_b32 v180, v184\n\t"
    "v_mov_b32 v182, v180\n\t"
    "v_mov_b32 v186, v184\n\t"
    "s_nop 0\n\t"
    "v_permlane16_swap_b32 v180, v182\n\t"
    "v_permlane16_swap_b32 v184, v186\n\t"
    "v_mov_b32 v168, v180\n\t"
    "v_mov_b32 v169, v180\n\t"
    "v_mov_b32 v170, v180\n\t"
    "v_mov_b32 v171, v180\n\t"
    "v_mov_b32 v172, v182\n\t"
    "v_mov_b32 v173, v182\n\t"
    "v_mov_b32 v174, v182\n\t"
    "v_mov_b32 v175, v182\n\t"
    "v_mov_b32 v176, v184\n\t"
    "v_mov_b32 v177, v184\n\t"
    "v_mov_b32 v178, v184\n\t"
    "v_mov_b32 v179, v184\n\t"
    "v_add_f32 v64, v64, v180\n\t"
    "v_add_f32 v65, v65, v180\n\t"
    "v_add_f32 v66, v66, v180\n\t"
    "v_add_f32 v67, v67, v180\n\t"
    "v_add_f32 v112, v112, v182\n\t"
    "v_add_f32 v113, v113, v182\n\t"
    "v_add_f32 v114, v114, v182\n\t"
    "v_add_f32 v115, v115, v182\n\t"
    "v_add_f32 v208, v208, v184\n\t"
    "v_add_f32 v209, v209, v184\n\t"
    "v_add_f32 v210, v210, v184\n\t"
    "v_add_f32 v211, v211, v184\n\t"
    "s_waitcnt lgkmcnt(6)\n\t"
    "v_mfma_f32_16x16x32_bf16 v[68:71], v[32:35], a[40:43], v[168:171]\n\t"
    "ds_read_b128 a[72:75], v16 offset:4608\n\t"
    "ds_read2_b32 v[50:51], v11 offset0:16 offset1:24\n\t"
    "v_mfma_f32_16x16x32_bf16 v[116:119], v[32:35], a[44:47], v[172:175]\n\t"
    "ds_read_b128 a[76:79], v16 offset:6912\n\t"
    "ds_read2_b32 v[52:53], v12 offset0:16 offset1:24\n\t"
    "v_mfma_f32_16x16x32_bf16 v[212:215], v[32:35], a[48:51], v[176:179]\n\t"
    "ds_read_b128 a[80:83], v16 offset:9216\n\t"
    "ds_read2_b32 v[54:55], v13 offset0:16 offset1:24\n\t"
    "s_waitcnt lgkmcnt(7)\n\t"
    "v_smfmac_f32_16x16x64_bf16 v[68:71], v[36:39], a[32:39], v9\n\t"
    "ds_read_b128 a[64:67], v16 offset:0\n\t"
    "v_and_b32 v32, v5, v48\n\t"
    "v_and_b32 v33, v6, v48\n\t"
    "v_smfmac_f32_16x16x64_bf16 v[116:119], v[36:39], a[36:43], v9\n\t"
    "ds_read_b128 a[68:71], v16 offset:2304\n\t"
    "s_add_u32 m0, s78, 90112\n\t"
    "s_nop 0\n\t"
    "buffer_load_dwordx4 v96, s[36:39], s72 offen lds\n\t"
    "v_and_b32 v34, v7, v48\n\t"
    "v_smfmac_f32_16x16x64_bf16 v[212:215], v[36:39], a[40:47], v9\n\t"
    "ds_read_b128 a[84:87], v20 offset:0\n\t"
    "v_and_b32 v35, v8, v48\n\t"
    "s_waitcnt lgkmcnt(7)\n\t"
    "v_smfmac_f32_16x16x64_bf16 v[68:71], v[40:43], a[52:59], v9\n\t"
    "ds_read_b128 a[88:91], v20 offset:4608\n\t"
    "v_and_b32 v36, v1, v50\n\t"
    "v_and_b32 v37, v2, v50\n\t"
    "v_smfmac_f32_16x16x64_bf16 v[212:215], v[40:43], a[56:63], v9\n\t"
    "ds_read_b128 a[92:95], v24 offset:0\n\t"
    "s_add_u32 m0, s78, 94208\n\t"
    "s_nop 0\n\t"
    "buffer_load_dwordx4 v97, s[36:39], s72 offen lds\n\t"
    "v_and_b32 v38, v3, v50\n\t"
    "v_smfmac_f32_16x16x64_bf16 v[116:119], v[44:47], a[52:59], v9\n\t"
    "v_and_b32 v39, v4, v50\n\t"
    "s_waitcnt lgkmcnt(5)\n\t"
    "v_and_b32 v40, v1, v52\n\t"
    "v_and_b32 v41, v2, v52\n\t"
    "v_and_b32 v42, v3, v52\n\t"
    "v_and_b32 v43, v4, v52\n\t"
    "v_and_b32 v44, v1, v54\n\t"
    "v_and_b32 v45, v2, v54\n\t"
    "v_and_b32 v46, v3, v54\n\t"
    "v_and_b32 v47, v4, v54\n\t"
    "v_mfma_f32_16x16x32_bf16 v[72:75], v[32:35], a[72:75], v[168:171]\n\t"
    "ds_read_b128 a[8:11], v17 offset:4608\n\t"
    "v_mfma_f32_16x16x32_bf16 v[120:123], v[32:35], a[76:79], v[172:175]\n\t"
    "ds_read_b128 a[12:15], v17 offset:6912\n\t"
    "s_add_u32 m0, s78, 98304\n\t"
    "s_nop 0\n\t"
    "buffer_load_dwordx4 v98, s[36:39], s72 offen lds\n\t"
    "v_mfma_f32_16x16x32_bf16 v[216:219], v[32:35], a[80:83], v[176:179]\n\t"
    "ds_read_b128 a[16:19], v17 offset:9216\n\t"
    "s_waitcnt lgkmcnt(6)\n\t"
    "v_smfmac_f32_16x16x64_bf16 v[72:75], v[36:39], a[64:71], v9\n\t"
    "ds_read_b128 a[0:3], v17 offset:0\n\t"
    "v_and_b32 v32, v5, v49\n\t"
    "v_and_b32 v33, v6, v49\n\t"
    "v_smfmac_f32_16x16x64_bf16 v[120:123], v[36:39], a[68:75], v9\n\t"
    "ds_read_b128 a[4:7], v17 offset:2304\n\t"
    "s_add_u32 m0, s78, 102400\n\t"
    "s_nop 0\n\t"
    "buffer_load_dwordx4 v99, s[36:39], s72 offen lds\n\t"
    "v_and_b32 v34, v7, v49\n\t"
    "v_smfmac_f32_16x16x64_bf16 v[216:219], v[36:39], a[72:79], v9\n\t"
    "ds_read_b128 a[20:23], v21 offset:0\n\t"
    "v_and_b32 v35, v8, v49\n\t"
    "s_waitcnt lgkmcnt(6)\n\t"
    "v_smfmac_f32_16x16x64_bf16 v[72:75], v[40:43], a[84:91], v9\n\t"
    "ds_read_b128 a[24:27], v21 offset:4608\n\t"
    "ds_read2_b32 v[48:49], v10 offset0:32 offset1:40\n\t"
    "v_and_b32 v36, v1, v51\n\t"
    "v_and_b32 v37, v2, v51\n\t"
    "v_smfmac_f32_16x16x64_bf16 v[216:219], v[40:43], a[88:95], v9\n\t"
    "ds_read_b128 a[28:31], v25 offset:0\n\t"
    "s_add_u32 m0, s78, 106496\n\t"
    "s_nop 0\n\t"
    "buffer_load_dwordx4 v100, s[36:39], s72 offen lds\n\t"
    "v_and_b32 v38, v3, v51\n\t"
    "v_smfmac_f32_16x16x64_bf16 v[120:123], v[44:47], a[84:91], v9\n\t"
    "v_and_b32 v39, v4, v51\n\t"
    "v_and_b32 v40, v1, v53\n\t"
    "v_and_b32 v41, v2, v53\n\t"
    "v_and_b32 v42, v3, v53\n\t"
    "v_and_b32 v43, v4, v53\n\t"
    "v_and_b32 v44, v1, v55\n\t"
    "v_and_b32 v45, v2, v55\n\t"
    "v_and_b32 v46, v3, v55\n\t"
    "v_and_b32 v47, v4, v55\n\t"
    "s_waitcnt lgkmcnt(6)\n\t"
    "v_mfma_f32_16x16x32_bf16 v[76:79], v[32:35], a[8:11], v[168:171]\n\t"
    "ds_read2_b32 v[50:51], v11 offset0:32 offset1:40\n\t"
    "v_mfma_f32_16x16x32_bf16 v[124:127], v[32:35], a[12:15], v[172:175]\n\t"
    "ds_read2_b32 v[52:53], v12 offset0:32 offset1:40\n\t"
    "v_mfma_f32_16x16x32_bf16 v[220:223], v[32:35], a[16:19], v[176:179]\n\t"
    "ds_read2_b32 v[54:55], v13 offset0:32 offset1:40\n\t"
    "s_waitcnt lgkmcnt(4)\n\t"
    "v_smfmac_f32_16x16x64_bf16 v[76:79], v[36:39], a[0:7], v9\n\t"
    "v_and_b32 v32, v5, v48\n\t"
    "v_and_b32 v33, v6, v48\n\t"
    "v_smfmac_f32_16x16x64_bf16 v[124:127], v[36:39], a[4:11], v9\n\t"
    "s_add_u32 m0, s78, 110592\n\t"
    "s_nop 0\n\t"
    "buffer_load_dwordx4 v101, s[36:39], s72 offen lds\n\t"
    "v_and_b32 v34, v7, v48\n\t"
    "v_smfmac_f32_16x16x64_bf16 v[220:223], v[36:39], a[8:15], v9\n\t"
    "v_and_b32 v35, v8, v48\n\t"
    "s_waitcnt lgkmcnt(2)\n\t"
    "v_smfmac_f32_16x16x64_bf16 v[76:79], v[40:43], a[20:27], v9\n\t"
    "v_and_b32 v36, v1, v50\n\t"
    "v_and_b32 v37, v2, v50\n\t"
    "v_smfmac_f32_16x16x64_bf16 v[220:223], v[40:43], a[24:31], v9\n\t"
    "s_add_u32 m0, s78, 114688\n\t"
    "s_nop 0\n\t"
    "buffer_load_dwordx4 v102, s[36:39], s72 offen lds\n\t"
    "v_and_b32 v38, v3, v50\n\t"
    "v_smfmac_f32_16x16x64_bf16 v[124:127], v[44:47], a[20:27], v9\n\t"
    "v_and_b32 v39, v4, v50\n\t"
    "s_waitcnt lgkmcnt(0)\n\t"
    "v_and_b32 v40, v1, v52\n\t"
    "v_and_b32 v41, v2, v52\n\t"
    "v_and_b32 v42, v3, v52\n\t"
    "v_and_b32 v43, v4, v52\n\t"
    "v_and_b32 v44, v1, v54\n\t"
    "v_and_b32 v45, v2, v54\n\t"
    "v_and_b32 v46, v3, v54\n\t"
    "v_and_b32 v47, v4, v54\n\t"
    "v_pk_mul_f32 v[190:191], v[64:65], v[64:65]\n\t"
    "v_pk_mul_f32 v[194:195], v[112:113], v[112:113]\n\t"
    "v_pk_mul_f32 v[198:199], v[208:209], v[208:209]\n\t"
    "v_pk_fma_f32 v[190:191], v[66:67], v[66:67], v[190:191]\n\t"
    "v_pk_fma_f32 v[194:195], v[114:115], v[114:115], v[194:195]\n\t"
    "v_pk_fma_f32 v[198:199], v[210:211], v[210:211], v[198:199]\n\t"
    "v_pk_add_f32 v[188:189], v[64:65], v[66:67]\n\t"
    "v_pk_add_f32 v[192:193], v[112:113], v[114:115]\n\t"
    "v_pk_add_f32 v[196:197], v[208:209], v[210:211]\n\t"
    "v_pk_fma_f32 v[190:191], v[68:69], v[68:69], v[190:191]\n\t"
    "v_pk_fma_f32 v[194:195], v[116:117], v[116:117], v[194:195]\n\t"
    "v_pk_fma_f32 v[198:199], v[212:213], v[212:213], v[198:199]\n\t"
    "v_pk_add_f32 v[188:189], v[188:189], v[68:69]\n\t"
    "v_pk_add_f32 v[192:193], v[192:193], v[116:117]\n\t"
    "v_pk_add_f32 v[196:197], v[196:197], v[212:213]\n\t"
    "v_pk_fma_f32 v[190:191], v[70:71], v[70:71], v[190:191]\n\t"
    "v_pk_fma_f32 v[194:195], v[118:119], v[118:119], v[194:195]\n\t"
    "v_pk_fma_f32 v[198:199], v[214:215], v[214:215], v[198:199]\n\t"
    "v_pk_add_f32 v[188:189], v[188:189], v[70:71]\n\t"
    "v_pk_add_f32 v[192:193], v[192:193], v[118:119]\n\t"
    "v_pk_add_f32 v[196:197], v[196:197], v[214:215]\n\t"
    "v_pk_fma_f32 v[190:191], v[72:73], v[72:73], v[190:191]\n\t"
    "v_pk_fma_f32 v[194:195], v[120:121], v[120:121], v[194:195]\n\t"
    "v_pk_fma_f32 v[198:199], v[216:217], v[216:217], v[198:199]\n\t"
    "v_pk_add_f32 v[188:189], v[188:189], v[72:73]\n\t"
    "v_pk_add_f32 v[192:193], v[192:193], v[120:121]\n\t"
    "v_pk_add_f32 v[196:197], v[196:197], v[216:217]\n\t"
    "v_pk_fma_f32 v[190:191], v[74:75], v[74:75], v[190:191]\n\t"
    "v_pk_fma_f32 v[194:195], v[122:123], v[122:123], v[194:195]\n\t"
    "v_pk_fma_f32 v[198:199], v[218:219], v[218:219], v[198:199]\n\t"
    "v_pk_add_f32 v[188:189], v[188:189], v[74:75]\n\t"
    "v_pk_add_f32 v[192:193], v[192:193], v[122:123]\n\t"
    "v_pk_add_f32 v[196:197], v[196:197], v[218:219]\n\t"
    "v_pk_fma_f32 v[190:191], v[76:77], v[76:77], v[190:191]\n\t"
    "v_pk_fma_f32 v[194:195], v[124:125], v[124:125], v[194:195]\n\t"
    "v_pk_fma_f32 v[198:199], v[220:221], v[220:221], v[198:199]\n\t"
    "v_pk_add_f32 v[188:189], v[188:189], v[76:77]\n\t"
    "v_pk_add_f32 v[192:193], v[192:193], v[124:125]\n\t"
    "v_pk_add_f32 v[196:197], v[196:197], v[220:221]\n\t"
    "s_waitcnt vmcnt(7)\n\t"
    "s_barrier\n\t"
    "ds_read_b128 a[40:43], v14 offset:37376\n\t"
    "ds_read_b128 a[44:47], v14 offset:39680\n\t"
    "ds_read_b128 a[48:51], v14 offset:41984\n\t"
    "ds_read_b128 a[32:35], v14 offset:32768\n\t"
    "ds_read_b128 a[36:39], v14 offset:35072\n\t"
    "ds_read_b128 a[52:55], v18 offset:32768\n\t"
    "ds_read_b128 a[56:59], v18 offset:37376\n\t"
    "ds_read_b128 a[60:63], v22 offset:32768\n\t"
    "v_pk_fma_f32 v[190:191], v[78:79], v[78:79], v[190:191]\n\t"
    "v_pk_fma_f32 v[194:195], v[126:127], v[126:127], v[194:195]\n\t"
    "v_pk_fma_f32 v[198:199], v[222:223], v[222:223], v[198:199]\n\t"
    "v_pk_add_f32 v[188:189], v[188:189], v[78:79]\n\t"
    "v_pk_add_f32 v[192:193], v[192:193], v[126:127]\n\t"
    "v_pk_add_f32 v[196:197], v[196:197], v[222:223]\n\t"
    "s_waitcnt lgkmcnt(5)\n\t"
    "v_mfma_f32_16x16x32_bf16 v[80:83], v[32:35], a[40:43], v[168:171]\n\t"
    "ds_read_b128 a[72:75], v15 offset:37376\n\t"
    "v_mfma_f32_16x16x32_bf16 v[128:131], v[32:35], a[44:47], v[172:175]\n\t"
    "ds_read_b128 a[76:79], v15 offset:39680\n\t"
    "s_add_u32 m0, s78, 118784\n\t"
    "s_nop 0\n\t"
    "buffer_load_dwordx4 v103, s[36:39], s72 offen lds\n\t"
    "v_mfma_f32_16x16x32_bf16 v[224:227], v[32:35], a[48:51], v[176:179]\n\t"
    "ds_read_b128 a[80:83], v15 offset:41984\n\t"
    "s_waitcnt lgkmcnt(6)\n\t"
    "v_smfmac_f32_16x16x64_bf16 v[80:83], v[36:39], a[32:39], v9\n\t"
    "ds_read_b128 a[64:67], v15 offset:32768\n\t"
    "v_and_b32 v32, v5, v49\n\t"
    "v_and_b32 v33, v6, v49\n\t"
    "v_smfmac_f32_16x16x64_bf16 v[128:131], v[36:39], a[36:43], v9\n\t"
    "ds_read_b128 a[68:71], v15 offset:35072\n\t"
    "s_add_u32 m0, s67, 0\n\t"
    "s_add_u32 s63, s65, 0\n\t"
    "buffer_load_dwordx4 v26, s[40:43], s63 offen offset:0 lds\n\t"
    "v_and_b32 v34, v7, v49\n\t"
    "v_smfmac_f32_16x16x64_bf16 v[224:227], v[36:39], a[40:47], v9\n\t"
    "ds_read_b128 a[84:87], v19 offset:32768\n\t"
    "v_and_b32 v35, v8, v49\n\t"
    "s_waitcnt lgkmcnt(6)\n\t"
    "v_smfmac_f32_16x16x64_bf16 v[80:83], v[40:43], a[52:59], v9\n\t"
    "ds_read_b128 a[88:91], v19 offset:37376\n\t"
    "ds_read2_b32 v[48:49], v10 offset0:48 offset1:56\n\t"
    "v_and_b32 v36, v1, v51\n\t"
    "v_and_b32 v37, v2, v51\n\t"
    "v_smfmac_f32_16x16x64_bf16 v[224:227], v[40:43], a[56:63], v9\n\t"
    "ds_read_b128 a[92:95], v23 offset:32768\n\t"
    "buffer_load_dwordx4 v27, s[40:43], s63 offen offset:1024 lds\n\t"
    "v_and_b32 v38, v3, v51\n\t"
    "v_smfmac_f32_16x16x64_bf16 v[128:131], v[44:47], a[52:59], v9\n\t"
    "v_and_b32 v39, v4, v51\n\t"
    "v_and_b32 v40, v1, v53\n\t"
    "v_and_b32 v41, v2, v53\n\t"
    "v_and_b32 v42, v3, v53\n\t"
    "v_and_b32 v43, v4, v53\n\t"
    "v_and_b32 v44, v1, v55\n\t"
    "v_and_b32 v45, v2, v55\n\t"
    "v_and_b32 v46, v3, v55\n\t"
    "v_and_b32 v47, v4, v55\n\t"
    "s_waitcnt lgkmcnt(6)\n\t"
    "v_mfma_f32_16x16x32_bf16 v[84:87], v[32:35], a[72:75], v[168:171]\n\t"
    "ds_read_b128 a[8:11], v16 offset:37376\n\t"
    "ds_read2_b32 v[50:51], v11 offset0:48 offset1:56\n\t"
    "v_mfma_f32_16x16x32_bf16 v[132:135], v[32:35], a[76:79], v[172:175]\n\t"
    "ds_read_b128 a[12:15], v16 offset:39680\n\t"
    "ds_read2_b32 v[52:53], v12 offset0:48 offset1:56\n\t"
    "v_mfma_f32_16x16x32_bf16 v[228:231], v[32:35], a[80:83], v[176:179]\n\t"
    "ds_read_b128 a[16:19], v16 offset:41984\n\t"
    "ds_read2_b32 v[54:55], v13 offset0:48 offset1:56\n\t"
    "s_waitcnt lgkmcnt(7)\n\t"
    "v_smfmac_f32_16x16x64_bf16 v[84:87], v[36:39], a[64:71], v9\n\t"
    "ds_read_b128 a[0:3], v16 offset:32768\n\t"
    "v_and_b32 v32, v5, v48\n\t"
    "v_and_b32 v33, v6, v48\n\t"
    "v_smfmac_f32_16x16x64_bf16 v[132:135], v[36:39], a[68:75], v9\n\t"
    "ds_read_b128 a[4:7], v16 offset:35072\n\t"
    "buffer_load_dwordx4 v28, s[40:43], s63 offen offset:2048 lds\n\t"
    "v_and_b32 v34, v7, v48\n\t"
    "v_smfmac_f32_16x16x64_bf16 v[228:231], v[36:39], a[72:79], v9\n\t"
    "ds_read_b128 a[20:23], v20 offset:32768\n\t"
    "v_and_b32 v35, v8, v48\n\t"
    "s_waitcnt lgkmcnt(7)\n\t"
    "v_smfmac_f32_16x16x64_bf16 v[84:87], v[40:43], a[84:91], v9\n\t"
    "ds_read_b128 a[24:27], v20 offset:37376\n\t"
    "v_and_b32 v36, v1, v50\n\t"
    "v_and_b32 v37, v2, v50\n\t"
    "v_smfmac_f32_16x16x64_bf16 v[228:231], v[40:43], a[88:95], v9\n\t"
    "ds_read_b128 a[28:31], v24 offset:32768\n\t"
    "s_add_u32 m0, s67, 12288\n\t"
    "s_add_u32 s63, s65, 147456\n\t"
    "buffer_load_dwordx4 v26, s[40:43], s63 offen offset:0 lds\n\t"
    "v_and_b32 v38, v3, v50\n\t"
    "v_smfmac_f32_16x16x64_bf16 v[132:135], v[44:47], a[84:91], v9\n\t"
    "v_and_b32 v39, v4, v50\n\t"
    "s_waitcnt lgkmcnt(5)\n\t"
    "v_and_b32 v40, v1, v52\n\t"
    "v_and_b32 v41, v2, v52\n\t"
    "v_and_b32 v42, v3, v52\n\t"
    "v_and_b32 v43, v4, v52\n\t"
    "v_and_b32 v44, v1, v54\n\t"
    "v_and_b32 v45, v2, v54\n\t"
    "v_and_b32 v46, v3, v54\n\t"
    "v_and_b32 v47, v4, v54\n\t"
    "v_mfma_f32_16x16x32_bf16 v[88:91], v[32:35], a[8:11], v[168:171]\n\t"
    "ds_read_b128 a[40:43], v17 offset:37376\n\t"
    "v_mfma_f32_16x16x32_bf16 v[136:139], v[32:35], a[12:15], v[172:175]\n\t"
    "ds_read_b128 a[44:47], v17 offset:39680\n\t"
    "buffer_load_dwordx4 v27, s[40:43], s63 offen offset:1024 lds\n\t"
    "v_mfma_f32_16x16x32_bf16 v[232:235], v[32:35], a[16:19], v[176:179]\n\t"
    "ds_read_b128 a[48:51], v17 offset:41984\n\t"
    "s_waitcnt lgkmcnt(6)\n\t"
    "v_smfmac_f32_16x16x64_bf16 v[88:91], v[36:39], a[0:7], v9\n\t"
    "ds_read_b128 a[32:35], v17 offset:32768\n\t"
    "v_and_b32 v32, v5, v49\n\t"
    "v_and_b32 v33, v6, v49\n\t"
    "v_smfmac_f32_16x16x64_bf16 v[136:139], v[36:39], a[4:11], v9\n\t"
    "ds_read_b128 a[36:39], v17 offset:35072\n\t"
    "buffer_load_dwordx4 v28, s[40:43], s63 offen offset:2048 lds\n\t"
    "s_add_u32 s65, s65, 128\n\t"
    "v_and_b32 v34, v7, v49\n\t"
    "v_smfmac_f32_16x16x64_bf16 v[232:235], v[36:39], a[8:15], v9\n\t"
    "ds_read_b128 a[52:55], v21 offset:32768\n\t"
    "v_and_b32 v35, v8, v49\n\t"
    "s_waitcnt lgkmcnt(6)\n\t"
    "v_smfmac_f32_16x16x64_bf16 v[88:91], v[40:43], a[20:27], v9\n\t"
    "ds_read_b128 a[56:59], v21 offset:37376\n\t"
    "ds_read2_b32 v[48:49], v10 offset0:64 offset1:72\n\t"
    "v_and_b32 v36, v1, v51\n\t"
    "v_and_b32 v37, v2, v51\n\t"
    "v_smfmac_f32_16x16x64_bf16 v[232:235], v[40:43], a[24:31], v9\n\t"
    "ds_read_b128 a[60:63], v25 offset:32768\n\t"
    "v_and_b32 v38, v3, v51\n\t"
    "v_smfmac_f32_16x16x64_bf16 v[136:139], v[44:47], a[20:27], v9\n\t"
    "v_and_b32 v39, v4, v51\n\t"
    "v_and_b32 v40, v1, v53\n\t"
    "v_and_b32 v41, v2, v53\n\t"
    "v_and_b32 v42, v3, v53\n\t"
    "v_and_b32 v43, v4, v53\n\t"
    "v_and_b32 v44, v1, v55\n\t"
    "v_and_b32 v45, v2, v55\n\t"
    "v_and_b32 v46, v3, v55\n\t"
    "v_and_b32 v47, v4, v55\n\t"
    "s_waitcnt lgkmcnt(6)\n\t"
    "v_mfma_f32_16x16x32_bf16 v[92:95], v[32:35], a[40:43], v[168:171]\n\t"
    "ds_read2_b32 v[50:51], v11 offset0:64 offset1:72\n\t"
    "v_mfma_f32_16x16x32_bf16 v[140:143], v[32:35], a[44:47], v[172:175]\n\t"
    "ds_read2_b32 v[52:53], v12 offset0:64 offset1:72\n\t"
    "v_mfma_f32_16x16x32_bf16 v[236:239], v[32:35], a[48:51], v[176:179]\n\t"
    "ds_read2_b32 v[54:55], v13 offset0:64 offset1:72\n\t"
    "s_waitcnt lgkmcnt(4)\n\t"
    "v_smfmac_f32_16x16x64_bf16 v[92:95], v[36:39], a[32:39], v9\n\t"
    "v_and_b32 v32, v5, v48\n\t"
    "v_and_b32 v33, v6, v48\n\t"
    "v_smfmac_f32_16x16x64_bf16 v[140:143], v[36:39], a[36:43], v9\n\t"
    "v_and_b32 v34, v7, v48\n\t"
    "v_smfmac_f32_16x16x64_bf16 v[236:239], v[36:39], a[40:47], v9\n\t"
    "v_and_b32 v35, v8, v48\n\t"
    "s_waitcnt lgkmcnt(2)\n\t"
    "v_smfmac_f32_16x16x64_bf16 v[92:95], v[40:43], a[52:59], v9\n\t"
    "v_and_b32 v36, v1, v50\n\t"
    "v_and_b32 v37, v2, v50\n\t"
    "v_smfmac_f32_16x16x64_bf16 v[236:239], v[40:43], a[56:63], v9\n\t"
    "v_and_b32 v38, v3, v50\n\t"
    "v_smfmac_f32_16x16x64_bf16 v[140:143], v[44:47], a[52:59], v9\n\t"
    "v_and_b32 v39, v4, v50\n\t"
    "s_waitcnt lgkmcnt(0)\n\t"
    "v_and_b32 v40, v1, v52\n\t"
    "v_and_b32 v41, v2, v52\n\t"
    "v_and_b32 v42, v3, v52\n\t"
    "v_and_b32 v43, v4, v52\n\t"
    "v_and_b32 v44, v1, v54\n\t"
    "v_and_b32 v45, v2, v54\n\t"
    "v_and_b32 v46, v3, v54\n\t"
    "v_and_b32 v47, v4, v54\n\t"
    "v_pk_fma_f32 v[190:191], v[80:81], v[80:81], v[190:191]\n\t"
    "v_pk_fma_f32 v[194:195], v[128:129], v[128:129], v[194:195]\n\t"
    "v_pk_fma_f32 v[198:199], v[224:225], v[224:225], v[198:199]\n\t"
    "v_pk_add_f32 v[188:189], v[188:189], v[80:81]\n\t"
    "v_pk_add_f32 v[192:193], v[192:193], v[128:129]\n\t"
    "v_pk_add_f32 v[196:197], v[196:197], v[224:225]\n\t"
    "v_pk_fma_f32 v[190:191], v[82:83], v[82:83], v[190:191]\n\t"
    "v_pk_fma_f32 v[194:195], v[130:131], v[130:131], v[194:195]\n\t"
    "v_pk_fma_f32 v[198:199], v[226:227], v[226:227], v[198:199]\n\t"
    "v_pk_add_f32 v[188:189], v[188:189], v[82:83]\n\t"
    "v_pk_add_f32 v[192:193], v[192:193], v[130:131]\n\t"
    "v_pk_add_f32 v[196:197], v[196:197], v[226:227]\n\t"
    "v_pk_fma_f32 v[190:191], v[84:85], v[84:85], v[190:191]\n\t"
    "v_pk_fma_f32 v[194:195], v[132:133], v[132:133], v[194:195]\n\t"
    "v_pk_fma_f32 v[198:199], v[228:229], v[228:229], v[198:199]\n\t"
    "v_pk_add_f32 v[188:189], v[188:189], v[84:85]\n\t"
    "v_pk_add_f32 v[192:193], v[192:193], v[132:133]\n\t"
    "v_pk_add_f32 v[196:197], v[196:197], v[228:229]\n\t"
    "v_pk_fma_f32 v[190:191], v[86:87], v[86:87], v[190:191]\n\t"
    "v_pk_fma_f32 v[194:195], v[134:135], v[134:135], v[194:195]\n\t"
    "v_pk_fma_f32 v[198:199], v[230:231], v[230:231], v[198:199]\n\t"
    "v_pk_add_f32 v[188:189], v[188:189], v[86:87]\n\t"
    "v_pk_add_f32 v[192:193], v[192:193], v[134:135]\n\t"
    "v_pk_add_f32 v[196:197], v[196:197], v[230:231]\n\t"
    "v_pk_fma_f32 v[190:191], v[88:89], v[88:89], v[190:191]\n\t"
    "v_pk_fma_f32 v[194:195], v[136:137], v[136:137], v[194:195]\n\t"
    "v_pk_fma_f32 v[198:199], v[232:233], v[232:233], v[198:199]\n\t"
    "v_pk_add_f32 v[188:189], v[188:189], v[88:89]\n\t"
    "v_pk_add_f32 v[192:193], v[192:193], v[136:137]\n\t"
    "v_pk_add_f32 v[196:197], v[196:197], v[232:233]\n\t"
    "v_pk_fma_f32 v[190:191], v[90:91], v[90:91], v[190:191]\n\t"
    "v_pk_fma_f32 v[194:195], v[138:139], v[138:139], v[194:195]\n\t"
    "v_pk_fma_f32 v[198:199], v[234:235], v[234:235], v[198:199]\n\t"
    "v_pk_add_f32 v[188:189], v[188:189], v[90:91]\n\t"
    "v_pk_add_f32 v[192:193], v[192:193], v[138:139]\n\t"
    "v_pk_add_f32 v[196:197], v[196:197], v[234:235]\n\t"
    "v_pk_fma_f32 v[190:191], v[92:93], v[92:93], v[190:191]\n\t"
    "v_pk_fma_f32 v[194:195], v[140:141], v[140:141], v[194:195]\n\t"
    "v_pk_fma_f32 v[198:199], v[236:237], v[236:237], v[198:199]\n\t"
    "v_pk_add_f32 v[188:189], v[188:189], v[92:93]\n\t"
    "v_pk_add_f32 v[192:193], v[192:193], v[140:141]\n\t"
    "v_pk_add_f32 v[196:197], v[196:197], v[236:237]\n\t"
    "v_add_u32 v14, 65536, v14\n\t"
    "v_add_u32 v15, 65536, v15\n\t"
    "v_add_u32 v16, 65536, v16\n\t"
    "v_add_u32 v17, 65536, v17\n\t"
    "v_add_u32 v18, 65536, v18\n\t"
    "v_add_u32 v19, 65536, v19\n\t"
    "v_add_u32 v20, 65536, v20\n\t"
    "v_add_u32 v21, 65536, v21\n\t"
    "v_add_u32 v22, 65536, v22\n\t"
    "v_add_u32 v23, 65536, v23\n\t"
    "v_add_u32 v24, 65536, v24\n\t"
    "v_add_u32 v25, 65536, v25\n\t"
    "s_waitcnt vmcnt(6)\n\t"
    "s_barrier\n\t"
    "ds_read_b128 a[72:75], v14 offset:4608\n\t"
    "ds_read_b128 a[76:79], v14 offset:6912\n\t"
    "ds_read_b128 a[80:83], v14 offset:9216\n\t"
    "ds_read_b128 a[64:67], v14 offset:0\n\t"
    "ds_read_b128 a[68:71], v14 offset:2304\n\t"
    "ds_read_b128 a[84:87], v18 offset:0\n\t"
    "ds_read_b128 a[88:91], v18 offset:4608\n\t"
    "ds_read_b128 a[92:95], v22 offset:0\n\t"
    "v_pk_fma_f32 v[190:191], v[94:95], v[94:95], v[190:191]\n\t"
    "v_pk_fma_f32 v[194:195], v[142:143], v[142:143], v[194:195]\n\t"
    "v_pk_fma_f32 v[198:199], v[238:239], v[238:239], v[198:199]\n\t"
    "v_pk_add_f32 v[188:189], v[188:189], v[94:95]\n\t"
    "v_pk_add_f32 v[192:193], v[192:193], v[142:143]\n\t"
    "v_pk_add_f32 v[196:197], v[196:197], v[238:239]\n\t"
    "s_waitcnt lgkmcnt(5)\n\t"
    "v_mfma_f32_16x16x32_bf16 v[96:99], v[32:35], a[72:75], v[168:171]\n\t"
    "ds_read_b128 a[8:11], v15 offset:4608\n\t"
    "v_mfma_f32_16x16x32_bf16 v[144:147], v[32:35], a[76:79], v[172:175]\n\t"
    "ds_read_b128 a[12:15], v15 offset:6912\n\t"
    "v_mfma_f32_16x16x32_bf16 v[240:243], v[32:35], a[80:83], v[176:179]\n\t"
    "ds_read_b128 a[16:19], v15 offset:9216\n\t"
    "s_waitcnt lgkmcnt(6)\n\t"
    "v_smfmac_f32_16x16x64_bf16 v[96:99], v[36:39], a[64:71], v9\n\t"
    "ds_read_b128 a[0:3], v15 offset:0\n\t"
    "v_and_b32 v32, v5, v49\n\t"
    "v_and_b32 v33, v6, v49\n\t"
    "v_smfmac_f32_16x16x64_bf16 v[144:147], v[36:39], a[68:75], v9\n\t"
    "ds_read_b128 a[4:7], v15 offset:2304\n\t"
    "v_and_b32 v34, v7, v49\n\t"
    "v_smfmac_f32_16x16x64_bf16 v[240:243], v[36:39], a[72:79], v9\n\t"
    "ds_read_b128 a[20:23], v19 offset:0\n\t"
    "v_and_b32 v35, v8, v49\n\t"
    "s_waitcnt lgkmcnt(6)\n\t"
    "v_smfmac_f32_16x16x64_bf16 v[96:99], v[40:43], a[84:91], v9\n\t"
    "ds_read_b128 a[24:27], v19 offset:4608\n\t"
    "ds_read2_b32 v[48:49], v10 offset0:80 offset1:88\n\t"
    "v_and_b32 v36, v1, v51\n\t"
    "v_and_b32 v37, v2, v51\n\t"
    "v_smfmac_f32_16x16x64_bf16 v[240:243], v[40:43], a[88:95], v9\n\t"
    "ds_read_b128 a[28:31], v23 offset:0\n\t"
    "v_and_b32 v38, v3, v51\n\t"
    "v_smfmac_f32_16x16x64_bf16 v[144:147], v[44:47], a[84:91], v9\n\t"
    "v_and_b32 v39, v4, v51\n\t"
    "v_and_b32 v40, v1, v53\n\t"
    "v_and_b32 v41, v2, v53\n\t"
    "v_and_b32 v42, v3, v53\n\t"
    "v_and_b32 v43, v4, v53\n\t"
    "v_and_b32 v44, v1, v55\n\t"
    "v_and_b32 v45, v2, v55\n\t"
    "v_and_b32 v46, v3, v55\n\t"
    "v_and_b32 v47, v4, v55\n\t"
    "s_waitcnt lgkmcnt(6)\n\t"
    "v_mfma_f32_16x16x32_bf16 v[100:103], v[32:35], a[8:11], v[168:171]\n\t"
    "ds_read_b128 a[40:43], v16 offset:4608\n\t"
    "ds_read2_b32 v[50:51], v11 offset0:80 offset1:88\n\t"
    "v_mfma_f32_16x16x32_bf16 v[148:151], v[32:35], a[12:15], v[172:175]\n\t"
    "ds_read_b128 a[44:47], v16 offset:6912\n\t"
    "ds_read2_b32 v[52:53], v12 offset0:80 offset1:88\n\t"
    "v_mfma_f32_16x16x32_bf16 v[244:247], v[32:35], a[16:19], v[176:179]\n\t"
    "ds_read_b128 a[48:51], v16 offset:9216\n\t"
    "ds_read2_b32 v[54:55], v13 offset0:80 offset1:88\n\t"
    "s_waitcnt lgkmcnt(7)\n\t"
    "v_smfmac_f32_16x16x64_bf16 v[100:103], v[36:39], a[0:7], v9\n\t"
    "ds_read_b128 a[32:35], v16 offset:0\n\t"
    "v_and_b32 v32, v5, v48\n\t"
    "v_and_b32 v33, v6, v48\n\t"
    "v_smfmac_f32_16x16x64_bf16 v[148:151], v[36:39], a[4:11], v9\n\t"
    "ds_read_b128 a[36:39], v16 offset:2304\n\t"
    "v_and_b32 v34, v7, v48\n\t"
    "v_smfmac_f32_16x16x64_bf16 v[244:247], v[36:39], a[8:15], v9\n\t"
    "ds_read_b128 a[52:55], v20 offset:0\n\t"
    "v_and_b32 v35, v8, v48\n\t"
    "s_waitcnt lgkmcnt(7)\n\t"
    "v_smfmac_f32_16x16x64_bf16 v[100:103], v[40:43], a[20:27], v9\n\t"
    "ds_read_b128 a[56:59], v20 offset:4608\n\t"
    "v_and_b32 v36, v1, v50\n\t"
    "v_and_b32 v37, v2, v50\n\t"
    "v_smfmac_f32_16x16x64_bf16 v[244:247], v[40:43], a[24:31], v9\n\t"
    "ds_read_b128 a[60:63], v24 offset:0\n\t"
    "v_and_b32 v38, v3, v50\n\t"
    "v_smfmac_f32_16x16x64_bf16 v[148:151], v[44:47], a[20:27], v9\n\t"
    "v_and_b32 v39, v4, v50\n\t"
    "s_waitcnt lgkmcnt(5)\n\t"
    "v_and_b32 v40, v1, v52\n\t"
    "v_and_b32 v41, v2, v52\n\t"
    "v_and_b32 v42, v3, v52\n\t"
    "v_and_b32 v43, v4, v52\n\t"
    "v_and_b32 v44, v1, v54\n\t"
    "v_and_b32 v45, v2, v54\n\t"
    "v_and_b32 v46, v3, v54\n\t"
    "v_and_b32 v47, v4, v54\n\t"
    "v_mfma_f32_16x16x32_bf16 v[104:107], v[32:35], a[40:43], v[168:171]\n\t"
    "ds_read_b128 a[72:75], v17 offset:4608\n\t"
    "v_mfma_f32_16x16x32_bf16 v[152:155], v[32:35], a[44:47], v[172:175]\n\t"
    "ds_read_b128 a[76:79], v17 offset:6912\n\t"
    "v_mfma_f32_16x16x32_bf16 v[248:251], v[32:35], a[48:51], v[176:179]\n\t"
    "ds_read_b128 a[80:83], v17 offset:9216\n\t"
    "s_waitcnt lgkmcnt(6)\n\t"
    "v_smfmac_f32_16x16x64_bf16 v[104:107], v[36:39], a[32:39], v9\n\t"
    "ds_read_b128 a[64:67], v17 offset:0\n\t"
    "v_and_b32 v32, v5, v49\n\t"
    "v_and_b32 v33, v6, v49\n\t"
    "v_smfmac_f32_16x16x64_bf16 v[152:155], v[36:39], a[36:43], v9\n\t"
    "ds_read_b128 a[68:71], v17 offset:2304\n\t"
    "v_and_b32 v34, v7, v49\n\t"
    "v_smfmac_f32_16x16x64_bf16 v[248:251], v[36:39], a[40:47], v9\n\t"
    "ds_read_b128 a[84:87], v21 offset:0\n\t"
    "v_and_b32 v35, v8, v49\n\t"
    "s_waitcnt lgkmcnt(6)\n\t"
    "v_smfmac_f32_16x16x64_bf16 v[104:107], v[40:43], a[52:59], v9\n\t"
    "ds_read_b128 a[88:91], v21 offset:4608\n\t"
    "v_and_b32 v36, v1, v51\n\t"
    "v_and_b32 v37, v2, v51\n\t"
    "v_smfmac_f32_16x16x64_bf16 v[248:251], v[40:43], a[56:63], v9\n\t"
    "ds_read_b128 a[92:95], v25 offset:0\n\t"
    "v_and_b32 v38, v3, v51\n\t"
    "v_smfmac_f32_16x16x64_bf16 v[152:155], v[44:47], a[52:59], v9\n\t"
    "v_and_b32 v39, v4, v51\n\t"
    "v_and_b32 v40, v1, v53\n\t"
    "v_and_b32 v41, v2, v53\n\t"
    "v_and_b32 v42, v3, v53\n\t"
    "v_and_b32 v43, v4, v53\n\t"
    "v_and_b32 v44, v1, v55\n\t"
    "v_and_b32 v45, v2, v55\n\t"
    "v_and_b32 v46, v3, v55\n\t"
    "v_and_b32 v47, v4, v55\n\t"
    "s_waitcnt lgkmcnt(5)\n\t"
    "v_mfma_f32_16x16x32_bf16 v[108:111], v[32:35], a[72:75], v[168:171]\n\t"
    "v_mfma_f32_16x16x32_bf16 v[156:159], v[32:35], a[76:79], v[172:175]\n\t"
    "v_mfma_f32_16x16x32_bf16 v[252:255], v[32:35], a[80:83], v[176:179]\n\t"
    "s_waitcnt lgkmcnt(3)\n\t"
    "v_smfmac_f32_16x16x64_bf16 v[108:111], v[36:39], a[64:71], v9\n\t"
    "v_smfmac_f32_16x16x64_bf16 v[156:159], v[36:39], a[68:75], v9\n\t"
    "v_smfmac_f32_16x16x64_bf16 v[252:255], v[36:39], a[72:79], v9\n\t"
    "s_waitcnt lgkmcnt(0)\n\t"
    "v_smfmac_f32_16x16x64_bf16 v[108:111], v[40:43], a[84:91], v9\n\t"
    "v_smfmac_f32_16x16x64_bf16 v[252:255], v[40:43], a[88:95], v9\n\t"
    "v_smfmac_f32_16x16x64_bf16 v[156:159], v[44:47], a[84:91], v9\n\t"
    "v_pk_fma_f32 v[190:191], v[96:97], v[96:97], v[190:191]\n\t"
    "v_pk_fma_f32 v[194:195], v[144:145], v[144:145], v[194:195]\n\t"
    "v_pk_fma_f32 v[198:199], v[240:241], v[240:241], v[198:199]\n\t"
    "v_pk_add_f32 v[188:189], v[188:189], v[96:97]\n\t"
    "v_pk_add_f32 v[192:193], v[192:193], v[144:145]\n\t"
    "v_pk_add_f32 v[196:197], v[196:197], v[240:241]\n\t"
    "v_pk_fma_f32 v[190:191], v[98:99], v[98:99], v[190:191]\n\t"
    "v_pk_fma_f32 v[194:195], v[146:147], v[146:147], v[194:195]\n\t"
    "v_pk_fma_f32 v[198:199], v[242:243], v[242:243], v[198:199]\n\t"
    "v_pk_add_f32 v[188:189], v[188:189], v[98:99]\n\t"
    "v_pk_add_f32 v[192:193], v[192:193], v[146:147]\n\t"
    "v_pk_add_f32 v[196:197], v[196:197], v[242:243]\n\t"
    "v_pk_fma_f32 v[190:191], v[100:101], v[100:101], v[190:191]\n\t"
    "v_pk_fma_f32 v[194:195], v[148:149], v[148:149], v[194:195]\n\t"
    "v_pk_fma_f32 v[198:199], v[244:245], v[244:245], v[198:199]\n\t"
    "v_pk_add_f32 v[188:189], v[188:189], v[100:101]\n\t"
    "v_pk_add_f32 v[192:193], v[192:193], v[148:149]\n\t"
    "v_pk_add_f32 v[196:197], v[196:197], v[244:245]\n\t"
    "v_pk_fma_f32 v[190:191], v[102:103], v[102:103], v[190:191]\n\t"
    "v_pk_fma_f32 v[194:195], v[150:151], v[150:151], v[194:195]\n\t"
    "v_pk_fma_f32 v[198:199], v[246:247], v[246:247], v[198:199]\n\t"
    "v_pk_add_f32 v[188:189], v[188:189], v[102:103]\n\t"
    "v_pk_add_f32 v[192:193], v[192:193], v[150:151]\n\t"
    "v_pk_add_f32 v[196:197], v[196:197], v[246:247]\n\t"
    "v_pk_fma_f32 v[190:191], v[104:105], v[104:105], v[190:191]\n\t"
    "v_pk_fma_f32 v[194:195], v[152:153], v[152:153], v[194:195]\n\t"
    "v_pk_fma_f32 v[198:199], v[248:249], v[248:249], v[198:199]\n\t"
    "v_pk_add_f32 v[188:189], v[188:189], v[104:105]\n\t"
    "v_pk_add_f32 v[192:193], v[192:193], v[152:153]\n\t"
    "v_pk_add_f32 v[196:197], v[196:197], v[248:249]\n\t"
    "v_pk_fma_f32 v[190:191], v[106:107], v[106:107], v[190:191]\n\t"
    "v_pk_fma_f32 v[194:195], v[154:155], v[154:155], v[194:195]\n\t"
    "v_pk_fma_f32 v[198:199], v[250:251], v[250:251], v[198:199]\n\t"
    "v_pk_add_f32 v[188:189], v[188:189], v[106:107]\n\t"
    "v_pk_add_f32 v[192:193], v[192:193], v[154:155]\n\t"
    "v_pk_add_f32 v[196:197], v[196:197], v[250:251]\n\t"
    "v_pk_fma_f32 v[190:191], v[108:109], v[108:109], v[190:191]\n\t"
    "v_pk_fma_f32 v[194:195], v[156:157], v[156:157], v[194:195]\n\t"
    "v_pk_fma_f32 v[198:199], v[252:253], v[252:253], v[198:199]\n\t"
    "v_pk_add_f32 v[188:189], v[188:189], v[108:109]\n\t"
    "v_pk_add_f32 v[192:193], v[192:193], v[156:157]\n\t"
    "v_pk_add_f32 v[196:197], v[196:197], v[252:253]\n\t"
    "v_pk_fma_f32 v[190:191], v[110:111], v[110:111], v[190:191]\n\t"
    "v_pk_fma_f32 v[194:195], v[158:159], v[158:159], v[194:195]\n\t"
    "v_pk_fma_f32 v[198:199], v[254:255], v[254:255], v[198:199]\n\t"
    "v_pk_add_f32 v[188:189], v[188:189], v[110:111]\n\t"
    "v_pk_add_f32 v[192:193], v[192:193], v[158:159]\n\t"
    "v_pk_add_f32 v[196:197], v[196:197], v[254:255]\n\t"
    "v_add_f32 v188, v188, v189\n\t"
    "v_add_f32 v190, v190, v191\n\t"
    "v_add_f32 v192, v192, v193\n\t"
    "v_add_f32 v194, v194, v195\n\t"
    "v_add_f32 v196, v196, v197\n\t"
    "v_add_f32 v198, v198, v199\n\t"
    "v_mov_b32 v200, v196\n\t"
    "v_mov_b32 v202, v198\n\t"
    "v_permlane16_swap_b32 v188, v192\n\t"
    "v_permlane16_swap_b32 v190, v194\n\t"
    "v_permlane16_swap_b32 v196, v200\n\t"
    "v_permlane16_swap_b32 v198, v202\n\t"
    "v_add_f32 v188, v188, v192\n\t"
    "v_add_f32 v190, v190, v194\n\t"
    "v_add_f32 v196, v196, v200\n\t"
    "v_add_f32 v198, v198, v202\n\t"
    "s_nop 0\n\t"
    "v_permlane32_swap_b32 v188, v196\n\t"
    "v_permlane32_swap_b32 v190, v198\n\t"
    "v_add_f32 v204, v188, v196\n\t"
    "v_add_f32 v205, v190, v198\n\t"
    "s_waitcnt vmcnt(0)\n\t"
    "s_barrier\n\t"
    "v_and_b32 v1, 15, v0\n\t"
    "v_lshrrev_b32 v2, 4, v0\n\t"
    "v_bfe_u32 v4, v0, 2, 2\n\t"
    "v_and_b32 v5, 3, v0\n\t"
    "v_lshlrev_b32 v5, 3, v5\n\t"
    "v_lshl_add_u32 v5, v4, 7, v5\n\t"
    "v_and_b32 v4, 2, v4\n\t"
    "v_lshl_add_u32 v5, v4, 5, v5\n\t"
    "v_and_b32 v3, 1, v2\n\t"
    "v_mul_u32_u24 v3, 544, v3\n\t"
    "v_add_u32 v5, v5, v3\n\t"
    "v_lshrrev_b32 v3, 1, v2\n\t"
    "v_lshl_add_u32 v14, v3, 10, v5\n\t"
    "v_mul_u32_u24 v3, 3072, v3\n\t"
    "v_add_u32 v3, 55296, v3\n\t"
    "v_add_u32 v18, v3, v5\n\t"
    "v_xor_b32 v15, 32, v14\n\t"
    "v_xor_b32 v19, 32, v18\n\t"
    "v_xor_b32 v16, 64, v14\n\t"
    "v_xor_b32 v20, 64, v18\n\t"
    "v_xor_b32 v17, 96, v14\n\t"
    "v_xor_b32 v21, 96, v18\n\t"
    "v_lshlrev_b32 v22, 4, v2\n\t"
    "v_add_u32 v22, 153600, v22\n\t"
    "v_mul_f32 v204, 0x3baaaaab, v204\n\t"
    "v_mul_f32 v205, 0x3baaaaab, v205\n\t"
    "v_fma_f32 v205, -v204, v204, v205\n\t"
    "v_max_f32 v205, 0, v205\n\t"
    "v_add_f32 v205, 0x3727c5ac, v205\n\t"
    "v_rsq_f32 v205, v205\n\t"
    "s_nop 0\n\t"
    "v_mul_f32 v32, 0x3eefe414, v205\n\t"
    "v_mul_f32_e64 v33, -v204, v32\n\t"
    "s_add_u32 m0, s68, 0\n\t"
    "s_nop 0\n\t"
    "buffer_load_dwordx4 v29, s[44:47], s66 offen offset:0 lds\n\t"
    "v_mov_b32 v36, v32\n\t"
    "v_mov_b32 v37, v33\n\t"
    "s_nop 0\n\t"
    "v_permlane32_swap_b32 v32, v36\n\t"
    "v_permlane32_swap_b32 v33, v37\n\t"
    "v_mov_b32 v34, v32\n\t"
    "v_mov_b32 v35, v33\n\t"
    "v_mov_b32 v38, v36\n\t"
    "v_mov_b32 v39, v37\n\t"
    "v_permlane16_swap_b32 v32, v34\n\t"
    "buffer_load_dwordx4 v30, s[44:47], s66 offen offset:1024 lds\n\t"
    "v_permlane16_swap_b32 v33, v35\n\t"
    "v_permlane16_swap_b32 v36, v38\n\t"
    "v_permlane16_swap_b32 v37, v39\n\t"
    "v_pk_fma_f32 v[64:65], v[64:65], v[32:33], v[32:33] op_sel:[0,0,1] op_sel_hi:[1,0,1]\n\t"
    "v_pk_fma_f32 v[112:113], v[112:113], v[34:35], v[34:35] op_sel:[0,0,1] op_sel_hi:[1,0,1]\n\t"
    "v_cvt_pk_bf16_f32 v160, v64, v65\n\t"
    "v_pk_fma_f32 v[208:209], v[208:209], v[36:37], v[36:37] op_sel:[0,0,1] op_sel_hi:[1,0,1]\n\t"
    "v_cvt_pk_bf16_f32 v184, v112, v113\n\t"
    "v_pk_fma_f32 v[66:67], v[66:67], v[32:33], v[32:33] op_sel:[0,0,1] op_sel_hi:[1,0,1]\n\t"
    "buffer_load_dwordx4 v31, s[44:47], s66 offen offset:2048 lds\n\t"
    "s_add_u32 s66, s66, 12288\n\t"
    "v_cvt_pk_bf16_f32 v208, v208, v209\n\t"
    "v_pk_fma_f32 v[114:115], v[114:115], v[34:35], v[34:35] op_sel:[0,0,1] op_sel_hi:[1,0,1]\n\t"
    "v_cvt_pk_bf16_f32 v161, v66, v67\n\t"
    "v_pk_fma_f32 v[210:211], v[210:211], v[36:37], v[36:37] op_sel:[0,0,1] op_sel_hi:[1,0,1]\n\t"
    "v_cvt_pk_bf16_f32 v185, v114, v115\n\t"
    "v_pk_fma_f32 v[68:69], v[68:69], v[32:33], v[32:33] op_sel:[0,0,1] op_sel_hi:[1,0,1]\n\t"
    "v_cvt_pk_bf16_f32 v209, v210, v211\n\t"
    "v_pk_fma_f32 v[116:117], v[116:117], v[34:35], v[34:35] op_sel:[0,0,1] op_sel_hi:[1,0,1]\n\t"
    "v_cvt_pk_bf16_f32 v162, v68, v69\n\t"
    "v_pk_fma_f32 v[212:213], v[212:213], v[36:37], v[36:37] op_sel:[0,0,1] op_sel_hi:[1,0,1]\n\t"
    "v_cvt_pk_bf16_f32 v186, v116, v117\n\t"
    "v_pk_fma_f32 v[70:71], v[70:71], v[32:33], v[32:33] op_sel:[0,0,1] op_sel_hi:[1,0,1]\n\t"
    "v_cvt_pk_bf16_f32 v210, v212, v213\n\t"
    "v_pk_fma_f32 v[118:119], v[118:119], v[34:35], v[34:35] op_sel:[0,0,1] op_sel_hi:[1,0,1]\n\t"
    "v_cvt_pk_bf16_f32 v163, v70, v71\n\t"
    "v_pk_fma_f32 v[214:215], v[214:215], v[36:37], v[36:37] op_sel:[0,0,1] op_sel_hi:[1,0,1]\n\t"
    "v_cvt_pk_bf16_f32 v187, v118, v119\n\t"
    "v_pk_fma_f32 v[72:73], v[72:73], v[32:33], v[32:33] op_sel:[0,0,1] op_sel_hi:[1,0,1]\n\t"
    "v_cvt_pk_bf16_f32 v211, v214, v215\n\t"
    "v_pk_fma_f32 v[120:121], v[120:121], v[34:35], v[34:35] op_sel:[0,0,1] op_sel_hi:[1,0,1]\n\t"
    "v_cvt_pk_bf16_f32 v164, v72, v73\n\t"
    "v_pk_fma_f32 v[216:217], v[216:217], v[36:37], v[36:37] op_sel:[0,0,1] op_sel_hi:[1,0,1]\n\t"
    "v_cvt_pk_bf16_f32 v188, v120, v121\n\t"
    "v_pk_fma_f32 v[74:75], v[74:75], v[32:33], v[32:33] op_sel:[0,0,1] op_sel_hi:[1,0,1]\n\t"
    "v_cvt_pk_bf16_f32 v212, v216, v217\n\t"
    "v_pk_fma_f32 v[122:123], v[122:123], v[34:35], v[34:35] op_sel:[0,0,1] op_sel_hi:[1,0,1]\n\t"
    "v_cvt_pk_bf16_f32 v165, v74, v75\n\t"
    "v_pk_fma_f32 v[218:219], v[218:219], v[36:37], v[36:37] op_sel:[0,0,1] op_sel_hi:[1,0,1]\n\t"
    "v_cvt_pk_bf16_f32 v189, v122, v123\n\t"
    "v_pk_fma_f32 v[76:77], v[76:77], v[32:33], v[32:33] op_sel:[0,0,1] op_sel_hi:[1,0,1]\n\t"
    "v_cvt_pk_bf16_f32 v213, v218, v219\n\t"
    "v_pk_fma_f32 v[124:125], v[124:125], v[34:35], v[34:35] op_sel:[0,0,1] op_sel_hi:[1,0,1]\n\t"
    "v_cvt_pk_bf16_f32 v166, v76, v77\n\t"
    "v_pk_fma_f32 v[220:221], v[220:221], v[36:37], v[36:37] op_sel:[0,0,1] op_sel_hi:[1,0,1]\n\t"
    "v_cvt_pk_bf16_f32 v190, v124, v125\n\t"
    "v_pk_fma_f32 v[78:79], v[78:79], v[32:33], v[32:33] op_sel:[0,0,1] op_sel_hi:[1,0,1]\n\t"
    "v_cvt_pk_bf16_f32 v214, v220, v221\n\t"
    "v_pk_fma_f32 v[126:127], v[126:127], v[34:35], v[34:35] op_sel:[0,0,1] op_sel_hi:[1,0,1]\n\t"
    "v_cvt_pk_bf16_f32 v167, v78, v79\n\t"
    "v_pk_fma_f32 v[222:223], v[222:223], v[36:37], v[36:37] op_sel:[0,0,1] op_sel_hi:[1,0,1]\n\t"
    "v_cvt_pk_bf16_f32 v191, v126, v127\n\t"
    "v_pk_fma_f32 v[80:81], v[80:81], v[32:33], v[32:33] op_sel:[0,0,1] op_sel_hi:[1,0,1]\n\t"
    "v_cvt_pk_bf16_f32 v215, v222, v223\n\t"
    "v_pk_fma_f32 v[128:129], v[128:129], v[34:35], v[34:35] op_sel:[0,0,1] op_sel_hi:[1,0,1]\n\t"
    "v_cvt_pk_bf16_f32 v168, v80, v81\n\t"
    "v_pk_fma_f32 v[224:225], v[224:225], v[36:37], v[36:37] op_sel:[0,0,1] op_sel_hi:[1,0,1]\n\t"
    "v_cvt_pk_bf16_f32 v192, v128, v129\n\t"
    "v_pk_fma_f32 v[82:83], v[82:83], v[32:33], v[32:33] op_sel:[0,0,1] op_sel_hi:[1,0,1]\n\t"
    "v_cvt_pk_bf16_f32 v216, v224, v225\n\t"
    "v_pk_fma_f32 v[130:131], v[130:131], v[34:35], v[34:35] op_sel:[0,0,1] op_sel_hi:[1,0,1]\n\t"
    "v_cvt_pk_bf16_f32 v169, v82, v83\n\t"
    "v_pk_fma_f32 v[226:227], v[226:227], v[36:37], v[36:37] op_sel:[0,0,1] op_sel_hi:[1,0,1]\n\t"
    "v_cvt_pk_bf16_f32 v193, v130, v131\n\t"
    "v_pk_fma_f32 v[84:85], v[84:85], v[32:33], v[32:33] op_sel:[0,0,1] op_sel_hi:[1,0,1]\n\t"
    "v_cvt_pk_bf16_f32 v217, v226, v227\n\t"
    "v_pk_fma_f32 v[132:133], v[132:133], v[34:35], v[34:35] op_sel:[0,0,1] op_sel_hi:[1,0,1]\n\t"
    "v_cvt_pk_bf16_f32 v170, v84, v85\n\t"
    "v_pk_fma_f32 v[228:229], v[228:229], v[36:37], v[36:37] op_sel:[0,0,1] op_sel_hi:[1,0,1]\n\t"
    "v_cvt_pk_bf16_f32 v194, v132, v133\n\t"
    "v_pk_fma_f32 v[86:87], v[86:87], v[32:33], v[32:33] op_sel:[0,0,1] op_sel_hi:[1,0,1]\n\t"
    "v_cvt_pk_bf16_f32 v218, v228, v229\n\t"
    "v_pk_fma_f32 v[134:135], v[134:135], v[34:35], v[34:35] op_sel:[0,0,1] op_sel_hi:[1,0,1]\n\t"
    "v_cvt_pk_bf16_f32 v171, v86, v87\n\t"
    "v_pk_fma_f32 v[230:231], v[230:231], v[36:37], v[36:37] op_sel:[0,0,1] op_sel_hi:[1,0,1]\n\t"
    "v_cvt_pk_bf16_f32 v195, v134, v135\n\t"
    "v_pk_fma_f32 v[88:89], v[88:89], v[32:33], v[32:33] op_sel:[0,0,1] op_sel_hi:[1,0,1]\n\t"
    "v_cvt_pk_bf16_f32 v219, v230, v231\n\t"
    "v_pk_fma_f32 v[136:137], v[136:137], v[34:35], v[34:35] op_sel:[0,0,1] op_sel_hi:[1,0,1]\n\t"
    "v_cvt_pk_bf16_f32 v172, v88, v89\n\t"
    "v_pk_fma_f32 v[232:233], v[232:233], v[36:37], v[36:37] op_sel:[0,0,1] op_sel_hi:[1,0,1]\n\t"
    "v_cvt_pk_bf16_f32 v196, v136, v137\n\t"
    "v_pk_fma_f32 v[90:91], v[90:91], v[32:33], v[32:33] op_sel:[0,0,1] op_sel_hi:[1,0,1]\n\t"
    "v_cvt_pk_bf16_f32 v220, v232, v233\n\t"
    "v_pk_fma_f32 v[138:139], v[138:139], v[34:35], v[34:35] op_sel:[0,0,1] op_sel_hi:[1,0,1]\n\t"
    "v_cvt_pk_bf16_f32 v173, v90, v91\n\t"
    "v_pk_fma_f32 v[234:235], v[234:235], v[36:37], v[36:37] op_sel:[0,0,1] op_sel_hi:[1,0,1]\n\t"
    "v_cvt_pk_bf16_f32 v197, v138, v139\n\t"
    "v_pk_fma_f32 v[92:93], v[92:93], v[32:33], v[32:33] op_sel:[0,0,1] op_sel_hi:[1,0,1]\n\t"
    "v_cvt_pk_bf16_f32 v221, v234, v235\n\t"
    "v_pk_fma_f32 v[140:141], v[140:141], v[34:35], v[34:35] op_sel:[0,0,1] op_sel_hi:[1,0,1]\n\t"
    "v_cvt_pk_bf16_f32 v174, v92, v93\n\t"
    "v_pk_fma_f32 v[236:237], v[236:237], v[36:37], v[36:37] op_sel:[0,0,1] op_sel_hi:[1,0,1]\n\t"
    "v_cvt_pk_bf16_f32 v198, v140, v141\n\t"
    "v_pk_fma_f32 v[94:95], v[94:95], v[32:33], v[32:33] op_sel:[0,0,1] op_sel_hi:[1,0,1]\n\t"
    "v_cvt_pk_bf16_f32 v222, v236, v237\n\t"
    "v_pk_fma_f32 v[142:143], v[142:143], v[34:35], v[34:35] op_sel:[0,0,1] op_sel_hi:[1,0,1]\n\t"
    "v_cvt_pk_bf16_f32 v175, v94, v95\n\t"
    "v_pk_fma_f32 v[238:239], v[238:239], v[36:37], v[36:37] op_sel:[0,0,1] op_sel_hi:[1,0,1]\n\t"
    "v_cvt_pk_bf16_f32 v199, v142, v143\n\t"
    "ds_read_b64_tr_b16 a[192:193], v14 offset:0\n\t"
    "ds_read_b64_tr_b16 a[194:195], v14 offset:2048\n\t"
    "ds_read_b64_tr_b16 a[196:197], v14 offset:4096\n\t"
    "ds_read_b64_tr_b16 a[198:199], v14 offset:6144\n\t"
    "ds_read_b64_tr_b16 a[200:201], v14 offset:8192\n\t"
    "ds_read_b64_tr_b16 a[202:203], v14 offset:10240\n\t"
    "ds_read_b64_tr_b16 a[204:205], v14 offset:12288\n\t"
    "ds_read_b64_tr_b16 a[206:207], v14 offset:14336\n\t"
    "v_pk_fma_f32 v[96:97], v[96:97], v[32:33], v[32:33] op_sel:[0,0,1] op_sel_hi:[1,0,1]\n\t"
    "v_cvt_pk_bf16_f32 v223, v238, v239\n\t"
    "v_pk_fma_f32 v[144:145], v[144:145], v[34:35], v[34:35] op_sel:[0,0,1] op_sel_hi:[1,0,1]\n\t"
    "v_cvt_pk_bf16_f32 v176, v96, v97\n\t"
    "v_pk_fma_f32 v[240:241], v[240:241], v[36:37], v[36:37] op_sel:[0,0,1] op_sel_hi:[1,0,1]\n\t"
    "v_cvt_pk_bf16_f32 v200, v144, v145\n\t"
    "v_pk_fma_f32 v[98:99], v[98:99], v[32:33], v[32:33] op_sel:[0,0,1] op_sel_hi:[1,0,1]\n\t"
    "v_cvt_pk_bf16_f32 v224, v240, v241\n\t"
    "v_pk_fma_f32 v[146:147], v[146:147], v[34:35], v[34:35] op_sel:[0,0,1] op_sel_hi:[1,0,1]\n\t"
    "v_cvt_pk_bf16_f32 v177, v98, v99\n\t"
    "v_pk_fma_f32 v[242:243], v[242:243], v[36:37], v[36:37] op_sel:[0,0,1] op_sel_hi:[1,0,1]\n\t"
    "v_cvt_pk_bf16_f32 v201, v146, v147\n\t"
    "v_pk_fma_f32 v[100:101], v[100:101], v[32:33], v[32:33] op_sel:[0,0,1] op_sel_hi:[1,0,1]\n\t"
    "v_cvt_pk_bf16_f32 v225, v242, v243\n\t"
    "v_pk_fma_f32 v[148:149], v[148:149], v[34:35], v[34:35] op_sel:[0,0,1] op_sel_hi:[1,0,1]\n\t"
    "v_cvt_pk_bf16_f32 v178, v100, v101\n\t"
    "v_pk_fma_f32 v[244:245], v[244:245], v[36:37], v[36:37] op_sel:[0,0,1] op_sel_hi:[1,0,1]\n\t"
    "v_cvt_pk_bf16_f32 v202, v148, v149\n\t"
    "v_pk_fma_f32 v[102:103], v[102:103], v[32:33], v[32:33] op_sel:[0,0,1] op_sel_hi:[1,0,1]\n\t"
    "v_cvt_pk_bf16_f32 v226, v244, v245\n\t"
    "v_pk_fma_f32 v[150:151], v[150:151], v[34:35], v[34:35] op_sel:[0,0,1] op_sel_hi:[1,0,1]\n\t"
    "v_cvt_pk_bf16_f32 v179, v102, v103\n\t"
    "v_pk_fma_f32 v[246:247], v[246:247], v[36:37], v[36:37] op_sel:[0,0,1] op_sel_hi:[1,0,1]\n\t"
    "v_cvt_pk_bf16_f32 v203, v150, v151\n\t"
    "ds_read_b64_tr_b16 a[208:209], v14 offset:16384\n\t"
    "ds_read_b64_tr_b16 a[210:211], v14 offset:18432\n\t"
    "ds_read_b64_tr_b16 a[212:213], v14 offset:20480\n\t"
    "ds_read_b64_tr_b16 a[214:215], v14 offset:22528\n\t"
    "ds_read_b128 v[48:51], v22 offset:0\n\t"
    "ds_read_b128 v[52:55], v22 offset:64\n\t"
    "v_pk_fma_f32 v[104:105], v[104:105], v[32:33], v[32:33] op_sel:[0,0,1] op_sel_hi:[1,0,1]\n\t"
    "v_cvt_pk_bf16_f32 v227, v246, v247\n\t"
    "v_pk_fma_f32 v[152:153], v[152:153], v[34:35], v[34:35] op_sel:[0,0,1] op_sel_hi:[1,0,1]\n\t"
    "v_cvt_pk_bf16_f32 v180, v104, v105\n\t"
    "v_pk_fma_f32 v[248:249], v[248:249], v[36:37], v[36:37] op_sel:[0,0,1] op_sel_hi:[1,0,1]\n\t"
    "v_cvt_pk_bf16_f32 v204, v152, v153\n\t"
    "v_pk_fma_f32 v[106:107], v[106:107], v[32:33], v[32:33] op_sel:[0,0,1] op_sel_hi:[1,0,1]\n\t"
    "v_cvt_pk_bf16_f32 v228, v248, v249\n\t"
    "v_pk_fma_f32 v[154:155], v[154:155], v[34:35], v[34:35] op_sel:[0,0,1] op_sel_hi:[1,0,1]\n\t"
    "v_cvt_pk_bf16_f32 v181, v106, v107\n\t"
    "v_pk_fma_f32 v[250:251], v[250:251], v[36:37], v[36:37] op_sel:[0,0,1] op_sel_hi:[1,0,1]\n\t"
    "v_cvt_pk_bf16_f32 v205, v154, v155\n\t"
    "v_pk_fma_f32 v[108:109], v[108:109], v[32:33], v[32:33] op_sel:[0,0,1] op_sel_hi:[1,0,1]\n\t"
    "v_cvt_pk_bf16_f32 v229, v250, v251\n\t"
    "v_pk_fma_f32 v[156:157], v[156:157], v[34:35], v[34:35] op_sel:[0,0,1] op_sel_hi:[1,0,1]\n\t"
    "v_cvt_pk_bf16_f32 v182, v108, v109\n\t"
    "v_pk_fma_f32 v[252:253], v[252:253], v[36:37], v[36:37] op_sel:[0,0,1] op_sel_hi:[1,0,1]\n\t"
    "v_cvt_pk_bf16_f32 v206, v156, v157\n\t"
    "v_pk_fma_f32 v[110:111], v[110:111], v[32:33], v[32:33] op_sel:[0,0,1] op_sel_hi:[1,0,1]\n\t"
    "v_cvt_pk_bf16_f32 v230, v252, v253\n\t"
    "v_pk_fma_f32 v[158:159], v[158:159], v[34:35], v[34:35] op_sel:[0,0,1] op_sel_hi:[1,0,1]\n\t"
    "v_cvt_pk_bf16_f32 v183, v110, v111\n\t"
    "v_pk_fma_f32 v[254:255], v[254:255], v[36:37], v[36:37] op_sel:[0,0,1] op_sel_hi:[1,0,1]\n\t"
    "v_cvt_pk_bf16_f32 v207, v158, v159\n\t"
    "v_cvt_pk_bf16_f32 v231, v254, v255\n\t"
    "v_add_u32 v23, 3072, v22\n\t"
    "s_waitcnt lgkmcnt(0)\n\t"
    "v_mfma_f32_16x16x32_bf16 v[64:67], a[192:195], v[160:163], v[48:51]\n\t"
    "ds_read_b64_tr_b16 a[216:217], v15 offset:0\n\t"
    "ds_read_b128 a[0:3], v23 offset:0\n\t"
    "v_mfma_f32_16x16x32_bf16 v[68:71], a[192:195], v[184:187], v[48:51]\n\t"
    "ds_read_b64_tr_b16 a[218:219], v15 offset:2048\n\t"
    "ds_read_b128 a[4:7], v23 offset:0\n\t"
    "s_add_u32 m0, s67, 24576\n\t"
    "s_add_u32 s63, s65, 0\n\t"
    "v_mfma_f32_16x16x32_bf16 v[72:75], a[192:195], v[208:211], v[48:51]\n\t"
    "buffer_load_dwordx4 v26, s[40:43], s63 offen offset:0 lds\n\t"
    "ds_read_b128 a[8:11], v23 offset:0\n\t"
    "v_mfma_f32_16x16x32_bf16 v[64:67], a[196:199], v[164:167], v[64:67]\n\t"
    "ds_read_b64_tr_b16 a[220:221], v15 offset:4096\n\t"
    "ds_read_b128 a[12:15], v23 offset:64\n\t"
    "v_mfma_f32_16x16x32_bf16 v[68:71], a[196:199], v[188:191], v[68:71]\n\t"
    "ds_read_b64_tr_b16 a[222:223], v15 offset:6144\n\t"
    "ds_read_b128 a[16:19], v23 offset:64\n\t"
    "v_mfma_f32_16x16x32_bf16 v[72:75], a[196:199], v[212:215], v[72:75]\n\t"
    "buffer_load_dwordx4 v27, s[40:43], s63 offen offset:1024 lds\n\t"
    "ds_read_b128 a[20:23], v23 offset:64\n\t"
    "s_waitcnt lgkmcnt(7)\n\t"
    "v_mfma_f32_16x16x32_bf16 v[64:67], a[200:203], v[168:171], v[64:67]\n\t"
    "ds_read_b64_tr_b16 a[224:225], v15 offset:8192\n\t"
    "ds_read_b128 a[24:27], v23 offset:128\n\t"
    "v_mfma_f32_16x16x32_bf16 v[68:71], a[200:203], v[192:195], v[68:71]\n\t"
    "ds_read_b64_tr_b16 a[226:227], v15 offset:10240\n\t"
    "ds_read_b128 a[28:31], v23 offset:128\n\t"
    "v_mfma_f32_16x16x32_bf16 v[72:75], a[200:203], v[216:219], v[72:75]\n\t"
    "ds_read_b128 v[48:51], v22 offset:128\n\t"
    "buffer_load_dwordx4 v28, s[40:43], s63 offen offset:2048 lds\n\t"
    "ds_read_b128 a[32:35], v23 offset:128\n\t"
    "s_waitcnt lgkmcnt(8)\n\t"
    "v_mfma_f32_16x16x32_bf16 v[64:67], a[204:207], v[172:175], v[64:67]\n\t"
    "ds_read_b64_tr_b16 a[228:229], v15 offset:12288\n\t"
    "ds_read_b128 a[36:39], v23 offset:192\n\t"
    "v_mfma_f32_16x16x32_bf16 v[68:71], a[204:207], v[196:199], v[68:71]\n\t"
    "ds_read_b64_tr_b16 a[230:231], v15 offset:14336\n\t"
    "ds_read_b128 a[40:43], v23 offset:192\n\t"
    "s_add_u32 m0, s67, 36864\n\t"
    "s_add_u32 s63, s65, 147456\n\t"
    "v_mfma_f32_16x16x32_bf16 v[72:75], a[204:207], v[220:223], v[72:75]\n\t"
    "buffer_load_dwordx4 v26, s[40:43], s63 offen offset:0 lds\n\t"
    "ds_read_b128 a[44:47], v23 offset:192\n\t"
    "s_waitcnt lgkmcnt(8)\n\t"
    "v_mfma_f32_16x16x32_bf16 v[64:67], a[208:211], v[176:179], v[64:67]\n\t"
    "ds_read_b64_tr_b16 a[232:233], v15 offset:16384\n\t"
    "ds_read_b128 a[48:51], v23 offset:256\n\t"
    "v_mfma_f32_16x16x32_bf16 v[68:71], a[208:211], v[200:203], v[68:71]\n\t"
    "ds_read_b64_tr_b16 a[234:235], v15 offset:18432\n\t"
    "ds_read_b128 a[52:55], v23 offset:256\n\t"
    "v_mfma_f32_16x16x32_bf16 v[72:75], a[208:211], v[224:227], v[72:75]\n\t"
    "buffer_load_dwordx4 v27, s[40:43], s63 offen offset:1024 lds\n\t"
    "ds_read_b128 a[56:59], v23 offset:256\n\t"
    "s_waitcnt lgkmcnt(7)\n\t"
    "v_mfma_f32_16x16x32_bf16 v[64:67], a[212:215], v[180:183], v[64:67]\n\t"
    "ds_read_b64_tr_b16 a[236:237], v15 offset:20480\n\t"
    "ds_read_b128 a[60:63], v23 offset:320\n\t"
    "v_mfma_f32_16x16x32_bf16 v[68:71], a[212:215], v[204:207], v[68:71]\n\t"
    "ds_read_b64_tr_b16 a[238:239], v15 offset:22528\n\t"
    "ds_read_b128 a[64:67], v23 offset:320\n\t"
    "v_mfma_f32_16x16x32_bf16 v[72:75], a[212:215], v[228:231], v[72:75]\n\t"
    "buffer_load_dwordx4 v28, s[40:43], s63 offen offset:2048 lds\n\t"
    "ds_read_b128 a[68:71], v23 offset:320\n\t"
    "s_waitcnt lgkmcnt(7)\n\t"
    "v_mfma_f32_16x16x32_bf16 v[80:83], a[216:219], v[160:163], v[52:55]\n\t"
    "ds_read_b64_tr_b16 a[192:193], v16 offset:0\n\t"
    "ds_read_b128 a[72:75], v23 offset:384\n\t"
    "v_mfma_f32_16x16x32_bf16 v[84:87], a[216:219], v[184:187], v[52:55]\n\t"
    "ds_read_b64_tr_b16 a[194:195], v16 offset:2048\n\t"
    "ds_read_b128 a[76:79], v23 offset:384\n\t"
    "s_add_u32 s65, s65, 128\n\t"
    "s_add_u32 m0, s68, 12288\n\t"
    "v_mfma_f32_16x16x32_bf16 v[88:91], a[216:219], v[208:211], v[52:55]\n\t"
    "buffer_load_dwordx4 v29, s[44:47], s66 offen offset:0 lds\n\t"
    "ds_read_b128 a[80:83], v23 offset:384\n\t"
    "s_waitcnt lgkmcnt(7)\n\t"
    "v_mfma_f32_16x16x32_bf16 v[80:83], a[220:223], v[164:167], v[80:83]\n\t"
    "ds_read_b64_tr_b16 a[196:197], v16 offset:4096\n\t"
    "ds_read_b128 a[84:87], v23 offset:448\n\t"
    "v_mfma_f32_16x16x32_bf16 v[84:87], a[220:223], v[188:191], v[84:87]\n\t"
    "ds_read_b64_tr_b16 a[198:199], v16 offset:6144\n\t"
    "ds_read_b128 a[88:91], v23 offset:448\n\t"
    "v_mfma_f32_16x16x32_bf16 v[88:91], a[220:223], v[212:215], v[88:91]\n\t"
    "buffer_load_dwordx4 v30, s[44:47], s66 offen offset:1024 lds\n\t"
    "ds_read_b128 a[92:95], v23 offset:448\n\t"
    "s_waitcnt lgkmcnt(7)\n\t"
    "v_mfma_f32_16x16x32_bf16 v[80:83], a[224:227], v[168:171], v[80:83]\n\t"
    "ds_read_b64_tr_b16 a[200:201], v16 offset:8192\n\t"
    "ds_read_b128 a[96:99], v23 offset:512\n\t"
    "v_cvt_pk_f16_f32 v32, v64, v65\n\t"
    "v_cvt_pk_f16_f32 v33, v66, v67\n\t"
    "v_pk_fma_f16 v34, v32, v32, s60 neg_lo:[1,0,1] neg_hi:[1,0,1]\n\t"
    "v_mfma_f32_16x16x32_bf16 v[84:87], a[224:227], v[192:195], v[84:87]\n\t"
    "ds_read_b64_tr_b16 a[202:203], v16 offset:10240\n\t"
    "ds_read_b128 a[100:103], v23 offset:512\n\t"
    "v_pk_fma_f16 v35, v33, v33, s60 neg_lo:[1,0,1] neg_hi:[1,0,1]\n\t"
    "v_pk_mul_f16 v34, v34, v32\n\t"
    "v_pk_mul_f16 v35, v35, v33\n\t"
    "v_mfma_f32_16x16x32_bf16 v[88:91], a[224:227], v[216:219], v[88:91]\n\t"
    "ds_read_b128 v[52:55], v22 offset:192\n\t"
    "buffer_load_dwordx4 v31, s[44:47], s66 offen offset:2048 lds\n\t"
    "ds_read_b128 a[104:107], v23 offset:512\n\t"
    "s_waitcnt lgkmcnt(8)\n\t"
    "v_mfma_f32_16x16x32_bf16 v[80:83], a[228:231], v[172:175], v[80:83]\n\t"
    "ds_read_b64_tr_b16 a[204:205], v16 offset:12288\n\t"
    "ds_read_b128 a[108:111], v23 offset:576\n\t"
    "v_exp_f16 v36, v34\n\t"
    "v_mfma_f32_16x16x32_bf16 v[84:87], a[228:231], v[196:199], v[84:87]\n\t"
    "ds_read_b64_tr_b16 a[206:207], v16 offset:14336\n\t"
    "ds_read_b128 a[112:115], v23 offset:576\n\t"
    "v_exp_f16 v37, v35\n\t"
    "s_add_u32 s66, s66, 12288\n\t"
    "v_mfma_f32_16x16x32_bf16 v[88:91], a[228:231], v[220:223], v[88:91]\n\t"
    "ds_read_b128 a[116:119], v23 offset:576\n\t"
    "v_exp_f16_sdwa v36, v34 dst_sel:WORD_1 dst_unused:UNUSED_PRESERVE src0_sel:WORD_1\n\t"
    "s_waitcnt lgkmcnt(8)\n\t"
    "v_mfma_f32_16x16x32_bf16 v[80:83], a[232:235], v[176:179], v[80:83]\n\t"
    "ds_read_b64_tr_b16 a[208:209], v16 offset:16384\n\t"
    "ds_read_b128 a[120:123], v23 offset:640\n\t"
    "v_exp_f16_sdwa v37, v35 dst_sel:WORD_1 dst_unused:UNUSED_PRESERVE src0_sel:WORD_1\n\t"
    "v_pk_add_f16 v36, v36, 1.0 op_sel_hi:[1,0]\n\t"
    "v_mfma_f32_16x16x32_bf16 v[84:87], a[232:235], v[200:203], v[84:87]\n\t"
    "ds_read_b64_tr_b16 a[210:211], v16 offset:18432\n\t"
    "ds_read_b128 a[124:127], v23 offset:640\n\t"
    "v_pk_add_f16 v37, v37, 1.0 op_sel_hi:[1,0]\n\t"
    "v_rcp_f16 v34, v36\n\t"
    "v_mfma_f32_16x16x32_bf16 v[88:91], a[232:235], v[224:227], v[88:91]\n\t"
    "ds_read_b128 a[128:131], v23 offset:640\n\t"
    "v_rcp_f16 v35, v37\n\t"
    "s_waitcnt lgkmcnt(7)\n\t"
    "v_mfma_f32_16x16x32_bf16 v[80:83], a[236:239], v[180:183], v[80:83]\n\t"
    "ds_read_b64_tr_b16 a[212:213], v16 offset:20480\n\t"
    "ds_read_b128 a[132:135], v23 offset:704\n\t"
    "v_rcp_f16_sdwa v34, v36 dst_sel:WORD_1 dst_unused:UNUSED_PRESERVE src0_sel:WORD_1\n\t"
    "v_rcp_f16_sdwa v35, v37 dst_sel:WORD_1 dst_unused:UNUSED_PRESERVE src0_sel:WORD_1\n\t"
    "v_mfma_f32_16x16x32_bf16 v[84:87], a[236:239], v[204:207], v[84:87]\n\t"
    "ds_read_b64_tr_b16 a[214:215], v16 offset:22528\n\t"
    "ds_read_b128 a[136:139], v23 offset:704\n\t"
    "v_fma_mix_f32 v64, v64, v34, 0 op_sel_hi:[0,1,0]\n\t"
    "v_fma_mix_f32 v66, v66, v35, 0 op_sel_hi:[0,1,0]\n\t"
    "v_mfma_f32_16x16x32_bf16 v[88:91], a[236:239], v[228:231], v[88:91]\n\t"
    "ds_read_b128 a[140:143], v23 offset:704\n\t"
    "v_fma_mix_f32 v65, v65, v34, 0 op_sel:[0,1,0] op_sel_hi:[0,1,0]\n\t"
    "v_fma_mix_f32 v67, v67, v35, 0 op_sel:[0,1,0] op_sel_hi:[0,1,0]\n\t"
    "v_cvt_pk_bf16_f32 v128, v64, v65\n\t"
    "v_cvt_pk_bf16_f32 v129, v66, v67\n\t"
    "v_mfma_f32_16x16x32_bf16 v[96:99], a[192:195], v[160:163], v[48:51]\n\t"
    "ds_read_b64_tr_b16 a[216:217], v17 offset:0\n\t"
    "v_cvt_pk_f16_f32 v38, v68, v69\n\t"
    "v_cvt_pk_f16_f32 v39, v70, v71\n\t"
    "v_pk_fma_f16 v40, v38, v38, s60 neg_lo:[1,0,1] neg_hi:[1,0,1]\n\t"
    "v_pk_fma_f16 v41, v39, v39, s60 neg_lo:[1,0,1] neg_hi:[1,0,1]\n\t"
    "v_pk_mul_f16 v40, v40, v38\n\t"
    "v_mfma_f32_16x16x32_bf16 v[100:103], a[192:195], v[184:187], v[48:51]\n\t"
    "ds_read_b64_tr_b16 a[218:219], v17 offset:2048\n\t"
    "v_pk_mul_f16 v41, v41, v39\n\t"
    "v_exp_f16 v42, v40\n\t"
    "v_exp_f16 v43, v41\n\t"
    "v_mfma_f32_16x16x32_bf16 v[104:107], a[192:195], v[208:211], v[48:51]\n\t"
    "v_exp_f16_sdwa v42, v40 dst_sel:WORD_1 dst_unused:UNUSED_PRESERVE src0_sel:WORD_1\n\t"
    "v_exp_f16_sdwa v43, v41 dst_sel:WORD_1 dst_unused:UNUSED_PRESERVE src0_sel:WORD_1\n\t"
    "v_pk_add_f16 v42, v42, 1.0 op_sel_hi:[1,0]\n\t"
    "v_pk_add_f16 v43, v43, 1.0 op_sel_hi:[1,0]\n\t"
    "v_mfma_f32_16x16x32_bf16 v[96:99], a[196:199], v[164:167], v[96:99]\n\t"
    "ds_read_b64_tr_b16 a[220:221], v17 offset:4096\n\t"
    "v_rcp_f16 v40, v42\n\t"
    "v_rcp_f16 v41, v43\n\t"
    "v_rcp_f16_sdwa v40, v42 dst_sel:WORD_1 dst_unused:UNUSED_PRESERVE src0_sel:WORD_1\n\t"
    "v_mfma_f32_16x16x32_bf16 v[100:103], a[196:199], v[188:191], v[100:103]\n\t"
    "s_waitcnt lgkmcnt(11)\n\t"
    "ds_read_b64_tr_b16 a[222:223], v17 offset:6144\n\t"
    "v_rcp_f16_sdwa v41, v43 dst_sel:WORD_1 dst_unused:UNUSED_PRESERVE src0_sel:WORD_1\n\t"
    "v_fma_mix_f32 v68, v68, v40, 0 op_sel_hi:[0,1,0]\n\t"
    "v_fma_mix_f32 v70, v70, v41, 0 op_sel_hi:[0,1,0]\n\t"
    "v_mfma_f32_16x16x32_bf16 v[104:107], a[196:199], v[212:215], v[104:107]\n\t"
    "v_fma_mix_f32 v69, v69, v40, 0 op_sel:[0,1,0] op_sel_hi:[0,1,0]\n\t"
    "v_fma_mix_f32 v71, v71, v41, 0 op_sel:[0,1,0] op_sel_hi:[0,1,0]\n\t"
    "v_cvt_pk_bf16_f32 v132, v68, v69\n\t"
    "v_cvt_pk_bf16_f32 v133, v70, v71\n\t"
    "v_cvt_pk_f16_f32 v32, v72, v73\n\t"
    "v_cvt_pk_f16_f32 v33, v74, v75\n\t"
    "v_mfma_f32_16x16x32_bf16 v[96:99], a[200:203], v[168:171], v[96:99]\n\t"
    "ds_read_b64_tr_b16 a[224:225], v17 offset:8192\n\t"
    "v_pk_fma_f16 v34, v32, v32, s60 neg_lo:[1,0,1] neg_hi:[1,0,1]\n\t"
    "v_pk_fma_f16 v35, v33, v33, s60 neg_lo:[1,0,1] neg_hi:[1,0,1]\n\t"
    "v_pk_mul_f16 v34, v34, v32\n\t"
    "v_pk_mul_f16 v35, v35, v33\n\t"
    "v_exp_f16 v36, v34\n\t"
    "v_mfma_f32_16x16x32_bf16 v[100:103], a[200:203], v[192:195], v[100:103]\n\t"
    "ds_read_b64_tr_b16 a[226:227], v17 offset:10240\n\t"
    "v_exp_f16 v37, v35\n\t"
    "v_exp_f16_sdwa v36, v34 dst_sel:WORD_1 dst_unused:UNUSED_PRESERVE src0_sel:WORD_1\n\t"
    "v_mfma_f32_16x16x32_bf16 v[104:107], a[200:203], v[216:219], v[104:107]\n\t"
    "v_exp_f16_sdwa v37, v35 dst_sel:WORD_1 dst_unused:UNUSED_PRESERVE src0_sel:WORD_1\n\t"
    "v_pk_add_f16 v36, v36, 1.0 op_sel_hi:[1,0]\n\t"
    "v_pk_add_f16 v37, v37, 1.0 op_sel_hi:[1,0]\n\t"
    "v_rcp_f16 v34, v36\n\t"
    "v_mfma_f32_16x16x32_bf16 v[96:99], a[204:207], v[172:175], v[96:99]\n\t"
    "ds_read_b64_tr_b16 a[228:229], v17 offset:12288\n\t"
    "v_rcp_f16 v35, v37\n\t"
    "v_rcp_f16_sdwa v34, v36 dst_sel:WORD_1 dst_unused:UNUSED_PRESERVE src0_sel:WORD_1\n\t"
    "v_rcp_f16_sdwa v35, v37 dst_sel:WORD_1 dst_unused:UNUSED_PRESERVE src0_sel:WORD_1\n\t"
    "v_mfma_f32_16x16x32_bf16 v[100:103], a[204:207], v[196:199], v[100:103]\n\t"
    "s_waitcnt lgkmcnt(11)\n\t"
    "ds_read_b64_tr_b16 a[230:231], v17 offset:14336\n\t"
    "v_fma_mix_f32 v72, v72, v34, 0 op_sel_hi:[0,1,0]\n\t"
    "v_fma_mix_f32 v74, v74, v35, 0 op_sel_hi:[0,1,0]\n\t"
    "v_fma_mix_f32 v73, v73, v34, 0 op_sel:[0,1,0] op_sel_hi:[0,1,0]\n\t"
    "v_fma_mix_f32 v75, v75, v35, 0 op_sel:[0,1,0] op_sel_hi:[0,1,0]\n\t"
    "v_mfma_f32_16x16x32_bf16 v[104:107], a[204:207], v[220:223], v[104:107]\n\t"
    "v_cvt_pk_bf16_f32 v136, v72, v73\n\t"
    "v_cvt_pk_bf16_f32 v137, v74, v75\n\t"
    "v_cvt_pk_f16_f32 v38, v80, v81\n\t"
    "s_waitcnt vmcnt(9)\n\t"
    "s_barrier\n\t"
    "s_waitcnt lgkmcnt(4)\n\t"
    "v_mfma_f32_16x16x32_bf16 v[96:99], a[208:211], v[176:179], v[96:99]\n\t"
    "ds_read_b64_tr_b16 a[232:233], v17 offset:16384\n\t"
    "v_cvt_pk_f16_f32 v39, v82, v83\n\t"
    "v_pk_fma_f16 v40, v38, v38, s60 neg_lo:[1,0,1] neg_hi:[1,0,1]\n\t"
    "v_pk_fma_f16 v41, v39, v39, s60 neg_lo:[1,0,1] neg_hi:[1,0,1]\n\t"
    "v_pk_mul_f16 v40, v40, v38\n\t"
    "v_pk_mul_f16 v41, v41, v39\n\t"
    "v_mfma_f32_16x16x32_bf16 v[100:103], a[208:211], v[200:203], v[100:103]\n\t"
    "ds_read_b64_tr_b16 a[234:235], v17 offset:18432\n\t"
    "v_exp_f16 v42, v40\n\t"
    "v_exp_f16 v43, v41\n\t"
    "s_add_u32 m0, s68, 24576\n\t"
    "v_mfma_f32_16x16x32_bf16 v[104:107], a[208:211], v[224:227], v[104:107]\n\t"
    "buffer_load_dwordx4 v29, s[44:47], s66 offen offset:0 lds\n\t"
    "v_exp_f16_sdwa v42, v40 dst_sel:WORD_1 dst_unused:UNUSED_PRESERVE src0_sel:WORD_1\n\t"
    "v_exp_f16_sdwa v43, v41 dst_sel:WORD_1 dst_unused:UNUSED_PRESERVE src0_sel:WORD_1\n\t"
    "v_mfma_f32_16x16x32_bf16 v[96:99], a[212:215], v[180:183], v[96:99]\n\t"
    "ds_read_b64_tr_b16 a[236:237], v17 offset:20480\n\t"
    "v_pk_add_f16 v42, v42, 1.0 op_sel_hi:[1,0]\n\t"
    "v_pk_add_f16 v43, v43, 1.0 op_sel_hi:[1,0]\n\t"
    "v_rcp_f16 v40, v42\n\t"
    "v_rcp_f16 v41, v43\n\t"
    "v_mfma_f32_16x16x32_bf16 v[100:103], a[212:215], v[204:207], v[100:103]\n\t"
    "ds_read_b64_tr_b16 a[238:239], v17 offset:22528\n\t"
    "v_rcp_f16_sdwa v40, v42 dst_sel:WORD_1 dst_unused:UNUSED_PRESERVE src0_sel:WORD_1\n\t"
    "v_rcp_f16_sdwa v41, v43 dst_sel:WORD_1 dst_unused:UNUSED_PRESERVE src0_sel:WORD_1\n\t"
    "v_mfma_f32_16x16x32_bf16 v[104:107], a[212:215], v[228:231], v[104:107]\n\t"
    "buffer_load_dwordx4 v30, s[44:47], s66 offen offset:1024 lds\n\t"
    "v_fma_mix_f32 v80, v80, v40, 0 op_sel_hi:[0,1,0]\n\t"
    "v_fma_mix_f32 v82, v82, v41, 0 op_sel_hi:[0,1,0]\n\t"
    "v_fma_mix_f32 v81, v81, v40, 0 op_sel:[0,1,0] op_sel_hi:[0,1,0]\n\t"
    "v_fma_mix_f32 v83, v83, v41, 0 op_sel:[0,1,0] op_sel_hi:[0,1,0]\n\t"
    "v_mfma_f32_16x16x32_bf16 v[112:115], a[216:219], v[160:163], v[52:55]\n\t"
    "ds_read_b64_tr_b16 a[192:193], v18 offset:0\n\t"
    "v_cvt_pk_bf16_f32 v130, v80, v81\n\t"
    "v_cvt_pk_bf16_f32 v131, v82, v83\n\t"
    "v_cvt_pk_f16_f32 v32, v84, v85\n\t"
    "v_cvt_pk_f16_f32 v33, v86, v87\n\t"
    "v_pk_fma_f16 v34, v32, v32, s60 neg_lo:[1,0,1] neg_hi:[1,0,1]\n\t"
    "v_mfma_f32_16x16x32_bf16 v[116:119], a[216:219], v[184:187], v[52:55]\n\t"
    "ds_read_b64_tr_b16 a[194:195], v18 offset:6144\n\t"
    "v_pk_fma_f16 v35, v33, v33, s60 neg_lo:[1,0,1] neg_hi:[1,0,1]\n\t"
    "v_pk_mul_f16 v34, v34, v32\n\t"
    "v_pk_mul_f16 v35, v35, v33\n\t"
    "v_exp_f16 v36, v34\n\t"
    "v_mfma_f32_16x16x32_bf16 v[120:123], a[216:219], v[208:211], v[52:55]\n\t"
    "buffer_load_dwordx4 v31, s[44:47], s66 offen offset:2048 lds\n\t"
    "v_exp_f16 v37, v35\n\t"
    "v_exp_f16_sdwa v36, v34 dst_sel:WORD_1 dst_unused:UNUSED_PRESERVE src0_sel:WORD_1\n\t"
    "v_mfma_f32_16x16x32_bf16 v[112:115], a[220:223], v[164:167], v[112:115]\n\t"
    "ds_read_b64_tr_b16 a[196:197], v19 offset:0\n\t"
    "v_exp_f16_sdwa v37, v35 dst_sel:WORD_1 dst_unused:UNUSED_PRESERVE src0_sel:WORD_1\n\t"
    "v_pk_add_f16 v36, v36, 1.0 op_sel_hi:[1,0]\n\t"
    "v_pk_add_f16 v37, v37, 1.0 op_sel_hi:[1,0]\n\t"
    "v_rcp_f16 v34, v36\n\t"
    "v_mfma_f32_16x16x32_bf16 v[116:119], a[220:223], v[188:191], v[116:119]\n\t"
    "ds_read_b64_tr_b16 a[198:199], v19 offset:6144\n\t"
    "v_rcp_f16 v35, v37\n\t"
    "v_rcp_f16_sdwa v34, v36 dst_sel:WORD_1 dst_unused:UNUSED_PRESERVE src0_sel:WORD_1\n\t"
    "s_add_u32 s66, s66, 12288\n\t"
    "v_mfma_f32_16x16x32_bf16 v[120:123], a[220:223], v[212:215], v[120:123]\n\t"
    "v_rcp_f16_sdwa v35, v37 dst_sel:WORD_1 dst_unused:UNUSED_PRESERVE src0_sel:WORD_1\n\t"
    "v_fma_mix_f32 v84, v84, v34, 0 op_sel_hi:[0,1,0]\n\t"
    "v_fma_mix_f32 v86, v86, v35, 0 op_sel_hi:[0,1,0]\n\t"
    "s_waitcnt lgkmcnt(4)\n\t"
    "v_mfma_f32_16x16x32_bf16 v[112:115], a[224:227], v[168:171], v[112:115]\n\t"
    "ds_read_b64_tr_b16 a[200:201], v20 offset:0\n\t"
    "v_fma_mix_f32 v85, v85, v34, 0 op_sel:[0,1,0] op_sel_hi:[0,1,0]\n\t"
    "v_fma_mix_f32 v87, v87, v35, 0 op_sel:[0,1,0] op_sel_hi:[0,1,0]\n\t"
    "v_cvt_pk_bf16_f32 v134, v84, v85\n\t"
    "v_cvt_pk_bf16_f32 v135, v86, v87\n\t"
    "v_cvt_pk_f16_f32 v38, v88, v89\n\t"
    "v_mfma_f32_16x16x32_bf16 v[116:119], a[224:227], v[192:195], v[116:119]\n\t"
    "ds_read_b64_tr_b16 a[202:203], v20 offset:6144\n\t"
    "v_cvt_pk_f16_f32 v39, v90, v91\n\t"
    "v_pk_fma_f16 v40, v38, v38, s60 neg_lo:[1,0,1] neg_hi:[1,0,1]\n\t"
    "v_pk_fma_f16 v41, v39, v39, s60 neg_lo:[1,0,1] neg_hi:[1,0,1]\n\t"
    "v_pk_mul_f16 v40, v40, v38\n\t"
    "v_pk_mul_f16 v41, v41, v39\n\t"
    "v_mfma_f32_16x16x32_bf16 v[120:123], a[224:227], v[216:219], v[120:123]\n\t"
    "v_exp_f16 v42, v40\n\t"
    "v_exp_f16 v43, v41\n\t"
    "v_exp_f16_sdwa v42, v40 dst_sel:WORD_1 dst_unused:UNUSED_PRESERVE src0_sel:WORD_1\n\t"
    "v_mfma_f32_16x16x32_bf16 v[112:115], a[228:231], v[172:175], v[112:115]\n\t"
    "ds_read_b64_tr_b16 a[204:205], v21 offset:0\n\t"
    "v_exp_f16_sdwa v43, v41 dst_sel:WORD_1 dst_unused:UNUSED_PRESERVE src0_sel:WORD_1\n\t"
    "v_pk_add_f16 v42, v42, 1.0 op_sel_hi:[1,0]\n\t"
    "v_pk_add_f16 v43, v43, 1.0 op_sel_hi:[1,0]\n\t"
    "v_rcp_f16 v40, v42\n\t"
    "v_mfma_f32_16x16x32_bf16 v[116:119], a[228:231], v[196:199], v[116:119]\n\t"
    "ds_read_b64_tr_b16 a[206:207], v21 offset:6144\n\t"
    "v_rcp_f16 v41, v43\n\t"
    "v_rcp_f16_sdwa v40, v42 dst_sel:WORD_1 dst_unused:UNUSED_PRESERVE src0_sel:WORD_1\n\t"
    "v_mfma_f32_16x16x32_bf16 v[120:123], a[228:231], v[220:223], v[120:123]\n\t"
    "v_rcp_f16_sdwa v41, v43 dst_sel:WORD_1 dst_unused:UNUSED_PRESERVE src0_sel:WORD_1\n\t"
    "v_fma_mix_f32 v88, v88, v40, 0 op_sel_hi:[0,1,0]\n\t"
    "v_fma_mix_f32 v90, v90, v41, 0 op_sel_hi:[0,1,0]\n\t"
    "v_fma_mix_f32 v89, v89, v40, 0 op_sel:[0,1,0] op_sel_hi:[0,1,0]\n\t"
    "v_fma_mix_f32 v91, v91, v41, 0 op_sel:[0,1,0] op_sel_hi:[0,1,0]\n\t"
    "v_mfma_f32_16x16x32_bf16 v[112:115], a[232:235], v[176:179], v[112:115]\n\t"
    "ds_read_b64_tr_b16 a[208:209], v18 offset:1024\n\t"
    "v_cvt_pk_bf16_f32 v138, v88, v89\n\t"
    "v_cvt_pk_bf16_f32 v139, v90, v91\n\t"
    "v_cvt_pk_f16_f32 v32, v96, v97\n\t"
    "v_cvt_pk_f16_f32 v33, v98, v99\n\t"
    "v_pk_fma_f16 v34, v32, v32, s60 neg_lo:[1,0,1] neg_hi:[1,0,1]\n\t"
    "v_mfma_f32_16x16x32_bf16 v[116:119], a[232:235], v[200:203], v[116:119]\n\t"
    "ds_read_b64_tr_b16 a[210:211], v18 offset:7168\n\t"
    "v_pk_fma_f16 v35, v33, v33, s60 neg_lo:[1,0,1] neg_hi:[1,0,1]\n\t"
    "v_pk_mul_f16 v34, v34, v32\n\t"
    "v_pk_mul_f16 v35, v35, v33\n\t"
    "v_exp_f16 v36, v34\n\t"
    "v_mfma_f32_16x16x32_bf16 v[120:123], a[232:235], v[224:227], v[120:123]\n\t"
    "v_exp_f16 v37, v35\n\t"
    "v_exp_f16_sdwa v36, v34 dst_sel:WORD_1 dst_unused:UNUSED_PRESERVE src0_sel:WORD_1\n\t"
    "v_exp_f16_sdwa v37, v35 dst_sel:WORD_1 dst_unused:UNUSED_PRESERVE src0_sel:WORD_1\n\t"
    "v_mfma_f32_16x16x32_bf16 v[112:115], a[236:239], v[180:183], v[112:115]\n\t"
    "ds_read_b64_tr_b16 a[212:213], v19 offset:1024\n\t"
    "v_pk_add_f16 v36, v36, 1.0 op_sel_hi:[1,0]\n\t"
    "v_pk_add_f16 v37, v37, 1.0 op_sel_hi:[1,0]\n\t"
    "v_rcp_f16 v34, v36\n\t"
    "v_rcp_f16 v35, v37\n\t"
    "v_mfma_f32_16x16x32_bf16 v[116:119], a[236:239], v[204:207], v[116:119]\n\t"
    "ds_read_b64_tr_b16 a[214:215], v19 offset:7168\n\t"
    "v_rcp_f16_sdwa v34, v36 dst_sel:WORD_1 dst_unused:UNUSED_PRESERVE src0_sel:WORD_1\n\t"
    "v_rcp_f16_sdwa v35, v37 dst_sel:WORD_1 dst_unused:UNUSED_PRESERVE src0_sel:WORD_1\n\t"
    "v_mfma_f32_16x16x32_bf16 v[120:123], a[236:239], v[228:231], v[120:123]\n\t"
    "v_fma_mix_f32 v96, v96, v34, 0 op_sel_hi:[0,1,0]\n\t"
    "v_fma_mix_f32 v98, v98, v35, 0 op_sel_hi:[0,1,0]\n\t"
    "v_fma_mix_f32 v97, v97, v34, 0 op_sel:[0,1,0] op_sel_hi:[0,1,0]\n\t"
    "v_fma_mix_f32 v99, v99, v35, 0 op_sel:[0,1,0] op_sel_hi:[0,1,0]\n\t"
    "v_cvt_pk_bf16_f32 v144, v96, v97\n\t"
    "v_cvt_pk_bf16_f32 v145, v98, v99\n\t"
    "s_mov_b32 s69, 6\n\t"
    "s_mov_b32 s30, 49152\n\t"
    "s_branch P2_a_%=\n\t"
    ".p2align 6\n\t"
    "loop_a_%=:\n\t"
    "s_waitcnt lgkmcnt(4)\n\t"
    "v_mfma_f32_16x16x32_bf16 a[0:3], a[192:195], v[128:131], a[0:3]\n\t"
    "ds_read_b64_tr_b16 a[216:217], v20 offset:25600\n\t"
    "v_cvt_pk_f16_f32 v38, v100, v101\n\t"
    "v_cvt_pk_f16_f32 v39, v102, v103\n\t"
    "v_pk_fma_f16 v40, v38, v38, s60 neg_lo:[1,0,1] neg_hi:[1,0,1]\n\t"
    "v_mfma_f32_16x16x32_bf16 a[4:7], a[192:195], v[132:135], a[4:7]\n\t"
    "ds_read_b64_tr_b16 a[218:219], v20 offset:31744\n\t"
    "v_pk_fma_f16 v41, v39, v39, s60 neg_lo:[1,0,1] neg_hi:[1,0,1]\n\t"
    "v_pk_mul_f16 v40, v40, v38\n\t"
    "v_mfma_f32_16x16x32_bf16 a[8:11], a[192:195], v[136:139], a[8:11]\n\t"
    "v_pk_mul_f16 v41, v41, v39\n\t"
    "v_exp_f16 v42, v40\n\t"
    "v_mfma_f32_16x16x32_bf16 a[12:15], a[196:199], v[128:131], a[12:15]\n\t"
    "ds_read_b64_tr_b16 a[220:221], v21 offset:25600\n\t"
    "v_exp_f16 v43, v41\n\t"
    "v_exp_f16_sdwa v42, v40 dst_sel:WORD_1 dst_unused:UNUSED_PRESERVE src0_sel:WORD_1\n\t"
    "v_mfma_f32_16x16x32_bf16 a[16:19], a[196:199], v[132:135], a[16:19]\n\t"
    "ds_read_b64_tr_b16 a[222:223], v21 offset:31744\n\t"
    "v_exp_f16_sdwa v43, v41 dst_sel:WORD_1 dst_unused:UNUSED_PRESERVE src0_sel:WORD_1\n\t"
    "v_mfma_f32_16x16x32_bf16 a[20:23], a[196:199], v[136:139], a[20:23]\n\t"
    "v_pk_add_f16 v42, v42, 1.0 op_sel_hi:[1,0]\n\t"
    "v_pk_add_f16 v43, v43, 1.0 op_sel_hi:[1,0]\n\t"
    "v_rcp_f16 v40, v42\n\t"
    "v_mfma_f32_16x16x32_bf16 a[24:27], a[200:203], v[128:131], a[24:27]\n\t"
    "ds_read_b64_tr_b16 a[224:225], v18 offset:26624\n\t"
    "v_rcp_f16 v41, v43\n\t"
    "v_mfma_f32_16x16x32_bf16 a[28:31], a[200:203], v[132:135], a[28:31]\n\t"
    "ds_read_b64_tr_b16 a[226:227], v18 offset:32768\n\t"
    "v_rcp_f16_sdwa v40, v42 dst_sel:WORD_1 dst_unused:UNUSED_PRESERVE src0_sel:WORD_1\n\t"
    "v_mfma_f32_16x16x32_bf16 a[32:35], a[200:203], v[136:139], a[32:35]\n\t"
    "v_rcp_f16_sdwa v41, v43 dst_sel:WORD_1 dst_unused:UNUSED_PRESERVE src0_sel:WORD_1\n\t"
    "v_fma_mix_f32 v100, v100, v40, 0 op_sel_hi:[0,1,0]\n\t"
    "v_fma_mix_f32 v102, v102, v41, 0 op_sel_hi:[0,1,0]\n\t"
    "v_mfma_f32_16x16x32_bf16 a[36:39], a[204:207], v[128:131], a[36:39]\n\t"
    "ds_read_b64_tr_b16 a[228:229], v19 offset:26624\n\t"
    "v_fma_mix_f32 v101, v101, v40, 0 op_sel:[0,1,0] op_sel_hi:[0,1,0]\n\t"
    "v_fma_mix_f32 v103, v103, v41, 0 op_sel:[0,1,0] op_sel_hi:[0,1,0]\n\t"
    "v_mfma_f32_16x16x32_bf16 a[40:43], a[204:207], v[132:135], a[40:43]\n\t"
    "ds_read_b64_tr_b16 a[230:231], v19 offset:32768\n\t"
    "v_cvt_pk_bf16_f32 v148, v100, v101\n\t"
    "v_cvt_pk_bf16_f32 v149, v102, v103\n\t"
    "v_cvt_pk_f16_f32 v32, v104, v105\n\t"
    "v_mfma_f32_16x16x32_bf16 a[44:47], a[204:207], v[136:139], a[44:47]\n\t"
    "s_waitcnt vmcnt(0)\n\t"
    "s_barrier\n\t"
    "s_waitcnt lgkmcnt(4)\n\t"
    "v_mfma_f32_16x16x32_bf16 a[48:51], a[208:211], v[128:131], a[48:51]\n\t"
    "ds_read_b64_tr_b16 a[232:233], v20 offset:26624\n\t"
    "v_cvt_pk_f16_f32 v33, v106, v107\n\t"
    "v_pk_fma_f16 v34, v32, v32, s60 neg_lo:[1,0,1] neg_hi:[1,0,1]\n\t"
    "v_pk_fma_f16 v35, v33, v33, s60 neg_lo:[1,0,1] neg_hi:[1,0,1]\n\t"
    "v_mfma_f32_16x16x32_bf16 a[52:55], a[208:211], v[132:135], a[52:55]\n\t"
    "ds_read_b64_tr_b16 a[234:235], v20 offset:32768\n\t"
    "s_add_u32 m0, s67, 24576\n\t"
    "s_add_u32 s63, s65, 0\n\t"
    "v_mfma_f32_16x16x32_bf16 a[56:59], a[208:211], v[136:139], a[56:59]\n\t"
    "buffer_load_dwordx4 v26, s[40:43], s63 offen offset:0 lds\n\t"
    "v_pk_mul_f16 v34, v34, v32\n\t"
    "v_pk_mul_f16 v35, v35, v33\n\t"
    "v_mfma_f32_16x16x32_bf16 a[60:63], a[212:215], v[128:131], a[60:63]\n\t"
    "ds_read_b64_tr_b16 a[236:237], v21 offset:26624\n\t"
    "v_exp_f16 v36, v34\n\t"
    "v_mfma_f32_16x16x32_bf16 a[64:67], a[212:215], v[132:135], a[64:67]\n\t"
    "ds_read_b64_tr_b16 a[238:239], v21 offset:32768\n\t"
    "v_exp_f16 v37, v35\n\t"
    "v_mfma_f32_16x16x32_bf16 a[68:71], a[212:215], v[136:139], a[68:71]\n\t"
    "v_exp_f16_sdwa v36, v34 dst_sel:WORD_1 dst_unused:UNUSED_PRESERVE src0_sel:WORD_1\n\t"
    "v_exp_f16_sdwa v37, v35 dst_sel:WORD_1 dst_unused:UNUSED_PRESERVE src0_sel:WORD_1\n\t"
    "v_mfma_f32_16x16x32_bf16 a[72:75], a[216:219], v[128:131], a[72:75]\n\t"
    "ds_read_b64_tr_b16 a[192:193], v14 offset:0\n\t"
    "v_pk_add_f16 v36, v36, 1.0 op_sel_hi:[1,0]\n\t"
    "v_pk_add_f16 v37, v37, 1.0 op_sel_hi:[1,0]\n\t"
    "v_mfma_f32_16x16x32_bf16 a[76:79], a[216:219], v[132:135], a[76:79]\n\t"
    "ds_read_b64_tr_b16 a[194:195], v14 offset:2048\n\t"
    "v_rcp_f16 v34, v36\n\t"
    "v_rcp_f16 v35, v37\n\t"
    "v_mfma_f32_16x16x32_bf16 a[80:83], a[216:219], v[136:139], a[80:83]\n\t"
    "buffer_load_dwordx4 v27, s[40:43], s63 offen offset:1024 lds\n\t"
    "v_mfma_f32_16x16x32_bf16 a[84:87], a[220:223], v[128:131], a[84:87]\n\t"
    "ds_read_b64_tr_b16 a[196:197], v14 offset:4096\n\t"
    "v_rcp_f16_sdwa v34, v36 dst_sel:WORD_1 dst_unused:UNUSED_PRESERVE src0_sel:WORD_1\n\t"
    "v_rcp_f16_sdwa v35, v37 dst_sel:WORD_1 dst_unused:UNUSED_PRESERVE src0_sel:WORD_1\n\t"
    "v_mfma_f32_16x16x32_bf16 a[88:91], a[220:223], v[132:135], a[88:91]\n\t"
    "ds_read_b64_tr_b16 a[198:199], v14 offset:6144\n\t"
    "v_fma_mix_f32 v104, v104, v34, 0 op_sel_hi:[0,1,0]\n\t"
    "v_fma_mix_f32 v106, v106, v35, 0 op_sel_hi:[0,1,0]\n\t"
    "v_mfma_f32_16x16x32_bf16 a[92:95], a[220:223], v[136:139], a[92:95]\n\t"
    "ds_read_b128 v[52:55], v22 offset:64\n\t"
    "v_fma_mix_f32 v105, v105, v34, 0 op_sel:[0,1,0] op_sel_hi:[0,1,0]\n\t"
    "s_waitcnt lgkmcnt(5)\n\t"
    "v_mfma_f32_16x16x32_bf16 a[96:99], a[224:227], v[128:131], a[96:99]\n\t"
    "ds_read_b64_tr_b16 a[200:201], v14 offset:8192\n\t"
    "v_fma_mix_f32 v107, v107, v35, 0 op_sel:[0,1,0] op_sel_hi:[0,1,0]\n\t"
    "v_cvt_pk_bf16_f32 v152, v104, v105\n\t"
    "v_cvt_pk_bf16_f32 v153, v106, v107\n\t"
    "v_mfma_f32_16x16x32_bf16 a[100:103], a[224:227], v[132:135], a[100:103]\n\t"
    "ds_read_b64_tr_b16 a[202:203], v14 offset:10240\n\t"
    "v_cvt_pk_f16_f32 v38, v112, v113\n\t"
    "v_cvt_pk_f16_f32 v39, v114, v115\n\t"
    "v_mfma_f32_16x16x32_bf16 a[104:107], a[224:227], v[136:139], a[104:107]\n\t"
    "buffer_load_dwordx4 v28, s[40:43], s63 offen offset:2048 lds\n\t"
    "v_pk_fma_f16 v40, v38, v38, s60 neg_lo:[1,0,1] neg_hi:[1,0,1]\n\t"
    "v_pk_fma_f16 v41, v39, v39, s60 neg_lo:[1,0,1] neg_hi:[1,0,1]\n\t"
    "v_mfma_f32_16x16x32_bf16 a[108:111], a[228:231], v[128:131], a[108:111]\n\t"
    "ds_read_b64_tr_b16 a[204:205], v14 offset:12288\n\t"
    "v_pk_mul_f16 v40, v40, v38\n\t"
    "v_pk_mul_f16 v41, v41, v39\n\t"
    "v_mfma_f32_16x16x32_bf16 a[112:115], a[228:231], v[132:135], a[112:115]\n\t"
    "ds_read_b64_tr_b16 a[206:207], v14 offset:14336\n\t"
    "v_exp_f16 v42, v40\n\t"
    "v_mfma_f32_16x16x32_bf16 a[116:119], a[228:231], v[136:139], a[116:119]\n\t"
    "ds_read_b128 v[48:51], v22 offset:0\n\t"
    "v_exp_f16 v43, v41\n\t"
    "v_exp_f16_sdwa v42, v40 dst_sel:WORD_1 dst_unused:UNUSED_PRESERVE src0_sel:WORD_1\n\t"
    "v_mfma_f32_16x16x32_bf16 a[120:123], a[232:235], v[128:131], a[120:123]\n\t"
    "ds_read_b64_tr_b16 a[208:209], v14 offset:16384\n\t"
    "v_exp_f16_sdwa v43, v41 dst_sel:WORD_1 dst_unused:UNUSED_PRESERVE src0_sel:WORD_1\n\t"
    "v_mfma_f32_16x16x32_bf16 a[124:127], a[232:235], v[132:135], a[124:127]\n\t"
    "ds_read_b64_tr_b16 a[210:211], v14 offset:18432\n\t"
    "v_pk_add_f16 v42, v42, 1.0 op_sel_hi:[1,0]\n\t"
    "s_add_u32 m0, s68, 12288\n\t"
    "v_mfma_f32_16x16x32_bf16 a[128:131], a[232:235], v[136:139], a[128:131]\n\t"
    "buffer_load_dwordx4 v29, s[44:47], s66 offen offset:0 lds\n\t"
    "v_pk_add_f16 v43, v43, 1.0 op_sel_hi:[1,0]\n\t"
    "v_mfma_f32_16x16x32_bf16 a[132:135], a[236:239], v[128:131], a[132:135]\n\t"
    "ds_read_b64_tr_b16 a[212:213], v14 offset:20480\n\t"
    "v_rcp_f16 v40, v42\n\t"
    "v_rcp_f16 v41, v43\n\t"
    "v_mfma_f32_16x16x32_bf16 a[136:139], a[236:239], v[132:135], a[136:139]\n\t"
    "ds_read_b64_tr_b16 a[214:215], v14 offset:22528\n\t"
    "v_rcp_f16_sdwa v40, v42 dst_sel:WORD_1 dst_unused:UNUSED_PRESERVE src0_sel:WORD_1\n\t"
    "v_mfma_f32_16x16x32_bf16 a[140:143], a[236:239], v[136:139], a[140:143]\n\t"
    "v_rcp_f16_sdwa v41, v43 dst_sel:WORD_1 dst_unused:UNUSED_PRESERVE src0_sel:WORD_1\n\t"
    "v_fma_mix_f32 v112, v112, v40, 0 op_sel_hi:[0,1,0]\n\t"
    "s_waitcnt lgkmcnt(4)\n\t"
    "v_mfma_f32_16x16x32_bf16 v[64:67], a[192:195], v[160:163], v[48:51]\n\t"
    "ds_read_b64_tr_b16 a[216:217], v15 offset:0\n\t"
    "v_fma_mix_f32 v114, v114, v41, 0 op_sel_hi:[0,1,0]\n\t"
    "v_fma_mix_f32 v113, v113, v40, 0 op_sel:[0,1,0] op_sel_hi:[0,1,0]\n\t"
    "v_mfma_f32_16x16x32_bf16 v[68:71], a[192:195], v[184:187], v[48:51]\n\t"
    "ds_read_b64_tr_b16 a[218:219], v15 offset:2048\n\t"
    "v_fma_mix_f32 v115, v115, v41, 0 op_sel:[0,1,0] op_sel_hi:[0,1,0]\n\t"
    "v_cvt_pk_bf16_f32 v146, v112, v113\n\t"
    "v_cvt_pk_bf16_f32 v147, v114, v115\n\t"
    "v_mfma_f32_16x16x32_bf16 v[72:75], a[192:195], v[208:211], v[48:51]\n\t"
    "buffer_load_dwordx4 v30, s[44:47], s66 offen offset:1024 lds\n\t"
    "v_cvt_pk_f16_f32 v32, v116, v117\n\t"
    "v_mfma_f32_16x16x32_bf16 v[64:67], a[196:199], v[164:167], v[64:67]\n\t"
    "ds_read_b64_tr_b16 a[220:221], v15 offset:4096\n\t"
    "v_cvt_pk_f16_f32 v33, v118, v119\n\t"
    "v_pk_fma_f16 v34, v32, v32, s60 neg_lo:[1,0,1] neg_hi:[1,0,1]\n\t"
    "v_pk_fma_f16 v35, v33, v33, s60 neg_lo:[1,0,1] neg_hi:[1,0,1]\n\t"
    "v_mfma_f32_16x16x32_bf16 v[68:71], a[196:199], v[188:191], v[68:71]\n\t"
    "ds_read_b64_tr_b16 a[222:223], v15 offset:6144\n\t"
    "v_pk_mul_f16 v34, v34, v32\n\t"
    "v_pk_mul_f16 v35, v35, v33\n\t"
    "v_mfma_f32_16x16x32_bf16 v[72:75], a[196:199], v[212:215], v[72:75]\n\t"
    "v_exp_f16 v36, v34\n\t"
    "v_exp_f16 v37, v35\n\t"
    "v_mfma_f32_16x16x32_bf16 v[64:67], a[200:203], v[168:171], v[64:67]\n\t"
    "ds_read_b64_tr_b16 a[224:225], v15 offset:8192\n\t"
    "v_exp_f16_sdwa v36, v34 dst_sel:WORD_1 dst_unused:UNUSED_PRESERVE src0_sel:WORD_1\n\t"
    "v_mfma_f32_16x16x32_bf16 v[68:71], a[200:203], v[192:195], v[68:71]\n\t"
    "ds_read_b64_tr_b16 a[226:227], v15 offset:10240\n\t"
    "v_exp_f16_sdwa v37, v35 dst_sel:WORD_1 dst_unused:UNUSED_PRESERVE src0_sel:WORD_1\n\t"
    "v_pk_add_f16 v36, v36, 1.0 op_sel_hi:[1,0]\n\t"
    "v_mfma_f32_16x16x32_bf16 v[72:75], a[200:203], v[216:219], v[72:75]\n\t"
    "buffer_load_dwordx4 v31, s[44:47], s66 offen offset:2048 lds\n\t"
    "v_pk_add_f16 v37, v37, 1.0 op_sel_hi:[1,0]\n\t"
    "v_mfma_f32_16x16x32_bf16 v[64:67], a[204:207], v[172:175], v[64:67]\n\t"
    "ds_read_b64_tr_b16 a[228:229], v15 offset:12288\n\t"
    "v_rcp_f16 v34, v36\n\t"
    "v_mfma_f32_16x16x32_bf16 v[68:71], a[204:207], v[196:199], v[68:71]\n\t"
    "ds_read_b64_tr_b16 a[230:231], v15 offset:14336\n\t"
    "v_rcp_f16 v35, v37\n\t"
    "v_rcp_f16_sdwa v34, v36 dst_sel:WORD_1 dst_unused:UNUSED_PRESERVE src0_sel:WORD_1\n\t"
    "v_mfma_f32_16x16x32_bf16 v[72:75], a[204:207], v[220:223], v[72:75]\n\t"
    "v_rcp_f16_sdwa v35, v37 dst_sel:WORD_1 dst_unused:UNUSED_PRESERVE src0_sel:WORD_1\n\t"
    "s_waitcnt lgkmcnt(4)\n\t"
    "v_mfma_f32_16x16x32_bf16 v[64:67], a[208:211], v[176:179], v[64:67]\n\t"
    "ds_read_b64_tr_b16 a[232:233], v15 offset:16384\n\t"
    "v_fma_mix_f32 v116, v116, v34, 0 op_sel_hi:[0,1,0]\n\t"
    "v_fma_mix_f32 v118, v118, v35, 0 op_sel_hi:[0,1,0]\n\t"
    "v_mfma_f32_16x16x32_bf16 v[68:71], a[208:211], v[200:203], v[68:71]\n\t"
    "ds_read_b64_tr_b16 a[234:235], v15 offset:18432\n\t"
    "v_fma_mix_f32 v117, v117, v34, 0 op_sel:[0,1,0] op_sel_hi:[0,1,0]\n\t"
    "v_fma_mix_f32 v119, v119, v35, 0 op_sel:[0,1,0] op_sel_hi:[0,1,0]\n\t"
    "s_add_u32 s66, s66, 12288\n\t"
    "v_mfma_f32_16x16x32_bf16 v[72:75], a[208:211], v[224:227], v[72:75]\n\t"
    "v_cvt_pk_bf16_f32 v150, v116, v117\n\t"
    "v_cvt_pk_bf16_f32 v151, v118, v119\n\t"
    "v_cvt_pk_f16_f32 v38, v120, v121\n\t"
    "v_mfma_f32_16x16x32_bf16 v[64:67], a[212:215], v[180:183], v[64:67]\n\t"
    "ds_read_b64_tr_b16 a[236:237], v15 offset:20480\n\t"
    "v_cvt_pk_f16_f32 v39, v122, v123\n\t"
    "v_pk_fma_f16 v40, v38, v38, s60 neg_lo:[1,0,1] neg_hi:[1,0,1]\n\t"
    "v_pk_fma_f16 v41, v39, v39, s60 neg_lo:[1,0,1] neg_hi:[1,0,1]\n\t"
    "v_mfma_f32_16x16x32_bf16 v[68:71], a[212:215], v[204:207], v[68:71]\n\t"
    "ds_read_b64_tr_b16 a[238:239], v15 offset:22528\n\t"
    "v_pk_mul_f16 v40, v40, v38\n\t"
    "v_pk_mul_f16 v41, v41, v39\n\t"
    "v_mfma_f32_16x16x32_bf16 v[72:75], a[212:215], v[228:231], v[72:75]\n\t"
    "v_exp_f16 v42, v40\n\t"
    "v_exp_f16 v43, v41\n\t"
    "v_mfma_f32_16x16x32_bf16 v[80:83], a[216:219], v[160:163], v[52:55]\n\t"
    "ds_read_b64_tr_b16 a[192:193], v18 offset:36864\n\t"
    "v_exp_f16_sdwa v42, v40 dst_sel:WORD_1 dst_unused:UNUSED_PRESERVE src0_sel:WORD_1\n\t"
    "v_mfma_f32_16x16x32_bf16 v[84:87], a[216:219], v[184:187], v[52:55]\n\t"
    "ds_read_b64_tr_b16 a[194:195], v18 offset:43008\n\t"
    "v_exp_f16_sdwa v43, v41 dst_sel:WORD_1 dst_unused:UNUSED_PRESERVE src0_sel:WORD_1\n\t"
    "v_pk_add_f16 v42, v42, 1.0 op_sel_hi:[1,0]\n\t"
    "v_mfma_f32_16x16x32_bf16 v[88:91], a[216:219], v[208:211], v[52:55]\n\t"
    "v_pk_add_f16 v43, v43, 1.0 op_sel_hi:[1,0]\n\t"
    "v_rcp_f16 v40, v42\n\t"
    "v_mfma_f32_16x16x32_bf16 v[80:83], a[220:223], v[164:167], v[80:83]\n\t"
    "ds_read_b64_tr_b16 a[196:197], v19 offset:36864\n\t"
    "v_rcp_f16 v41, v43\n\t"
    "v_mfma_f32_16x16x32_bf16 v[84:87], a[220:223], v[188:191], v[84:87]\n\t"
    "ds_read_b64_tr_b16 a[198:199], v19 offset:43008\n\t"
    "v_rcp_f16_sdwa v40, v42 dst_sel:WORD_1 dst_unused:UNUSED_PRESERVE src0_sel:WORD_1\n\t"
    "v_rcp_f16_sdwa v41, v43 dst_sel:WORD_1 dst_unused:UNUSED_PRESERVE src0_sel:WORD_1\n\t"
    "v_mfma_f32_16x16x32_bf16 v[88:91], a[220:223], v[212:215], v[88:91]\n\t"
    "v_fma_mix_f32 v120, v120, v40, 0 op_sel_hi:[0,1,0]\n\t"
    "v_fma_mix_f32 v122, v122, v41, 0 op_sel_hi:[0,1,0]\n\t"
    "s_waitcnt lgkmcnt(4)\n\t"
    "v_mfma_f32_16x16x32_bf16 v[80:83], a[224:227], v[168:171], v[80:83]\n\t"
    "ds_read_b64_tr_b16 a[200:201], v20 offset:36864\n\t"
    "v_fma_mix_f32 v121, v121, v40, 0 op_sel:[0,1,0] op_sel_hi:[0,1,0]\n\t"
    "v_fma_mix_f32 v123, v123, v41, 0 op_sel:[0,1,0] op_sel_hi:[0,1,0]\n\t"
    "v_mfma_f32_16x16x32_bf16 v[84:87], a[224:227], v[192:195], v[84:87]\n\t"
    "ds_read_b64_tr_b16 a[202:203], v20 offset:43008\n\t"
    "v_cvt_pk_bf16_f32 v154, v120, v121\n\t"
    "v_cvt_pk_bf16_f32 v155, v122, v123\n\t"
    "v_cvt_pk_f16_f32 v32, v64, v65\n\t"
    "v_mfma_f32_16x16x32_bf16 v[88:91], a[224:227], v[216:219], v[88:91]\n\t"
    "v_cvt_pk_f16_f32 v33, v66, v67\n\t"
    "v_pk_fma_f16 v34, v32, v32, s60 neg_lo:[1,0,1] neg_hi:[1,0,1]\n\t"
    "v_pk_fma_f16 v35, v33, v33, s60 neg_lo:[1,0,1] neg_hi:[1,0,1]\n\t"
    "v_mfma_f32_16x16x32_bf16 v[80:83], a[228:231], v[172:175], v[80:83]\n\t"
    "ds_read_b64_tr_b16 a[204:205], v21 offset:36864\n\t"
    "v_pk_mul_f16 v34, v34, v32\n\t"
    "v_pk_mul_f16 v35, v35, v33\n\t"
    "v_mfma_f32_16x16x32_bf16 v[84:87], a[228:231], v[196:199], v[84:87]\n\t"
    "ds_read_b64_tr_b16 a[206:207], v21 offset:43008\n\t"
    "v_exp_f16 v36, v34\n\t"
    "v_exp_f16 v37, v35\n\t"
    "v_mfma_f32_16x16x32_bf16 v[88:91], a[228:231], v[220:223], v[88:91]\n\t"
    "v_exp_f16_sdwa v36, v34 dst_sel:WORD_1 dst_unused:UNUSED_PRESERVE src0_sel:WORD_1\n\t"
    "v_mfma_f32_16x16x32_bf16 v[80:83], a[232:235], v[176:179], v[80:83]\n\t"
    "ds_read_b64_tr_b16 a[208:209], v18 offset:37888\n\t"
    "v_exp_f16_sdwa v37, v35 dst_sel:WORD_1 dst_unused:UNUSED_PRESERVE src0_sel:WORD_1\n\t"
    "v_pk_add_f16 v36, v36, 1.0 op_sel_hi:[1,0]\n\t"
    "v_mfma_f32_16x16x32_bf16 v[84:87], a[232:235], v[200:203], v[84:87]\n\t"
    "ds_read_b64_tr_b16 a[210:211], v18 offset:44032\n\t"
    "v_pk_add_f16 v37, v37, 1.0 op_sel_hi:[1,0]\n\t"
    "v_rcp_f16 v34, v36\n\t"
    "v_mfma_f32_16x16x32_bf16 v[88:91], a[232:235], v[224:227], v[88:91]\n\t"
    "v_rcp_f16 v35, v37\n\t"
    "v_rcp_f16_sdwa v34, v36 dst_sel:WORD_1 dst_unused:UNUSED_PRESERVE src0_sel:WORD_1\n\t"
    "v_mfma_f32_16x16x32_bf16 v[80:83], a[236:239], v[180:183], v[80:83]\n\t"
    "ds_read_b64_tr_b16 a[212:213], v19 offset:37888\n\t"
    "v_rcp_f16_sdwa v35, v37 dst_sel:WORD_1 dst_unused:UNUSED_PRESERVE src0_sel:WORD_1\n\t"
    "v_mfma_f32_16x16x32_bf16 v[84:87], a[236:239], v[204:207], v[84:87]\n\t"
    "ds_read_b64_tr_b16 a[214:215], v19 offset:44032\n\t"
    "v_fma_mix_f32 v64, v64, v34, 0 op_sel_hi:[0,1,0]\n\t"
    "v_fma_mix_f32 v66, v66, v35, 0 op_sel_hi:[0,1,0]\n\t"
    "v_mfma_f32_16x16x32_bf16 v[88:91], a[236:239], v[228:231], v[88:91]\n\t"
    "v_fma_mix_f32 v65, v65, v34, 0 op_sel:[0,1,0] op_sel_hi:[0,1,0]\n\t"
    "v_fma_mix_f32 v67, v67, v35, 0 op_sel:[0,1,0] op_sel_hi:[0,1,0]\n\t"
    "v_cvt_pk_bf16_f32 v128, v64, v65\n\t"
    "v_cvt_pk_bf16_f32 v129, v66, v67\n\t"
    "s_waitcnt lgkmcnt(4)\n\t"
    "v_mfma_f32_16x16x32_bf16 a[0:3], a[192:195], v[144:147], a[0:3]\n\t"
    "ds_read_b64_tr_b16 a[216:217], v20 offset:37888\n\t"
    "v_cvt_pk_f16_f32 v38, v68, v69\n\t"
    "v_cvt_pk_f16_f32 v39, v70, v71\n\t"
    "v_pk_fma_f16 v40, v38, v38, s60 neg_lo:[1,0,1] neg_hi:[1,0,1]\n\t"
    "v_mfma_f32_16x16x32_bf16 a[4:7], a[192:195], v[148:151], a[4:7]\n\t"
    "ds_read_b64_tr_b16 a[218:219], v20 offset:44032\n\t"
    "s_add_u32 m0, s67, 36864\n\t"
    "s_add_u32 s63, s65, 147456\n\t"
    "v_mfma_f32_16x16x32_bf16 a[8:11], a[192:195], v[152:155], a[8:11]\n\t"
    "buffer_load_dwordx4 v26, s[40:43], s63 offen offset:0 lds\n\t"
    "v_pk_fma_f16 v41, v39, v39, s60 neg_lo:[1,0,1] neg_hi:[1,0,1]\n\t"
    "v_pk_mul_f16 v40, v40, v38\n\t"
    "v_mfma_f32_16x16x32_bf16 a[12:15], a[196:199], v[144:147], a[12:15]\n\t"
    "ds_read_b64_tr_b16 a[220:221], v21 offset:37888\n\t"
    "v_pk_mul_f16 v41, v41, v39\n\t"
    "v_exp_f16 v42, v40\n\t"
    "v_mfma_f32_16x16x32_bf16 a[16:19], a[196:199], v[148:151], a[16:19]\n\t"
    "ds_read_b64_tr_b16 a[222:223], v21 offset:44032\n\t"
    "v_exp_f16 v43, v41\n\t"
    "v_mfma_f32_16x16x32_bf16 a[20:23], a[196:199], v[152:155], a[20:23]\n\t"
    "buffer_load_dwordx4 v27, s[40:43], s63 offen offset:1024 lds\n\t"
    "v_exp_f16_sdwa v42, v40 dst_sel:WORD_1 dst_unused:UNUSED_PRESERVE src0_sel:WORD_1\n\t"
    "v_mfma_f32_16x16x32_bf16 a[24:27], a[200:203], v[144:147], a[24:27]\n\t"
    "ds_read_b64_tr_b16 a[224:225], v18 offset:38912\n\t"
    "v_exp_f16_sdwa v43, v41 dst_sel:WORD_1 dst_unused:UNUSED_PRESERVE src0_sel:WORD_1\n\t"
    "v_mfma_f32_16x16x32_bf16 a[28:31], a[200:203], v[148:151], a[28:31]\n\t"
    "ds_read_b64_tr_b16 a[226:227], v18 offset:45056\n\t"
    "v_pk_add_f16 v42, v42, 1.0 op_sel_hi:[1,0]\n\t"
    "v_pk_add_f16 v43, v43, 1.0 op_sel_hi:[1,0]\n\t"
    "v_mfma_f32_16x16x32_bf16 a[32:35], a[200:203], v[152:155], a[32:35]\n\t"
    "buffer_load_dwordx4 v28, s[40:43], s63 offen offset:2048 lds\n\t"
    "v_rcp_f16 v40, v42\n\t"
    "v_mfma_f32_16x16x32_bf16 a[36:39], a[204:207], v[144:147], a[36:39]\n\t"
    "ds_read_b64_tr_b16 a[228:229], v19 offset:38912\n\t"
    "v_rcp_f16 v41, v43\n\t"
    "v_mfma_f32_16x16x32_bf16 a[40:43], a[204:207], v[148:151], a[40:43]\n\t"
    "ds_read_b64_tr_b16 a[230:231], v19 offset:45056\n\t"
    "v_rcp_f16_sdwa v40, v42 dst_sel:WORD_1 dst_unused:UNUSED_PRESERVE src0_sel:WORD_1\n\t"
    "s_add_u32 s65, s65, 128\n\t"
    "s_add_u32 m0, s68, 24576\n\t"
    "v_mfma_f32_16x16x32_bf16 a[44:47], a[204:207], v[152:155], a[44:47]\n\t"
    "buffer_load_dwordx4 v29, s[44:47], s66 offen offset:0 lds\n\t"
    "s_waitcnt lgkmcnt(4)\n\t"
    "v_mfma_f32_16x16x32_bf16 a[48:51], a[208:211], v[144:147], a[48:51]\n\t"
    "ds_read_b64_tr_b16 a[232:233], v20 offset:38912\n\t"
    "v_rcp_f16_sdwa v41, v43 dst_sel:WORD_1 dst_unused:UNUSED_PRESERVE src0_sel:WORD_1\n\t"
    "v_mfma_f32_16x16x32_bf16 a[52:55], a[208:211], v[148:151], a[52:55]\n\t"
    "ds_read_b64_tr_b16 a[234:235], v20 offset:45056\n\t"
    "v_fma_mix_f32 v68, v68, v40, 0 op_sel_hi:[0,1,0]\n\t"
    "v_fma_mix_f32 v70, v70, v41, 0 op_sel_hi:[0,1,0]\n\t"
    "v_fma_mix_f32 v69, v69, v40, 0 op_sel:[0,1,0] op_sel_hi:[0,1,0]\n\t"
    "v_mfma_f32_16x16x32_bf16 a[56:59], a[208:211], v[152:155], a[56:59]\n\t"
    "buffer_load_dwordx4 v30, s[44:47], s66 offen offset:1024 lds\n\t"
    "v_fma_mix_f32 v71, v71, v41, 0 op_sel:[0,1,0] op_sel_hi:[0,1,0]\n\t"
    "v_mfma_f32_16x16x32_bf16 a[60:63], a[212:215], v[144:147], a[60:63]\n\t"
    "ds_read_b64_tr_b16 a[236:237], v21 offset:38912\n\t"
    "v_cvt_pk_bf16_f32 v132, v68, v69\n\t"
    "v_cvt_pk_bf16_f32 v133, v70, v71\n\t"
    "v_cvt_pk_f16_f32 v32, v72, v73\n\t"
    "v_mfma_f32_16x16x32_bf16 a[64:67], a[212:215], v[148:151], a[64:67]\n\t"
    "ds_read_b64_tr_b16 a[238:239], v21 offset:45056\n\t"
    "v_cvt_pk_f16_f32 v33, v74, v75\n\t"
    "v_pk_fma_f16 v34, v32, v32, s60 neg_lo:[1,0,1] neg_hi:[1,0,1]\n\t"
    "v_mfma_f32_16x16x32_bf16 a[68:71], a[212:215], v[152:155], a[68:71]\n\t"
    "buffer_load_dwordx4 v31, s[44:47], s66 offen offset:2048 lds\n\t"
    "v_pk_fma_f16 v35, v33, v33, s60 neg_lo:[1,0,1] neg_hi:[1,0,1]\n\t"
    "v_pk_mul_f16 v34, v34, v32\n\t"
    "v_mfma_f32_16x16x32_bf16 a[72:75], a[216:219], v[144:147], a[72:75]\n\t"
    "ds_read_b64_tr_b16 a[192:193], v16 offset:0\n\t"
    "v_pk_mul_f16 v35, v35, v33\n\t"
    "v_exp_f16 v36, v34\n\t"
    "v_mfma_f32_16x16x32_bf16 a[76:79], a[216:219], v[148:151], a[76:79]\n\t"
    "ds_read_b64_tr_b16 a[194:195], v16 offset:2048\n\t"
    "v_exp_f16 v37, v35\n\t"
    "s_add_u32 s66, s66, 12288\n\t"
    "v_mfma_f32_16x16x32_bf16 a[80:83], a[216:219], v[152:155], a[80:83]\n\t"
    "v_exp_f16_sdwa v36, v34 dst_sel:WORD_1 dst_unused:UNUSED_PRESERVE src0_sel:WORD_1\n\t"
    "v_mfma_f32_16x16x32_bf16 a[84:87], a[220:223], v[144:147], a[84:87]\n\t"
    "ds_read_b64_tr_b16 a[196:197], v16 offset:4096\n\t"
    "v_exp_f16_sdwa v37, v35 dst_sel:WORD_1 dst_unused:UNUSED_PRESERVE src0_sel:WORD_1\n\t"
    "v_pk_add_f16 v36, v36, 1.0 op_sel_hi:[1,0]\n\t"
    "v_mfma_f32_16x16x32_bf16 a[88:91], a[220:223], v[148:151], a[88:91]\n\t"
    "ds_read_b64_tr_b16 a[198:199], v16 offset:6144\n\t"
    "v_pk_add_f16 v37, v37, 1.0 op_sel_hi:[1,0]\n\t"
    "v_rcp_f16 v34, v36\n\t"
    "v_mfma_f32_16x16x32_bf16 a[92:95], a[220:223], v[152:155], a[92:95]\n\t"
    "ds_read_b128 v[52:55], v22 offset:192\n\t"
    "v_rcp_f16 v35, v37\n\t"
    "s_waitcnt lgkmcnt(5)\n\t"
    "v_mfma_f32_16x16x32_bf16 a[96:99], a[224:227], v[144:147], a[96:99]\n\t"
    "ds_read_b64_tr_b16 a[200:201], v16 offset:8192\n\t"
    "v_rcp_f16_sdwa v34, v36 dst_sel:WORD_1 dst_unused:UNUSED_PRESERVE src0_sel:WORD_1\n\t"
    "v_mfma_f32_16x16x32_bf16 a[100:103], a[224:227], v[148:151], a[100:103]\n\t"
    "ds_read_b64_tr_b16 a[202:203], v16 offset:10240\n\t"
    "v_rcp_f16_sdwa v35, v37 dst_sel:WORD_1 dst_unused:UNUSED_PRESERVE src0_sel:WORD_1\n\t"
    "v_mfma_f32_16x16x32_bf16 a[104:107], a[224:227], v[152:155], a[104:107]\n\t"
    "v_fma_mix_f32 v72, v72, v34, 0 op_sel_hi:[0,1,0]\n\t"
    "v_fma_mix_f32 v74, v74, v35, 0 op_sel_hi:[0,1,0]\n\t"
    "v_fma_mix_f32 v73, v73, v34, 0 op_sel:[0,1,0] op_sel_hi:[0,1,0]\n\t"
    "v_fma_mix_f32 v75, v75, v35, 0 op_sel:[0,1,0] op_sel_hi:[0,1,0]\n\t"
    "v_mfma_f32_16x16x32_bf16 a[108:111], a[228:231], v[144:147], a[108:111]\n\t"
    "ds_read_b64_tr_b16 a[204:205], v16 offset:12288\n\t"
    "v_cvt_pk_bf16_f32 v136, v72, v73\n\t"
    "v_cvt_pk_bf16_f32 v137, v74, v75\n\t"
    "v_cvt_pk_f16_f32 v38, v80, v81\n\t"
    "v_mfma_f32_16x16x32_bf16 a[112:115], a[228:231], v[148:151], a[112:115]\n\t"
    "ds_read_b64_tr_b16 a[206:207], v16 offset:14336\n\t"
    "v_cvt_pk_f16_f32 v39, v82, v83\n\t"
    "v_pk_fma_f16 v40, v38, v38, s60 neg_lo:[1,0,1] neg_hi:[1,0,1]\n\t"
    "v_mfma_f32_16x16x32_bf16 a[116:119], a[228:231], v[152:155], a[116:119]\n\t"
    "ds_read_b128 v[48:51], v22 offset:128\n\t"
    "v_pk_fma_f16 v41, v39, v39, s60 neg_lo:[1,0,1] neg_hi:[1,0,1]\n\t"
    "v_pk_mul_f16 v40, v40, v38\n\t"
    "v_pk_mul_f16 v41, v41, v39\n\t"
    "v_mfma_f32_16x16x32_bf16 a[120:123], a[232:235], v[144:147], a[120:123]\n\t"
    "ds_read_b64_tr_b16 a[208:209], v16 offset:16384\n\t"
    "v_exp_f16 v42, v40\n\t"
    "v_mfma_f32_16x16x32_bf16 a[124:127], a[232:235], v[148:151], a[124:127]\n\t"
    "ds_read_b64_tr_b16 a[210:211], v16 offset:18432\n\t"
    "v_exp_f16 v43, v41\n\t"
    "v_mfma_f32_16x16x32_bf16 a[128:131], a[232:235], v[152:155], a[128:131]\n\t"
    "v_exp_f16_sdwa v42, v40 dst_sel:WORD_1 dst_unused:UNUSED_PRESERVE src0_sel:WORD_1\n\t"
    "v_exp_f16_sdwa v43, v41 dst_sel:WORD_1 dst_unused:UNUSED_PRESERVE src0_sel:WORD_1\n\t"
    "v_mfma_f32_16x16x32_bf16 a[132:135], a[236:239], v[144:147], a[132:135]\n\t"
    "ds_read_b64_tr_b16 a[212:213], v16 offset:20480\n\t"
    "v_pk_add_f16 v42, v42, 1.0 op_sel_hi:[1,0]\n\t"
    "v_pk_add_f16 v43, v43, 1.0 op_sel_hi:[1,0]\n\t"
    "v_mfma_f32_16x16x32_bf16 a[136:139], a[236:239], v[148:151], a[136:139]\n\t"
    "ds_read_b64_tr_b16 a[214:215], v16 offset:22528\n\t"
    "v_rcp_f16 v40, v42\n\t"
    "v_rcp_f16 v41, v43\n\t"
    "v_mfma_f32_16x16x32_bf16 a[140:143], a[236:239], v[152:155], a[140:143]\n\t"
    "v_rcp_f16_sdwa v40, v42 dst_sel:WORD_1 dst_unused:UNUSED_PRESERVE src0_sel:WORD_1\n\t"
    "s_waitcnt lgkmcnt(4)\n\t"
    "v_mfma_f32_16x16x32_bf16 v[96:99], a[192:195], v[160:163], v[48:51]\n\t"
    "ds_read_b64_tr_b16 a[216:217], v17 offset:0\n\t"
    "v_rcp_f16_sdwa v41, v43 dst_sel:WORD_1 dst_unused:UNUSED_PRESERVE src0_sel:WORD_1\n\t"
    "v_fma_mix_f32 v80, v80, v40, 0 op_sel_hi:[0,1,0]\n\t"
    "v_mfma_f32_16x16x32_bf16 v[100:103], a[192:195], v[184:187], v[48:51]\n\t"
    "ds_read_b64_tr_b16 a[218:219], v17 offset:2048\n\t"
    "v_fma_mix_f32 v82, v82, v41, 0 op_sel_hi:[0,1,0]\n\t"
    "v_fma_mix_f32 v81, v81, v40, 0 op_sel:[0,1,0] op_sel_hi:[0,1,0]\n\t"
    "v_mfma_f32_16x16x32_bf16 v[104:107], a[192:195], v[208:211], v[48:51]\n\t"
    "v_fma_mix_f32 v83, v83, v41, 0 op_sel:[0,1,0] op_sel_hi:[0,1,0]\n\t"
    "v_cvt_pk_bf16_f32 v130, v80, v81\n\t"
    "v_cvt_pk_bf16_f32 v131, v82, v83\n\t"
    "v_subrev_u32 v18, s30, v18\n\t"
    "v_mfma_f32_16x16x32_bf16 v[96:99], a[196:199], v[164:167], v[96:99]\n\t"
    "ds_read_b64_tr_b16 a[220:221], v17 offset:4096\n\t"
    "v_subrev_u32 v19, s30, v19\n\t"
    "v_subrev_u32 v20, s30, v20\n\t"
    "v_mfma_f32_16x16x32_bf16 v[100:103], a[196:199], v[188:191], v[100:103]\n\t"
    "ds_read_b64_tr_b16 a[222:223], v17 offset:6144\n\t"
    "v_subrev_u32 v21, s30, v21\n\t"
    "v_cvt_pk_f16_f32 v32, v84, v85\n\t"
    "v_cvt_pk_f16_f32 v33, v86, v87\n\t"
    "v_mfma_f32_16x16x32_bf16 v[104:107], a[196:199], v[212:215], v[104:107]\n\t"
    "v_pk_fma_f16 v34, v32, v32, s60 neg_lo:[1,0,1] neg_hi:[1,0,1]\n\t"
    "v_pk_fma_f16 v35, v33, v33, s60 neg_lo:[1,0,1] neg_hi:[1,0,1]\n\t"
    "v_pk_mul_f16 v34, v34, v32\n\t"
    "v_mfma_f32_16x16x32_bf16 v[96:99], a[200:203], v[168:171], v[96:99]\n\t"
    "ds_read_b64_tr_b16 a[224:225], v17 offset:8192\n\t"
    "v_pk_mul_f16 v35, v35, v33\n\t"
    "v_exp_f16 v36, v34\n\t"
    "v_mfma_f32_16x16x32_bf16 v[100:103], a[200:203], v[192:195], v[100:103]\n\t"
    "ds_read_b64_tr_b16 a[226:227], v17 offset:10240\n\t"
    "v_exp_f16 v37, v35\n\t"
    "v_mfma_f32_16x16x32_bf16 v[104:107], a[200:203], v[216:219], v[104:107]\n\t"
    "v_exp_f16_sdwa v36, v34 dst_sel:WORD_1 dst_unused:UNUSED_PRESERVE src0_sel:WORD_1\n\t"
    "v_exp_f16_sdwa v37, v35 dst_sel:WORD_1 dst_unused:UNUSED_PRESERVE src0_sel:WORD_1\n\t"
    "v_mfma_f32_16x16x32_bf16 v[96:99], a[204:207], v[172:175], v[96:99]\n\t"
    "ds_read_b64_tr_b16 a[228:229], v17 offset:12288\n\t"
    "v_pk_add_f16 v36, v36, 1.0 op_sel_hi:[1,0]\n\t"
    "v_pk_add_f16 v37, v37, 1.0 op_sel_hi:[1,0]\n\t"
    "v_mfma_f32_16x16x32_bf16 v[100:103], a[204:207], v[196:199], v[100:103]\n\t"
    "ds_read_b64_tr_b16 a[230:231], v17 offset:14336\n\t"
    "v_rcp_f16 v34, v36\n\t"
    "v_rcp_f16 v35, v37\n\t"
    "v_mfma_f32_16x16x32_bf16 v[104:107], a[204:207], v[220:223], v[104:107]\n\t"
    "v_rcp_f16_sdwa v34, v36 dst_sel:WORD_1 dst_unused:UNUSED_PRESERVE src0_sel:WORD_1\n\t"
    "s_waitcnt lgkmcnt(4)\n\t"
    "v_mfma_f32_16x16x32_bf16 v[96:99], a[208:211], v[176:179], v[96:99]\n\t"
    "ds_read_b64_tr_b16 a[232:233], v17 offset:16384\n\t"
    "v_rcp_f16_sdwa v35, v37 dst_sel:WORD_1 dst_unused:UNUSED_PRESERVE src0_sel:WORD_1\n\t"
    "v_mfma_f32_16x16x32_bf16 v[100:103], a[208:211], v[200:203], v[100:103]\n\t"
    "ds_read_b64_tr_b16 a[234:235], v17 offset:18432\n\t"
    "v_fma_mix_f32 v84, v84, v34, 0 op_sel_hi:[0,1,0]\n\t"
    "v_fma_mix_f32 v86, v86, v35, 0 op_sel_hi:[0,1,0]\n\t"
    "v_fma_mix_f32 v85, v85, v34, 0 op_sel:[0,1,0] op_sel_hi:[0,1,0]\n\t"
    "v_mfma_f32_16x16x32_bf16 v[104:107], a[208:211], v[224:227], v[104:107]\n\t"
    "v_fma_mix_f32 v87, v87, v35, 0 op_sel:[0,1,0] op_sel_hi:[0,1,0]\n\t"
    "v_cvt_pk_bf16_f32 v134, v84, v85\n\t"
    "v_cvt_pk_bf16_f32 v135, v86, v87\n\t"
    "v_mfma_f32_16x16x32_bf16 v[96:99], a[212:215], v[180:183], v[96:99]\n\t"
    "ds_read_b64_tr_b16 a[236:237], v17 offset:20480\n\t"
    "v_cvt_pk_f16_f32 v38, v88, v89\n\t"
    "v_cvt_pk_f16_f32 v39, v90, v91\n\t"
    "v_pk_fma_f16 v40, v38, v38, s60 neg_lo:[1,0,1] neg_hi:[1,0,1]\n\t"
    "v_mfma_f32_16x16x32_bf16 v[100:103], a[212:215], v[204:207], v[100:103]\n\t"
    "ds_read_b64_tr_b16 a[238:239], v17 offset:22528\n\t"
    "v_pk_fma_f16 v41, v39, v39, s60 neg_lo:[1,0,1] neg_hi:[1,0,1]\n\t"
    "v_pk_mul_f16 v40, v40, v38\n\t"
    "v_mfma_f32_16x16x32_bf16 v[104:107], a[212:215], v[228:231], v[104:107]\n\t"
    "v_pk_mul_f16 v41, v41, v39\n\t"
    "v_exp_f16 v42, v40\n\t"
    "v_exp_f16 v43, v41\n\t"
    "v_mfma_f32_16x16x32_bf16 v[112:115], a[216:219], v[160:163], v[52:55]\n\t"
    "ds_read_b64_tr_b16 a[192:193], v18 offset:0\n\t"
    "v_exp_f16_sdwa v42, v40 dst_sel:WORD_1 dst_unused:UNUSED_PRESERVE src0_sel:WORD_1\n\t"
    "v_mfma_f32_16x16x32_bf16 v[116:119], a[216:219], v[184:187], v[52:55]\n\t"
    "ds_read_b64_tr_b16 a[194:195], v18 offset:6144\n\t"
    "v_exp_f16_sdwa v43, v41 dst_sel:WORD_1 dst_unused:UNUSED_PRESERVE src0_sel:WORD_1\n\t"
    "v_mfma_f32_16x16x32_bf16 v[120:123], a[216:219], v[208:211], v[52:55]\n\t"
    "v_pk_add_f16 v42, v42, 1.0 op_sel_hi:[1,0]\n\t"
    "v_pk_add_f16 v43, v43, 1.0 op_sel_hi:[1,0]\n\t"
    "v_rcp_f16 v40, v42\n\t"
    "v_mfma_f32_16x16x32_bf16 v[112:115], a[220:223], v[164:167], v[112:115]\n\t"
    "ds_read_b64_tr_b16 a[196:197], v19 offset:0\n\t"
    "v_rcp_f16 v41, v43\n\t"
    "v_mfma_f32_16x16x32_bf16 v[116:119], a[220:223], v[188:191], v[116:119]\n\t"
    "ds_read_b64_tr_b16 a[198:199], v19 offset:6144\n\t"
    "v_rcp_f16_sdwa v40, v42 dst_sel:WORD_1 dst_unused:UNUSED_PRESERVE src0_sel:WORD_1\n\t"
    "v_mfma_f32_16x16x32_bf16 v[120:123], a[220:223], v[212:215], v[120:123]\n\t"
    "v_rcp_f16_sdwa v41, v43 dst_sel:WORD_1 dst_unused:UNUSED_PRESERVE src0_sel:WORD_1\n\t"
    "v_fma_mix_f32 v88, v88, v40, 0 op_sel_hi:[0,1,0]\n\t"
    "s_waitcnt lgkmcnt(4)\n\t"
    "v_mfma_f32_16x16x32_bf16 v[112:115], a[224:227], v[168:171], v[112:115]\n\t"
    "ds_read_b64_tr_b16 a[200:201], v20 offset:0\n\t"
    "v_fma_mix_f32 v90, v90, v41, 0 op_sel_hi:[0,1,0]\n\t"
    "v_fma_mix_f32 v89, v89, v40, 0 op_sel:[0,1,0] op_sel_hi:[0,1,0]\n\t"
    "v_fma_mix_f32 v91, v91, v41, 0 op_sel:[0,1,0] op_sel_hi:[0,1,0]\n\t"
    "v_mfma_f32_16x16x32_bf16 v[116:119], a[224:227], v[192:195], v[116:119]\n\t"
    "ds_read_b64_tr_b16 a[202:203], v20 offset:6144\n\t"
    "v_cvt_pk_bf16_f32 v138, v88, v89\n\t"
    "v_cvt_pk_bf16_f32 v139, v90, v91\n\t"
    "v_mfma_f32_16x16x32_bf16 v[120:123], a[224:227], v[216:219], v[120:123]\n\t"
    "v_cvt_pk_f16_f32 v32, v96, v97\n\t"
    "v_cvt_pk_f16_f32 v33, v98, v99\n\t"
    "v_pk_fma_f16 v34, v32, v32, s60 neg_lo:[1,0,1] neg_hi:[1,0,1]\n\t"
    "v_pk_fma_f16 v35, v33, v33, s60 neg_lo:[1,0,1] neg_hi:[1,0,1]\n\t"
    "v_mfma_f32_16x16x32_bf16 v[112:115], a[228:231], v[172:175], v[112:115]\n\t"
    "ds_read_b64_tr_b16 a[204:205], v21 offset:0\n\t"
    "v_pk_mul_f16 v34, v34, v32\n\t"
    "v_pk_mul_f16 v35, v35, v33\n\t"
    "v_mfma_f32_16x16x32_bf16 v[116:119], a[228:231], v[196:199], v[116:119]\n\t"
    "ds_read_b64_tr_b16 a[206:207], v21 offset:6144\n\t"
    "v_exp_f16 v36, v34\n\t"
    "v_exp_f16 v37, v35\n\t"
    "v_mfma_f32_16x16x32_bf16 v[120:123], a[228:231], v[220:223], v[120:123]\n\t"
    "v_exp_f16_sdwa v36, v34 dst_sel:WORD_1 dst_unused:UNUSED_PRESERVE src0_sel:WORD_1\n\t"
    "v_mfma_f32_16x16x32_bf16 v[112:115], a[232:235], v[176:179], v[112:115]\n\t"
    "ds_read_b64_tr_b16 a[208:209], v18 offset:1024\n\t"
    "v_exp_f16_sdwa v37, v35 dst_sel:WORD_1 dst_unused:UNUSED_PRESERVE src0_sel:WORD_1\n\t"
    "v_pk_add_f16 v36, v36, 1.0 op_sel_hi:[1,0]\n\t"
    "v_mfma_f32_16x16x32_bf16 v[116:119], a[232:235], v[200:203], v[116:119]\n\t"
    "ds_read_b64_tr_b16 a[210:211], v18 offset:7168\n\t"
    "v_pk_add_f16 v37, v37, 1.0 op_sel_hi:[1,0]\n\t"
    "v_rcp_f16 v34, v36\n\t"
    "v_mfma_f32_16x16x32_bf16 v[120:123], a[232:235], v[224:227], v[120:123]\n\t"
    "v_rcp_f16 v35, v37\n\t"
    "v_rcp_f16_sdwa v34, v36 dst_sel:WORD_1 dst_unused:UNUSED_PRESERVE src0_sel:WORD_1\n\t"
    "v_mfma_f32_16x16x32_bf16 v[112:115], a[236:239], v[180:183], v[112:115]\n\t"
    "ds_read_b64_tr_b16 a[212:213], v19 offset:1024\n\t"
    "v_rcp_f16_sdwa v35, v37 dst_sel:WORD_1 dst_unused:UNUSED_PRESERVE src0_sel:WORD_1\n\t"
    "v_mfma_f32_16x16x32_bf16 v[116:119], a[236:239], v[204:207], v[116:119]\n\t"
    "ds_read_b64_tr_b16 a[214:215], v19 offset:7168\n\t"
    "v_fma_mix_f32 v96, v96, v34, 0 op_sel_hi:[0,1,0]\n\t"
    "v_fma_mix_f32 v98, v98, v35, 0 op_sel_hi:[0,1,0]\n\t"
    "v_mfma_f32_16x16x32_bf16 v[120:123], a[236:239], v[228:231], v[120:123]\n\t"
    "v_fma_mix_f32 v97, v97, v34, 0 op_sel:[0,1,0] op_sel_hi:[0,1,0]\n\t"
    "v_fma_mix_f32 v99, v99, v35, 0 op_sel:[0,1,0] op_sel_hi:[0,1,0]\n\t"
    "v_cvt_pk_bf16_f32 v144, v96, v97\n\t"
    "v_cvt_pk_bf16_f32 v145, v98, v99\n\t"
    "P2_a_%=:\n\t"
    "s_waitcnt lgkmcnt(4)\n\t"
    "v_mfma_f32_16x16x32_bf16 a[0:3], a[192:195], v[128:131], a[0:3]\n\t"
    "ds_read_b64_tr_b16 a[216:217], v20 offset:1024\n\t"
    "v_cvt_pk_f16_f32 v38, v100, v101\n\t"
    "v_cvt_pk_f16_f32 v39, v102, v103\n\t"
    "v_pk_fma_f16 v40, v38, v38, s60 neg_lo:[1,0,1] neg_hi:[1,0,1]\n\t"
    "v_mfma_f32_16x16x32_bf16 a[4:7], a[192:195], v[132:135], a[4:7]\n\t"
    "ds_read_b64_tr_b16 a[218:219], v20 offset:7168\n\t"
    "v_pk_fma_f16 v41, v39, v39, s60 neg_lo:[1,0,1] neg_hi:[1,0,1]\n\t"
    "v_pk_mul_f16 v40, v40, v38\n\t"
    "v_mfma_f32_16x16x32_bf16 a[8:11], a[192:195], v[136:139], a[8:11]\n\t"
    "v_pk_mul_f16 v41, v41, v39\n\t"
    "v_exp_f16 v42, v40\n\t"
    "v_mfma_f32_16x16x32_bf16 a[12:15], a[196:199], v[128:131], a[12:15]\n\t"
    "ds_read_b64_tr_b16 a[220:221], v21 offset:1024\n\t"
    "v_exp_f16 v43, v41\n\t"
    "v_exp_f16_sdwa v42, v40 dst_sel:WORD_1 dst_unused:UNUSED_PRESERVE src0_sel:WORD_1\n\t"
    "v_mfma_f32_16x16x32_bf16 a[16:19], a[196:199], v[132:135], a[16:19]\n\t"
    "ds_read_b64_tr_b16 a[222:223], v21 offset:7168\n\t"
    "v_exp_f16_sdwa v43, v41 dst_sel:WORD_1 dst_unused:UNUSED_PRESERVE src0_sel:WORD_1\n\t"
    "v_mfma_f32_16x16x32_bf16 a[20:23], a[196:199], v[136:139], a[20:23]\n\t"
    "v_pk_add_f16 v42, v42, 1.0 op_sel_hi:[1,0]\n\t"
    "v_pk_add_f16 v43, v43, 1.0 op_sel_hi:[1,0]\n\t"
    "v_rcp_f16 v40, v42\n\t"
    "v_mfma_f32_16x16x32_bf16 a[24:27], a[200:203], v[128:131], a[24:27]\n\t"
    "ds_read_b64_tr_b16 a[224:225], v18 offset:2048\n\t"
    "v_rcp_f16 v41, v43\n\t"
    "v_mfma_f32_16x16x32_bf16 a[28:31], a[200:203], v[132:135], a[28:31]\n\t"
    "ds_read_b64_tr_b16 a[226:227], v18 offset:8192\n\t"
    "v_rcp_f16_sdwa v40, v42 dst_sel:WORD_1 dst_unused:UNUSED_PRESERVE src0_sel:WORD_1\n\t"
    "v_mfma_f32_16x16x32_bf16 a[32:35], a[200:203], v[136:139], a[32:35]\n\t"
    "v_rcp_f16_sdwa v41, v43 dst_sel:WORD_1 dst_unused:UNUSED_PRESERVE src0_sel:WORD_1\n\t"
    "v_fma_mix_f32 v100, v100, v40, 0 op_sel_hi:[0,1,0]\n\t"
    "v_fma_mix_f32 v102, v102, v41, 0 op_sel_hi:[0,1,0]\n\t"
    "v_mfma_f32_16x16x32_bf16 a[36:39], a[204:207], v[128:131], a[36:39]\n\t"
    "ds_read_b64_tr_b16 a[228:229], v19 offset:2048\n\t"
    "v_fma_mix_f32 v101, v101, v40, 0 op_sel:[0,1,0] op_sel_hi:[0,1,0]\n\t"
    "v_fma_mix_f32 v103, v103, v41, 0 op_sel:[0,1,0] op_sel_hi:[0,1,0]\n\t"
    "v_mfma_f32_16x16x32_bf16 a[40:43], a[204:207], v[132:135], a[40:43]\n\t"
    "ds_read_b64_tr_b16 a[230:231], v19 offset:8192\n\t"
    "v_cvt_pk_bf16_f32 v148, v100, v101\n\t"
    "v_cvt_pk_bf16_f32 v149, v102, v103\n\t"
    "v_cvt_pk_f16_f32 v32, v104, v105\n\t"
    "v_mfma_f32_16x16x32_bf16 a[44:47], a[204:207], v[136:139], a[44:47]\n\t"
    "s_waitcnt vmcnt(0)\n\t"
    "s_barrier\n\t"
    "s_waitcnt lgkmcnt(4)\n\t"
    "v_mfma_f32_16x16x32_bf16 a[48:51], a[208:211], v[128:131], a[48:51]\n\t"
    "ds_read_b64_tr_b16 a[232:233], v20 offset:2048\n\t"
    "v_cvt_pk_f16_f32 v33, v106, v107\n\t"
    "v_pk_fma_f16 v34, v32, v32, s60 neg_lo:[1,0,1] neg_hi:[1,0,1]\n\t"
    "v_pk_fma_f16 v35, v33, v33, s60 neg_lo:[1,0,1] neg_hi:[1,0,1]\n\t"
    "v_mfma_f32_16x16x32_bf16 a[52:55], a[208:211], v[132:135], a[52:55]\n\t"
    "ds_read_b64_tr_b16 a[234:235], v20 offset:8192\n\t"
    "s_add_u32 m0, s67, 0\n\t"
    "s_add_u32 s63, s65, 0\n\t"
    "v_mfma_f32_16x16x32_bf16 a[56:59], a[208:211], v[136:139], a[56:59]\n\t"
    "buffer_load_dwordx4 v26, s[40:43], s63 offen offset:0 lds\n\t"
    "v_pk_mul_f16 v34, v34, v32\n\t"
    "v_pk_mul_f16 v35, v35, v33\n\t"
    "v_mfma_f32_16x16x32_bf16 a[60:63], a[212:215], v[128:131], a[60:63]\n\t"
    "ds_read_b64_tr_b16 a[236:237], v21 offset:2048\n\t"
    "v_exp_f16 v36, v34\n\t"
    "v_mfma_f32_16x16x32_bf16 a[64:67], a[212:215], v[132:135], a[64:67]\n\t"
    "ds_read_b64_tr_b16 a[238:239], v21 offset:8192\n\t"
    "v_exp_f16 v37, v35\n\t"
    "v_mfma_f32_16x16x32_bf16 a[68:71], a[212:215], v[136:139], a[68:71]\n\t"
    "v_exp_f16_sdwa v36, v34 dst_sel:WORD_1 dst_unused:UNUSED_PRESERVE src0_sel:WORD_1\n\t"
    "v_exp_f16_sdwa v37, v35 dst_sel:WORD_1 dst_unused:UNUSED_PRESERVE src0_sel:WORD_1\n\t"
    "v_mfma_f32_16x16x32_bf16 a[72:75], a[216:219], v[128:131], a[72:75]\n\t"
    "ds_read_b64_tr_b16 a[192:193], v14 offset:24576\n\t"
    "v_pk_add_f16 v36, v36, 1.0 op_sel_hi:[1,0]\n\t"
    "v_pk_add_f16 v37, v37, 1.0 op_sel_hi:[1,0]\n\t"
    "v_mfma_f32_16x16x32_bf16 a[76:79], a[216:219], v[132:135], a[76:79]\n\t"
    "ds_read_b64_tr_b16 a[194:195], v14 offset:26624\n\t"
    "v_rcp_f16 v34, v36\n\t"
    "v_rcp_f16 v35, v37\n\t"
    "v_mfma_f32_16x16x32_bf16 a[80:83], a[216:219], v[136:139], a[80:83]\n\t"
    "buffer_load_dwordx4 v27, s[40:43], s63 offen offset:1024 lds\n\t"
    "v_mfma_f32_16x16x32_bf16 a[84:87], a[220:223], v[128:131], a[84:87]\n\t"
    "ds_read_b64_tr_b16 a[196:197], v14 offset:28672\n\t"
    "v_rcp_f16_sdwa v34, v36 dst_sel:WORD_1 dst_unused:UNUSED_PRESERVE src0_sel:WORD_1\n\t"
    "v_rcp_f16_sdwa v35, v37 dst_sel:WORD_1 dst_unused:UNUSED_PRESERVE src0_sel:WORD_1\n\t"
    "v_mfma_f32_16x16x32_bf16 a[88:91], a[220:223], v[132:135], a[88:91]\n\t"
    "ds_read_b64_tr_b16 a[198:199], v14 offset:30720\n\t"
    "v_fma_mix_f32 v104, v104, v34, 0 op_sel_hi:[0,1,0]\n\t"
    "v_fma_mix_f32 v106, v106, v35, 0 op_sel_hi:[0,1,0]\n\t"
    "v_mfma_f32_16x16x32_bf16 a[92:95], a[220:223], v[136:139], a[92:95]\n\t"
    "ds_read_b128 v[52:55], v22 offset:320\n\t"
    "v_fma_mix_f32 v105, v105, v34, 0 op_sel:[0,1,0] op_sel_hi:[0,1,0]\n\t"
    "s_waitcnt lgkmcnt(5)\n\t"
    "v_mfma_f32_16x16x32_bf16 a[96:99], a[224:227], v[128:131], a[96:99]\n\t"
    "ds_read_b64_tr_b16 a[200:201], v14 offset:32768\n\t"
    "v_fma_mix_f32 v107, v107, v35, 0 op_sel:[0,1,0] op_sel_hi:[0,1,0]\n\t"
    "v_cvt_pk_bf16_f32 v152, v104, v105\n\t"
    "v_cvt_pk_bf16_f32 v153, v106, v107\n\t"
    "v_mfma_f32_16x16x32_bf16 a[100:103], a[224:227], v[132:135], a[100:103]\n\t"
    "ds_read_b64_tr_b16 a[202:203], v14 offset:34816\n\t"
    "v_cvt_pk_f16_f32 v38, v112, v113\n\t"
    "v_cvt_pk_f16_f32 v39, v114, v115\n\t"
    "v_mfma_f32_16x16x32_bf16 a[104:107], a[224:227], v[136:139], a[104:107]\n\t"
    "buffer_load_dwordx4 v28, s[40:43], s63 offen offset:2048 lds\n\t"
    "v_pk_fma_f16 v40, v38, v38, s60 neg_lo:[1,0,1] neg_hi:[1,0,1]\n\t"
    "v_pk_fma_f16 v41, v39, v39, s60 neg_lo:[1,0,1] neg_hi:[1,0,1]\n\t"
    "v_mfma_f32_16x16x32_bf16 a[108:111], a[228:231], v[128:131], a[108:111]\n\t"
    "ds_read_b64_tr_b16 a[204:205], v14 offset:36864\n\t"
    "v_pk_mul_f16 v40, v40, v38\n\t"
    "v_pk_mul_f16 v41, v41, v39\n\t"
    "v_mfma_f32_16x16x32_bf16 a[112:115], a[228:231], v[132:135], a[112:115]\n\t"
    "ds_read_b64_tr_b16 a[206:207], v14 offset:38912\n\t"
    "v_exp_f16 v42, v40\n\t"
    "v_mfma_f32_16x16x32_bf16 a[116:119], a[228:231], v[136:139], a[116:119]\n\t"
    "ds_read_b128 v[48:51], v22 offset:256\n\t"
    "v_exp_f16 v43, v41\n\t"
    "v_exp_f16_sdwa v42, v40 dst_sel:WORD_1 dst_unused:UNUSED_PRESERVE src0_sel:WORD_1\n\t"
    "v_mfma_f32_16x16x32_bf16 a[120:123], a[232:235], v[128:131], a[120:123]\n\t"
    "ds_read_b64_tr_b16 a[208:209], v14 offset:40960\n\t"
    "v_exp_f16_sdwa v43, v41 dst_sel:WORD_1 dst_unused:UNUSED_PRESERVE src0_sel:WORD_1\n\t"
    "v_mfma_f32_16x16x32_bf16 a[124:127], a[232:235], v[132:135], a[124:127]\n\t"
    "ds_read_b64_tr_b16 a[210:211], v14 offset:43008\n\t"
    "v_pk_add_f16 v42, v42, 1.0 op_sel_hi:[1,0]\n\t"
    "s_add_u32 m0, s68, 36864\n\t"
    "v_mfma_f32_16x16x32_bf16 a[128:131], a[232:235], v[136:139], a[128:131]\n\t"
    "buffer_load_dwordx4 v29, s[44:47], s66 offen offset:0 lds\n\t"
    "v_pk_add_f16 v43, v43, 1.0 op_sel_hi:[1,0]\n\t"
    "v_mfma_f32_16x16x32_bf16 a[132:135], a[236:239], v[128:131], a[132:135]\n\t"
    "ds_read_b64_tr_b16 a[212:213], v14 offset:45056\n\t"
    "v_rcp_f16 v40, v42\n\t"
    "v_rcp_f16 v41, v43\n\t"
    "v_mfma_f32_16x16x32_bf16 a[136:139], a[236:239], v[132:135], a[136:139]\n\t"
    "ds_read_b64_tr_b16 a[214:215], v14 offset:47104\n\t"
    "v_rcp_f16_sdwa v40, v42 dst_sel:WORD_1 dst_unused:UNUSED_PRESERVE src0_sel:WORD_1\n\t"
    "v_mfma_f32_16x16x32_bf16 a[140:143], a[236:239], v[136:139], a[140:143]\n\t"
    "v_rcp_f16_sdwa v41, v43 dst_sel:WORD_1 dst_unused:UNUSED_PRESERVE src0_sel:WORD_1\n\t"
    "v_fma_mix_f32 v112, v112, v40, 0 op_sel_hi:[0,1,0]\n\t"
    "s_waitcnt lgkmcnt(4)\n\t"
    "v_mfma_f32_16x16x32_bf16 v[64:67], a[192:195], v[160:163], v[48:51]\n\t"
    "ds_read_b64_tr_b16 a[216:217], v15 offset:24576\n\t"
    "v_fma_mix_f32 v114, v114, v41, 0 op_sel_hi:[0,1,0]\n\t"
    "v_fma_mix_f32 v113, v113, v40, 0 op_sel:[0,1,0] op_sel_hi:[0,1,0]\n\t"
    "v_mfma_f32_16x16x32_bf16 v[68:71], a[192:195], v[184:187], v[48:51]\n\t"
    "ds_read_b64_tr_b16 a[218:219], v15 offset:26624\n\t"
    "v_fma_mix_f32 v115, v115, v41, 0 op_sel:[0,1,0] op_sel_hi:[0,1,0]\n\t"
    "v_cvt_pk_bf16_f32 v146, v112, v113\n\t"
    "v_cvt_pk_bf16_f32 v147, v114, v115\n\t"
    "v_mfma_f32_16x16x32_bf16 v[72:75], a[192:195], v[208:211], v[48:51]\n\t"
    "buffer_load_dwordx4 v30, s[44:47], s66 offen offset:1024 lds\n\t"
    "v_cvt_pk_f16_f32 v32, v116, v117\n\t"
    "v_mfma_f32_16x16x32_bf16 v[64:67], a[196:199], v[164:167], v[64:67]\n\t"
    "ds_read_b64_tr_b16 a[220:221], v15 offset:28672\n\t"
    "v_cvt_pk_f16_f32 v33, v118, v119\n\t"
    "v_pk_fma_f16 v34, v32, v32, s60 neg_lo:[1,0,1] neg_hi:[1,0,1]\n\t"
    "v_pk_fma_f16 v35, v33, v33, s60 neg_lo:[1,0,1] neg_hi:[1,0,1]\n\t"
    "v_mfma_f32_16x16x32_bf16 v[68:71], a[196:199], v[188:191], v[68:71]\n\t"
    "ds_read_b64_tr_b16 a[222:223], v15 offset:30720\n\t"
    "v_pk_mul_f16 v34, v34, v32\n\t"
    "v_pk_mul_f16 v35, v35, v33\n\t"
    "v_mfma_f32_16x16x32_bf16 v[72:75], a[196:199], v[212:215], v[72:75]\n\t"
    "v_exp_f16 v36, v34\n\t"
    "v_exp_f16 v37, v35\n\t"
    "v_mfma_f32_16x16x32_bf16 v[64:67], a[200:203], v[168:171], v[64:67]\n\t"
    "ds_read_b64_tr_b16 a[224:225], v15 offset:32768\n\t"
    "v_exp_f16_sdwa v36, v34 dst_sel:WORD_1 dst_unused:UNUSED_PRESERVE src0_sel:WORD_1\n\t"
    "v_mfma_f32_16x16x32_bf16 v[68:71], a[200:203], v[192:195], v[68:71]\n\t"
    "ds_read_b64_tr_b16 a[226:227], v15 offset:34816\n\t"
    "v_exp_f16_sdwa v37, v35 dst_sel:WORD_1 dst_unused:UNUSED_PRESERVE src0_sel:WORD_1\n\t"
    "v_pk_add_f16 v36, v36, 1.0 op_sel_hi:[1,0]\n\t"
    "v_mfma_f32_16x16x32_bf16 v[72:75], a[200:203], v[216:219], v[72:75]\n\t"
    "buffer_load_dwordx4 v31, s[44:47], s66 offen offset:2048 lds\n\t"
    "v_pk_add_f16 v37, v37, 1.0 op_sel_hi:[1,0]\n\t"
    "v_mfma_f32_16x16x32_bf16 v[64:67], a[204:207], v[172:175], v[64:67]\n\t"
    "ds_read_b64_tr_b16 a[228:229], v15 offset:36864\n\t"
    "v_rcp_f16 v34, v36\n\t"
    "v_mfma_f32_16x16x32_bf16 v[68:71], a[204:207], v[196:199], v[68:71]\n\t"
    "ds_read_b64_tr_b16 a[230:231], v15 offset:38912\n\t"
    "v_rcp_f16 v35, v37\n\t"
    "v_rcp_f16_sdwa v34, v36 dst_sel:WORD_1 dst_unused:UNUSED_PRESERVE src0_sel:WORD_1\n\t"
    "v_mfma_f32_16x16x32_bf16 v[72:75], a[204:207], v[220:223], v[72:75]\n\t"
    "v_rcp_f16_sdwa v35, v37 dst_sel:WORD_1 dst_unused:UNUSED_PRESERVE src0_sel:WORD_1\n\t"
    "s_waitcnt lgkmcnt(4)\n\t"
    "v_mfma_f32_16x16x32_bf16 v[64:67], a[208:211], v[176:179], v[64:67]\n\t"
    "ds_read_b64_tr_b16 a[232:233], v15 offset:40960\n\t"
    "v_fma_mix_f32 v116, v116, v34, 0 op_sel_hi:[0,1,0]\n\t"
    "v_fma_mix_f32 v118, v118, v35, 0 op_sel_hi:[0,1,0]\n\t"
    "v_mfma_f32_16x16x32_bf16 v[68:71], a[208:211], v[200:203], v[68:71]\n\t"
    "ds_read_b64_tr_b16 a[234:235], v15 offset:43008\n\t"
    "v_fma_mix_f32 v117, v117, v34, 0 op_sel:[0,1,0] op_sel_hi:[0,1,0]\n\t"
    "v_fma_mix_f32 v119, v119, v35, 0 op_sel:[0,1,0] op_sel_hi:[0,1,0]\n\t"
    "s_add_u32 s66, s66, 12288\n\t"
    "v_mfma_f32_16x16x32_bf16 v[72:75], a[208:211], v[224:227], v[72:75]\n\t"
    "v_cvt_pk_bf16_f32 v150, v116, v117\n\t"
    "v_cvt_pk_bf16_f32 v151, v118, v119\n\t"
    "v_cvt_pk_f16_f32 v38, v120, v121\n\t"
    "v_mfma_f32_16x16x32_bf16 v[64:67], a[212:215], v[180:183], v[64:67]\n\t"
    "ds_read_b64_tr_b16 a[236:237], v15 offset:45056\n\t"
    "v_cvt_pk_f16_f32 v39, v122, v123\n\t"
    "v_pk_fma_f16 v40, v38, v38, s60 neg_lo:[1,0,1] neg_hi:[1,0,1]\n\t"
    "v_pk_fma_f16 v41, v39, v39, s60 neg_lo:[1,0,1] neg_hi:[1,0,1]\n\t"
    "v_mfma_f32_16x16x32_bf16 v[68:71], a[212:215], v[204:207], v[68:71]\n\t"
    "ds_read_b64_tr_b16 a[238:239], v15 offset:47104\n\t"
    "v_pk_mul_f16 v40, v40, v38\n\t"
    "v_pk_mul_f16 v41, v41, v39\n\t"
    "v_mfma_f32_16x16x32_bf16 v[72:75], a[212:215], v[228:231], v[72:75]\n\t"
    "v_exp_f16 v42, v40\n\t"
    "v_exp_f16 v43, v41\n\t"
    "v_mfma_f32_16x16x32_bf16 v[80:83], a[216:219], v[160:163], v[52:55]\n\t"
    "ds_read_b64_tr_b16 a[192:193], v18 offset:12288\n\t"
    "v_exp_f16_sdwa v42, v40 dst_sel:WORD_1 dst_unused:UNUSED_PRESERVE src0_sel:WORD_1\n\t"
    "v_mfma_f32_16x16x32_bf16 v[84:87], a[216:219], v[184:187], v[52:55]\n\t"
    "ds_read_b64_tr_b16 a[194:195], v18 offset:18432\n\t"
    "v_exp_f16_sdwa v43, v41 dst_sel:WORD_1 dst_unused:UNUSED_PRESERVE src0_sel:WORD_1\n\t"
    "v_pk_add_f16 v42, v42, 1.0 op_sel_hi:[1,0]\n\t"
    "v_mfma_f32_16x16x32_bf16 v[88:91], a[216:219], v[208:211], v[52:55]\n\t"
    "v_pk_add_f16 v43, v43, 1.0 op_sel_hi:[1,0]\n\t"
    "v_rcp_f16 v40, v42\n\t"
    "v_mfma_f32_16x16x32_bf16 v[80:83], a[220:223], v[164:167], v[80:83]\n\t"
    "ds_read_b64_tr_b16 a[196:197], v19 offset:12288\n\t"
    "v_rcp_f16 v41, v43\n\t"
    "v_mfma_f32_16x16x32_bf16 v[84:87], a[220:223], v[188:191], v[84:87]\n\t"
    "ds_read_b64_tr_b16 a[198:199], v19 offset:18432\n\t"
    "v_rcp_f16_sdwa v40, v42 dst_sel:WORD_1 dst_unused:UNUSED_PRESERVE src0_sel:WORD_1\n\t"
    "v_rcp_f16_sdwa v41, v43 dst_sel:WORD_1 dst_unused:UNUSED_PRESERVE src0_sel:WORD_1\n\t"
    "v_mfma_f32_16x16x32_bf16 v[88:91], a[220:223], v[212:215], v[88:91]\n\t"
    "v_fma_mix_f32 v120, v120, v40, 0 op_sel_hi:[0,1,0]\n\t"
    "v_fma_mix_f32 v122, v122, v41, 0 op_sel_hi:[0,1,0]\n\t"
    "s_waitcnt lgkmcnt(4)\n\t"
    "v_mfma_f32_16x16x32_bf16 v[80:83], a[224:227], v[168:171], v[80:83]\n\t"
    "ds_read_b64_tr_b16 a[200:201], v20 offset:12288\n\t"
    "v_fma_mix_f32 v121, v121, v40, 0 op_sel:[0,1,0] op_sel_hi:[0,1,0]\n\t"
    "v_fma_mix_f32 v123, v123, v41, 0 op_sel:[0,1,0] op_sel_hi:[0,1,0]\n\t"
    "v_mfma_f32_16x16x32_bf16 v[84:87], a[224:227], v[192:195], v[84:87]\n\t"
    "ds_read_b64_tr_b16 a[202:203], v20 offset:18432\n\t"
    "v_cvt_pk_bf16_f32 v154, v120, v121\n\t"
    "v_cvt_pk_bf16_f32 v155, v122, v123\n\t"
    "v_cvt_pk_f16_f32 v32, v64, v65\n\t"
    "v_mfma_f32_16x16x32_bf16 v[88:91], a[224:227], v[216:219], v[88:91]\n\t"
    "v_cvt_pk_f16_f32 v33, v66, v67\n\t"
    "v_pk_fma_f16 v34, v32, v32, s60 neg_lo:[1,0,1] neg_hi:[1,0,1]\n\t"
    "v_pk_fma_f16 v35, v33, v33, s60 neg_lo:[1,0,1] neg_hi:[1,0,1]\n\t"
    "v_mfma_f32_16x16x32_bf16 v[80:83], a[228:231], v[172:175], v[80:83]\n\t"
    "ds_read_b64_tr_b16 a[204:205], v21 offset:12288\n\t"
    "v_pk_mul_f16 v34, v34, v32\n\t"
    "v_pk_mul_f16 v35, v35, v33\n\t"
    "v_mfma_f32_16x16x32_bf16 v[84:87], a[228:231], v[196:199], v[84:87]\n\t"
    "ds_read_b64_tr_b16 a[206:207], v21 offset:18432\n\t"
    "v_exp_f16 v36, v34\n\t"
    "v_exp_f16 v37, v35\n\t"
    "v_mfma_f32_16x16x32_bf16 v[88:91], a[228:231], v[220:223], v[88:91]\n\t"
    "v_exp_f16_sdwa v36, v34 dst_sel:WORD_1 dst_unused:UNUSED_PRESERVE src0_sel:WORD_1\n\t"
    "v_mfma_f32_16x16x32_bf16 v[80:83], a[232:235], v[176:179], v[80:83]\n\t"
    "ds_read_b64_tr_b16 a[208:209], v18 offset:13312\n\t"
    "v_exp_f16_sdwa v37, v35 dst_sel:WORD_1 dst_unused:UNUSED_PRESERVE src0_sel:WORD_1\n\t"
    "v_pk_add_f16 v36, v36, 1.0 op_sel_hi:[1,0]\n\t"
    "v_mfma_f32_16x16x32_bf16 v[84:87], a[232:235], v[200:203], v[84:87]\n\t"
    "ds_read_b64_tr_b16 a[210:211], v18 offset:19456\n\t"
    "v_pk_add_f16 v37, v37, 1.0 op_sel_hi:[1,0]\n\t"
    "v_rcp_f16 v34, v36\n\t"
    "v_mfma_f32_16x16x32_bf16 v[88:91], a[232:235], v[224:227], v[88:91]\n\t"
    "v_rcp_f16 v35, v37\n\t"
    "v_rcp_f16_sdwa v34, v36 dst_sel:WORD_1 dst_unused:UNUSED_PRESERVE src0_sel:WORD_1\n\t"
    "v_mfma_f32_16x16x32_bf16 v[80:83], a[236:239], v[180:183], v[80:83]\n\t"
    "ds_read_b64_tr_b16 a[212:213], v19 offset:13312\n\t"
    "v_rcp_f16_sdwa v35, v37 dst_sel:WORD_1 dst_unused:UNUSED_PRESERVE src0_sel:WORD_1\n\t"
    "v_mfma_f32_16x16x32_bf16 v[84:87], a[236:239], v[204:207], v[84:87]\n\t"
    "ds_read_b64_tr_b16 a[214:215], v19 offset:19456\n\t"
    "v_fma_mix_f32 v64, v64, v34, 0 op_sel_hi:[0,1,0]\n\t"
    "v_fma_mix_f32 v66, v66, v35, 0 op_sel_hi:[0,1,0]\n\t"
    "v_mfma_f32_16x16x32_bf16 v[88:91], a[236:239], v[228:231], v[88:91]\n\t"
    "v_fma_mix_f32 v65, v65, v34, 0 op_sel:[0,1,0] op_sel_hi:[0,1,0]\n\t"
    "v_fma_mix_f32 v67, v67, v35, 0 op_sel:[0,1,0] op_sel_hi:[0,1,0]\n\t"
    "v_cvt_pk_bf16_f32 v128, v64, v65\n\t"
    "v_cvt_pk_bf16_f32 v129, v66, v67\n\t"
    "s_waitcnt lgkmcnt(4)\n\t"
    "v_mfma_f32_16x16x32_bf16 a[0:3], a[192:195], v[144:147], a[0:3]\n\t"
    "ds_read_b64_tr_b16 a[216:217], v20 offset:13312\n\t"
    "v_cvt_pk_f16_f32 v38, v68, v69\n\t"
    "v_cvt_pk_f16_f32 v39, v70, v71\n\t"
    "v_pk_fma_f16 v40, v38, v38, s60 neg_lo:[1,0,1] neg_hi:[1,0,1]\n\t"
    "v_mfma_f32_16x16x32_bf16 a[4:7], a[192:195], v[148:151], a[4:7]\n\t"
    "ds_read_b64_tr_b16 a[218:219], v20 offset:19456\n\t"
    "s_add_u32 m0, s67, 12288\n\t"
    "s_add_u32 s63, s65, 147456\n\t"
    "v_mfma_f32_16x16x32_bf16 a[8:11], a[192:195], v[152:155], a[8:11]\n\t"
    "buffer_load_dwordx4 v26, s[40:43], s63 offen offset:0 lds\n\t"
    "v_pk_fma_f16 v41, v39, v39, s60 neg_lo:[1,0,1] neg_hi:[1,0,1]\n\t"
    "v_pk_mul_f16 v40, v40, v38\n\t"
    "v_mfma_f32_16x16x32_bf16 a[12:15], a[196:199], v[144:147], a[12:15]\n\t"
    "ds_read_b64_tr_b16 a[220:221], v21 offset:13312\n\t"
    "v_pk_mul_f16 v41, v41, v39\n\t"
    "v_exp_f16 v42, v40\n\t"
    "v_mfma_f32_16x16x32_bf16 a[16:19], a[196:199], v[148:151], a[16:19]\n\t"
    "ds_read_b64_tr_b16 a[222:223], v21 offset:19456\n\t"
    "v_exp_f16 v43, v41\n\t"
    "v_mfma_f32_16x16x32_bf16 a[20:23], a[196:199], v[152:155], a[20:23]\n\t"
    "buffer_load_dwordx4 v27, s[40:43], s63 offen offset:1024 lds\n\t"
    "v_exp_f16_sdwa v42, v40 dst_sel:WORD_1 dst_unused:UNUSED_PRESERVE src0_sel:WORD_1\n\t"
    "v_mfma_f32_16x16x32_bf16 a[24:27], a[200:203], v[144:147], a[24:27]\n\t"
    "ds_read_b64_tr_b16 a[224:225], v18 offset:14336\n\t"
    "v_exp_f16_sdwa v43, v41 dst_sel:WORD_1 dst_unused:UNUSED_PRESERVE src0_sel:WORD_1\n\t"
    "v_mfma_f32_16x16x32_bf16 a[28:31], a[200:203], v[148:151], a[28:31]\n\t"
    "ds_read_b64_tr_b16 a[226:227], v18 offset:20480\n\t"
    "v_pk_add_f16 v42, v42, 1.0 op_sel_hi:[1,0]\n\t"
    "v_pk_add_f16 v43, v43, 1.0 op_sel_hi:[1,0]\n\t"
    "v_mfma_f32_16x16x32_bf16 a[32:35], a[200:203], v[152:155], a[32:35]\n\t"
    "buffer_load_dwordx4 v28, s[40:43], s63 offen offset:2048 lds\n\t"
    "v_rcp_f16 v40, v42\n\t"
    "v_mfma_f32_16x16x32_bf16 a[36:39], a[204:207], v[144:147], a[36:39]\n\t"
    "ds_read_b64_tr_b16 a[228:229], v19 offset:14336\n\t"
    "v_rcp_f16 v41, v43\n\t"
    "v_mfma_f32_16x16x32_bf16 a[40:43], a[204:207], v[148:151], a[40:43]\n\t"
    "ds_read_b64_tr_b16 a[230:231], v19 offset:20480\n\t"
    "s_add_u32 s65, s65, 128\n\t"
    "s_add_u32 s68, s68, s30\n\t"
    "s_add_u32 m0, s68, 0\n\t"
    "v_mfma_f32_16x16x32_bf16 a[44:47], a[204:207], v[152:155], a[44:47]\n\t"
    "buffer_load_dwordx4 v29, s[44:47], s66 offen offset:0 lds\n\t"
    "s_waitcnt lgkmcnt(4)\n\t"
    "v_mfma_f32_16x16x32_bf16 a[48:51], a[208:211], v[144:147], a[48:51]\n\t"
    "ds_read_b64_tr_b16 a[232:233], v20 offset:14336\n\t"
    "v_rcp_f16_sdwa v40, v42 dst_sel:WORD_1 dst_unused:UNUSED_PRESERVE src0_sel:WORD_1\n\t"
    "v_mfma_f32_16x16x32_bf16 a[52:55], a[208:211], v[148:151], a[52:55]\n\t"
    "ds_read_b64_tr_b16 a[234:235], v20 offset:20480\n\t"
    "v_rcp_f16_sdwa v41, v43 dst_sel:WORD_1 dst_unused:UNUSED_PRESERVE src0_sel:WORD_1\n\t"
    "v_fma_mix_f32 v68, v68, v40, 0 op_sel_hi:[0,1,0]\n\t"
    "v_mfma_f32_16x16x32_bf16 a[56:59], a[208:211], v[152:155], a[56:59]\n\t"
    "buffer_load_dwordx4 v30, s[44:47], s66 offen offset:1024 lds\n\t"
    "v_fma_mix_f32 v70, v70, v41, 0 op_sel_hi:[0,1,0]\n\t"
    "v_fma_mix_f32 v69, v69, v40, 0 op_sel:[0,1,0] op_sel_hi:[0,1,0]\n\t"
    "v_mfma_f32_16x16x32_bf16 a[60:63], a[212:215], v[144:147], a[60:63]\n\t"
    "ds_read_b64_tr_b16 a[236:237], v21 offset:14336\n\t"
    "v_fma_mix_f32 v71, v71, v41, 0 op_sel:[0,1,0] op_sel_hi:[0,1,0]\n\t"
    "v_cvt_pk_bf16_f32 v132, v68, v69\n\t"
    "v_mfma_f32_16x16x32_bf16 a[64:67], a[212:215], v[148:151], a[64:67]\n\t"
    "ds_read_b64_tr_b16 a[238:239], v21 offset:20480\n\t"
    "v_cvt_pk_bf16_f32 v133, v70, v71\n\t"
    "v_cvt_pk_f16_f32 v32, v72, v73\n\t"
    "v_cvt_pk_f16_f32 v33, v74, v75\n\t"
    "v_mfma_f32_16x16x32_bf16 a[68:71], a[212:215], v[152:155], a[68:71]\n\t"
    "buffer_load_dwordx4 v31, s[44:47], s66 offen offset:2048 lds\n\t"
    "v_pk_fma_f16 v34, v32, v32, s60 neg_lo:[1,0,1] neg_hi:[1,0,1]\n\t"
    "v_mfma_f32_16x16x32_bf16 a[72:75], a[216:219], v[144:147], a[72:75]\n\t"
    "ds_read_b64_tr_b16 a[192:193], v16 offset:24576\n\t"
    "v_pk_fma_f16 v35, v33, v33, s60 neg_lo:[1,0,1] neg_hi:[1,0,1]\n\t"
    "v_pk_mul_f16 v34, v34, v32\n\t"
    "v_pk_mul_f16 v35, v35, v33\n\t"
    "v_mfma_f32_16x16x32_bf16 a[76:79], a[216:219], v[148:151], a[76:79]\n\t"
    "ds_read_b64_tr_b16 a[194:195], v16 offset:26624\n\t"
    "v_exp_f16 v36, v34\n\t"
    "s_add_u32 s66, s66, 12288\n\t"
    "v_mfma_f32_16x16x32_bf16 a[80:83], a[216:219], v[152:155], a[80:83]\n\t"
    "v_exp_f16 v37, v35\n\t"
    "v_mfma_f32_16x16x32_bf16 a[84:87], a[220:223], v[144:147], a[84:87]\n\t"
    "ds_read_b64_tr_b16 a[196:197], v16 offset:28672\n\t"
    "v_exp_f16_sdwa v36, v34 dst_sel:WORD_1 dst_unused:UNUSED_PRESERVE src0_sel:WORD_1\n\t"
    "v_exp_f16_sdwa v37, v35 dst_sel:WORD_1 dst_unused:UNUSED_PRESERVE src0_sel:WORD_1\n\t"
    "v_mfma_f32_16x16x32_bf16 a[88:91], a[220:223], v[148:151], a[88:91]\n\t"
    "ds_read_b64_tr_b16 a[198:199], v16 offset:30720\n\t"
    "v_pk_add_f16 v36, v36, 1.0 op_sel_hi:[1,0]\n\t"
    "v_pk_add_f16 v37, v37, 1.0 op_sel_hi:[1,0]\n\t"
    "v_mfma_f32_16x16x32_bf16 a[92:95], a[220:223], v[152:155], a[92:95]\n\t"
    "ds_read_b128 v[52:55], v22 offset:448\n\t"
    "v_rcp_f16 v34, v36\n\t"
    "s_waitcnt lgkmcnt(5)\n\t"
    "v_mfma_f32_16x16x32_bf16 a[96:99], a[224:227], v[144:147], a[96:99]\n\t"
    "ds_read_b64_tr_b16 a[200:201], v16 offset:32768\n\t"
    "v_rcp_f16 v35, v37\n\t"
    "v_mfma_f32_16x16x32_bf16 a[100:103], a[224:227], v[148:151], a[100:103]\n\t"
    "ds_read_b64_tr_b16 a[202:203], v16 offset:34816\n\t"
    "v_rcp_f16_sdwa v34, v36 dst_sel:WORD_1 dst_unused:UNUSED_PRESERVE src0_sel:WORD_1\n\t"
    "v_mfma_f32_16x16x32_bf16 a[104:107], a[224:227], v[152:155], a[104:107]\n\t"
    "v_rcp_f16_sdwa v35, v37 dst_sel:WORD_1 dst_unused:UNUSED_PRESERVE src0_sel:WORD_1\n\t"
    "v_fma_mix_f32 v72, v72, v34, 0 op_sel_hi:[0,1,0]\n\t"
    "v_fma_mix_f32 v74, v74, v35, 0 op_sel_hi:[0,1,0]\n\t"
    "v_mfma_f32_16x16x32_bf16 a[108:111], a[228:231], v[144:147], a[108:111]\n\t"
    "ds_read_b64_tr_b16 a[204:205], v16 offset:36864\n\t"
    "v_fma_mix_f32 v73, v73, v34, 0 op_sel:[0,1,0] op_sel_hi:[0,1,0]\n\t"
    "v_fma_mix_f32 v75, v75, v35, 0 op_sel:[0,1,0] op_sel_hi:[0,1,0]\n\t"
    "v_mfma_f32_16x16x32_bf16 a[112:115], a[228:231], v[148:151], a[112:115]\n\t"
    "ds_read_b64_tr_b16 a[206:207], v16 offset:38912\n\t"
    "v_cvt_pk_bf16_f32 v136, v72, v73\n\t"
    "v_cvt_pk_bf16_f32 v137, v74, v75\n\t"
    "v_cvt_pk_f16_f32 v38, v80, v81\n\t"
    "v_mfma_f32_16x16x32_bf16 a[116:119], a[228:231], v[152:155], a[116:119]\n\t"
    "ds_read_b128 v[48:51], v22 offset:384\n\t"
    "v_cvt_pk_f16_f32 v39, v82, v83\n\t"
    "v_pk_fma_f16 v40, v38, v38, s60 neg_lo:[1,0,1] neg_hi:[1,0,1]\n\t"
    "v_mfma_f32_16x16x32_bf16 a[120:123], a[232:235], v[144:147], a[120:123]\n\t"
    "ds_read_b64_tr_b16 a[208:209], v16 offset:40960\n\t"
    "v_pk_fma_f16 v41, v39, v39, s60 neg_lo:[1,0,1] neg_hi:[1,0,1]\n\t"
    "v_pk_mul_f16 v40, v40, v38\n\t"
    "v_pk_mul_f16 v41, v41, v39\n\t"
    "v_mfma_f32_16x16x32_bf16 a[124:127], a[232:235], v[148:151], a[124:127]\n\t"
    "ds_read_b64_tr_b16 a[210:211], v16 offset:43008\n\t"
    "v_exp_f16 v42, v40\n\t"
    "v_mfma_f32_16x16x32_bf16 a[128:131], a[232:235], v[152:155], a[128:131]\n\t"
    "v_exp_f16 v43, v41\n\t"
    "v_exp_f16_sdwa v42, v40 dst_sel:WORD_1 dst_unused:UNUSED_PRESERVE src0_sel:WORD_1\n\t"
    "v_mfma_f32_16x16x32_bf16 a[132:135], a[236:239], v[144:147], a[132:135]\n\t"
    "ds_read_b64_tr_b16 a[212:213], v16 offset:45056\n\t"
    "v_exp_f16_sdwa v43, v41 dst_sel:WORD_1 dst_unused:UNUSED_PRESERVE src0_sel:WORD_1\n\t"
    "v_mfma_f32_16x16x32_bf16 a[136:139], a[236:239], v[148:151], a[136:139]\n\t"
    "ds_read_b64_tr_b16 a[214:215], v16 offset:47104\n\t"
    "v_pk_add_f16 v42, v42, 1.0 op_sel_hi:[1,0]\n\t"
    "v_pk_add_f16 v43, v43, 1.0 op_sel_hi:[1,0]\n\t"
    "v_mfma_f32_16x16x32_bf16 a[140:143], a[236:239], v[152:155], a[140:143]\n\t"
    "v_rcp_f16 v40, v42\n\t"
    "v_rcp_f16 v41, v43\n\t"
    "s_waitcnt lgkmcnt(4)\n\t"
    "v_mfma_f32_16x16x32_bf16 v[96:99], a[192:195], v[160:163], v[48:51]\n\t"
    "ds_read_b64_tr_b16 a[216:217], v17 offset:24576\n\t"
    "v_rcp_f16_sdwa v40, v42 dst_sel:WORD_1 dst_unused:UNUSED_PRESERVE src0_sel:WORD_1\n\t"
    "v_mfma_f32_16x16x32_bf16 v[100:103], a[192:195], v[184:187], v[48:51]\n\t"
    "ds_read_b64_tr_b16 a[218:219], v17 offset:26624\n\t"
    "v_rcp_f16_sdwa v41, v43 dst_sel:WORD_1 dst_unused:UNUSED_PRESERVE src0_sel:WORD_1\n\t"
    "v_fma_mix_f32 v80, v80, v40, 0 op_sel_hi:[0,1,0]\n\t"
    "v_mfma_f32_16x16x32_bf16 v[104:107], a[192:195], v[208:211], v[48:51]\n\t"
    "v_fma_mix_f32 v82, v82, v41, 0 op_sel_hi:[0,1,0]\n\t"
    "v_fma_mix_f32 v81, v81, v40, 0 op_sel:[0,1,0] op_sel_hi:[0,1,0]\n\t"
    "v_fma_mix_f32 v83, v83, v41, 0 op_sel:[0,1,0] op_sel_hi:[0,1,0]\n\t"
    "v_mfma_f32_16x16x32_bf16 v[96:99], a[196:199], v[164:167], v[96:99]\n\t"
    "ds_read_b64_tr_b16 a[220:221], v17 offset:28672\n\t"
    "v_cvt_pk_bf16_f32 v130, v80, v81\n\t"
    "v_cvt_pk_bf16_f32 v131, v82, v83\n\t"
    "v_cvt_pk_f16_f32 v32, v84, v85\n\t"
    "v_mfma_f32_16x16x32_bf16 v[100:103], a[196:199], v[188:191], v[100:103]\n\t"
    "ds_read_b64_tr_b16 a[222:223], v17 offset:30720\n\t"
    "v_cvt_pk_f16_f32 v33, v86, v87\n\t"
    "v_pk_fma_f16 v34, v32, v32, s60 neg_lo:[1,0,1] neg_hi:[1,0,1]\n\t"
    "v_mfma_f32_16x16x32_bf16 v[104:107], a[196:199], v[212:215], v[104:107]\n\t"
    "v_pk_fma_f16 v35, v33, v33, s60 neg_lo:[1,0,1] neg_hi:[1,0,1]\n\t"
    "v_pk_mul_f16 v34, v34, v32\n\t"
    "v_pk_mul_f16 v35, v35, v33\n\t"
    "v_mfma_f32_16x16x32_bf16 v[96:99], a[200:203], v[168:171], v[96:99]\n\t"
    "ds_read_b64_tr_b16 a[224:225], v17 offset:32768\n\t"
    "v_exp_f16 v36, v34\n\t"
    "v_exp_f16 v37, v35\n\t"
    "v_mfma_f32_16x16x32_bf16 v[100:103], a[200:203], v[192:195], v[100:103]\n\t"
    "ds_read_b64_tr_b16 a[226:227], v17 offset:34816\n\t"
    "v_exp_f16_sdwa v36, v34 dst_sel:WORD_1 dst_unused:UNUSED_PRESERVE src0_sel:WORD_1\n\t"
    "v_mfma_f32_16x16x32_bf16 v[104:107], a[200:203], v[216:219], v[104:107]\n\t"
    "v_exp_f16_sdwa v37, v35 dst_sel:WORD_1 dst_unused:UNUSED_PRESERVE src0_sel:WORD_1\n\t"
    "v_pk_add_f16 v36, v36, 1.0 op_sel_hi:[1,0]\n\t"
    "v_mfma_f32_16x16x32_bf16 v[96:99], a[204:207], v[172:175], v[96:99]\n\t"
    "ds_read_b64_tr_b16 a[228:229], v17 offset:36864\n\t"
    "v_pk_add_f16 v37, v37, 1.0 op_sel_hi:[1,0]\n\t"
    "v_rcp_f16 v34, v36\n\t"
    "v_mfma_f32_16x16x32_bf16 v[100:103], a[204:207], v[196:199], v[100:103]\n\t"
    "ds_read_b64_tr_b16 a[230:231], v17 offset:38912\n\t"
    "v_rcp_f16 v35, v37\n\t"
    "v_mfma_f32_16x16x32_bf16 v[104:107], a[204:207], v[220:223], v[104:107]\n\t"
    "v_rcp_f16_sdwa v34, v36 dst_sel:WORD_1 dst_unused:UNUSED_PRESERVE src0_sel:WORD_1\n\t"
    "s_waitcnt lgkmcnt(4)\n\t"
    "v_mfma_f32_16x16x32_bf16 v[96:99], a[208:211], v[176:179], v[96:99]\n\t"
    "ds_read_b64_tr_b16 a[232:233], v17 offset:40960\n\t"
    "v_rcp_f16_sdwa v35, v37 dst_sel:WORD_1 dst_unused:UNUSED_PRESERVE src0_sel:WORD_1\n\t"
    "v_fma_mix_f32 v84, v84, v34, 0 op_sel_hi:[0,1,0]\n\t"
    "v_mfma_f32_16x16x32_bf16 v[100:103], a[208:211], v[200:203], v[100:103]\n\t"
    "ds_read_b64_tr_b16 a[234:235], v17 offset:43008\n\t"
    "v_fma_mix_f32 v86, v86, v35, 0 op_sel_hi:[0,1,0]\n\t"
    "v_fma_mix_f32 v85, v85, v34, 0 op_sel:[0,1,0] op_sel_hi:[0,1,0]\n\t"
    "v_fma_mix_f32 v87, v87, v35, 0 op_sel:[0,1,0] op_sel_hi:[0,1,0]\n\t"
    "v_mfma_f32_16x16x32_bf16 v[104:107], a[208:211], v[224:227], v[104:107]\n\t"
    "v_cvt_pk_bf16_f32 v134, v84, v85\n\t"
    "v_cvt_pk_bf16_f32 v135, v86, v87\n\t"
    "v_cvt_pk_f16_f32 v38, v88, v89\n\t"
    "v_mfma_f32_16x16x32_bf16 v[96:99], a[212:215], v[180:183], v[96:99]\n\t"
    "ds_read_b64_tr_b16 a[236:237], v17 offset:45056\n\t"
    "v_cvt_pk_f16_f32 v39, v90, v91\n\t"
    "v_pk_fma_f16 v40, v38, v38, s60 neg_lo:[1,0,1] neg_hi:[1,0,1]\n\t"
    "v_pk_fma_f16 v41, v39, v39, s60 neg_lo:[1,0,1] neg_hi:[1,0,1]\n\t"
    "v_mfma_f32_16x16x32_bf16 v[100:103], a[212:215], v[204:207], v[100:103]\n\t"
    "ds_read_b64_tr_b16 a[238:239], v17 offset:47104\n\t"
    "v_pk_mul_f16 v40, v40, v38\n\t"
    "v_pk_mul_f16 v41, v41, v39\n\t"
    "v_mfma_f32_16x16x32_bf16 v[104:107], a[212:215], v[228:231], v[104:107]\n\t"
    "v_exp_f16 v42, v40\n\t"
    "v_exp_f16 v43, v41\n\t"
    "v_mfma_f32_16x16x32_bf16 v[112:115], a[216:219], v[160:163], v[52:55]\n\t"
    "ds_read_b64_tr_b16 a[192:193], v18 offset:24576\n\t"
    "v_exp_f16_sdwa v42, v40 dst_sel:WORD_1 dst_unused:UNUSED_PRESERVE src0_sel:WORD_1\n\t"
    "v_mfma_f32_16x16x32_bf16 v[116:119], a[216:219], v[184:187], v[52:55]\n\t"
    "ds_read_b64_tr_b16 a[194:195], v18 offset:30720\n\t"
    "v_exp_f16_sdwa v43, v41 dst_sel:WORD_1 dst_unused:UNUSED_PRESERVE src0_sel:WORD_1\n\t"
    "v_pk_add_f16 v42, v42, 1.0 op_sel_hi:[1,0]\n\t"
    "v_mfma_f32_16x16x32_bf16 v[120:123], a[216:219], v[208:211], v[52:55]\n\t"
    "v_pk_add_f16 v43, v43, 1.0 op_sel_hi:[1,0]\n\t"
    "v_rcp_f16 v40, v42\n\t"
    "v_mfma_f32_16x16x32_bf16 v[112:115], a[220:223], v[164:167], v[112:115]\n\t"
    "ds_read_b64_tr_b16 a[196:197], v19 offset:24576\n\t"
    "v_rcp_f16 v41, v43\n\t"
    "v_mfma_f32_16x16x32_bf16 v[116:119], a[220:223], v[188:191], v[116:119]\n\t"
    "ds_read_b64_tr_b16 a[198:199], v19 offset:30720\n\t"
    "v_rcp_f16_sdwa v40, v42 dst_sel:WORD_1 dst_unused:UNUSED_PRESERVE src0_sel:WORD_1\n\t"
    "v_rcp_f16_sdwa v41, v43 dst_sel:WORD_1 dst_unused:UNUSED_PRESERVE src0_sel:WORD_1\n\t"
    "v_mfma_f32_16x16x32_bf16 v[120:123], a[220:223], v[212:215], v[120:123]\n\t"
    "v_fma_mix_f32 v88, v88, v40, 0 op_sel_hi:[0,1,0]\n\t"
    "v_fma_mix_f32 v90, v90, v41, 0 op_sel_hi:[0,1,0]\n\t"
    "s_waitcnt lgkmcnt(4)\n\t"
    "v_mfma_f32_16x16x32_bf16 v[112:115], a[224:227], v[168:171], v[112:115]\n\t"
    "ds_read_b64_tr_b16 a[200:201], v20 offset:24576\n\t"
    "v_fma_mix_f32 v89, v89, v40, 0 op_sel:[0,1,0] op_sel_hi:[0,1,0]\n\t"
    "v_fma_mix_f32 v91, v91, v41, 0 op_sel:[0,1,0] op_sel_hi:[0,1,0]\n\t"
    "v_mfma_f32_16x16x32_bf16 v[116:119], a[224:227], v[192:195], v[116:119]\n\t"
    "ds_read_b64_tr_b16 a[202:203], v20 offset:30720\n\t"
    "v_cvt_pk_bf16_f32 v138, v88, v89\n\t"
    "v_cvt_pk_bf16_f32 v139, v90, v91\n\t"
    "v_cvt_pk_f16_f32 v32, v96, v97\n\t"
    "v_mfma_f32_16x16x32_bf16 v[120:123], a[224:227], v[216:219], v[120:123]\n\t"
    "v_cvt_pk_f16_f32 v33, v98, v99\n\t"
    "v_pk_fma_f16 v34, v32, v32, s60 neg_lo:[1,0,1] neg_hi:[1,0,1]\n\t"
    "v_pk_fma_f16 v35, v33, v33, s60 neg_lo:[1,0,1] neg_hi:[1,0,1]\n\t"
    "v_mfma_f32_16x16x32_bf16 v[112:115], a[228:231], v[172:175], v[112:115]\n\t"
    "ds_read_b64_tr_b16 a[204:205], v21 offset:24576\n\t"
    "v_pk_mul_f16 v34, v34, v32\n\t"
    "v_pk_mul_f16 v35, v35, v33\n\t"
    "v_mfma_f32_16x16x32_bf16 v[116:119], a[228:231], v[196:199], v[116:119]\n\t"
    "ds_read_b64_tr_b16 a[206:207], v21 offset:30720\n\t"
    "v_exp_f16 v36, v34\n\t"
    "v_exp_f16 v37, v35\n\t"
    "v_mfma_f32_16x16x32_bf16 v[120:123], a[228:231], v[220:223], v[120:123]\n\t"
    "v_exp_f16_sdwa v36, v34 dst_sel:WORD_1 dst_unused:UNUSED_PRESERVE src0_sel:WORD_1\n\t"
    "v_mfma_f32_16x16x32_bf16 v[112:115], a[232:235], v[176:179], v[112:115]\n\t"
    "ds_read_b64_tr_b16 a[208:209], v18 offset:25600\n\t"
    "v_exp_f16_sdwa v37, v35 dst_sel:WORD_1 dst_unused:UNUSED_PRESERVE src0_sel:WORD_1\n\t"
    "v_pk_add_f16 v36, v36, 1.0 op_sel_hi:[1,0]\n\t"
    "v_mfma_f32_16x16x32_bf16 v[116:119], a[232:235], v[200:203], v[116:119]\n\t"
    "ds_read_b64_tr_b16 a[210:211], v18 offset:31744\n\t"
    "v_pk_add_f16 v37, v37, 1.0 op_sel_hi:[1,0]\n\t"
    "v_rcp_f16 v34, v36\n\t"
    "v_mfma_f32_16x16x32_bf16 v[120:123], a[232:235], v[224:227], v[120:123]\n\t"
    "v_rcp_f16 v35, v37\n\t"
    "v_rcp_f16_sdwa v34, v36 dst_sel:WORD_1 dst_unused:UNUSED_PRESERVE src0_sel:WORD_1\n\t"
    "v_mfma_f32_16x16x32_bf16 v[112:115], a[236:239], v[180:183], v[112:115]\n\t"
    "ds_read_b64_tr_b16 a[212:213], v19 offset:25600\n\t"
    "v_rcp_f16_sdwa v35, v37 dst_sel:WORD_1 dst_unused:UNUSED_PRESERVE src0_sel:WORD_1\n\t"
    "v_mfma_f32_16x16x32_bf16 v[116:119], a[236:239], v[204:207], v[116:119]\n\t"
    "ds_read_b64_tr_b16 a[214:215], v19 offset:31744\n\t"
    "v_fma_mix_f32 v96, v96, v34, 0 op_sel_hi:[0,1,0]\n\t"
    "v_fma_mix_f32 v98, v98, v35, 0 op_sel_hi:[0,1,0]\n\t"
    "v_mfma_f32_16x16x32_bf16 v[120:123], a[236:239], v[228:231], v[120:123]\n\t"
    "v_fma_mix_f32 v97, v97, v34, 0 op_sel:[0,1,0] op_sel_hi:[0,1,0]\n\t"
    "v_fma_mix_f32 v99, v99, v35, 0 op_sel:[0,1,0] op_sel_hi:[0,1,0]\n\t"
    "v_cvt_pk_bf16_f32 v144, v96, v97\n\t"
    "v_cvt_pk_bf16_f32 v145, v98, v99\n\t"
    "v_add_u32 v22, 512, v22\n\t"
    "s_sub_u32 s30, 0, s30\n\t"
    "s_sub_u32 s69, s69, 1\n\t"
    "s_cmp_lg_u32 s69, 0\n\t"
    "s_cbranch_scc1 loop_a_%=\n\t"
    "s_add_u32 s61, s75, s57\n\t"
    "s_mul_i32 s61, s61, 224\n\t"
    "s_add_u32 s61, s61, s58\n\t"
    "s_mul_i32 s70, s61, 384\n\t"
    "s_add_u32 s71, s70, 86016\n\t"
    "s_add_u32 s72, s70, 172032\n\t"
    "v_lshlrev_b32 v26, 4, v0\n\t"
    "v_add_u32 v27, 4096, v26\n\t"
    "s_mov_b32 s64, 0xffff0000\n\t"
    "s_waitcnt lgkmcnt(0)\n\t"
    "v_mfma_f32_16x16x32_bf16 a[0:3], a[192:195], v[128:131], a[0:3]\n\t"
    "ds_read_b64_tr_b16 a[216:217], v20 offset:25600\n\t"
    "ds_read_b64_tr_b16 a[218:219], v20 offset:31744\n\t"
    "v_cvt_pk_f16_f32 v38, v112, v113\n\t"
    "v_cvt_pk_f16_f32 v39, v114, v115\n\t"
    "v_pk_fma_f16 v40, v38, v38, s60 neg_lo:[1,0,1] neg_hi:[1,0,1]\n\t"
    "v_pk_fma_f16 v41, v39, v39, s60 neg_lo:[1,0,1] neg_hi:[1,0,1]\n\t"
    "v_pk_mul_f16 v40, v40, v38\n\t"
    "v_mfma_f32_16x16x32_bf16 a[12:15], a[196:199], v[128:131], a[12:15]\n\t"
    "ds_read_b64_tr_b16 a[220:221], v21 offset:25600\n\t"
    "ds_read_b64_tr_b16 a[222:223], v21 offset:31744\n\t"
    "v_pk_mul_f16 v41, v41, v39\n\t"
    "v_exp_f16 v42, v40\n\t"
    "v_exp_f16 v43, v41\n\t"
    "v_mfma_f32_16x16x32_bf16 a[24:27], a[200:203], v[128:131], a[24:27]\n\t"
    "ds_read_b64_tr_b16 a[224:225], v18 offset:26624\n\t"
    "ds_read_b64_tr_b16 a[226:227], v18 offset:32768\n\t"
    "v_exp_f16_sdwa v42, v40 dst_sel:WORD_1 dst_unused:UNUSED_PRESERVE src0_sel:WORD_1\n\t"
    "v_exp_f16_sdwa v43, v41 dst_sel:WORD_1 dst_unused:UNUSED_PRESERVE src0_sel:WORD_1\n\t"
    "v_pk_add_f16 v42, v42, 1.0 op_sel_hi:[1,0]\n\t"
    "s_waitcnt vmcnt(0)\n\t"
    "s_barrier\n\t"
    "v_mfma_f32_16x16x32_bf16 a[36:39], a[204:207], v[128:131], a[36:39]\n\t"
    "ds_read_b64_tr_b16 a[228:229], v19 offset:26624\n\t"
    "ds_read_b64_tr_b16 a[230:231], v19 offset:32768\n\t"
    "buffer_load_dwordx4 v[160:163], v26, s[36:39], s70 offen offset:0\n\t"
    "v_pk_add_f16 v43, v43, 1.0 op_sel_hi:[1,0]\n\t"
    "v_rcp_f16 v40, v42\n\t"
    "v_rcp_f16 v41, v43\n\t"
    "v_mfma_f32_16x16x32_bf16 a[48:51], a[208:211], v[128:131], a[48:51]\n\t"
    "ds_read_b64_tr_b16 a[232:233], v20 offset:26624\n\t"
    "ds_read_b64_tr_b16 a[234:235], v20 offset:32768\n\t"
    "v_rcp_f16_sdwa v40, v42 dst_sel:WORD_1 dst_unused:UNUSED_PRESERVE src0_sel:WORD_1\n\t"
    "v_rcp_f16_sdwa v41, v43 dst_sel:WORD_1 dst_unused:UNUSED_PRESERVE src0_sel:WORD_1\n\t"
    "v_fma_mix_f32 v112, v112, v40, 0 op_sel_hi:[0,1,0]\n\t"
    "v_mfma_f32_16x16x32_bf16 a[60:63], a[212:215], v[128:131], a[60:63]\n\t"
    "ds_read_b64_tr_b16 a[236:237], v21 offset:26624\n\t"
    "ds_read_b64_tr_b16 a[238:239], v21 offset:32768\n\t"
    "buffer_load_dwordx4 v[164:167], v26, s[36:39], s70 offen offset:1024\n\t"
    "v_fma_mix_f32 v114, v114, v41, 0 op_sel_hi:[0,1,0]\n\t"
    "v_fma_mix_f32 v113, v113, v40, 0 op_sel:[0,1,0] op_sel_hi:[0,1,0]\n\t"
    "v_fma_mix_f32 v115, v115, v41, 0 op_sel:[0,1,0] op_sel_hi:[0,1,0]\n\t"
    "v_cvt_pk_bf16_f32 v146, v112, v113\n\t"
    "v_cvt_pk_bf16_f32 v147, v114, v115\n\t"
    "s_waitcnt lgkmcnt(6)\n\t"
    "v_mfma_f32_16x16x32_bf16 a[72:75], a[216:219], v[128:131], a[72:75]\n\t"
    "ds_read_b64_tr_b16 a[144:145], v18 offset:36864\n\t"
    "ds_read_b64_tr_b16 a[146:147], v18 offset:43008\n\t"
    "s_mul_i32 s61, s79, 25088\n\t"
    "v_and_b32 v40, 15, v0\n\t"
    "v_mul_u32_u24 v40, 784, v40\n\t"
    "v_bfe_u32 v41, v0, 4, 1\n\t"
    "v_mul_u32_u24 v41, 384, v41\n\t"
    "v_mfma_f32_16x16x32_bf16 a[84:87], a[220:223], v[128:131], a[84:87]\n\t"
    "ds_read_b64_tr_b16 a[148:149], v19 offset:36864\n\t"
    "ds_read_b64_tr_b16 a[150:151], v19 offset:43008\n\t"
    "buffer_load_dwordx4 v[168:171], v26, s[36:39], s70 offen offset:2048\n\t"
    "v_add3_u32 v40, v40, v41, s61\n\t"
    "v_lshrrev_b32 v41, 5, v0\n\t"
    "v_lshl_add_u32 v29, v41, 4, v40\n\t"
    "v_add_u32 v40, 0, v0\n\t"
    "v_mul_u32_u24 v41, 2731, v40\n\t"
    "v_mfma_f32_16x16x32_bf16 a[96:99], a[224:227], v[128:131], a[96:99]\n\t"
    "ds_read_b64_tr_b16 a[152:153], v20 offset:36864\n\t"
    "ds_read_b64_tr_b16 a[154:155], v20 offset:43008\n\t"
    "v_lshrrev_b32 v41, 16, v41\n\t"
    "v_mul_u32_u24 v42, 24, v41\n\t"
    "v_sub_u32 v40, v40, v42\n\t"
    "v_mul_u32_u24 v41, 784, v41\n\t"
    "v_lshl_add_u32 v41, v40, 4, v41\n\t"
    "s_waitcnt lgkmcnt(6)\n\t"
    "v_mfma_f32_16x16x32_bf16 a[108:111], a[228:231], v[128:131], a[108:111]\n\t"
    "ds_read_b64_tr_b16 a[156:157], v21 offset:36864\n\t"
    "ds_read_b64_tr_b16 a[158:159], v21 offset:43008\n\t"
    "v_add_u32 v14, s61, v41\n\t"
    "v_lshlrev_b32 v40, 5, v40\n\t"
    "v_add_u32 v44, 157440, v40\n\t"
    "v_add_u32 v40, 64, v0\n\t"
    "v_mul_u32_u24 v41, 2731, v40\n\t"
    "v_mfma_f32_16x16x32_bf16 a[120:123], a[232:235], v[128:131], a[120:123]\n\t"
    "ds_read_b64_tr_b16 a[160:161], v18 offset:37888\n\t"
    "ds_read_b64_tr_b16 a[162:163], v18 offset:44032\n\t"
    "v_lshrrev_b32 v41, 16, v41\n\t"
    "v_mul_u32_u24 v42, 24, v41\n\t"
    "v_sub_u32 v40, v40, v42\n\t"
    "v_mul_u32_u24 v41, 784, v41\n\t"
    "v_lshl_add_u32 v41, v40, 4, v41\n\t"
    "v_mfma_f32_16x16x32_bf16 a[132:135], a[236:239], v[128:131], a[132:135]\n\t"
    "ds_read_b64_tr_b16 a[164:165], v19 offset:37888\n\t"
    "ds_read_b64_tr_b16 a[166:167], v19 offset:44032\n\t"
    "v_add_u32 v15, s61, v41\n\t"
    "v_lshlrev_b32 v40, 5, v40\n\t"
    "v_add_u32 v45, 157440, v40\n\t"
    "v_add_u32 v40, 128, v0\n\t"
    "v_mul_u32_u24 v41, 2731, v40\n\t"
    "s_waitcnt lgkmcnt(6)\n\t"
    "v_mfma_f32_16x16x32_bf16 a[0:3], a[144:147], v[144:147], a[0:3]\n\t"
    "ds_read_b64_tr_b16 a[168:169], v20 offset:37888\n\t"
    "ds_read_b64_tr_b16 a[170:171], v20 offset:44032\n\t"
    "v_lshrrev_b32 v41, 16, v41\n\t"
    "v_mul_u32_u24 v42, 24, v41\n\t"
    "v_sub_u32 v40, v40, v42\n\t"
    "v_mul_u32_u24 v41, 784, v41\n\t"
    "v_lshl_add_u32 v41, v40, 4, v41\n\t"
    "v_mfma_f32_16x16x32_bf16 a[12:15], a[148:151], v[144:147], a[12:15]\n\t"
    "ds_read_b64_tr_b16 a[172:173], v21 offset:37888\n\t"
    "ds_read_b64_tr_b16 a[174:175], v21 offset:44032\n\t"
    "v_add_u32 v16, s61, v41\n\t"
    "v_lshlrev_b32 v40, 5, v40\n\t"
    "v_add_u32 v46, 157440, v40\n\t"
    "v_mfma_f32_16x16x32_bf16 a[24:27], a[152:155], v[144:147], a[24:27]\n\t"
    "ds_read_b64_tr_b16 a[176:177], v18 offset:38912\n\t"
    "ds_read_b64_tr_b16 a[178:179], v18 offset:45056\n\t"
    "s_waitcnt lgkmcnt(6)\n\t"
    "v_mfma_f32_16x16x32_bf16 a[36:39], a[156:159], v[144:147], a[36:39]\n\t"
    "ds_read_b64_tr_b16 a[180:181], v19 offset:38912\n\t"
    "ds_read_b64_tr_b16 a[182:183], v19 offset:45056\n\t"
    "v_mfma_f32_16x16x32_bf16 a[48:51], a[160:163], v[144:147], a[48:51]\n\t"
    "ds_read_b64_tr_b16 a[184:185], v20 offset:38912\n\t"
    "ds_read_b64_tr_b16 a[186:187], v20 offset:45056\n\t"
    "ds_write_b128 v29, a[0:3] offset:0\n\t"
    "v_mfma_f32_16x16x32_bf16 a[60:63], a[164:167], v[144:147], a[60:63]\n\t"
    "ds_read_b64_tr_b16 a[188:189], v21 offset:38912\n\t"
    "ds_read_b64_tr_b16 a[190:191], v21 offset:45056\n\t"
    "ds_write_b128 v29, a[12:15] offset:32\n\t"
    "s_waitcnt lgkmcnt(8)\n\t"
    "v_mfma_f32_16x16x32_bf16 a[72:75], a[168:171], v[144:147], a[72:75]\n\t"
    "ds_write_b128 v29, a[24:27] offset:64\n\t"
    "v_mfma_f32_16x16x32_bf16 a[84:87], a[172:175], v[144:147], a[84:87]\n\t"
    "ds_write_b128 v29, a[36:39] offset:96\n\t"
    "v_mfma_f32_16x16x32_bf16 a[96:99], a[176:179], v[144:147], a[96:99]\n\t"
    "ds_write_b128 v29, a[48:51] offset:128\n\t"
    "s_waitcnt lgkmcnt(4)\n\t"
    "v_mfma_f32_16x16x32_bf16 a[108:111], a[180:183], v[144:147], a[108:111]\n\t"
    "ds_write_b128 v29, a[60:63] offset:160\n\t"
    "v_mfma_f32_16x16x32_bf16 a[120:123], a[184:187], v[144:147], a[120:123]\n\t"
    "ds_write_b128 v29, a[72:75] offset:192\n\t"
    "ds_read_b128 v[64:67], v44\n\t"
    "v_mfma_f32_16x16x32_bf16 a[132:135], a[188:191], v[144:147], a[132:135]\n\t"
    "ds_write_b128 v29, a[84:87] offset:224\n\t"
    "ds_read_b128 v[68:71], v44 offset:16\n\t"
    "v_mfma_f32_16x16x32_bf16 a[4:7], a[192:195], v[132:135], a[4:7]\n\t"
    "ds_write_b128 v29, a[96:99] offset:256\n\t"
    "ds_read_b128 v[72:75], v45\n\t"
    "v_mfma_f32_16x16x32_bf16 a[16:19], a[196:199], v[132:135], a[16:19]\n\t"
    "ds_write_b128 v29, a[108:111] offset:288\n\t"
    "ds_read_b128 v[76:79], v45 offset:16\n\t"
    "v_mfma_f32_16x16x32_bf16 a[28:31], a[200:203], v[132:135], a[28:31]\n\t"
    "ds_write_b128 v29, a[120:123] offset:320\n\t"
    "ds_read_b128 v[80:83], v46\n\t"
    "v_mfma_f32_16x16x32_bf16 a[40:43], a[204:207], v[132:135], a[40:43]\n\t"
    "s_waitcnt lgkmcnt(11)\n\t"
    "ds_write_b128 v29, a[132:135] offset:352\n\t"
    "ds_read_b128 v[84:87], v46 offset:16\n\t"
    "v_mfma_f32_16x16x32_bf16 a[52:55], a[208:211], v[132:135], a[52:55]\n\t"
    "v_mfma_f32_16x16x32_bf16 a[64:67], a[212:215], v[132:135], a[64:67]\n\t"
    "buffer_load_dwordx4 v[172:175], v26, s[36:39], s70 offen offset:3072\n\t"
    "ds_read_b128 v[88:91], v14 offset:0\n\t"
    "ds_read_b128 v[92:95], v14 offset:384\n\t"
    "s_waitcnt vmcnt(3)\n\t"
    "v_lshlrev_b32 v108, 16, v160\n\t"
    "v_and_b32 v109, s64, v160\n\t"
    "v_lshlrev_b32 v110, 16, v161\n\t"
    "v_and_b32 v111, s64, v161\n\t"
    "v_lshlrev_b32 v112, 16, v162\n\t"
    "v_and_b32 v113, s64, v162\n\t"
    "v_lshlrev_b32 v114, 16, v163\n\t"
    "v_and_b32 v115, s64, v163\n\t"
    "v_cvt_pk_f16_f32 v38, v100, v101\n\t"
    "v_cvt_pk_f16_f32 v39, v102, v103\n\t"
    "v_pk_fma_f16 v40, v38, v38, s60 neg_lo:[1,0,1] neg_hi:[1,0,1]\n\t"
    "v_mfma_f32_16x16x32_bf16 a[76:79], a[216:219], v[132:135], a[76:79]\n\t"
    "v_pk_fma_f16 v41, v39, v39, s60 neg_lo:[1,0,1] neg_hi:[1,0,1]\n\t"
    "v_pk_mul_f16 v40, v40, v38\n\t"
    "v_pk_mul_f16 v41, v41, v39\n\t"
    "v_exp_f16 v42, v40\n\t"
    "v_exp_f16 v43, v41\n\t"
    "v_exp_f16_sdwa v42, v40 dst_sel:WORD_1 dst_unused:UNUSED_PRESERVE src0_sel:WORD_1\n\t"
    "v_exp_f16_sdwa v43, v41 dst_sel:WORD_1 dst_unused:UNUSED_PRESERVE src0_sel:WORD_1\n\t"
    "v_pk_add_f16 v42, v42, 1.0 op_sel_hi:[1,0]\n\t"
    "v_mfma_f32_16x16x32_bf16 a[88:91], a[220:223], v[132:135], a[88:91]\n\t"
    "buffer_load_dwordx4 v[176:179], v27, s[36:39], s70 offen offset:0\n\t"
    "v_pk_add_f16 v43, v43, 1.0 op_sel_hi:[1,0]\n\t"
    "v_rcp_f16 v40, v42\n\t"
    "v_rcp_f16 v41, v43\n\t"
    "v_rcp_f16_sdwa v40, v42 dst_sel:WORD_1 dst_unused:UNUSED_PRESERVE src0_sel:WORD_1\n\t"
    "v_rcp_f16_sdwa v41, v43 dst_sel:WORD_1 dst_unused:UNUSED_PRESERVE src0_sel:WORD_1\n\t"
    "v_fma_mix_f32 v100, v100, v40, 0 op_sel_hi:[0,1,0]\n\t"
    "v_fma_mix_f32 v102, v102, v41, 0 op_sel_hi:[0,1,0]\n\t"
    "v_fma_mix_f32 v101, v101, v40, 0 op_sel:[0,1,0] op_sel_hi:[0,1,0]\n\t"
    "v_mfma_f32_16x16x32_bf16 a[100:103], a[224:227], v[132:135], a[100:103]\n\t"
    "v_fma_mix_f32 v103, v103, v41, 0 op_sel:[0,1,0] op_sel_hi:[0,1,0]\n\t"
    "v_cvt_pk_bf16_f32 v148, v100, v101\n\t"
    "v_cvt_pk_bf16_f32 v149, v102, v103\n\t"
    "v_cvt_pk_f16_f32 v32, v116, v117\n\t"
    "v_cvt_pk_f16_f32 v33, v118, v119\n\t"
    "v_pk_fma_f16 v34, v32, v32, s60 neg_lo:[1,0,1] neg_hi:[1,0,1]\n\t"
    "v_pk_fma_f16 v35, v33, v33, s60 neg_lo:[1,0,1] neg_hi:[1,0,1]\n\t"
    "v_pk_mul_f16 v34, v34, v32\n\t"
    "v_pk_mul_f16 v35, v35, v33\n\t"
    "v_exp_f16 v36, v34\n\t"
    "v_mfma_f32_16x16x32_bf16 a[112:115], a[228:231], v[132:135], a[112:115]\n\t"
    "buffer_load_dwordx4 v[180:183], v27, s[36:39], s70 offen offset:1024\n\t"
    "v_exp_f16 v37, v35\n\t"
    "v_exp_f16_sdwa v36, v34 dst_sel:WORD_1 dst_unused:UNUSED_PRESERVE src0_sel:WORD_1\n\t"
    "v_exp_f16_sdwa v37, v35 dst_sel:WORD_1 dst_unused:UNUSED_PRESERVE src0_sel:WORD_1\n\t"
    "v_pk_add_f16 v36, v36, 1.0 op_sel_hi:[1,0]\n\t"
    "v_pk_add_f16 v37, v37, 1.0 op_sel_hi:[1,0]\n\t"
    "v_rcp_f16 v34, v36\n\t"
    "v_rcp_f16 v35, v37\n\t"
    "v_mfma_f32_16x16x32_bf16 a[124:127], a[232:235], v[132:135], a[124:127]\n\t"
    "v_rcp_f16_sdwa v34, v36 dst_sel:WORD_1 dst_unused:UNUSED_PRESERVE src0_sel:WORD_1\n\t"
    "v_rcp_f16_sdwa v35, v37 dst_sel:WORD_1 dst_unused:UNUSED_PRESERVE src0_sel:WORD_1\n\t"
    "v_fma_mix_f32 v116, v116, v34, 0 op_sel_hi:[0,1,0]\n\t"
    "v_fma_mix_f32 v118, v118, v35, 0 op_sel_hi:[0,1,0]\n\t"
    "v_fma_mix_f32 v117, v117, v34, 0 op_sel:[0,1,0] op_sel_hi:[0,1,0]\n\t"
    "v_fma_mix_f32 v119, v119, v35, 0 op_sel:[0,1,0] op_sel_hi:[0,1,0]\n\t"
    "v_cvt_pk_bf16_f32 v150, v116, v117\n\t"
    "v_cvt_pk_bf16_f32 v151, v118, v119\n\t"
    "s_waitcnt lgkmcnt(0)\n\t"
    "v_fmac_f32 v108, v88, v64\n\t"
    "v_mfma_f32_16x16x32_bf16 a[136:139], a[236:239], v[132:135], a[136:139]\n\t"
    "buffer_load_dwordx4 v[184:187], v26, s[36:39], s71 offen offset:0\n\t"
    "v_fmac_f32 v109, v89, v65\n\t"
    "v_fmac_f32 v110, v90, v66\n\t"
    "v_fmac_f32 v111, v91, v67\n\t"
    "v_fmac_f32 v112, v92, v68\n\t"
    "v_fmac_f32 v113, v93, v69\n\t"
    "v_fmac_f32 v114, v94, v70\n\t"
    "v_fmac_f32 v115, v95, v71\n\t"
    "v_cvt_pk_bf16_f32 v116, v108, v109\n\t"
    "v_cvt_pk_bf16_f32 v117, v110, v111\n\t"
    "v_cvt_pk_bf16_f32 v118, v112, v113\n\t"
    "v_cvt_pk_bf16_f32 v119, v114, v115\n\t"
    "v_mfma_f32_16x16x32_bf16 a[4:7], a[144:147], v[148:151], a[4:7]\n\t"
    "buffer_store_dwordx4 v[116:119], v26, s[48:51], s70 offen offset:0 sc1 nt\n\t"
    "v_mfma_f32_16x16x32_bf16 a[16:19], a[148:151], v[148:151], a[16:19]\n\t"
    "buffer_load_dwordx4 v[188:191], v26, s[36:39], s71 offen offset:1024\n\t"
    "v_mfma_f32_16x16x32_bf16 a[28:31], a[152:155], v[148:151], a[28:31]\n\t"
    "v_mfma_f32_16x16x32_bf16 a[40:43], a[156:159], v[148:151], a[40:43]\n\t"
    "buffer_load_dwordx4 v[192:195], v26, s[36:39], s71 offen offset:2048\n\t"
    "ds_read_b128 v[96:99], v15 offset:0\n\t"
    "ds_read_b128 v[100:103], v15 offset:384\n\t"
    "s_waitcnt vmcnt(8)\n\t"
    "v_lshlrev_b32 v108, 16, v164\n\t"
    "v_and_b32 v109, s64, v164\n\t"
    "v_lshlrev_b32 v110, 16, v165\n\t"
    "v_and_b32 v111, s64, v165\n\t"
    "v_lshlrev_b32 v112, 16, v166\n\t"
    "v_mfma_f32_16x16x32_bf16 a[52:55], a[160:163], v[148:151], a[52:55]\n\t"
    "v_and_b32 v113, s64, v166\n\t"
    "v_lshlrev_b32 v114, 16, v167\n\t"
    "v_and_b32 v115, s64, v167\n\t"
    "v_mfma_f32_16x16x32_bf16 a[64:67], a[164:167], v[148:151], a[64:67]\n\t"
    "buffer_load_dwordx4 v[196:199], v26, s[36:39], s71 offen offset:3072\n\t"
    "v_mfma_f32_16x16x32_bf16 a[76:79], a[168:171], v[148:151], a[76:79]\n\t"
    "ds_write_b128 v29, a[4:7] offset:12544\n\t"
    "s_waitcnt lgkmcnt(1)\n\t"
    "v_fmac_f32 v108, v96, v72\n\t"
    "v_fmac_f32 v109, v97, v73\n\t"
    "v_fmac_f32 v110, v98, v74\n\t"
    "v_fmac_f32 v111, v99, v75\n\t"
    "v_fmac_f32 v112, v100, v76\n\t"
    "v_mfma_f32_16x16x32_bf16 a[88:91], a[172:175], v[148:151], a[88:91]\n\t"
    "buffer_load_dwordx4 v[200:203], v27, s[36:39], s71 offen offset:0\n\t"
    "v_fmac_f32 v113, v101, v77\n\t"
    "v_fmac_f32 v114, v102, v78\n\t"
    "v_fmac_f32 v115, v103, v79\n\t"
    "v_cvt_pk_bf16_f32 v124, v108, v109\n\t"
    "v_cvt_pk_bf16_f32 v125, v110, v111\n\t"
    "v_cvt_pk_bf16_f32 v126, v112, v113\n\t"
    "v_mfma_f32_16x16x32_bf16 a[100:103], a[176:179], v[148:151], a[100:103]\n\t"
    "ds_write_b128 v29, a[16:19] offset:12576\n\t"
    "v_cvt_pk_bf16_f32 v127, v114, v115\n\t"
    "v_mfma_f32_16x16x32_bf16 a[112:115], a[180:183], v[148:151], a[112:115]\n\t"
    "buffer_load_dwordx4 v[204:207], v27, s[36:39], s71 offen offset:1024\n\t"
    "v_mfma_f32_16x16x32_bf16 a[124:127], a[184:187], v[148:151], a[124:127]\n\t"
    "ds_write_b128 v29, a[28:31] offset:12608\n\t"
    "buffer_store_dwordx4 v[124:127], v26, s[48:51], s70 offen offset:1024 sc1 nt\n\t"
    "v_mfma_f32_16x16x32_bf16 a[136:139], a[188:191], v[148:151], a[136:139]\n\t"
    "buffer_load_dwordx4 v[208:211], v26, s[36:39], s72 offen offset:0\n\t"
    "v_mfma_f32_16x16x32_bf16 a[8:11], a[192:195], v[136:139], a[8:11]\n\t"
    "ds_write_b128 v29, a[40:43] offset:12640\n\t"
    "v_mfma_f32_16x16x32_bf16 a[20:23], a[196:199], v[136:139], a[20:23]\n\t"
    "buffer_load_dwordx4 v[212:215], v26, s[36:39], s72 offen offset:1024\n\t"
    "ds_read_b128 v[88:91], v16 offset:0\n\t"
    "ds_read_b128 v[92:95], v16 offset:384\n\t"
    "s_waitcnt vmcnt(13)\n\t"
    "v_lshlrev_b32 v108, 16, v168\n\t"
    "v_and_b32 v109, s64, v168\n\t"
    "v_lshlrev_b32 v110, 16, v169\n\t"
    "v_and_b32 v111, s64, v169\n\t"
    "v_lshlrev_b32 v112, 16, v170\n\t"
    "v_and_b32 v113, s64, v170\n\t"
    "v_lshlrev_b32 v114, 16, v171\n\t"
    "v_and_b32 v115, s64, v171\n\t"
    "v_mfma_f32_16x16x32_bf16 a[32:35], a[200:203], v[136:139], a[32:35]\n\t"
    "ds_write_b128 v29, a[52:55] offset:12672\n\t"
    "v_mfma_f32_16x16x32_bf16 a[44:47], a[204:207], v[136:139], a[44:47]\n\t"
    "buffer_load_dwordx4 v[216:219], v26, s[36:39], s72 offen offset:2048\n\t"
    "v_mfma_f32_16x16x32_bf16 a[56:59], a[208:211], v[136:139], a[56:59]\n\t"
    "ds_write_b128 v29, a[64:67] offset:12704\n\t"
    "s_waitcnt lgkmcnt(2)\n\t"
    "v_fmac_f32 v108, v88, v80\n\t"
    "v_fmac_f32 v109, v89, v81\n\t"
    "v_fmac_f32 v110, v90, v82\n\t"
    "v_fmac_f32 v111, v91, v83\n\t"
    "v_fmac_f32 v112, v92, v84\n\t"
    "v_fmac_f32 v113, v93, v85\n\t"
    "v_fmac_f32 v114, v94, v86\n\t"
    "v_fmac_f32 v115, v95, v87\n\t"
    "v_cvt_pk_bf16_f32 v116, v108, v109\n\t"
    "v_cvt_pk_bf16_f32 v117, v110, v111\n\t"
    "v_mfma_f32_16x16x32_bf16 a[68:71], a[212:215], v[136:139], a[68:71]\n\t"
    "buffer_load_dwordx4 v[220:223], v26, s[36:39], s72 offen offset:3072\n\t"
    "v_cvt_pk_bf16_f32 v118, v112, v113\n\t"
    "v_cvt_pk_bf16_f32 v119, v114, v115\n\t"
    "v_cvt_pk_f16_f32 v32, v104, v105\n\t"
    "v_cvt_pk_f16_f32 v33, v106, v107\n\t"
    "v_pk_fma_f16 v34, v32, v32, s60 neg_lo:[1,0,1] neg_hi:[1,0,1]\n\t"
    "v_pk_fma_f16 v35, v33, v33, s60 neg_lo:[1,0,1] neg_hi:[1,0,1]\n\t"
    "v_pk_mul_f16 v34, v34, v32\n\t"
    "v_pk_mul_f16 v35, v35, v33\n\t"
    "v_exp_f16 v36, v34\n\t"
    "v_mfma_f32_16x16x32_bf16 a[80:83], a[216:219], v[136:139], a[80:83]\n\t"
    "v_exp_f16 v37, v35\n\t"
    "v_exp_f16_sdwa v36, v34 dst_sel:WORD_1 dst_unused:UNUSED_PRESERVE src0_sel:WORD_1\n\t"
    "v_exp_f16_sdwa v37, v35 dst_sel:WORD_1 dst_unused:UNUSED_PRESERVE src0_sel:WORD_1\n\t"
    "v_pk_add_f16 v36, v36, 1.0 op_sel_hi:[1,0]\n\t"
    "v_pk_add_f16 v37, v37, 1.0 op_sel_hi:[1,0]\n\t"
    "v_rcp_f16 v34, v36\n\t"
    "v_mfma_f32_16x16x32_bf16 a[92:95], a[220:223], v[136:139], a[92:95]\n\t"
    "ds_write_b128 v29, a[76:79] offset:12736\n\t"
    "v_rcp_f16 v35, v37\n\t"
    "v_rcp_f16_sdwa v34, v36 dst_sel:WORD_1 dst_unused:UNUSED_PRESERVE src0_sel:WORD_1\n\t"
    "v_rcp_f16_sdwa v35, v37 dst_sel:WORD_1 dst_unused:UNUSED_PRESERVE src0_sel:WORD_1\n\t"
    "v_fma_mix_f32 v104, v104, v34, 0 op_sel_hi:[0,1,0]\n\t"
    "v_fma_mix_f32 v106, v106, v35, 0 op_sel_hi:[0,1,0]\n\t"
    "v_fma_mix_f32 v105, v105, v34, 0 op_sel:[0,1,0] op_sel_hi:[0,1,0]\n\t"
    "v_fma_mix_f32 v107, v107, v35, 0 op_sel:[0,1,0] op_sel_hi:[0,1,0]\n\t"
    "v_cvt_pk_bf16_f32 v152, v104, v105\n\t"
    "v_mfma_f32_16x16x32_bf16 a[104:107], a[224:227], v[136:139], a[104:107]\n\t"
    "buffer_store_dwordx4 v[116:119], v26, s[48:51], s70 offen offset:2048 sc1 nt\n\t"
    "v_cvt_pk_bf16_f32 v153, v106, v107\n\t"
    "v_cvt_pk_f16_f32 v38, v120, v121\n\t"
    "v_cvt_pk_f16_f32 v39, v122, v123\n\t"
    "v_pk_fma_f16 v40, v38, v38, s60 neg_lo:[1,0,1] neg_hi:[1,0,1]\n\t"
    "v_pk_fma_f16 v41, v39, v39, s60 neg_lo:[1,0,1] neg_hi:[1,0,1]\n\t"
    "v_pk_mul_f16 v40, v40, v38\n\t"
    "v_pk_mul_f16 v41, v41, v39\n\t"
    "v_exp_f16 v42, v40\n\t"
    "v_exp_f16 v43, v41\n\t"
    "v_mfma_f32_16x16x32_bf16 a[116:119], a[228:231], v[136:139], a[116:119]\n\t"
    "ds_write_b128 v29, a[88:91] offset:12768\n\t"
    "v_exp_f16_sdwa v42, v40 dst_sel:WORD_1 dst_unused:UNUSED_PRESERVE src0_sel:WORD_1\n\t"
    "v_exp_f16_sdwa v43, v41 dst_sel:WORD_1 dst_unused:UNUSED_PRESERVE src0_sel:WORD_1\n\t"
    "v_pk_add_f16 v42, v42, 1.0 op_sel_hi:[1,0]\n\t"
    "v_pk_add_f16 v43, v43, 1.0 op_sel_hi:[1,0]\n\t"
    "v_rcp_f16 v40, v42\n\t"
    "v_rcp_f16 v41, v43\n\t"
    "v_mfma_f32_16x16x32_bf16 a[128:131], a[232:235], v[136:139], a[128:131]\n\t"
    "v_rcp_f16_sdwa v40, v42 dst_sel:WORD_1 dst_unused:UNUSED_PRESERVE src0_sel:WORD_1\n\t"
    "v_rcp_f16_sdwa v41, v43 dst_sel:WORD_1 dst_unused:UNUSED_PRESERVE src0_sel:WORD_1\n\t"
    "v_fma_mix_f32 v120, v120, v40, 0 op_sel_hi:[0,1,0]\n\t"
    "v_fma_mix_f32 v122, v122, v41, 0 op_sel_hi:[0,1,0]\n\t"
    "v_fma_mix_f32 v121, v121, v40, 0 op_sel:[0,1,0] op_sel_hi:[0,1,0]\n\t"
    "v_fma_mix_f32 v123, v123, v41, 0 op_sel:[0,1,0] op_sel_hi:[0,1,0]\n\t"
    "v_cvt_pk_bf16_f32 v154, v120, v121\n\t"
    "v_cvt_pk_bf16_f32 v155, v122, v123\n\t"
    "v_mfma_f32_16x16x32_bf16 a[140:143], a[236:239], v[136:139], a[140:143]\n\t"
    "ds_write_b128 v29, a[100:103] offset:12800\n\t"
    "ds_read_b128 v[96:99], v14 offset:6272\n\t"
    "ds_read_b128 v[100:103], v14 offset:6656\n\t"
    "s_waitcnt vmcnt(15)\n\t"
    "v_lshlrev_b32 v108, 16, v172\n\t"
    "v_and_b32 v109, s64, v172\n\t"
    "v_lshlrev_b32 v110, 16, v173\n\t"
    "v_mfma_f32_16x16x32_bf16 a[8:11], a[144:147], v[152:155], a[8:11]\n\t"
    "v_and_b32 v111, s64, v173\n\t"
    "v_lshlrev_b32 v112, 16, v174\n\t"
    "v_and_b32 v113, s64, v174\n\t"
    "v_mfma_f32_16x16x32_bf16 a[20:23], a[148:151], v[152:155], a[20:23]\n\t"
    "ds_write_b128 v29, a[112:115] offset:12832\n\t"
    "v_lshlrev_b32 v114, 16, v175\n\t"
    "v_and_b32 v115, s64, v175\n\t"
    "v_mfma_f32_16x16x32_bf16 a[32:35], a[152:155], v[152:155], a[32:35]\n\t"
    "s_waitcnt lgkmcnt(1)\n\t"
    "v_fmac_f32 v108, v96, v64\n\t"
    "v_fmac_f32 v109, v97, v65\n\t"
    "v_fmac_f32 v110, v98, v66\n\t"
    "v_mfma_f32_16x16x32_bf16 a[44:47], a[156:159], v[152:155], a[44:47]\n\t"
    "ds_write_b128 v29, a[124:127] offset:12864\n\t"
    "v_fmac_f32 v111, v99, v67\n\t"
    "v_fmac_f32 v112, v100, v68\n\t"
    "v_fmac_f32 v113, v101, v69\n\t"
    "v_mfma_f32_16x16x32_bf16 a[56:59], a[160:163], v[152:155], a[56:59]\n\t"
    "v_fmac_f32 v114, v102, v70\n\t"
    "v_fmac_f32 v115, v103, v71\n\t"
    "v_cvt_pk_bf16_f32 v124, v108, v109\n\t"
    "v_mfma_f32_16x16x32_bf16 a[68:71], a[164:167], v[152:155], a[68:71]\n\t"
    "ds_write_b128 v29, a[136:139] offset:12896\n\t"
    "v_cvt_pk_bf16_f32 v125, v110, v111\n\t"
    "v_cvt_pk_bf16_f32 v126, v112, v113\n\t"
    "v_cvt_pk_bf16_f32 v127, v114, v115\n\t"
    "v_mfma_f32_16x16x32_bf16 a[80:83], a[168:171], v[152:155], a[80:83]\n\t"
    "buffer_store_dwordx4 v[124:127], v26, s[48:51], s70 offen offset:3072 sc1 nt\n\t"
    "v_mfma_f32_16x16x32_bf16 a[92:95], a[172:175], v[152:155], a[92:95]\n\t"
    "buffer_load_dwordx4 v[224:227], v27, s[36:39], s72 offen offset:0\n\t"
    "v_mfma_f32_16x16x32_bf16 a[104:107], a[176:179], v[152:155], a[104:107]\n\t"
    "v_mfma_f32_16x16x32_bf16 a[116:119], a[180:183], v[152:155], a[116:119]\n\t"
    "buffer_load_dwordx4 v[228:231], v27, s[36:39], s72 offen offset:1024\n\t"
    "v_mfma_f32_16x16x32_bf16 a[128:131], a[184:187], v[152:155], a[128:131]\n\t"
    "v_mfma_f32_16x16x32_bf16 a[140:143], a[188:191], v[152:155], a[140:143]\n\t"
    "ds_read_b128 v[96:99], v15 offset:6272\n\t"
    "ds_read_b128 v[100:103], v15 offset:6656\n\t"
    "ds_read_b128 v[104:107], v16 offset:6272\n\t"
    "ds_read_b128 v[108:111], v16 offset:6656\n\t"
    "ds_read_b128 v[88:91], v14 offset:12544\n\t"
    "ds_read_b128 v[92:95], v14 offset:12928\n\t"
    "ds_write_b128 v29, a[8:11] offset:0\n\t"
    "s_waitcnt vmcnt(17)\n\t"
    "v_lshlrev_b32 v128, 16, v176\n\t"
    "v_and_b32 v129, s64, v176\n\t"
    "v_lshlrev_b32 v130, 16, v177\n\t"
    "v_and_b32 v131, s64, v177\n\t"
    "v_lshlrev_b32 v132, 16, v178\n\t"
    "v_and_b32 v133, s64, v178\n\t"
    "v_lshlrev_b32 v134, 16, v179\n\t"
    "v_and_b32 v135, s64, v179\n\t"
    "ds_write_b128 v29, a[20:23] offset:32\n\t"
    "s_waitcnt lgkmcnt(6)\n\t"
    "v_pk_fma_f32 v[128:129], v[96:97], v[72:73], v[128:129]\n\t"
    "v_pk_fma_f32 v[130:131], v[98:99], v[74:75], v[130:131]\n\t"
    "v_pk_fma_f32 v[132:133], v[100:101], v[76:77], v[132:133]\n\t"
    "v_pk_fma_f32 v[134:135], v[102:103], v[78:79], v[134:135]\n\t"
    "v_cvt_pk_bf16_f32 v112, v128, v129\n\t"
    "v_cvt_pk_bf16_f32 v113, v130, v131\n\t"
    "v_cvt_pk_bf16_f32 v114, v132, v133\n\t"
    "v_cvt_pk_bf16_f32 v115, v134, v135\n\t"
    "buffer_store_dwordx4 v[112:115], v27, s[48:51], s70 offen offset:0 sc1 nt\n\t"
    "ds_read_b128 v[96:99], v15 offset:12544\n\t"
    "ds_read_b128 v[100:103], v15 offset:12928\n\t"
    "ds_write_b128 v29, a[32:35] offset:64\n\t"
    "s_waitcnt vmcnt(17)\n\t"
    "v_lshlrev_b32 v136, 16, v180\n\t"
    "v_and_b32 v137, s64, v180\n\t"
    "v_lshlrev_b32 v138, 16, v181\n\t"
    "v_and_b32 v139, s64, v181\n\t"
    "v_lshlrev_b32 v140, 16, v182\n\t"
    "v_and_b32 v141, s64, v182\n\t"
    "v_lshlrev_b32 v142, 16, v183\n\t"
    "v_and_b32 v143, s64, v183\n\t"
    "ds_write_b128 v29, a[44:47] offset:96\n\t"
    "s_waitcnt lgkmcnt(8)\n\t"
    "v_pk_fma_f32 v[136:137], v[104:105], v[80:81], v[136:137]\n\t"
    "v_pk_fma_f32 v[138:139], v[106:107], v[82:83], v[138:139]\n\t"
    "v_pk_fma_f32 v[140:141], v[108:109], v[84:85], v[140:141]\n\t"
    "v_pk_fma_f32 v[142:143], v[110:111], v[86:87], v[142:143]\n\t"
    "v_cvt_pk_bf16_f32 v116, v136, v137\n\t"
    "v_cvt_pk_bf16_f32 v117, v138, v139\n\t"
    "v_cvt_pk_bf16_f32 v118, v140, v141\n\t"
    "v_cvt_pk_bf16_f32 v119, v142, v143\n\t"
    "buffer_store_dwordx4 v[116:119], v27, s[48:51], s70 offen offset:1024 sc1 nt\n\t"
    "ds_read_b128 v[104:107], v16 offset:12544\n\t"
    "ds_read_b128 v[108:111], v16 offset:12928\n\t"
    "ds_write_b128 v29, a[56:59] offset:128\n\t"
    "s_waitcnt vmcnt(14)\n\t"
    "v_lshlrev_b32 v128, 16, v184\n\t"
    "v_and_b32 v129, s64, v184\n\t"
    "v_lshlrev_b32 v130, 16, v185\n\t"
    "v_and_b32 v131, s64, v185\n\t"
    "v_lshlrev_b32 v132, 16, v186\n\t"
    "v_and_b32 v133, s64, v186\n\t"
    "v_lshlrev_b32 v134, 16, v187\n\t"
    "v_and_b32 v135, s64, v187\n\t"
    "ds_write_b128 v29, a[68:71] offset:160\n\t"
    "s_waitcnt lgkmcnt(10)\n\t"
    "v_pk_fma_f32 v[128:129], v[88:89], v[64:65], v[128:129]\n\t"
    "v_pk_fma_f32 v[130:131], v[90:91], v[66:67], v[130:131]\n\t"
    "v_pk_fma_f32 v[132:133], v[92:93], v[68:69], v[132:133]\n\t"
    "v_pk_fma_f32 v[134:135], v[94:95], v[70:71], v[134:135]\n\t"
    "v_cvt_pk_bf16_f32 v112, v128, v129\n\t"
    "v_cvt_pk_bf16_f32 v113, v130, v131\n\t"
    "v_cvt_pk_bf16_f32 v114, v132, v133\n\t"
    "v_cvt_pk_bf16_f32 v115, v134, v135\n\t"
    "buffer_store_dwordx4 v[112:115], v26, s[48:51], s71 offen offset:0 sc1 nt\n\t"
    "ds_read_b128 v[88:91], v14 offset:18816\n\t"
    "ds_read_b128 v[92:95], v14 offset:19200\n\t"
    "ds_write_b128 v29, a[80:83] offset:192\n\t"
    "v_lshlrev_b32 v136, 16, v188\n\t"
    "v_and_b32 v137, s64, v188\n\t"
    "v_lshlrev_b32 v138, 16, v189\n\t"
    "v_and_b32 v139, s64, v189\n\t"
    "v_lshlrev_b32 v140, 16, v190\n\t"
    "v_and_b32 v141, s64, v190\n\t"
    "v_lshlrev_b32 v142, 16, v191\n\t"
    "v_and_b32 v143, s64, v191\n\t"
    "ds_write_b128 v29, a[92:95] offset:224\n\t"
    "s_waitcnt lgkmcnt(10)\n\t"
    "v_pk_fma_f32 v[136:137], v[96:97], v[72:73], v[136:137]\n\t"
    "v_pk_fma_f32 v[138:139], v[98:99], v[74:75], v[138:139]\n\t"
    "v_pk_fma_f32 v[140:141], v[100:101], v[76:77], v[140:141]\n\t"
    "v_pk_fma_f32 v[142:143], v[102:103], v[78:79], v[142:143]\n\t"
    "v_cvt_pk_bf16_f32 v116, v136, v137\n\t"
    "v_cvt_pk_bf16_f32 v117, v138, v139\n\t"
    "v_cvt_pk_bf16_f32 v118, v140, v141\n\t"
    "v_cvt_pk_bf16_f32 v119, v142, v143\n\t"
    "buffer_store_dwordx4 v[116:119], v26, s[48:51], s71 offen offset:1024 sc1 nt\n\t"
    "ds_read_b128 v[96:99], v15 offset:18816\n\t"
    "ds_read_b128 v[100:103], v15 offset:19200\n\t"
    "ds_write_b128 v29, a[104:107] offset:256\n\t"
    "v_lshlrev_b32 v128, 16, v192\n\t"
    "v_and_b32 v129, s64, v192\n\t"
    "v_lshlrev_b32 v130, 16, v193\n\t"
    "v_and_b32 v131, s64, v193\n\t"
    "v_lshlrev_b32 v132, 16, v194\n\t"
    "v_and_b32 v133, s64, v194\n\t"
    "v_lshlrev_b32 v134, 16, v195\n\t"
    "v_and_b32 v135, s64, v195\n\t"
    "ds_write_b128 v29, a[116:119] offset:288\n\t"
    "s_waitcnt lgkmcnt(10)\n\t"
    "v_pk_fma_f32 v[128:129], v[104:105], v[80:81], v[128:129]\n\t"
    "v_pk_fma_f32 v[130:131], v[106:107], v[82:83], v[130:131]\n\t"
    "v_pk_fma_f32 v[132:133], v[108:109], v[84:85], v[132:133]\n\t"
    "v_pk_fma_f32 v[134:135], v[110:111], v[86:87], v[134:135]\n\t"
    "v_cvt_pk_bf16_f32 v112, v128, v129\n\t"
    "v_cvt_pk_bf16_f32 v113, v130, v131\n\t"
    "v_cvt_pk_bf16_f32 v114, v132, v133\n\t"
    "v_cvt_pk_bf16_f32 v115, v134, v135\n\t"
    "buffer_store_dwordx4 v[112:115], v26, s[48:51], s71 offen offset:2048 sc1 nt\n\t"
    "ds_read_b128 v[104:107], v16 offset:18816\n\t"
    "ds_read_b128 v[108:111], v16 offset:19200\n\t"
    "ds_write_b128 v29, a[128:131] offset:320\n\t"
    "s_waitcnt vmcnt(14)\n\t"
    "v_lshlrev_b32 v136, 16, v196\n\t"
    "v_and_b32 v137, s64, v196\n\t"
    "v_lshlrev_b32 v138, 16, v197\n\t"
    "v_and_b32 v139, s64, v197\n\t"
    "v_lshlrev_b32 v140, 16, v198\n\t"
    "v_and_b32 v141, s64, v198\n\t"
    "v_lshlrev_b32 v142, 16, v199\n\t"
    "v_and_b32 v143, s64, v199\n\t"
    "ds_write_b128 v29, a[140:143] offset:352\n\t"
    "s_waitcnt lgkmcnt(10)\n\t"
    "v_pk_fma_f32 v[136:137], v[88:89], v[64:65], v[136:137]\n\t"
    "v_pk_fma_f32 v[138:139], v[90:91], v[66:67], v[138:139]\n\t"
    "v_pk_fma_f32 v[140:141], v[92:93], v[68:69], v[140:141]\n\t"
    "v_pk_fma_f32 v[142:143], v[94:95], v[70:71], v[142:143]\n\t"
    "v_cvt_pk_bf16_f32 v116, v136, v137\n\t"
    "v_cvt_pk_bf16_f32 v117, v138, v139\n\t"
    "v_cvt_pk_bf16_f32 v118, v140, v141\n\t"
    "v_cvt_pk_bf16_f32 v119, v142, v143\n\t"
    "buffer_store_dwordx4 v[116:119], v26, s[48:51], s71 offen offset:3072 sc1 nt\n\t"
    "ds_read_b128 v[88:91], v14 offset:0\n\t"
    "ds_read_b128 v[92:95], v14 offset:384\n\t"
    "v_lshlrev_b32 v128, 16, v200\n\t"
    "v_and_b32 v129, s64, v200\n\t"
    "v_lshlrev_b32 v130, 16, v201\n\t"
    "v_and_b32 v131, s64, v201\n\t"
    "v_lshlrev_b32 v132, 16, v202\n\t"
    "v_and_b32 v133, s64, v202\n\t"
    "v_lshlrev_b32 v134, 16, v203\n\t"
    "v_and_b32 v135, s64, v203\n\t"
    "s_waitcnt lgkmcnt(8)\n\t"
    "v_pk_fma_f32 v[128:129], v[96:97], v[72:73], v[128:129]\n\t"
    "v_pk_fma_f32 v[130:131], v[98:99], v[74:75], v[130:131]\n\t"
    "v_pk_fma_f32 v[132:133], v[100:101], v[76:77], v[132:133]\n\t"
    "v_pk_fma_f32 v[134:135], v[102:103], v[78:79], v[134:135]\n\t"
    "v_cvt_pk_bf16_f32 v112, v128, v129\n\t"
    "v_cvt_pk_bf16_f32 v113, v130, v131\n\t"
    "v_cvt_pk_bf16_f32 v114, v132, v133\n\t"
    "v_cvt_pk_bf16_f32 v115, v134, v135\n\t"
    "buffer_store_dwordx4 v[112:115], v27, s[48:51], s71 offen offset:0 sc1 nt\n\t"
    "ds_read_b128 v[96:99], v15 offset:0\n\t"
    "ds_read_b128 v[100:103], v15 offset:384\n\t"
    "v_lshlrev_b32 v136, 16, v204\n\t"
    "v_and_b32 v137, s64, v204\n\t"
    "v_lshlrev_b32 v138, 16, v205\n\t"
    "v_and_b32 v139, s64, v205\n\t"
    "v_lshlrev_b32 v140, 16, v206\n\t"
    "v_and_b32 v141, s64, v206\n\t"
    "v_lshlrev_b32 v142, 16, v207\n\t"
    "v_and_b32 v143, s64, v207\n\t"
    "s_waitcnt lgkmcnt(6)\n\t"
    "v_pk_fma_f32 v[136:137], v[104:105], v[80:81], v[136:137]\n\t"
    "v_pk_fma_f32 v[138:139], v[106:107], v[82:83], v[138:139]\n\t"
    "v_pk_fma_f32 v[140:141], v[108:109], v[84:85], v[140:141]\n\t"
    "v_pk_fma_f32 v[142:143], v[110:111], v[86:87], v[142:143]\n\t"
    "v_cvt_pk_bf16_f32 v116, v136, v137\n\t"
    "v_cvt_pk_bf16_f32 v117, v138, v139\n\t"
    "v_cvt_pk_bf16_f32 v118, v140, v141\n\t"
    "v_cvt_pk_bf16_f32 v119, v142, v143\n\t"
    "buffer_store_dwordx4 v[116:119], v27, s[48:51], s71 offen offset:1024 sc1 nt\n\t"
    "ds_read_b128 v[104:107], v16 offset:0\n\t"
    "ds_read_b128 v[108:111], v16 offset:384\n\t"
    "s_waitcnt vmcnt(13)\n\t"
    "v_lshlrev_b32 v128, 16, v208\n\t"
    "v_and_b32 v129, s64, v208\n\t"
    "v_lshlrev_b32 v130, 16, v209\n\t"
    "v_and_b32 v131, s64, v209\n\t"
    "v_lshlrev_b32 v132, 16, v210\n\t"
    "v_and_b32 v133, s64, v210\n\t"
    "v_lshlrev_b32 v134, 16, v211\n\t"
    "v_and_b32 v135, s64, v211\n\t"
    "s_waitcnt lgkmcnt(4)\n\t"
    "v_pk_fma_f32 v[128:129], v[88:89], v[64:65], v[128:129]\n\t"
    "v_pk_fma_f32 v[130:131], v[90:91], v[66:67], v[130:131]\n\t"
    "v_pk_fma_f32 v[132:133], v[92:93], v[68:69], v[132:133]\n\t"
    "v_pk_fma_f32 v[134:135], v[94:95], v[70:71], v[134:135]\n\t"
    "v_cvt_pk_bf16_f32 v112, v128, v129\n\t"
    "v_cvt_pk_bf16_f32 v113, v130, v131\n\t"
    "v_cvt_pk_bf16_f32 v114, v132, v133\n\t"
    "v_cvt_pk_bf16_f32 v115, v134, v135\n\t"
    "buffer_store_dwordx4 v[112:115], v26, s[48:51], s72 offen offset:0 sc1 nt\n\t"
    "ds_read_b128 v[88:91], v14 offset:6272\n\t"
    "ds_read_b128 v[92:95], v14 offset:6656\n\t"
    "v_lshlrev_b32 v136, 16, v212\n\t"
    "v_and_b32 v137, s64, v212\n\t"
    "v_lshlrev_b32 v138, 16, v213\n\t"
    "v_and_b32 v139, s64, v213\n\t"
    "v_lshlrev_b32 v140, 16, v214\n\t"
    "v_and_b32 v141, s64, v214\n\t"
    "v_lshlrev_b32 v142, 16, v215\n\t"
    "v_and_b32 v143, s64, v215\n\t"
    "s_waitcnt lgkmcnt(4)\n\t"
    "v_pk_fma_f32 v[136:137], v[96:97], v[72:73], v[136:137]\n\t"
    "v_pk_fma_f32 v[138:139], v[98:99], v[74:75], v[138:139]\n\t"
    "v_pk_fma_f32 v[140:141], v[100:101], v[76:77], v[140:141]\n\t"
    "v_pk_fma_f32 v[142:143], v[102:103], v[78:79], v[142:143]\n\t"
    "v_cvt_pk_bf16_f32 v116, v136, v137\n\t"
    "v_cvt_pk_bf16_f32 v117, v138, v139\n\t"
    "v_cvt_pk_bf16_f32 v118, v140, v141\n\t"
    "v_cvt_pk_bf16_f32 v119, v142, v143\n\t"
    "buffer_store_dwordx4 v[116:119], v26, s[48:51], s72 offen offset:1024 sc1 nt\n\t"
    "ds_read_b128 v[96:99], v15 offset:6272\n\t"
    "ds_read_b128 v[100:103], v15 offset:6656\n\t"
    "v_lshlrev_b32 v128, 16, v216\n\t"
    "v_and_b32 v129, s64, v216\n\t"
    "v_lshlrev_b32 v130, 16, v217\n\t"
    "v_and_b32 v131, s64, v217\n\t"
    "v_lshlrev_b32 v132, 16, v218\n\t"
    "v_and_b32 v133, s64, v218\n\t"
    "v_lshlrev_b32 v134, 16, v219\n\t"
    "v_and_b32 v135, s64, v219\n\t"
    "s_waitcnt lgkmcnt(4)\n\t"
    "v_pk_fma_f32 v[128:129], v[104:105], v[80:81], v[128:129]\n\t"
    "v_pk_fma_f32 v[130:131], v[106:107], v[82:83], v[130:131]\n\t"
    "v_pk_fma_f32 v[132:133], v[108:109], v[84:85], v[132:133]\n\t"
    "v_pk_fma_f32 v[134:135], v[110:111], v[86:87], v[134:135]\n\t"
    "v_cvt_pk_bf16_f32 v112, v128, v129\n\t"
    "v_cvt_pk_bf16_f32 v113, v130, v131\n\t"
    "v_cvt_pk_bf16_f32 v114, v132, v133\n\t"
    "v_cvt_pk_bf16_f32 v115, v134, v135\n\t"
    "buffer_store_dwordx4 v[112:115], v26, s[48:51], s72 offen offset:2048 sc1 nt\n\t"
    "ds_read_b128 v[104:107], v16 offset:6272\n\t"
    "ds_read_b128 v[108:111], v16 offset:6656\n\t"
    "s_waitcnt vmcnt(11)\n\t"
    "v_lshlrev_b32 v136, 16, v220\n\t"
    "v_and_b32 v137, s64, v220\n\t"
    "v_lshlrev_b32 v138, 16, v221\n\t"
    "v_and_b32 v139, s64, v221\n\t"
    "v_lshlrev_b32 v140, 16, v222\n\t"
    "v_and_b32 v141, s64, v222\n\t"
    "v_lshlrev_b32 v142, 16, v223\n\t"
    "v_and_b32 v143, s64, v223\n\t"
    "s_waitcnt lgkmcnt(4)\n\t"
    "v_pk_fma_f32 v[136:137], v[88:89], v[64:65], v[136:137]\n\t"
    "v_pk_fma_f32 v[138:139], v[90:91], v[66:67], v[138:139]\n\t"
    "v_pk_fma_f32 v[140:141], v[92:93], v[68:69], v[140:141]\n\t"
    "v_pk_fma_f32 v[142:143], v[94:95], v[70:71], v[142:143]\n\t"
    "v_cvt_pk_bf16_f32 v116, v136, v137\n\t"
    "v_cvt_pk_bf16_f32 v117, v138, v139\n\t"
    "v_cvt_pk_bf16_f32 v118, v140, v141\n\t"
    "v_cvt_pk_bf16_f32 v119, v142, v143\n\t"
    "buffer_store_dwordx4 v[116:119], v26, s[48:51], s72 offen offset:3072 sc1 nt\n\t"
    "v_lshlrev_b32 v128, 16, v224\n\t"
    "v_and_b32 v129, s64, v224\n\t"
    "v_lshlrev_b32 v130, 16, v225\n\t"
    "v_and_b32 v131, s64, v225\n\t"
    "v_lshlrev_b32 v132, 16, v226\n\t"
    "v_and_b32 v133, s64, v226\n\t"
    "v_lshlrev_b32 v134, 16, v227\n\t"
    "v_and_b32 v135, s64, v227\n\t"
    "s_waitcnt lgkmcnt(2)\n\t"
    "v_pk_fma_f32 v[128:129], v[96:97], v[72:73], v[128:129]\n\t"
    "v_pk_fma_f32 v[130:131], v[98:99], v[74:75], v[130:131]\n\t"
    "v_pk_fma_f32 v[132:133], v[100:101], v[76:77], v[132:133]\n\t"
    "v_pk_fma_f32 v[134:135], v[102:103], v[78:79], v[134:135]\n\t"
    "v_cvt_pk_bf16_f32 v112, v128, v129\n\t"
    "v_cvt_pk_bf16_f32 v113, v130, v131\n\t"
    "v_cvt_pk_bf16_f32 v114, v132, v133\n\t"
    "v_cvt_pk_bf16_f32 v115, v134, v135\n\t"
    "buffer_store_dwordx4 v[112:115], v27, s[48:51], s72 offen offset:0 sc1 nt\n\t"
    "v_lshlrev_b32 v136, 16, v228\n\t"
    "v_and_b32 v137, s64, v228\n\t"
    "v_lshlrev_b32 v138, 16, v229\n\t"
    "v_and_b32 v139, s64, v229\n\t"
    "v_lshlrev_b32 v140, 16, v230\n\t"
    "v_and_b32 v141, s64, v230\n\t"
    "v_lshlrev_b32 v142, 16, v231\n\t"
    "v_and_b32 v143, s64, v231\n\t"
    "s_waitcnt lgkmcnt(0)\n\t"
    "v_pk_fma_f32 v[136:137], v[104:105], v[80:81], v[136:137]\n\t"
    "v_pk_fma_f32 v[138:139], v[106:107], v[82:83], v[138:139]\n\t"
    "v_pk_fma_f32 v[140:141], v[108:109], v[84:85], v[140:141]\n\t"
    "v_pk_fma_f32 v[142:143], v[110:111], v[86:87], v[142:143]\n\t"
    "v_cvt_pk_bf16_f32 v116, v136, v137\n\t"
    "v_cvt_pk_bf16_f32 v117, v138, v139\n\t"
    "v_cvt_pk_bf16_f32 v118, v140, v141\n\t"
    "v_cvt_pk_bf16_f32 v119, v142, v143\n\t"
    "buffer_store_dwordx4 v[116:119], v27, s[48:51], s72 offen offset:1024 sc1 nt\n\t"
    "end_a_%=:\n\t"
    :: "s"(__builtin_amdgcn_kernarg_segment_ptr()), "s"(blockIdx.x), "s"(wid)
    : "v0", "v1", "v2", "v3", "v4", "v5", "v6", "v7", "v8", "v9", "v10", "v11", "v12", "v13", "v14", "v15", "v16", "v17", "v18", "v19", "v20", "v21", "v22", "v23", "v24", "v25", "v26", "v27", "v28", "v29", "v30", "v31", "v32", "v33", "v34", "v35", "v36", "v37", "v38", "v39", "v40", "v41", "v42", "v43", "v44", "v45", "v46", "v47", "v48", "v49", "v50", "v51", "v52", "v53", "v54", "v55", "v56", "v57", "v58", "v59", "v60", "v61", "v62", "v63", "v64", "v65", "v66", "v67", "v68", "v69", "v70", "v71", "v72", "v73", "v74", "v75", "v76", "v77", "v78", "v79", "v80", "v81", "v82", "v83", "v84", "v85", "v86", "v87", "v88", "v89", "v90", "v91", "v92", "v93", "v94", "v95", "v96", "v97", "v98", "v99", "v100", "v101", "v102", "v103", "v104", "v105", "v106", "v107", "v108", "v109", "v110", "v111", "v112", "v113", "v114", "v115", "v116", "v117", "v118", "v119", "v120", "v121", "v122", "v123", "v124", "v125", "v126", "v127", "v128", "v129", "v130", "v131", "v132", "v133", "v134", "v135", "v136", "v137", "v138", "v139", "v140", "v141", "v142", "v143", "v144", "v145", "v146", "v147", "v148", "v149", "v150", "v151", "v152", "v153", "v154", "v155", "v156", "v157", "v158", "v159", "v160", "v161", "v162", "v163", "v164", "v165", "v166", "v167", "v168", "v169", "v170", "v171", "v172", "v173", "v174", "v175", "v176", "v177", "v178", "v179", "v180", "v181", "v182", "v183", "v184", "v185", "v186", "v187", "v188", "v189", "v190", "v191", "v192", "v193", "v194", "v195", "v196", "v197", "v198", "v199", "v200", "v201", "v202", "v203", "v204", "v205", "v206", "v207", "v208", "v209", "v210", "v211", "v212", "v213", "v214", "v215", "v216", "v217", "v218", "v219", "v220", "v221", "v222", "v223", "v224", "v225", "v226", "v227", "v228", "v229", "v230", "v231", "v232", "v233", "v234", "v235", "v236", "v237", "v238", "v239", "v240", "v241", "v242", "v243", "v244", "v245", "v246", "v247", "v248", "v249", "v250", "v251", "v252", "v253", "v254", "v255", "a0", "a1", "a2", "a3", "a4", "a5", "a6", "a7", "a8", "a9", "a10", "a11", "a12", "a13", "a14", "a15", "a16", "a17", "a18", "a19", "a20", "a21", "a22", "a23", "a24", "a25", "a26", "a27", "a28", "a29", "a30", "a31", "a32", "a33", "a34", "a35", "a36", "a37", "a38", "a39", "a40", "a41", "a42", "a43", "a44", "a45", "a46", "a47", "a48", "a49", "a50", "a51", "a52", "a53", "a54", "a55", "a56", "a57", "a58", "a59", "a60", "a61", "a62", "a63", "a64", "a65", "a66", "a67", "a68", "a69", "a70", "a71", "a72", "a73", "a74", "a75", "a76", "a77", "a78", "a79", "a80", "a81", "a82", "a83", "a84", "a85", "a86", "a87", "a88", "a89", "a90", "a91", "a92", "a93", "a94", "a95", "a96", "a97", "a98", "a99", "a100", "a101", "a102", "a103", "a104", "a105", "a106", "a107", "a108", "a109", "a110", "a111", "a112", "a113", "a114", "a115", "a116", "a117", "a118", "a119", "a120", "a121", "a122", "a123", "a124", "a125", "a126", "a127", "a128", "a129", "a130", "a131", "a132", "a133", "a134", "a135", "a136", "a137", "a138", "a139", "a140", "a141", "a142", "a143", "a144", "a145", "a146", "a147", "a148", "a149", "a150", "a151", "a152", "a153", "a154", "a155", "a156", "a157", "a158", "a159", "a160", "a161", "a162", "a163", "a164", "a165", "a166", "a167", "a168", "a169", "a170", "a171", "a172", "a173", "a174", "a175", "a176", "a177", "a178", "a179", "a180", "a181", "a182", "a183", "a184", "a185", "a186", "a187", "a188", "a189", "a190", "a191", "a192", "a193", "a194", "a195", "a196", "a197", "a198", "a199", "a200", "a201", "a202", "a203", "a204", "a205", "a206", "a207", "a208", "a209", "a210", "a211", "a212", "a213", "a214", "a215", "a216", "a217", "a218", "a219", "a220", "a221", "a222", "a223", "a224", "a225", "a226", "a227", "a228", "a229", "a230", "a231", "a232", "a233", "a234", "a235", "a236", "a237", "a238", "a239", "a240", "a241", "a242", "a243", "a244", "a245", "a246", "a247", "a248", "a249", "a250", "a251", "a252", "a253", "a254", "a255", "s24", "s25", "s26", "s27", "s28", "s29", "s30", "s31", "s32", "s33", "s34", "s35", "s36", "s37", "s38", "s39", "s40", "s41", "s42", "s43", "s44", "s45", "s46", "s47", "s48", "s49", "s50", "s51", "s52", "s53", "s54", "s55", "s56", "s57", "s58", "s59", "s60", "s61", "s62", "s63", "s64", "s65", "s66", "s67", "s68", "s69", "s70", "s71", "s72", "s73", "s74", "s75", "s76", "s77", "s78", "s79", "s80", "s81", "s82", "s83", "s84", "s85", "s86", "s87", "s88", "s89", "s90", "s91", "s92", "s93", "s94", "s95", "s96", "s97", "s98", "s99", "vcc", "scc", "memory");
}
extern "C" void kernel_launch(void* const* d_in, const int* in_sizes, int n_in, void* d_out, int out_size, void* d_ws, size_t ws_size, hipStream_t stream) {
  convnext_block_kernel<<<256, 256, 0, stream>>>(d_in[0], d_in[1], d_in[2], d_in[3], d_in[4], d_in[5], d_in[6], d_out);
}
```
